```python
import torch

def _softmax(x, axis=-1):
    return torch.softmax(x, dim=axis)
def _gelu(x):
    return torch.nn.functional.gelu(x, approximate="tanh")
def _silu(x):
    return torch.nn.functional.silu(x)
def _sigmoid(x):
    return torch.sigmoid(x)
def _layernorm(x, axis=-1, eps=1e-5):
    m = x.mean(dim=axis, keepdim=True)
    v = x.var(dim=axis, keepdim=True, unbiased=False)
    return (x - m) / torch.sqrt(v + eps)
def _scatter_add(tgt, idx, src, axis=0):
    return tgt.clone().index_add_(axis, idx, src)
def _segment_sum(src, seg, n):
    out = torch.zeros((n,) + src.shape[1:], dtype=src.dtype)
    return out.index_add_(0, seg, src)
def _pad(x, pw):
    flat = tuple(v for pair in reversed(pw) for v in pair)
    return torch.nn.functional.pad(x, flat)

kernel_name = 'recurrent_scan__mha_block_cached__glu_gated_swiglu__prompt'

def setup_inputs():

    _w = (lambda a: (lambda t: t.to(torch.bfloat16) if t.is_floating_point() else t)(torch.as_tensor(a)))
    def _A_setup():
        B, Sq, D = 1, 2048, 576
        H, Sk, Dh = 32, 2048, 128
        return {
            "x": _w((lambda *s: torch.randn(*s, dtype=torch.bfloat16) * 0.05)(B, Sq, D)),
            "k_cache": _w((lambda *s: torch.randn(*s, dtype=torch.bfloat16) * 1.6)(B, H, Sk, Dh)),
            "v_cache": _w((lambda *s: torch.randn(*s, dtype=torch.bfloat16) * 1.6)(B, H, Sk, Dh)),
            "wq": _w((lambda *s: torch.randn(*s, dtype=torch.bfloat16) * 1.6)(D, H * Dh) * (D ** -0.5)),
            "wo": _w((lambda *s: torch.randn(*s, dtype=torch.bfloat16) * 1.6)(H * Dh, D) * ((H * Dh) ** -0.5)),
        }
    _A_in = _A_setup()

    def _B_setup():
        B, S, D, F = 1, 2048, 576, 4608
        return {
            "x": _w((lambda *s: torch.randn(*s, dtype=torch.bfloat16) * 0.05)(B, S, D)),
            "w1": _w((lambda *s: torch.randn(*s, dtype=torch.bfloat16) * 0.2)(D, F) * (2.0 / D) ** 0.5),
            "w3": _w((lambda *s: torch.randn(*s, dtype=torch.bfloat16) * 0.2)(D, F) * (2.0 / D) ** 0.5),
            "w2": _w((lambda *s: torch.randn(*s, dtype=torch.bfloat16) * 0.2)(F, D) * (2.0 / F) ** 0.5),
        }
    _B_in = _B_setup()

    return {
        "A_x": _A_in["x"],
        "A_k_cache": _A_in["k_cache"],
        "A_v_cache": _A_in["v_cache"],
        "A_wq": _A_in["wq"],
        "A_wo": _A_in["wo"],
        "B_x": _B_in["x"],
        "B_w1": _B_in["w1"],
        "B_w3": _B_in["w3"],
        "B_w2": _B_in["w2"],
    }

def reference(A_x, A_k_cache, A_v_cache, A_wq, A_wo, B_x, B_w1, B_w3, B_w2):
    def _A_ref(x, k_cache, v_cache, wq, wo):
        B, Sq, D = x.shape
        H, Dh = 32, 128
        q = (lambda x, *d: x.permute(*d))((x @ wq).reshape(B, Sq, H, Dh), 0, 2, 1, 3)
        scores = torch.einsum("bhqd,bhkd->bhqk", q, k_cache) * (Dh ** -0.5)
        p = _softmax(scores, axis=-1)
        o = torch.einsum("bhqk,bhkd->bhqd", p, v_cache)
        return (lambda x, *d: x.permute(*d))(o, 0, 2, 1, 3).reshape(B, Sq, H * Dh) @ wo

    def _B_ref(x, w1, w3, w2):
        gate = torch.einsum("bsd,df->bsf", x, w1)
        up = torch.einsum("bsd,df->bsf", x, w3)
        h = _silu(gate) * up
        out = torch.einsum("bsf,fd->bsd", h, w2)
        return out

    x = A_x
    xn = _layernorm(x, axis=-1)
    s = _A_ref(xn, A_k_cache, A_v_cache, A_wq, A_wo)
    g = _silu(_B_ref(xn, B_w1, B_w3, B_w2))
    return x + s * g
```

```cpp
#include <hip/hip_runtime.h>
#include <hip/hip_bf16.h>
typedef unsigned short u16;
typedef short bf16x8 __attribute__((ext_vector_type(8)));
typedef float f32x4 __attribute__((ext_vector_type(4)));
__device__ __forceinline__ float bf2f(u16 x){ return __uint_as_float(((unsigned)x)<<16); }
__device__ __forceinline__ u16 f2bf(float f){ unsigned u=__float_as_uint(f); u += 0x7FFFu + ((u>>16)&1u); return (u16)(u>>16); }
__device__ __forceinline__ float rbf(float f){ return bf2f(f2bf(f)); }

__global__ void ln_k(const u16* x, u16* xn){
  int row = blockIdx.x, lane = threadIdx.x;
  float v[9]; float s=0;
  for(int i=0;i<9;i++){ v[i]=bf2f(x[row*576+lane+64*i]); s+=v[i]; }
  for(int o=32;o;o>>=1) s+=__shfl_xor(s,o);
  float m=s/576.f; float q=0;
  for(int i=0;i<9;i++){ float d=v[i]-m; q+=d*d; }
  for(int o=32;o;o>>=1) q+=__shfl_xor(q,o);
  float var=rbf(q/576.f); float mb=rbf(m);
  float sd=rbf(sqrtf(rbf(var+1e-5f)));
  for(int i=0;i<9;i++){ xn[row*576+lane+64*i]=f2bf(rbf(v[i]-mb)/sd); }
}
template<int MODE>
__global__ void gemm_k(const u16* A, int lda, const u16* B, int ldb, u16* C, int ldc, int K){
  __shared__ u16 As[64][40]; __shared__ u16 Bs[64][40];
  int tid=threadIdx.x, w=tid>>6, lane=tid&63, wr=w>>1, wc=w&1;
  int m0=blockIdx.y*64, n0=blockIdx.x*64;
  f32x4 acc[2][2]={};
  for(int k0=0;k0<K;k0+=32){
    for(int i=tid;i<64*32;i+=256){ int r=i>>5,c=i&31; As[r][c]=A[(long)(m0+r)*lda+k0+c]; }
    for(int i=tid;i<64*32;i+=256){ int k=i>>6,n=i&63; Bs[n][k]=B[(long)(k0+k)*ldb+n0+n]; }
    __syncthreads();
    bf16x8 a[2],b[2];
    for(int i=0;i<2;i++){ a[i]=*(bf16x8*)&As[wr*32+i*16+(lane&15)][(lane>>4)*8]; b[i]=*(bf16x8*)&Bs[wc*32+i*16+(lane&15)][(lane>>4)*8]; }
    for(int i=0;i<2;i++)for(int j=0;j<2;j++) acc[i][j]=__builtin_amdgcn_mfma_f32_16x16x32_bf16(a[i],b[j],acc[i][j],0,0,0);
    __syncthreads();
  }
  for(int i=0;i<2;i++)for(int j=0;j<2;j++)for(int r=0;r<4;r++)
    C[(long)(m0+wr*32+i*16+(lane>>4)*4+r)*ldc+n0+wc*32+j*16+(lane&15)]=f2bf(acc[i][j][r]);
}
__global__ void attn_k(const u16* Q, const u16* Kc, const u16* Vc, u16* O){
  __shared__ float S[16][2048]; __shared__ float qs[16][128];
  int h=blockIdx.y, q0=blockIdx.x*16, tid=threadIdx.x;
  for(int i=tid;i<16*128;i+=256) qs[i>>7][i&127]=bf2f(Q[(long)(q0+(i>>7))*4096+h*128+(i&127)]);
  __syncthreads();
  for(int k=tid;k<2048;k+=256){
    float a[16]={};
    const u16* kr=Kc+((long)h*2048+k)*128;
    for(int d=0;d<128;d++){ float kv=bf2f(kr[d]);
      #pragma unroll
      for(int r=0;r<16;r++) a[r]+=qs[r][d]*kv; }
    #pragma unroll
    for(int r=0;r<16;r++) S[r][k]=a[r]*0.08838834764831845f;
  }
  __syncthreads();
  int w=tid>>6,lane=tid&63;
  for(int r=w;r<16;r+=4){
    float m=-1e30f; for(int k=lane;k<2048;k+=64) m=fmaxf(m,S[r][k]);
    for(int o=32;o;o>>=1) m=fmaxf(m,__shfl_xor(m,o));
    float s=0; for(int k=lane;k<2048;k+=64){ float e=__expf(S[r][k]-m); S[r][k]=e; s+=e; }
    for(int o=32;o;o>>=1) s+=__shfl_xor(s,o);
    float inv=1.f/s; for(int k=lane;k<2048;k+=64) S[r][k]=rbf(S[r][k]*inv);
  }
  __syncthreads();
  int d=tid&127, rh=tid>>7;
  float a[8]={};
  for(int k=0;k<2048;k++){ float v=bf2f(Vc[((long)h*2048+k)*128+d]);
    #pragma unroll
    for(int r=0;r<8;r++) a[r]+=S[rh*8+r][k]*v; }
  for(int r=0;r<8;r++) O[(long)(q0+rh*8+r)*4096+h*128+d]=f2bf(a[r]);
}
__global__ void swiglu_k(const u16* g,const u16* u,u16* h,int n){ int i=blockIdx.x*256+threadIdx.x; if(i<n){ float x=bf2f(g[i]); float s=rbf(x/(1.f+__expf(-x))); h[i]=f2bf(s*bf2f(u[i])); } }
__global__ void final_k(const u16* x,const u16* s,const u16* b,u16* o,int n){ int i=blockIdx.x*256+threadIdx.x; if(i<n){ float y=bf2f(b[i]); float g=rbf(y/(1.f+__expf(-y))); o[i]=f2bf(bf2f(x[i])+rbf(bf2f(s[i])*g)); } }

extern "C" void kernel_launch(void* const* d_in, const int* in_sizes, int n_in, void* d_out, int out_size, void* d_ws, size_t ws_size, hipStream_t stream){
  const u16* x=(const u16*)d_in[0]; const u16* kc=(const u16*)d_in[1]; const u16* vc=(const u16*)d_in[2];
  const u16* wq=(const u16*)d_in[3]; const u16* wo=(const u16*)d_in[4];
  const u16* w1=(const u16*)d_in[6]; const u16* w3=(const u16*)d_in[7]; const u16* w2=(const u16*)d_in[8];
  u16* ws=(u16*)d_ws; u16* xn=ws; u16* q=xn+2048*576; u16* o=q+2048*4096; u16* g=o+2048*4096; u16* u=g+2048*4608; u16* h=u+2048*4608; u16* s=h+2048*4608; u16* b=s+2048*576;
  ln_k<<<2048,64,0,stream>>>(x,xn);
  gemm_k<0><<<dim3(64,32),256,0,stream>>>(xn,576,wq,4096,q,4096,576);
  gemm_k<0><<<dim3(72,32),256,0,stream>>>(xn,576,w1,4608,g,4608,576);
  gemm_k<0><<<dim3(72,32),256,0,stream>>>(xn,576,w3,4608,u,4608,576);
  attn_k<<<dim3(128,32),256,0,stream>>>(q,kc,vc,o);
  swiglu_k<<<2048*4608/256,256,0,stream>>>(g,u,h,2048*4608);
  gemm_k<0><<<dim3(9,32),256,0,stream>>>(o,4096,wo,576,s,576,4096);
  gemm_k<0><<<dim3(9,32),256,0,stream>>>(h,4608,w2,576,b,576,4608);
  final_k<<<2048*576/256,256,0,stream>>>(x,s,b,(u16*)d_out,2048*576);
}
```

```cpp
#include <hip/hip_runtime.h>
#include <hip/hip_bf16.h>
typedef unsigned short u16;
typedef short bf16x8 __attribute__((ext_vector_type(8)));
typedef float f32x4 __attribute__((ext_vector_type(4)));
__device__ __forceinline__ float bf2f(u16 x){ return __uint_as_float(((unsigned)x)<<16); }
__device__ __forceinline__ u16 f2bf(float f){ unsigned u=__float_as_uint(f); u += 0x7FFFu + ((u>>16)&1u); return (u16)(u>>16); }
__device__ __forceinline__ float rbf(float f){ return bf2f(f2bf(f)); }

__global__ void ln_k(const u16* x, u16* xn){
  int row = blockIdx.x, lane = threadIdx.x;
  float v[9]; float s=0;
  for(int i=0;i<9;i++){ v[i]=bf2f(x[row*576+lane+64*i]); s+=v[i]; }
  for(int o=32;o;o>>=1) s+=__shfl_xor(s,o);
  float m=s/576.f; float q=0;
  for(int i=0;i<9;i++){ float d=v[i]-m; q+=d*d; }
  for(int o=32;o;o>>=1) q+=__shfl_xor(q,o);
  float var=rbf(q/576.f); float mb=rbf(m);
  float sd=rbf(sqrtf(rbf(var+1e-5f)));
  for(int i=0;i<9;i++){ xn[row*576+lane+64*i]=f2bf(rbf(v[i]-mb)/sd); }
}
template<int MODE>
__global__ void gemm_k(const u16* A, int lda, const u16* B, int ldb, u16* C, int ldc, int K){
  __shared__ u16 As[64][40]; __shared__ u16 Bs[64][40];
  int tid=threadIdx.x, w=tid>>6, lane=tid&63, wr=w>>1, wc=w&1;
  int m0=blockIdx.y*64, n0=blockIdx.x*64;
  f32x4 acc[2][2]={};
  for(int k0=0;k0<K;k0+=32){
    for(int i=tid;i<64*32;i+=256){ int r=i>>5,c=i&31; As[r][c]=A[(long)(m0+r)*lda+k0+c]; }
    for(int i=tid;i<64*32;i+=256){ int k=i>>6,n=i&63; Bs[n][k]=B[(long)(k0+k)*ldb+n0+n]; }
    __syncthreads();
    bf16x8 a[2],b[2];
    for(int i=0;i<2;i++){ a[i]=*(bf16x8*)&As[wr*32+i*16+(lane&15)][(lane>>4)*8]; b[i]=*(bf16x8*)&Bs[wc*32+i*16+(lane&15)][(lane>>4)*8]; }
    for(int i=0;i<2;i++)for(int j=0;j<2;j++) acc[i][j]=__builtin_amdgcn_mfma_f32_16x16x32_bf16(a[i],b[j],acc[i][j],0,0,0);
    __syncthreads();
  }
  for(int i=0;i<2;i++)for(int j=0;j<2;j++)for(int r=0;r<4;r++)
    C[(long)(m0+wr*32+i*16+(lane>>4)*4+r)*ldc+n0+wc*32+j*16+(lane&15)]=f2bf(acc[i][j][r]);
}
__global__ void attn_k(const u16* Q, const u16* Kc, const u16* Vc, u16* O){
  __shared__ float S[16][2048]; __shared__ float qs[16][128];
  int h=blockIdx.y, q0=blockIdx.x*16, tid=threadIdx.x;
  for(int i=tid;i<16*128;i+=256) qs[i>>7][i&127]=bf2f(Q[(long)(q0+(i>>7))*4096+h*128+(i&127)]);
  __syncthreads();
  for(int k=tid;k<2048;k+=256){
    float a[16]={};
    const u16* kr=Kc+((long)h*2048+k)*128;
    for(int d=0;d<128;d++){ float kv=bf2f(kr[d]);
      #pragma unroll
      for(int r=0;r<16;r++) a[r]+=qs[r][d]*kv; }
    #pragma unroll
    for(int r=0;r<16;r++) S[r][k]=a[r]*0.08838834764831845f;
  }
  __syncthreads();
  int w=tid>>6,lane=tid&63;
  for(int r=w;r<16;r+=4){
    float m=-1e30f; for(int k=lane;k<2048;k+=64) m=fmaxf(m,S[r][k]);
    for(int o=32;o;o>>=1) m=fmaxf(m,__shfl_xor(m,o));
    float s=0; for(int k=lane;k<2048;k+=64){ float e=__expf(S[r][k]-m); S[r][k]=e; s+=e; }
    for(int o=32;o;o>>=1) s+=__shfl_xor(s,o);
    float inv=1.f/s; for(int k=lane;k<2048;k+=64) S[r][k]=rbf(S[r][k]*inv);
  }
  __syncthreads();
  int d=tid&127, rh=tid>>7;
  float a[8]={};
  for(int k=0;k<2048;k++){ float v=bf2f(Vc[((long)h*2048+k)*128+d]);
    #pragma unroll
    for(int r=0;r<8;r++) a[r]+=S[rh*8+r][k]*v; }
  for(int r=0;r<8;r++) O[(long)(q0+rh*8+r)*4096+h*128+d]=f2bf(a[r]);
}
__global__ void swiglu_k(const u16* g,const u16* u,u16* h,int n){ int i=blockIdx.x*256+threadIdx.x; if(i<n){ float x=bf2f(g[i]); float s=rbf(x/(1.f+__expf(-x))); h[i]=f2bf(s*bf2f(u[i])); } }
__global__ void final_k(const u16* x,const u16* s,const u16* b,u16* o,int n){ int i=blockIdx.x*256+threadIdx.x; if(i<n){ float y=bf2f(b[i]); float g=rbf(y/(1.f+__expf(-y))); o[i]=f2bf(bf2f(x[i])+rbf(bf2f(s[i])*g)); } }

static void body(void* const* d_in, void* d_out, void* d_ws, hipStream_t stream){
  const u16* x=(const u16*)d_in[0]; const u16* kc=(const u16*)d_in[1]; const u16* vc=(const u16*)d_in[2];
  const u16* wq=(const u16*)d_in[3]; const u16* wo=(const u16*)d_in[4];
  const u16* w1=(const u16*)d_in[6]; const u16* w3=(const u16*)d_in[7]; const u16* w2=(const u16*)d_in[8];
  u16* ws=(u16*)d_ws; u16* xn=ws; u16* q=xn+2048*576; u16* o=q+2048*4096; u16* g=o+2048*4096; u16* u=g+2048*4608; u16* h=u+2048*4608; u16* s=h+2048*4608; u16* b=s+2048*576;
  ln_k<<<2048,64,0,stream>>>(x,xn);
  gemm_k<0><<<dim3(64,32),256,0,stream>>>(xn,576,wq,4096,q,4096,576);
  gemm_k<0><<<dim3(72,32),256,0,stream>>>(xn,576,w1,4608,g,4608,576);
  gemm_k<0><<<dim3(72,32),256,0,stream>>>(xn,576,w3,4608,u,4608,576);
  attn_k<<<dim3(128,32),256,0,stream>>>(q,kc,vc,o);
  swiglu_k<<<2048*4608/256,256,0,stream>>>(g,u,h,2048*4608);
  gemm_k<0><<<dim3(9,32),256,0,stream>>>(o,4096,wo,576,s,576,4096);
  gemm_k<0><<<dim3(9,32),256,0,stream>>>(h,4608,w2,576,b,576,4608);
  final_k<<<2048*576/256,256,0,stream>>>(x,s,b,(u16*)d_out,2048*576);
}

typedef float f32x16 __attribute__((ext_vector_type(16)));
__global__ __launch_bounds__(256,1) void probe_k(const u16* src, unsigned* res, int n){
  bf16x8 a=*(const bf16x8*)(src+ (blockIdx.x*256+threadIdx.x)*8), b=*(const bf16x8*)(src+ 600000+(blockIdx.x*256+threadIdx.x)*8);
  f32x16 c0={},c1={},c2={},c3={};
  unsigned long long t0=__builtin_amdgcn_s_memrealtime();
  for(int i=0;i<n;i++){
    c0=__builtin_amdgcn_mfma_f32_32x32x16_bf16(a,b,c0,0,0,0);
    c1=__builtin_amdgcn_mfma_f32_32x32x16_bf16(b,a,c1,0,0,0);
    c2=__builtin_amdgcn_mfma_f32_32x32x16_bf16(a,a,c2,0,0,0);
    c3=__builtin_amdgcn_mfma_f32_32x32x16_bf16(b,b,c3,0,0,0);
  }
  float s=0; for(int i=0;i<16;i++) s+=c0[i]+c1[i]+c2[i]+c3[i];
  asm volatile("" :: "v"(s));
  unsigned long long t1=__builtin_amdgcn_s_memrealtime();
  if(threadIdx.x==0) res[blockIdx.x]=(unsigned)(t1-t0);
}
__global__ void delay_k(const unsigned* res, int mult){
  unsigned T=res[5];
  unsigned long long t0=__builtin_amdgcn_s_memrealtime();
  unsigned long long lim=(unsigned long long)T*mult; if(lim>3000000ull) lim=3000000ull;
  while(__builtin_amdgcn_s_memrealtime()-t0<lim) __builtin_amdgcn_s_sleep(1);
}
extern "C" void kernel_launch(void* const* d_in, const int* in_sizes, int n_in, void* d_out, int out_size, void* d_ws, size_t ws_size, hipStream_t stream){
  unsigned* res=(unsigned*)((char*)d_ws+200000000);
  probe_k<<<256,256,0,stream>>>((const u16*)d_in[1],res,1000);
  body(d_in,d_out,d_ws,stream);
  delay_k<<<1,64,0,stream>>>(res,0);
}
```

```cpp
#define PMODE 0
#include <hip/hip_runtime.h>
#include <hip/hip_bf16.h>
typedef unsigned short u16;
typedef short bf16x8 __attribute__((ext_vector_type(8)));
typedef float f32x4 __attribute__((ext_vector_type(4)));
__device__ __forceinline__ float bf2f(u16 x){ return __uint_as_float(((unsigned)x)<<16); }
__device__ __forceinline__ u16 f2bf(float f){ unsigned u=__float_as_uint(f); u += 0x7FFFu + ((u>>16)&1u); return (u16)(u>>16); }
__device__ __forceinline__ float rbf(float f){ return bf2f(f2bf(f)); }

__global__ void ln_k(const u16* x, u16* xn){
  int row = blockIdx.x, lane = threadIdx.x;
  float v[9]; float s=0;
  for(int i=0;i<9;i++){ v[i]=bf2f(x[row*576+lane+64*i]); s+=v[i]; }
  for(int o=32;o;o>>=1) s+=__shfl_xor(s,o);
  float m=s/576.f; float q=0;
  for(int i=0;i<9;i++){ float d=v[i]-m; q+=d*d; }
  for(int o=32;o;o>>=1) q+=__shfl_xor(q,o);
  float var=rbf(q/576.f); float mb=rbf(m);
  float sd=rbf(sqrtf(rbf(var+1e-5f)));
  for(int i=0;i<9;i++){ xn[row*576+lane+64*i]=f2bf(rbf(v[i]-mb)/sd); }
}
template<int MODE>
__global__ void gemm_k(const u16* A, int lda, const u16* B, int ldb, u16* C, int ldc, int K){
  __shared__ u16 As[64][40]; __shared__ u16 Bs[64][40];
  int tid=threadIdx.x, w=tid>>6, lane=tid&63, wr=w>>1, wc=w&1;
  int m0=blockIdx.y*64, n0=blockIdx.x*64;
  f32x4 acc[2][2]={};
  for(int k0=0;k0<K;k0+=32){
    for(int i=tid;i<64*32;i+=256){ int r=i>>5,c=i&31; As[r][c]=A[(long)(m0+r)*lda+k0+c]; }
    for(int i=tid;i<64*32;i+=256){ int k=i>>6,n=i&63; Bs[n][k]=B[(long)(k0+k)*ldb+n0+n]; }
    __syncthreads();
    bf16x8 a[2],b[2];
    for(int i=0;i<2;i++){ a[i]=*(bf16x8*)&As[wr*32+i*16+(lane&15)][(lane>>4)*8]; b[i]=*(bf16x8*)&Bs[wc*32+i*16+(lane&15)][(lane>>4)*8]; }
    for(int i=0;i<2;i++)for(int j=0;j<2;j++) acc[i][j]=__builtin_amdgcn_mfma_f32_16x16x32_bf16(a[i],b[j],acc[i][j],0,0,0);
    __syncthreads();
  }
  for(int i=0;i<2;i++)for(int j=0;j<2;j++)for(int r=0;r<4;r++)
    C[(long)(m0+wr*32+i*16+(lane>>4)*4+r)*ldc+n0+wc*32+j*16+(lane&15)]=f2bf(acc[i][j][r]);
}
__global__ void attn_k(const u16* Q, const u16* Kc, const u16* Vc, u16* O){
  __shared__ float S[16][2048]; __shared__ float qs[16][128];
  int h=blockIdx.y, q0=blockIdx.x*16, tid=threadIdx.x;
  for(int i=tid;i<16*128;i+=256) qs[i>>7][i&127]=bf2f(Q[(long)(q0+(i>>7))*4096+h*128+(i&127)]);
  __syncthreads();
  for(int k=tid;k<2048;k+=256){
    float a[16]={};
    const u16* kr=Kc+((long)h*2048+k)*128;
    for(int d=0;d<128;d++){ float kv=bf2f(kr[d]);
      #pragma unroll
      for(int r=0;r<16;r++) a[r]+=qs[r][d]*kv; }
    #pragma unroll
    for(int r=0;r<16;r++) S[r][k]=a[r]*0.08838834764831845f;
  }
  __syncthreads();
  int w=tid>>6,lane=tid&63;
  for(int r=w;r<16;r+=4){
    float m=-1e30f; for(int k=lane;k<2048;k+=64) m=fmaxf(m,S[r][k]);
    for(int o=32;o;o>>=1) m=fmaxf(m,__shfl_xor(m,o));
    float s=0; for(int k=lane;k<2048;k+=64){ float e=__expf(S[r][k]-m); S[r][k]=e; s+=e; }
    for(int o=32;o;o>>=1) s+=__shfl_xor(s,o);
    float inv=1.f/s; for(int k=lane;k<2048;k+=64) S[r][k]=rbf(S[r][k]*inv);
  }
  __syncthreads();
  int d=tid&127, rh=tid>>7;
  float a[8]={};
  for(int k=0;k<2048;k++){ float v=bf2f(Vc[((long)h*2048+k)*128+d]);
    #pragma unroll
    for(int r=0;r<8;r++) a[r]+=S[rh*8+r][k]*v; }
  for(int r=0;r<8;r++) O[(long)(q0+rh*8+r)*4096+h*128+d]=f2bf(a[r]);
}
__global__ void swiglu_k(const u16* g,const u16* u,u16* h,int n){ int i=blockIdx.x*256+threadIdx.x; if(i<n){ float x=bf2f(g[i]); float s=rbf(x/(1.f+__expf(-x))); h[i]=f2bf(s*bf2f(u[i])); } }
__global__ void final_k(const u16* x,const u16* s,const u16* b,u16* o,int n){ int i=blockIdx.x*256+threadIdx.x; if(i<n){ float y=bf2f(b[i]); float g=rbf(y/(1.f+__expf(-y))); o[i]=f2bf(bf2f(x[i])+rbf(bf2f(s[i])*g)); } }

static void body(void* const* d_in, void* d_out, void* d_ws, hipStream_t stream){
  const u16* x=(const u16*)d_in[0]; const u16* kc=(const u16*)d_in[1]; const u16* vc=(const u16*)d_in[2];
  const u16* wq=(const u16*)d_in[3]; const u16* wo=(const u16*)d_in[4];
  const u16* w1=(const u16*)d_in[6]; const u16* w3=(const u16*)d_in[7]; const u16* w2=(const u16*)d_in[8];
  u16* ws=(u16*)d_ws; u16* xn=ws; u16* q=xn+2048*576; u16* o=q+2048*4096; u16* g=o+2048*4096; u16* u=g+2048*4608; u16* h=u+2048*4608; u16* s=h+2048*4608; u16* b=s+2048*576;
  ln_k<<<2048,64,0,stream>>>(x,xn);
  gemm_k<0><<<dim3(64,32),256,0,stream>>>(xn,576,wq,4096,q,4096,576);
  gemm_k<0><<<dim3(72,32),256,0,stream>>>(xn,576,w1,4608,g,4608,576);
  gemm_k<0><<<dim3(72,32),256,0,stream>>>(xn,576,w3,4608,u,4608,576);
  attn_k<<<dim3(128,32),256,0,stream>>>(q,kc,vc,o);
  swiglu_k<<<2048*4608/256,256,0,stream>>>(g,u,h,2048*4608);
  gemm_k<0><<<dim3(9,32),256,0,stream>>>(o,4096,wo,576,s,576,4096);
  gemm_k<0><<<dim3(9,32),256,0,stream>>>(h,4608,w2,576,b,576,4608);
  final_k<<<2048*576/256,256,0,stream>>>(x,s,b,(u16*)d_out,2048*576);
}

typedef float f32x16 __attribute__((ext_vector_type(16)));
__global__ __launch_bounds__(256,1) void probe_k(const u16* src, unsigned* res, int n){
  bf16x8 a=*(const bf16x8*)(src+ (blockIdx.x*256+threadIdx.x)*8), b=*(const bf16x8*)(src+ 600000+(blockIdx.x*256+threadIdx.x)*8);
  asm volatile("s_waitcnt vmcnt(0)":"+v"(a),"+v"(b));
  unsigned long long t0=__builtin_amdgcn_s_memrealtime();
  unsigned long long c0=__builtin_amdgcn_s_memtime();
  while(__builtin_amdgcn_s_memtime()-c0 < 128000ull) {}
  unsigned long long t1=__builtin_amdgcn_s_memrealtime();
  if(threadIdx.x==0) res[blockIdx.x]=(unsigned)(t1-t0);
}
__global__ void delay_k(const unsigned* res, int mult){
  unsigned T=res[5];
  unsigned long long t0=__builtin_amdgcn_s_memrealtime();
  unsigned long long lim=(unsigned long long)T*mult; if(lim>3000000ull) lim=3000000ull;
  while(__builtin_amdgcn_s_memrealtime()-t0<lim) __builtin_amdgcn_s_sleep(1);
}
extern "C" void kernel_launch(void* const* d_in, const int* in_sizes, int n_in, void* d_out, int out_size, void* d_ws, size_t ws_size, hipStream_t stream){
  unsigned* res=(unsigned*)((char*)d_ws+200000000);
  probe_k<<<256,256,0,stream>>>((const u16*)d_in[1],res,1000);
  body(d_in,d_out,d_ws,stream);
  delay_k<<<1,64,0,stream>>>(res,100);
}
```

```cpp
#include <hip/hip_runtime.h>
#include <hip/hip_bf16.h>

__global__ __launch_bounds__(256, 1) void fused_block_kernel(const void* x, void* ws, const void* w1, const void* w3, const void* wq, const void* kc, const void* vc, void* out,
                                                           const void* wo, const void* w2) {
  __shared__ __attribute__((aligned(1024))) char lds[163840];
  const unsigned wid = __builtin_amdgcn_readfirstlane(threadIdx.x >> 6);
  const unsigned ldsb = (unsigned)(size_t)lds;
  asm volatile(
"L_start_%=:\n"
"s_getpc_b64 s[16:17]\n"
"s_load_dwordx16 s[52:67], %[ka], 0x0\n"
"s_load_dwordx4 s[20:23], %[ka], 0x40\n"
"v_mbcnt_lo_u32_b32 v0, -1, 0\n"
"v_mbcnt_hi_u32_b32 v0, -1, v0\n"
"s_mov_b32 s72, %[wid]\n"
"s_and_b32 s70, %[blk], 7\n"
"s_lshr_b32 s78, %[blk], 3\n"
"s_mov_b32 s75, s78\n"
"s_lshr_b32 s101, s70, 1\n"
"v_lshlrev_b32 v1, 4, v0\n"
"s_lshl_b32 s96, s78, 2\n"
"s_add_u32 s96, s96, s72\n"
"s_lshl_b32 s96, s96, 10\n"
"s_and_b32 s17, s17, 0xffff\n"
"s_mov_b32 s18, L_end_%=-L_start_%=-16\n"
"s_mov_b32 s19, 0x20000\n"
"s_and_b32 s71, s78, 3\n"
"s_lshr_b32 s69, s78, 2\n"
"s_lshl_b32 s68, s70, 2\n"
"s_add_u32 s68, s68, s71\n"
"s_lshl_b32 s73, s69, 2\n"
"s_add_u32 s73, s73, s72\n"
"v_lshrrev_b32 v8, 5, v0\n"
"v_and_b32 v9, 31, v0\n"
"v_mul_u32_u24 v9, 12, v9\n"
"v_mul_u32_u24 v10, 1152, v8\n"
"v_add_u32 v10, v10, v9\n"
"s_lshl_b32 s78, s69, 5\n"
"s_add_u32 s78, s78, s68\n"
"s_lshl_b32 s78, s78, 2\n"
"s_add_u32 s78, s78, s72\n"
"s_mul_i32 s78, s78, 2304\n"
"v_cmp_eq_u32 vcc, 0, v0\n"
"v_bfrev_b32 v14, 1\n"
"s_waitcnt lgkmcnt(0)\n"
"v_cndmask_b32_e64 v14, v14, 0, vcc\n"
"s_mov_b32 s44, s52\n"
"s_and_b32 s45, s53, 0xffff\n"
"s_mov_b32 s46, 2359296\n"
"s_mov_b32 s47, 0x20000\n"
"buffer_load_dwordx3 v[16:18], v10, s[44:47], s78 offen\n"
"buffer_load_dwordx3 v[20:22], v10, s[44:47], s78 offen offset:384\n"
"buffer_load_dwordx3 v[24:26], v10, s[44:47], s78 offen offset:768\n"
"s_mov_b32 s40, s54\n"
"s_and_b32 s41, s55, 0xffff\n"
"s_mov_b32 s42, 79822848\n"
"s_mov_b32 s43, 0x20000\n"
"s_lshl_b32 s78, s69, 7\n"
"s_add_u32 s78, s78, 79691776\n"
"s_lshl_b32 s79, s101, 7\n"
"s_add_u32 s79, s79, 79695872\n"
"buffer_load_dword v28, v14, s[40:43], s78 offen sc1\n"
"buffer_load_dword v29, v14, s[40:43], s79 offen sc1\n"
"buffer_load_dwordx4 v[228:231], v1, s[16:19], s96 offen\n"
"s_mul_i32 s78, s72, 2336\n"
"v_mul_u32_u24 v30, 1168, v8\n"
"v_add3_u32 v30, v30, v9, s78\n"
"s_mov_b32 s28, s60\n"
"s_and_b32 s29, s61, 0xffff\n"
"s_mov_b32 s30, 4718592\n"
"s_mov_b32 s31, 0x20000\n"
"v_lshrrev_b32 v11, 1, v0\n"
"v_and_b32 v15, 1, v0\n"
"v_min_u32 v226, 17, v11\n"
"v_lshlrev_b32 v226, 13, v226\n"
"v_lshl_add_u32 v226, v15, 7, v226\n"
"s_mul_i32 s89, s73, 147456\n"
"s_lshl_b32 s77, s68, 8\n"
"s_add_u32 s89, s89, s77\n"
"buffer_load_dword v227, v226, s[28:31], s89 offen\n"
"v_lshrrev_b32 v8, 4, v0\n"
"v_and_b32 v10, 3, v0\n"
"v_bfe_u32 v11, v0, 2, 3\n"
"v_and_b32 v15, 1, v8\n"
"v_lshlrev_b32 v7, 1, v15\n"
"v_xor_b32 v7, v10, v7\n"
"v_lshlrev_b32 v7, 4, v7\n"
"v_lshl_add_u32 v7, v11, 13, v7\n"
"v_lshrrev_b32 v11, 5, v0\n"
"v_lshl_add_u32 v7, v11, 6, v7\n"
"s_lshl_b32 s74, s72, 10\n"
"s_lshl_b32 s77, s68, 8\n"
"s_lshl_b32 s78, s72, 16\n"
"s_add_u32 s77, s77, s78\n"
"v_bfe_u32 v9, v0, 2, 2\n"
"v_mul_u32_u24 v2, 288, v15\n"
"v_lshl_add_u32 v2, v9, 6, v2\n"
"v_lshl_add_u32 v2, v10, 3, v2\n"
"v_lshrrev_b32 v15, 1, v8\n"
"v_lshl_add_u32 v2, v15, 10, v2\n"
"v_add_u32 v12, 90112, v2\n"
"v_xor_b32 v13, 32, v12\n"
"v_add_u32 v2, 53248, v2\n"
"v_xor_b32 v3, 32, v2\n"
"v_and_b32 v8, 7, v0\n"
"v_bfe_u32 v9, v0, 3, 2\n"
"v_lshrrev_b32 v10, 5, v0\n"
"v_mul_u32_u24 v31, 1168, v8\n"
"v_lshl_add_u32 v31, v9, 3, v31\n"
"v_lshl_add_u32 v31, v10, 6, v31\n"
"s_lshl_b32 s78, s72, 7\n"
"v_add_u32 v31, s78, v31\n"
"v_lshlrev_b32 v19, 4, v8\n"
"v_lshl_add_u32 v19, v9, 8, v19\n"
"v_lshl_add_u32 v19, v10, 12, v19\n"
"s_lshl_b32 s79, s69, 2\n"
"s_add_u32 s78, s101, s79\n"
"s_mul_i32 s85, s78, 73728\n"
"s_bfe_u32 s78, s68, 0x20001\n"
"s_lshl_b32 s78, s78, 10\n"
"s_add_u32 s85, s85, s78\n"
"s_and_b32 s78, s68, 1\n"
"s_lshl_b32 s78, s78, 7\n"
"s_add_u32 s85, s85, s78\n"
"s_lshl_b32 s78, s72, 13\n"
"s_add_u32 s85, s85, s78\n"
"s_add_u32 s85, s85, 0\n"
"s_waitcnt vmcnt(6)\n"
"v_lshlrev_b32 v32, 16, v16\n"
"v_and_b32 v33, 0xffff0000, v16\n"
"v_lshlrev_b32 v34, 16, v17\n"
"v_and_b32 v35, 0xffff0000, v17\n"
"v_lshlrev_b32 v36, 16, v18\n"
"v_and_b32 v37, 0xffff0000, v18\n"
"s_waitcnt vmcnt(5)\n"
"v_lshlrev_b32 v38, 16, v20\n"
"v_and_b32 v39, 0xffff0000, v20\n"
"v_lshlrev_b32 v40, 16, v21\n"
"v_and_b32 v41, 0xffff0000, v21\n"
"v_lshlrev_b32 v42, 16, v22\n"
"v_and_b32 v43, 0xffff0000, v22\n"
"s_waitcnt vmcnt(4)\n"
"v_lshlrev_b32 v44, 16, v24\n"
"v_and_b32 v45, 0xffff0000, v24\n"
"v_lshlrev_b32 v46, 16, v25\n"
"v_and_b32 v47, 0xffff0000, v25\n"
"v_lshlrev_b32 v48, 16, v26\n"
"v_and_b32 v49, 0xffff0000, v26\n"
"v_pk_add_f32 v[70:71], v[32:33], v[34:35]\n"
"v_pk_add_f32 v[72:73], v[36:37], v[38:39]\n"
"v_pk_add_f32 v[70:71], v[70:71], v[40:41]\n"
"v_pk_add_f32 v[72:73], v[72:73], v[42:43]\n"
"v_pk_add_f32 v[70:71], v[70:71], v[44:45]\n"
"v_pk_add_f32 v[72:73], v[72:73], v[46:47]\n"
"v_pk_add_f32 v[70:71], v[70:71], v[48:49]\n"
"v_pk_add_f32 v[70:71], v[70:71], v[72:73]\n"
"v_add_f32 v70, v70, v71\n"
"s_nop 1\n"
"v_add_f32_dpp v70, v70, v70 quad_perm:[1,0,3,2] row_mask:0xf bank_mask:0xf\n"
"s_nop 1\n"
"v_add_f32_dpp v70, v70, v70 quad_perm:[2,3,0,1] row_mask:0xf bank_mask:0xf\n"
"s_nop 1\n"
"v_add_f32_dpp v70, v70, v70 row_half_mirror row_mask:0xf bank_mask:0xf\n"
"s_nop 1\n"
"v_add_f32_dpp v70, v70, v70 row_mirror row_mask:0xf bank_mask:0xf\n"
"v_mov_b32 v71, v70\n"
"s_nop 1\n"
"v_permlane16_swap_b32 v70, v71\n"
"v_add_f32 v70, v70, v71\n"
"v_mul_f32 v68, 0xbae38e39, v70\n"
"v_mov_b32 v69, v68\n"
"v_pk_add_f32 v[50:51], v[32:33], v[68:69]\n"
"v_pk_add_f32 v[52:53], v[34:35], v[68:69]\n"
"v_pk_add_f32 v[54:55], v[36:37], v[68:69]\n"
"v_pk_add_f32 v[56:57], v[38:39], v[68:69]\n"
"v_pk_add_f32 v[58:59], v[40:41], v[68:69]\n"
"v_pk_add_f32 v[60:61], v[42:43], v[68:69]\n"
"v_pk_add_f32 v[62:63], v[44:45], v[68:69]\n"
"v_pk_add_f32 v[64:65], v[46:47], v[68:69]\n"
"v_pk_add_f32 v[66:67], v[48:49], v[68:69]\n"
"v_pk_mul_f32 v[70:71], v[50:51], v[50:51]\n"
"v_pk_mul_f32 v[72:73], v[52:53], v[52:53]\n"
"v_pk_fma_f32 v[70:71], v[54:55], v[54:55], v[70:71]\n"
"v_pk_fma_f32 v[72:73], v[56:57], v[56:57], v[72:73]\n"
"v_pk_fma_f32 v[70:71], v[58:59], v[58:59], v[70:71]\n"
"v_pk_fma_f32 v[72:73], v[60:61], v[60:61], v[72:73]\n"
"v_pk_fma_f32 v[70:71], v[62:63], v[62:63], v[70:71]\n"
"v_pk_fma_f32 v[72:73], v[64:65], v[64:65], v[72:73]\n"
"v_pk_fma_f32 v[70:71], v[66:67], v[66:67], v[70:71]\n"
"v_cvt_pk_bf16_f32 v68, v68, v68\n"
"v_lshlrev_b32 v68, 16, v68\n"
"v_mov_b32 v69, v68\n"
"v_pk_add_f32 v[70:71], v[70:71], v[72:73]\n"
"v_add_f32 v70, v70, v71\n"
"s_nop 1\n"
"v_add_f32_dpp v70, v70, v70 quad_perm:[1,0,3,2] row_mask:0xf bank_mask:0xf\n"
"s_nop 1\n"
"v_add_f32_dpp v70, v70, v70 quad_perm:[2,3,0,1] row_mask:0xf bank_mask:0xf\n"
"s_nop 1\n"
"v_add_f32_dpp v70, v70, v70 row_half_mirror row_mask:0xf bank_mask:0xf\n"
"s_nop 1\n"
"v_add_f32_dpp v70, v70, v70 row_mirror row_mask:0xf bank_mask:0xf\n"
"v_mov_b32 v71, v70\n"
"s_nop 1\n"
"v_permlane16_swap_b32 v70, v71\n"
"v_add_f32 v70, v70, v71\n"
"v_pk_add_f32 v[50:51], v[32:33], v[68:69]\n"
"v_pk_add_f32 v[52:53], v[34:35], v[68:69]\n"
"v_pk_add_f32 v[54:55], v[36:37], v[68:69]\n"
"v_pk_add_f32 v[56:57], v[38:39], v[68:69]\n"
"v_pk_add_f32 v[58:59], v[40:41], v[68:69]\n"
"v_pk_add_f32 v[60:61], v[42:43], v[68:69]\n"
"v_pk_add_f32 v[62:63], v[44:45], v[68:69]\n"
"v_pk_add_f32 v[64:65], v[46:47], v[68:69]\n"
"v_pk_add_f32 v[66:67], v[48:49], v[68:69]\n"
"v_mul_f32 v70, 0x3ae38e39, v70\n"
"v_cvt_pk_bf16_f32 v70, v70, v70\n"
"v_lshlrev_b32 v70, 16, v70\n"
"v_add_f32 v70, 0x3727c5ac, v70\n"
"v_cvt_pk_bf16_f32 v70, v70, v70\n"
"v_lshlrev_b32 v70, 16, v70\n"
"v_sqrt_f32 v70, v70\n"
"s_nop 0\n"
"v_cvt_pk_bf16_f32 v70, v70, v70\n"
"v_lshlrev_b32 v70, 16, v70\n"
"v_rcp_f32 v70, v70\n"
"v_cvt_pk_bf16_f32 v32, v50, v51\n"
"v_cvt_pk_bf16_f32 v33, v52, v53\n"
"v_cvt_pk_bf16_f32 v34, v54, v55\n"
"v_cvt_pk_bf16_f32 v35, v56, v57\n"
"v_cvt_pk_bf16_f32 v36, v58, v59\n"
"v_cvt_pk_bf16_f32 v37, v60, v61\n"
"v_cvt_pk_bf16_f32 v38, v62, v63\n"
"v_cvt_pk_bf16_f32 v39, v64, v65\n"
"v_cvt_pk_bf16_f32 v40, v66, v67\n"
"v_mov_b32 v71, v70\n"
"v_lshlrev_b32 v50, 16, v32\n"
"v_and_b32 v51, 0xffff0000, v32\n"
"v_lshlrev_b32 v52, 16, v33\n"
"v_and_b32 v53, 0xffff0000, v33\n"
"v_lshlrev_b32 v54, 16, v34\n"
"v_and_b32 v55, 0xffff0000, v34\n"
"v_lshlrev_b32 v56, 16, v35\n"
"v_and_b32 v57, 0xffff0000, v35\n"
"v_lshlrev_b32 v58, 16, v36\n"
"v_and_b32 v59, 0xffff0000, v36\n"
"v_lshlrev_b32 v60, 16, v37\n"
"v_and_b32 v61, 0xffff0000, v37\n"
"v_lshlrev_b32 v62, 16, v38\n"
"v_and_b32 v63, 0xffff0000, v38\n"
"v_lshlrev_b32 v64, 16, v39\n"
"v_and_b32 v65, 0xffff0000, v39\n"
"v_lshlrev_b32 v66, 16, v40\n"
"v_and_b32 v67, 0xffff0000, v40\n"
"v_pk_mul_f32 v[50:51], v[50:51], v[70:71]\n"
"v_pk_mul_f32 v[52:53], v[52:53], v[70:71]\n"
"v_pk_mul_f32 v[54:55], v[54:55], v[70:71]\n"
"v_pk_mul_f32 v[56:57], v[56:57], v[70:71]\n"
"v_pk_mul_f32 v[58:59], v[58:59], v[70:71]\n"
"v_pk_mul_f32 v[60:61], v[60:61], v[70:71]\n"
"v_pk_mul_f32 v[62:63], v[62:63], v[70:71]\n"
"v_pk_mul_f32 v[64:65], v[64:65], v[70:71]\n"
"v_pk_mul_f32 v[66:67], v[66:67], v[70:71]\n"
"v_cvt_pk_bf16_f32 v32, v50, v51\n"
"v_cvt_pk_bf16_f32 v33, v52, v53\n"
"v_cvt_pk_bf16_f32 v34, v54, v55\n"
"v_cvt_pk_bf16_f32 v35, v56, v57\n"
"v_cvt_pk_bf16_f32 v36, v58, v59\n"
"v_cvt_pk_bf16_f32 v37, v60, v61\n"
"v_cvt_pk_bf16_f32 v38, v62, v63\n"
"v_cvt_pk_bf16_f32 v39, v64, v65\n"
"v_cvt_pk_bf16_f32 v40, v66, v67\n"
"ds_write2_b32 v30, v32, v33 offset0:0 offset1:1\n"
"ds_write_b32 v30, v34 offset:8\n"
"ds_write2_b32 v30, v35, v36 offset0:96 offset1:97\n"
"ds_write_b32 v30, v37 offset:392\n"
"ds_write2_b32 v30, v38, v39 offset0:192 offset1:193\n"
"ds_write_b32 v30, v40 offset:776\n"
"s_waitcnt vmcnt(3)\n"
"v_readfirstlane_b32 s98, v28\n"
"s_waitcnt vmcnt(2)\n"
"v_readfirstlane_b32 s99, v29\n"
"s_waitcnt lgkmcnt(0)\n"
"s_barrier\n"
"ds_read2_b64 v[32:35], v31 offset0:0 offset1:4\n"
"ds_read2_b64 v[36:39], v31 offset0:64 offset1:68\n"
"ds_read2_b64 v[40:43], v31 offset0:128 offset1:132\n"
"s_mul_i32 s92, s98, 0x9e3779b1\n"
"s_add_u32 s92, s92, 0x7f4a7c15\n"
"s_mul_i32 s93, s98, 0x85ebca6b\n"
"s_add_u32 s93, s93, 0x2545f491\n"
"s_mul_i32 s94, s99, 0x9e3779b1\n"
"s_add_u32 s94, s94, 0x7f4a7c15\n"
"s_mul_i32 s95, s99, 0x85ebca6b\n"
"s_add_u32 s95, s95, 0x2545f491\n"
"s_lshl_b32 s79, s72, 28\n"
"s_waitcnt lgkmcnt(2)\n"
"buffer_store_dwordx4 v[32:35], v19, s[40:43], s85 offen sc1\n"
"s_add_u32 s85, s85, 32768\n"
"s_waitcnt lgkmcnt(1)\n"
"buffer_store_dwordx4 v[36:39], v19, s[40:43], s85 offen sc1\n"
"s_add_u32 s85, s85, 32768\n"
"s_or_b32 s85, s85, s79\n"
"s_waitcnt lgkmcnt(0)\n"
"buffer_store_dwordx4 v[40:43], v19, s[40:43], s85 offen sc1\n"
"s_add_u32 s85, s85, 32768\n"
"s_lshl_b32 s80, s68, 19\n"
"s_add_u32 s78, s62, s80\n"
"s_addc_u32 s79, s63, 0\n"
"s_mov_b32 s32, s78\n"
"s_and_b32 s33, s79, 0xffff\n"
"s_mov_b32 s34, 524288\n"
"s_mov_b32 s35, 0x20000\n"
"s_add_u32 s78, s64, s80\n"
"s_addc_u32 s79, s65, 0\n"
"s_sub_u32 s78, s78, 896\n"
"s_subb_u32 s79, s79, 0\n"
"s_mov_b32 s36, s78\n"
"s_and_b32 s37, s79, 0xffff\n"
"s_mov_b32 s38, 525184\n"
"s_mov_b32 s39, 0x20000\n"
"s_cmp_ge_u32 s72, 2\n"
"s_cselect_b32 s78, s58, s56\n"
"s_cselect_b32 s79, s59, s57\n"
"s_mov_b32 s24, s78\n"
"s_and_b32 s25, s79, 0xffff\n"
"s_mov_b32 s26, 5308416\n"
"s_mov_b32 s27, 0x20000\n"
"s_and_b32 s79, s68, 1\n"
"s_mul_i32 s83, s73, 144\n"
"s_mul_i32 s81, s101, 36\n"
"s_add_u32 s83, s83, s81\n"
"s_bfe_u32 s81, s68, 0x20001\n"
"s_add_u32 s91, s83, s81\n"
"s_and_b32 s81, s68, 7\n"
"s_add_u32 s83, s83, s81\n"
"s_lshl_b32 s83, s83, 12\n"
"s_add_u32 s83, s83, 2359296\n"
"s_lshl_b32 s91, s91, 12\n"
"s_lshl_b32 s81, s79, 3\n"
"s_add_u32 s91, s91, s81\n"
"s_add_u32 s91, s91, 2424832\n"
"s_mov_b32 s48, s66\n"
"s_and_b32 s49, s67, 0xffff\n"
"s_mov_b32 s50, 2359296\n"
"s_mov_b32 s51, 0x20000\n"
"s_xor_b32 s67, s70, s69\n"
"s_and_b32 s67, s67, 1\n"
"s_and_b32 s78, s70, 1\n"
"s_cselect_b32 s78, s20, s22\n"
"s_cselect_b32 s79, s21, s23\n"
"s_cselect_b32 s80, 8, 9\n"
"s_mul_i32 s80, s80, 589824\n"
"s_mov_b32 s60, s78\n"
"s_and_b32 s61, s79, 0xffff\n"
"s_mov_b32 s62, s80\n"
"s_mov_b32 s63, 0x20000\n"
"s_lshl_b32 s100, s67, 2\n"
"s_add_u32 s100, s100, s101\n"
"s_lshl_b32 s66, s72, 12\n"
"s_mov_b32 s86, 0xbfb8aa3b\n"
"s_mov_b32 s87, 0x3e0293ee\n"
"s_mov_b32 s22, 0\n"
"s_add_u32 m0, s74, 53248\n"
"s_add_u32 s82, s77, 0\n"
"buffer_load_dwordx4 v7, s[28:31], s82 offen lds\n"
"s_add_u32 m0, s74, 90112\n"
"s_add_u32 s82, s77, 128\n"
"buffer_load_dwordx4 v7, s[28:31], s82 offen lds\n"
"s_add_u32 m0, s74, 57344\n"
"s_add_u32 s82, s77, 262144\n"
"buffer_load_dwordx4 v7, s[28:31], s82 offen lds\n"
"s_add_u32 m0, s74, 94208\n"
"s_add_u32 s82, s77, 262272\n"
"buffer_load_dwordx4 v7, s[28:31], s82 offen lds\n"
"s_add_u32 m0, s74, 61440\n"
"s_add_u32 s82, s77, 524288\n"
"buffer_load_dwordx4 v7, s[28:31], s82 offen lds\n"
"s_add_u32 m0, s74, 98304\n"
"s_add_u32 s82, s77, 524416\n"
"buffer_load_dwordx4 v7, s[28:31], s82 offen lds\n"
"s_lshl_b32 s78, s69, 5\n"
"s_add_u32 s78, s78, s68\n"
"s_lshl_b32 s78, s78, 2\n"
"s_add_u32 s78, s78, s72\n"
"s_lshl_b32 s78, s78, 6\n"
"s_add_u32 s78, s78, 78905344\n"
"v_mov_b32 v8, s92\n"
"v_mov_b32 v9, s93\n"
"s_waitcnt vmcnt(6)\n"
"buffer_store_dwordx2 v[8:9], v14, s[40:43], s78 offen sc1\n"
"s_add_u32 m0, s74, 65536\n"
"s_add_u32 s82, s77, 786432\n"
"buffer_load_dwordx4 v7, s[28:31], s82 offen lds\n"
"v_lshrrev_b32 v144, 4, v0\n"
"v_bfe_u32 v145, v0, 2, 2\n"
"v_and_b32 v146, 3, v0\n"
"v_and_b32 v147, 1, v144\n"
"v_mul_u32_u24 v148, 288, v147\n"
"v_lshl_add_u32 v148, v145, 6, v148\n"
"v_lshl_add_u32 v148, v146, 3, v148\n"
"v_lshrrev_b32 v145, 1, v144\n"
"v_lshl_add_u32 v254, v145, 11, v148\n"
"v_or_b32 v254, 0x10000, v254\n"
"v_xor_b32 v255, 32, v254\n"
"s_add_u32 m0, s74, 102400\n"
"s_add_u32 s82, s77, 786560\n"
"buffer_load_dwordx4 v7, s[28:31], s82 offen lds\n"
"v_lshlrev_b32 v148, 1, v147\n"
"v_xor_b32 v148, v146, v148\n"
"v_lshlrev_b32 v148, 4, v148\n"
"v_bfe_u32 v146, v0, 2, 3\n"
"v_lshl_add_u32 v249, v146, 8, v148\n"
"v_lshl_add_u32 v249, v145, 6, v249\n"
"v_add_u32 v248, 896, v249\n"
"v_and_b32 v146, 15, v0\n"
"v_xor_b32 v148, v146, v144\n"
"v_lshlrev_b32 v148, 4, v148\n"
"v_lshl_add_u32 v244, v144, 8, v148\n"
"v_xor_b32 v245, 64, v244\n"
"s_add_u32 m0, s74, 69632\n"
"s_add_u32 s82, s77, 1048576\n"
"buffer_load_dwordx4 v7, s[28:31], s82 offen lds\n"
"v_xor_b32 v246, 128, v244\n"
"v_xor_b32 v247, 192, v244\n"
"v_lshl_or_b32 v148, v147, 1, v145\n"
"v_xor_b32 v148, v148, v146\n"
"v_lshlrev_b32 v148, 4, v148\n"
"v_lshl_add_u32 v250, v146, 8, v148\n"
"v_or_b32 v250, 0x10000, v250\n"
"v_xor_b32 v251, 64, v250\n"
"v_xor_b32 v252, 128, v250\n"
"v_xor_b32 v253, 192, v250\n"
"v_mov_b32 v155, 0x7fff0000\n"
"s_add_u32 m0, s74, 106496\n"
"s_add_u32 s82, s77, 1048704\n"
"buffer_load_dwordx4 v7, s[28:31], s82 offen lds\n"
"s_and_b32 s88, s72, 1\n"
"s_lshl_b32 s65, s75, 1\n"
"s_add_u32 s88, s88, s65\n"
"v_subrev_u32 v144, 0, v0\n"
"v_bfe_u32 v145, v144, 2, 2\n"
"v_lshl_add_u32 v145, v145, 6, s88\n"
"v_and_b32 v148, 3, v144\n"
"v_mul_u32_u24 v146, 911, v145\n"
"v_lshrrev_b32 v146, 13, v146\n"
"v_mul_u32_u24 v147, 9, v146\n"
"v_sub_u32 v147, v145, v147\n"
"v_lshl_add_u32 v149, v146, 2, v148\n"
"s_add_u32 m0, s74, 73728\n"
"s_add_u32 s82, s77, 1310720\n"
"buffer_load_dwordx4 v7, s[28:31], s82 offen lds\n"
"v_mul_u32_u24 v149, 73728, v149\n"
"v_lshl_add_u32 v149, v147, 7, v149\n"
"s_mul_i32 s65, s70, 1152\n"
"v_add_u32 v149, s65, v149\n"
"v_cmp_gt_u32 vcc, 162, v145\n"
"s_nop 1\n"
"v_cndmask_b32 v149, v155, v149, vcc\n"
"v_cmp_gt_u32 vcc, 12, v144\n"
"s_nop 1\n"
"v_cndmask_b32 v232, v155, v149, vcc\n"
"v_subrev_u32 v144, 5, v0\n"
"v_bfe_u32 v145, v144, 2, 2\n"
"v_lshl_add_u32 v145, v145, 6, s88\n"
"s_add_u32 m0, s74, 110592\n"
"s_add_u32 s82, s77, 1310848\n"
"buffer_load_dwordx4 v7, s[28:31], s82 offen lds\n"
"v_and_b32 v148, 3, v144\n"
"v_mul_u32_u24 v146, 911, v145\n"
"v_lshrrev_b32 v146, 13, v146\n"
"v_mul_u32_u24 v147, 9, v146\n"
"v_sub_u32 v147, v145, v147\n"
"v_lshl_add_u32 v149, v147, 2, v148\n"
"v_mul_u32_u24 v152, 911, v149\n"
"v_lshrrev_b32 v152, 13, v152\n"
"v_mul_u32_u24 v151, 9, v152\n"
"v_sub_u32 v151, v149, v151\n"
"v_add_u32 v150, 9, v146\n"
"v_subrev_u32 v146, 9, v146\n"
"s_add_u32 m0, s74, 77824\n"
"s_add_u32 s82, s77, 1572864\n"
"buffer_load_dwordx4 v7, s[28:31], s82 offen lds\n"
"v_min_u32 v146, v146, v150\n"
"v_and_b32 v150, 1, v152\n"
"v_sub_u32 v153, v151, v150\n"
"v_min_u32 v153, 8, v153\n"
"v_mul_u32_u24 v154, 43, v146\n"
"v_lshrrev_b32 v154, 7, v154\n"
"v_mul_u32_u24 v147, 3, v154\n"
"v_sub_u32 v147, v146, v147\n"
"v_lshrrev_b32 v148, 1, v153\n"
"v_mul_u32_u24 v149, 24, v154\n"
"v_mul_u32_u24 v150, 12, v148\n"
"v_add_u32 v149, v149, v150\n"
"s_add_u32 m0, s74, 114688\n"
"s_add_u32 s82, s77, 1572992\n"
"buffer_load_dwordx4 v7, s[28:31], s82 offen lds\n"
"v_mul_u32_u24 v151, 12, v154\n"
"v_mul_u32_u24 v150, 36, v148\n"
"v_add3_u32 v151, v151, v150, 36\n"
"v_cmp_gt_u32 vcc, 9, v146\n"
"s_nop 1\n"
"v_cndmask_b32 v149, v151, v149, vcc\n"
"v_add_u32 v151, v151, v150\n"
"v_cmp_gt_u32 vcc, 2, v148\n"
"s_nop 1\n"
"v_cndmask_b32 v149, v151, v149, vcc\n"
"s_lshr_b32 s65, s72, 1\n"
"s_mul_i32 s65, s65, 3\n"
"v_and_b32 v150, 1, v153\n"
"s_add_u32 m0, s74, 81920\n"
"s_add_u32 s82, s77, 1835008\n"
"buffer_load_dwordx4 v7, s[28:31], s82 offen lds\n"
"v_mul_u32_u24 v150, 3, v150\n"
"v_add3_u32 v149, v149, v150, s65\n"
"v_mul_u32_u24 v150, 6, v154\n"
"v_add_u32 v150, 144, v150\n"
"v_cmp_gt_u32 vcc, 8, v153\n"
"s_nop 1\n"
"v_cndmask_b32 v149, v150, v149, vcc\n"
"v_add3_u32 v149, v149, v147, s65\n"
"v_mul_u32_u24 v152, 672, v152\n"
"v_add_u32 v149, v149, v152\n"
"s_mul_i32 s76, s70, 2752512\n"
"s_add_u32 s76, s76, 38010880\n"
"v_lshl_add_u32 v149, v149, 10, s76\n"
"s_add_u32 m0, s74, 118784\n"
"s_add_u32 s82, s77, 1835136\n"
"buffer_load_dwordx4 v7, s[28:31], s82 offen lds\n"
"v_cmp_gt_u32 vcc, 162, v145\n"
"s_nop 1\n"
"v_cndmask_b32 v149, v155, v149, vcc\n"
"v_cmp_gt_u32 vcc, 12, v144\n"
"s_nop 1\n"
"v_cndmask_b32 v233, v155, v149, vcc\n"
"s_lshl_b32 s88, s75, 2\n"
"s_add_u32 s88, s88, s72\n"
"v_subrev_u32 v144, 12, v0\n"
"v_bfe_u32 v145, v144, 2, 2\n"
"v_lshl_add_u32 v145, v145, 7, s88\n"
"v_and_b32 v148, 3, v144\n"
"v_mul_u32_u24 v146, 911, v145\n"
"s_add_u32 m0, s74, 86016\n"
"s_add_u32 s82, s77, 2097152\n"
"buffer_load_dwordx4 v7, s[28:31], s82 offen lds\n"
"v_lshrrev_b32 v146, 13, v146\n"
"v_mul_u32_u24 v147, 9, v146\n"
"v_sub_u32 v147, v145, v147\n"
"v_subrev_u32 v149, 4, v146\n"
"v_cmp_gt_u32 vcc, 16, v146\n"
"s_nop 1\n"
"v_cndmask_b32 v149, v149, v146, vcc\n"
"v_and_b32 v150, 7, v149\n"
"v_mul_u32_u24 v151, 9, v150\n"
"v_and_b32 v150, 1, v150\n"
"v_lshrrev_b32 v149, 3, v149\n"
"v_lshl_add_u32 v151, v149, 1, v151\n"
"v_add_u32 v151, v151, v150\n"
"s_add_u32 m0, s74, 122880\n"
"s_add_u32 s82, s77, 2097280\n"
"buffer_load_dwordx4 v7, s[28:31], s82 offen lds\n"
"v_subrev_u32 v149, 16, v146\n"
"v_mul_u32_u24 v150, 18, v149\n"
"v_add_u32 v150, 8, v150\n"
"v_cmp_gt_u32 vcc, 4, v149\n"
"s_nop 1\n"
"v_cndmask_b32 v151, v151, v150, vcc\n"
"s_mul_i32 s65, s101, 72\n"
"v_add_u32 v151, s65, v151\n"
"s_lshl_b32 s65, s101, 6\n"
"v_lshl_add_u32 v149, v146, 1, s65\n"
"s_and_b32 s65, s70, 1\n"
"v_mov_b32 v150, s65\n"
"s_add_u32 m0, s74, 16384\n"
"s_add_u32 s82, s77, 2359296\n"
"buffer_load_dwordx4 v7, s[28:31], s82 offen lds\n"
"v_cmp_eq_u32 vcc, 0, v150\n"
"s_nop 1\n"
"v_cndmask_b32 v151, v149, v151, vcc\n"
"v_lshl_add_u32 v151, v151, 1, v148\n"
"v_mul_u32_u24 v151, 9216, v151\n"
"v_lshl_add_u32 v149, v147, 7, v151\n"
"s_mul_i32 s56, s65, 36\n"
"s_sub_u32 s56, 324, s56\n"
"v_cmp_gt_u32 vcc, s56, v145\n"
"s_nop 1\n"
"v_cndmask_b32 v149, v155, v149, vcc\n"
"v_cmp_gt_u32 vcc, 12, v144\n"
"s_nop 1\n"
"v_cndmask_b32 v232, v232, v149, vcc\n"
"v_subrev_u32 v144, 17, v0\n"
"s_add_u32 m0, s74, 20480\n"
"s_add_u32 s82, s77, 2621440\n"
"buffer_load_dwordx4 v7, s[28:31], s82 offen lds\n"
"v_bfe_u32 v145, v144, 2, 2\n"
"v_lshl_add_u32 v145, v145, 7, s88\n"
"v_and_b32 v148, 3, v144\n"
"v_mul_u32_u24 v146, 911, v145\n"
"v_lshrrev_b32 v146, 13, v146\n"
"v_mul_u32_u24 v147, 9, v146\n"
"v_sub_u32 v147, v145, v147\n"
"v_lshl_add_u32 v149, v147, 2, v148\n"
"v_mul_u32_u24 v152, 911, v149\n"
"v_lshrrev_b32 v152, 13, v152\n"
"v_mul_u32_u24 v151, 9, v152\n"
"v_sub_u32 v151, v149, v151\n"
"s_add_u32 m0, s74, 24576\n"
"s_add_u32 s82, s77, 2883584\n"
"buffer_load_dwordx4 v7, s[28:31], s82 offen lds\n"
"s_lshl_b32 s55, s101, 1\n"
"s_add_u32 s55, s55, 2\n"
"v_subrev_u32 v153, s55, v151\n"
"v_and_b32 v153, 7, v153\n"
"v_add_u32 v150, 1, v153\n"
"s_lshl_b32 s55, s65, 1\n"
"s_add_u32 s55, s55, 6\n"
"v_cmp_gt_u32 vcc, s55, v153\n"
"s_nop 1\n"
"v_cndmask_b32 v153, v150, v153, vcc\n"
"v_mov_b32 v150, s55\n"
"v_cmp_eq_u32 vcc, 8, v151\n"
"s_add_u32 m0, s74, 28672\n"
"s_add_u32 s82, s77, 3145728\n"
"buffer_load_dwordx4 v7, s[28:31], s82 offen lds\n"
"v_cndmask_b32 v153, v153, v150, vcc\n"
"s_mul_i32 s88, s65, 1\n"
"s_add_u32 s88, s88, 2\n"
"s_lshl_b32 s65, s65, 2\n"
"s_sub_u32 s65, 20, s65\n"
"s_mul_i32 s55, s65, 9\n"
"v_subrev_u32 v149, s65, v146\n"
"v_lshl_add_u32 v150, v153, 4, v149\n"
"v_and_b32 v149, 30, v149\n"
"v_mul_u32_u24 v149, s88, v149\n"
"v_and_b32 v151, 1, v146\n"
"v_lshl_add_u32 v151, v153, 1, v151\n"
"s_add_u32 m0, s74, 32768\n"
"s_add_u32 s82, s77, 3407872\n"
"buffer_load_dwordx4 v7, s[28:31], s82 offen lds\n"
"v_add_u32 v149, v149, v151\n"
"v_cmp_gt_u32 vcc, s88, v153\n"
"s_nop 1\n"
"v_cndmask_b32 v149, v150, v149, vcc\n"
"v_add_u32 v149, s55, v149\n"
"v_lshrrev_b32 v150, 1, v146\n"
"v_mul_u32_u24 v150, 18, v150\n"
"v_add_u32 v150, v150, v151\n"
"v_cmp_gt_u32 vcc, s65, v146\n"
"s_nop 1\n"
"v_cndmask_b32 v149, v149, v150, vcc\n"
"v_mul_u32_u24 v152, 672, v152\n"
"v_add_u32 v149, v149, v152\n"
"s_add_u32 m0, s74, 36864\n"
"s_add_u32 s82, s77, 3670016\n"
"buffer_load_dwordx4 v7, s[28:31], s82 offen lds\n"
"v_add_u32 v149, 336, v149\n"
"v_lshl_add_u32 v149, v149, 10, s76\n"
"v_cmp_gt_u32 vcc, s56, v145\n"
"s_nop 1\n"
"v_cndmask_b32 v149, v155, v149, vcc\n"
"v_cmp_gt_u32 vcc, 12, v144\n"
"s_nop 1\n"
"v_cndmask_b32 v233, v233, v149, vcc\n"
"v_and_b32 v149, 127, v249\n"
"v_bfe_u32 v150, v0, 2, 3\n"
"v_mul_u32_u24 v6, 9216, v150\n"
"v_add_u32 v6, v6, v149\n"
"v_mul_u32_u24 v238, 1152, v150\n"
"v_add_u32 v238, v238, v149\n"
"s_add_u32 m0, s74, 40960\n"
"s_add_u32 s82, s77, 3932160\n"
"buffer_load_dwordx4 v7, s[28:31], s82 offen lds\n"
"v_and_b32 v149, 511, v254\n"
"v_lshrrev_b32 v150, 5, v0\n"
"v_lshl_add_u32 v149, v150, 10, v149\n"
"s_lshl_b32 s54, s72, 13\n"
"s_add_u32 s54, s54, 131072\n"
"v_add_u32 v4, s54, v149\n"
"s_mul_i32 s58, s68, 688128\n"
"s_add_u32 s58, s58, 38010880\n"
"s_add_u32 s58, s58, s66\n"
"s_lshl_b32 s64, s101, 1\n"
"s_add_u32 s64, s64, s67\n"
"s_add_u32 m0, s74, 45056\n"
"s_add_u32 s82, s77, 4194304\n"
"buffer_load_dwordx4 v7, s[28:31], s82 offen lds\n"
"s_lshl_b32 s64, s64, 2\n"
"s_add_u32 s64, s64, s71\n"
"s_mul_i32 s64, s64, 688128\n"
"s_add_u32 s64, s64, 38010880\n"
"s_add_u32 s64, s64, s66\n"
"v_mov_b32 v236, s94\n"
"v_mov_b32 v237, s95\n"
"s_lshl_b32 s57, s70, 5\n"
"s_add_u32 s57, s57, s75\n"
"s_lshl_b32 s57, s57, 6\n"
"s_add_u32 s57, s57, 79560704\n"
"s_lshl_b32 s65, s72, 28\n"
"s_add_u32 m0, s74, 49152\n"
"s_add_u32 s82, s77, 4456448\n"
"buffer_load_dwordx4 v7, s[28:31], s82 offen lds\n"
"s_add_u32 s57, s57, s65\n"
"v_and_b32 v144, 3, v0\n"
"v_cmp_gt_u32 vcc, 2, v144\n"
"v_mov_b32 v145, 0x3f803f80\n"
"s_nop 0\n"
"v_cndmask_b32 v228, 0, v145, vcc\n"
"v_mov_b32 v229, v228\n"
"v_mov_b32 v230, v228\n"
"v_mov_b32 v231, v228\n"
"v_cmp_eq_u32 vcc, 0, v144\n"
"v_mov_b32 v145, 0x44444444\n"
"v_mov_b32 v146, 0xeeeeeeee\n"
"v_cndmask_b32 v239, v146, v145, vcc\n"
"s_lshl_b32 s23, s73, 11\n"
"s_add_u32 s23, s23, 78905344\n"
"v_and_b32 v15, 31, v0\n"
"v_lshlrev_b32 v15, 6, v15\n"
"buffer_load_dwordx2 v[10:11], v15, s[40:43], s23 offen sc1\n"
"s_waitcnt vmcnt(0)\n"
"v_cmp_ne_u64 vcc, s[92:93], v[10:11]\n"
"s_nop 0\n"
"s_cbranch_vccnz L_slow_1_%=\n"
"L_back_2_%=:\n"
"s_mul_i32 s84, s73, 73728\n"
"s_add_u32 s84, s84, 0\n"
"buffer_load_dwordx4 a[0:3], v1, s[40:43], s84 offen sc1\n"
"buffer_load_dwordx4 a[4:7], v1, s[40:43], s84 offen offset:1024 sc1\n"
"buffer_load_dwordx4 a[8:11], v1, s[40:43], s84 offen offset:2048 sc1\n"
"buffer_load_dwordx4 a[12:15], v1, s[40:43], s84 offen offset:3072 sc1\n"
"s_add_u32 s84, s84, 4096\n"
"buffer_load_dwordx4 a[16:19], v1, s[40:43], s84 offen sc1\n"
"buffer_load_dwordx4 a[20:23], v1, s[40:43], s84 offen offset:1024 sc1\n"
"buffer_load_dwordx4 a[24:27], v1, s[40:43], s84 offen offset:2048 sc1\n"
"buffer_load_dwordx4 a[28:31], v1, s[40:43], s84 offen offset:3072 sc1\n"
"s_add_u32 s84, s84, 4096\n"
"buffer_load_dwordx4 a[32:35], v1, s[40:43], s84 offen sc1\n"
"buffer_load_dwordx4 a[36:39], v1, s[40:43], s84 offen offset:1024 sc1\n"
"buffer_load_dwordx4 a[40:43], v1, s[40:43], s84 offen offset:2048 sc1\n"
"buffer_load_dwordx4 a[44:47], v1, s[40:43], s84 offen offset:3072 sc1\n"
"s_add_u32 s84, s84, 4096\n"
"s_barrier\n"
"ds_read_b64_tr_b16 a[80:81], v2\n"
"ds_read_b64_tr_b16 a[82:83], v2 offset:2048\n"
"ds_read_b64_tr_b16 a[84:85], v2 offset:4096\n"
"ds_read_b64_tr_b16 a[86:87], v2 offset:6144\n"
"ds_read_b64_tr_b16 a[88:89], v2 offset:8192\n"
"ds_read_b64_tr_b16 a[90:91], v2 offset:10240\n"
"ds_read_b64_tr_b16 a[92:93], v3\n"
"ds_read_b64_tr_b16 a[94:95], v3 offset:2048\n"
"ds_read_b64_tr_b16 a[96:97], v3 offset:4096\n"
"ds_read_b64_tr_b16 a[98:99], v3 offset:6144\n"
"ds_read_b64_tr_b16 a[100:101], v3 offset:8192\n"
"ds_read_b64_tr_b16 a[102:103], v3 offset:10240\n"
"s_add_u32 s78, s98, 1\n"
"v_mov_b32 v8, s78\n"
"s_lshl_b32 s79, s69, 7\n"
"s_add_u32 s79, s79, 79691776\n"
"s_or_b32 s80, s68, s72\n"
"s_cmp_eq_u32 s80, 0\n"
"s_cselect_b32 s79, s79, 0x7fff0000\n"
"buffer_store_dword v8, v14, s[40:43], s79 offen sc1\n"
"s_waitcnt vmcnt(12) lgkmcnt(10)\n"
"v_mfma_f32_16x16x32_bf16 v[16:19], a[80:83], a[0:3], 0\n"
"s_waitcnt vmcnt(8) lgkmcnt(8)\n"
"ds_read_b64_tr_b16 a[104:105], v2 offset:512\n"
"v_mfma_f32_16x16x32_bf16 v[16:19], a[84:87], a[16:19], v[16:19]\n"
"s_waitcnt vmcnt(4) lgkmcnt(7)\n"
"ds_read_b64_tr_b16 a[106:107], v2 offset:2560\n"
"v_mfma_f32_16x16x32_bf16 v[16:19], a[88:91], a[32:35], v[16:19]\n"
"buffer_load_dwordx4 a[48:51], v1, s[40:43], s84 offen sc1\n"
"v_mfma_f32_16x16x32_bf16 v[20:23], a[80:83], a[4:7], 0\n"
"ds_read_b64_tr_b16 a[108:109], v2 offset:4608\n"
"v_mfma_f32_16x16x32_bf16 v[20:23], a[84:87], a[20:23], v[20:23]\n"
"s_waitcnt vmcnt(4)\n"
"ds_read_b64_tr_b16 a[110:111], v2 offset:6656\n"
"v_mfma_f32_16x16x32_bf16 v[20:23], a[88:91], a[36:39], v[20:23]\n"
"v_mfma_f32_16x16x32_bf16 v[24:27], a[80:83], a[8:11], 0\n"
"ds_read_b64_tr_b16 a[112:113], v2 offset:8704\n"
"v_mfma_f32_16x16x32_bf16 v[24:27], a[84:87], a[24:27], v[24:27]\n"
"s_waitcnt vmcnt(3)\n"
"ds_read_b64_tr_b16 a[114:115], v2 offset:10752\n"
"v_mfma_f32_16x16x32_bf16 v[24:27], a[88:91], a[40:43], v[24:27]\n"
"buffer_load_dwordx4 a[52:55], v1, s[40:43], s84 offen offset:1024 sc1\n"
"v_mfma_f32_16x16x32_bf16 v[28:31], a[80:83], a[12:15], 0\n"
"v_mfma_f32_16x16x32_bf16 v[28:31], a[84:87], a[28:31], v[28:31]\n"
"s_waitcnt vmcnt(3)\n"
"v_mfma_f32_16x16x32_bf16 v[28:31], a[88:91], a[44:47], v[28:31]\n"
"s_waitcnt lgkmcnt(10)\n"
"buffer_load_dwordx4 a[56:59], v1, s[40:43], s84 offen offset:2048 sc1\n"
"v_mfma_f32_16x16x32_bf16 v[32:35], a[92:95], a[0:3], 0\n"
"s_waitcnt lgkmcnt(8)\n"
"ds_read_b64_tr_b16 a[80:81], v3 offset:512\n"
"v_mfma_f32_16x16x32_bf16 v[32:35], a[96:99], a[16:19], v[32:35]\n"
"s_waitcnt lgkmcnt(7)\n"
"ds_read_b64_tr_b16 a[82:83], v3 offset:2560\n"
"v_mfma_f32_16x16x32_bf16 v[32:35], a[100:103], a[32:35], v[32:35]\n"
"v_mfma_f32_16x16x32_bf16 v[36:39], a[92:95], a[4:7], 0\n"
"ds_read_b64_tr_b16 a[84:85], v3 offset:4608\n"
"v_mfma_f32_16x16x32_bf16 v[36:39], a[96:99], a[20:23], v[36:39]\n"
"ds_read_b64_tr_b16 a[86:87], v3 offset:6656\n"
"v_mfma_f32_16x16x32_bf16 v[36:39], a[100:103], a[36:39], v[36:39]\n"
"buffer_load_dwordx4 a[60:63], v1, s[40:43], s84 offen offset:3072 sc1\n"
"v_mfma_f32_16x16x32_bf16 v[40:43], a[92:95], a[8:11], 0\n"
"ds_read_b64_tr_b16 a[88:89], v3 offset:8704\n"
"v_mfma_f32_16x16x32_bf16 v[40:43], a[96:99], a[24:27], v[40:43]\n"
"ds_read_b64_tr_b16 a[90:91], v3 offset:10752\n"
"v_mfma_f32_16x16x32_bf16 v[40:43], a[100:103], a[40:43], v[40:43]\n"
"s_add_u32 s84, s84, 4096\n"
"buffer_load_dwordx4 a[64:67], v1, s[40:43], s84 offen sc1\n"
"v_mfma_f32_16x16x32_bf16 v[44:47], a[92:95], a[12:15], 0\n"
"v_mfma_f32_16x16x32_bf16 v[44:47], a[96:99], a[28:31], v[44:47]\n"
"s_waitcnt lgkmcnt(6)\n"
"v_mfma_f32_16x16x32_bf16 v[44:47], a[100:103], a[44:47], v[44:47]\n"
"v_mfma_f32_16x16x32_bf16 v[48:51], a[104:107], a[0:3], 0\n"
"ds_read_b64_tr_b16 a[92:93], v12\n"
"v_mfma_f32_16x16x32_bf16 v[48:51], a[108:111], a[16:19], v[48:51]\n"
"ds_read_b64_tr_b16 a[94:95], v12 offset:2048\n"
"v_mfma_f32_16x16x32_bf16 v[48:51], a[112:115], a[32:35], v[48:51]\n"
"buffer_load_dwordx4 a[68:71], v1, s[40:43], s84 offen offset:1024 sc1\n"
"v_mfma_f32_16x16x32_bf16 v[52:55], a[104:107], a[4:7], 0\n"
"ds_read_b64_tr_b16 a[96:97], v12 offset:4096\n"
"v_mfma_f32_16x16x32_bf16 v[52:55], a[108:111], a[20:23], v[52:55]\n"
"ds_read_b64_tr_b16 a[98:99], v12 offset:6144\n"
"v_mfma_f32_16x16x32_bf16 v[52:55], a[112:115], a[36:39], v[52:55]\n"
"buffer_load_dwordx4 a[72:75], v1, s[40:43], s84 offen offset:2048 sc1\n"
"v_mfma_f32_16x16x32_bf16 v[56:59], a[104:107], a[8:11], 0\n"
"ds_read_b64_tr_b16 a[100:101], v12 offset:8192\n"
"v_mfma_f32_16x16x32_bf16 v[56:59], a[108:111], a[24:27], v[56:59]\n"
"ds_read_b64_tr_b16 a[102:103], v12 offset:10240\n"
"v_mfma_f32_16x16x32_bf16 v[56:59], a[112:115], a[40:43], v[56:59]\n"
"v_mfma_f32_16x16x32_bf16 v[60:63], a[104:107], a[12:15], 0\n"
"buffer_load_dwordx4 a[76:79], v1, s[40:43], s84 offen offset:3072 sc1\n"
"v_mfma_f32_16x16x32_bf16 v[60:63], a[108:111], a[28:31], v[60:63]\n"
"s_waitcnt lgkmcnt(6)\n"
"v_mfma_f32_16x16x32_bf16 v[60:63], a[112:115], a[44:47], v[60:63]\n"
"v_mfma_f32_16x16x32_bf16 v[64:67], a[80:83], a[0:3], 0\n"
"ds_read_b64_tr_b16 a[104:105], v13\n"
"v_mfma_f32_16x16x32_bf16 v[64:67], a[84:87], a[16:19], v[64:67]\n"
"ds_read_b64_tr_b16 a[106:107], v13 offset:2048\n"
"v_mfma_f32_16x16x32_bf16 v[64:67], a[88:91], a[32:35], v[64:67]\n"
"s_add_u32 s84, s84, 4096\n"
"buffer_load_dwordx4 a[128:131], v1, s[40:43], s84 offen sc1\n"
"v_mfma_f32_16x16x32_bf16 v[68:71], a[80:83], a[4:7], 0\n"
"ds_read_b64_tr_b16 a[108:109], v13 offset:4096\n"
"v_mfma_f32_16x16x32_bf16 v[68:71], a[84:87], a[20:23], v[68:71]\n"
"ds_read_b64_tr_b16 a[110:111], v13 offset:6144\n"
"v_mfma_f32_16x16x32_bf16 v[68:71], a[88:91], a[36:39], v[68:71]\n"
"v_mfma_f32_16x16x32_bf16 v[72:75], a[80:83], a[8:11], 0\n"
"ds_read_b64_tr_b16 a[112:113], v13 offset:8192\n"
"v_mfma_f32_16x16x32_bf16 v[72:75], a[84:87], a[24:27], v[72:75]\n"
"ds_read_b64_tr_b16 a[114:115], v13 offset:10240\n"
"v_mfma_f32_16x16x32_bf16 v[72:75], a[88:91], a[40:43], v[72:75]\n"
"buffer_load_dwordx4 a[132:135], v1, s[40:43], s84 offen offset:1024 sc1\n"
"v_mfma_f32_16x16x32_bf16 v[76:79], a[80:83], a[12:15], 0\n"
"v_mfma_f32_16x16x32_bf16 v[76:79], a[84:87], a[28:31], v[76:79]\n"
"s_waitcnt lgkmcnt(6)\n"
"v_mfma_f32_16x16x32_bf16 v[76:79], a[88:91], a[44:47], v[76:79]\n"
"buffer_load_dwordx4 a[136:139], v1, s[40:43], s84 offen offset:2048 sc1\n"
"v_mfma_f32_16x16x32_bf16 v[80:83], a[92:95], a[0:3], 0\n"
"ds_read_b64_tr_b16 a[80:81], v12 offset:512\n"
"v_mfma_f32_16x16x32_bf16 v[80:83], a[96:99], a[16:19], v[80:83]\n"
"ds_read_b64_tr_b16 a[82:83], v12 offset:2560\n"
"v_mfma_f32_16x16x32_bf16 v[80:83], a[100:103], a[32:35], v[80:83]\n"
"v_mfma_f32_16x16x32_bf16 v[84:87], a[92:95], a[4:7], 0\n"
"ds_read_b64_tr_b16 a[84:85], v12 offset:4608\n"
"v_mfma_f32_16x16x32_bf16 v[84:87], a[96:99], a[20:23], v[84:87]\n"
"ds_read_b64_tr_b16 a[86:87], v12 offset:6656\n"
"v_mfma_f32_16x16x32_bf16 v[84:87], a[100:103], a[36:39], v[84:87]\n"
"buffer_load_dwordx4 a[140:143], v1, s[40:43], s84 offen offset:3072 sc1\n"
"v_mfma_f32_16x16x32_bf16 v[88:91], a[92:95], a[8:11], 0\n"
"ds_read_b64_tr_b16 a[88:89], v12 offset:8704\n"
"v_mfma_f32_16x16x32_bf16 v[88:91], a[96:99], a[24:27], v[88:91]\n"
"ds_read_b64_tr_b16 a[90:91], v12 offset:10752\n"
"v_mfma_f32_16x16x32_bf16 v[88:91], a[100:103], a[40:43], v[88:91]\n"
"s_add_u32 s84, s84, 4096\n"
"buffer_load_dwordx4 a[144:147], v1, s[40:43], s84 offen sc1\n"
"v_mfma_f32_16x16x32_bf16 v[92:95], a[92:95], a[12:15], 0\n"
"v_mfma_f32_16x16x32_bf16 v[92:95], a[96:99], a[28:31], v[92:95]\n"
"s_waitcnt lgkmcnt(6)\n"
"v_mfma_f32_16x16x32_bf16 v[92:95], a[100:103], a[44:47], v[92:95]\n"
"v_mfma_f32_16x16x32_bf16 v[96:99], a[104:107], a[0:3], 0\n"
"ds_read_b64_tr_b16 a[92:93], v13 offset:512\n"
"v_mfma_f32_16x16x32_bf16 v[96:99], a[108:111], a[16:19], v[96:99]\n"
"ds_read_b64_tr_b16 a[94:95], v13 offset:2560\n"
"v_mfma_f32_16x16x32_bf16 v[96:99], a[112:115], a[32:35], v[96:99]\n"
"buffer_load_dwordx4 a[148:151], v1, s[40:43], s84 offen offset:1024 sc1\n"
"v_mfma_f32_16x16x32_bf16 v[100:103], a[104:107], a[4:7], 0\n"
"ds_read_b64_tr_b16 a[96:97], v13 offset:4608\n"
"v_mfma_f32_16x16x32_bf16 v[100:103], a[108:111], a[20:23], v[100:103]\n"
"ds_read_b64_tr_b16 a[98:99], v13 offset:6656\n"
"v_mfma_f32_16x16x32_bf16 v[100:103], a[112:115], a[36:39], v[100:103]\n"
"buffer_load_dwordx4 a[152:155], v1, s[40:43], s84 offen offset:2048 sc1\n"
"v_mfma_f32_16x16x32_bf16 v[104:107], a[104:107], a[8:11], 0\n"
"ds_read_b64_tr_b16 a[100:101], v13 offset:8704\n"
"v_mfma_f32_16x16x32_bf16 v[104:107], a[108:111], a[24:27], v[104:107]\n"
"ds_read_b64_tr_b16 a[102:103], v13 offset:10752\n"
"v_mfma_f32_16x16x32_bf16 v[104:107], a[112:115], a[40:43], v[104:107]\n"
"v_mfma_f32_16x16x32_bf16 v[108:111], a[104:107], a[12:15], 0\n"
"buffer_load_dwordx4 a[156:159], v1, s[40:43], s84 offen offset:3072 sc1\n"
"v_mfma_f32_16x16x32_bf16 v[108:111], a[108:111], a[28:31], v[108:111]\n"
"s_waitcnt lgkmcnt(6)\n"
"v_mfma_f32_16x16x32_bf16 v[108:111], a[112:115], a[44:47], v[108:111]\n"
"v_mfma_f32_16x16x32_bf16 v[112:115], a[80:83], a[0:3], 0\n"
"ds_read_b64_tr_b16 a[104:105], v2 offset:12288\n"
"v_mfma_f32_16x16x32_bf16 v[112:115], a[84:87], a[16:19], v[112:115]\n"
"ds_read_b64_tr_b16 a[106:107], v2 offset:14336\n"
"v_mfma_f32_16x16x32_bf16 v[112:115], a[88:91], a[32:35], v[112:115]\n"
"s_add_u32 s84, s84, 4096\n"
"buffer_load_dwordx4 a[160:163], v1, s[40:43], s84 offen sc1\n"
"v_mfma_f32_16x16x32_bf16 v[116:119], a[80:83], a[4:7], 0\n"
"ds_read_b64_tr_b16 a[108:109], v2 offset:16384\n"
"v_mfma_f32_16x16x32_bf16 v[116:119], a[84:87], a[20:23], v[116:119]\n"
"ds_read_b64_tr_b16 a[110:111], v2 offset:18432\n"
"v_mfma_f32_16x16x32_bf16 v[116:119], a[88:91], a[36:39], v[116:119]\n"
"v_mfma_f32_16x16x32_bf16 v[120:123], a[80:83], a[8:11], 0\n"
"ds_read_b64_tr_b16 a[112:113], v2 offset:20480\n"
"v_mfma_f32_16x16x32_bf16 v[120:123], a[84:87], a[24:27], v[120:123]\n"
"ds_read_b64_tr_b16 a[114:115], v2 offset:22528\n"
"v_mfma_f32_16x16x32_bf16 v[120:123], a[88:91], a[40:43], v[120:123]\n"
"buffer_load_dwordx4 a[164:167], v1, s[40:43], s84 offen offset:1024 sc1\n"
"v_mfma_f32_16x16x32_bf16 v[124:127], a[80:83], a[12:15], 0\n"
"v_mfma_f32_16x16x32_bf16 v[124:127], a[84:87], a[28:31], v[124:127]\n"
"s_waitcnt lgkmcnt(6)\n"
"v_mfma_f32_16x16x32_bf16 v[124:127], a[88:91], a[44:47], v[124:127]\n"
"buffer_load_dwordx4 a[168:171], v1, s[40:43], s84 offen offset:2048 sc1\n"
"v_mfma_f32_16x16x32_bf16 v[128:131], a[92:95], a[0:3], 0\n"
"ds_read_b64_tr_b16 a[80:81], v3 offset:12288\n"
"v_mfma_f32_16x16x32_bf16 v[128:131], a[96:99], a[16:19], v[128:131]\n"
"ds_read_b64_tr_b16 a[82:83], v3 offset:14336\n"
"v_mfma_f32_16x16x32_bf16 v[128:131], a[100:103], a[32:35], v[128:131]\n"
"v_mfma_f32_16x16x32_bf16 v[132:135], a[92:95], a[4:7], 0\n"
"ds_read_b64_tr_b16 a[84:85], v3 offset:16384\n"
"v_mfma_f32_16x16x32_bf16 v[132:135], a[96:99], a[20:23], v[132:135]\n"
"ds_read_b64_tr_b16 a[86:87], v3 offset:18432\n"
"v_mfma_f32_16x16x32_bf16 v[132:135], a[100:103], a[36:39], v[132:135]\n"
"buffer_load_dwordx4 a[172:175], v1, s[40:43], s84 offen offset:3072 sc1\n"
"v_mfma_f32_16x16x32_bf16 v[136:139], a[92:95], a[8:11], 0\n"
"ds_read_b64_tr_b16 a[88:89], v3 offset:20480\n"
"v_mfma_f32_16x16x32_bf16 v[136:139], a[96:99], a[24:27], v[136:139]\n"
"ds_read_b64_tr_b16 a[90:91], v3 offset:22528\n"
"v_mfma_f32_16x16x32_bf16 v[136:139], a[100:103], a[40:43], v[136:139]\n"
"s_add_u32 s84, s84, 4096\n"
"buffer_load_dwordx4 a[176:179], v1, s[40:43], s84 offen sc1\n"
"v_mfma_f32_16x16x32_bf16 v[140:143], a[92:95], a[12:15], 0\n"
"v_mfma_f32_16x16x32_bf16 v[140:143], a[96:99], a[28:31], v[140:143]\n"
"s_waitcnt vmcnt(9) lgkmcnt(6)\n"
"v_mfma_f32_16x16x32_bf16 v[140:143], a[100:103], a[44:47], v[140:143]\n"
"v_mfma_f32_16x16x32_bf16 v[16:19], a[104:107], a[48:51], v[16:19]\n"
"ds_read_b64_tr_b16 a[92:93], v2 offset:12800\n"
"v_mfma_f32_16x16x32_bf16 v[16:19], a[108:111], a[64:67], v[16:19]\n"
"ds_read_b64_tr_b16 a[94:95], v2 offset:14848\n"
"v_mfma_f32_16x16x32_bf16 v[16:19], a[112:115], a[128:131], v[16:19]\n"
"buffer_load_dwordx4 a[180:183], v1, s[40:43], s84 offen offset:1024 sc1\n"
"v_mfma_f32_16x16x32_bf16 v[20:23], a[104:107], a[52:55], v[20:23]\n"
"ds_read_b64_tr_b16 a[96:97], v2 offset:16896\n"
"v_mfma_f32_16x16x32_bf16 v[20:23], a[108:111], a[68:71], v[20:23]\n"
"ds_read_b64_tr_b16 a[98:99], v2 offset:18944\n"
"v_mfma_f32_16x16x32_bf16 v[20:23], a[112:115], a[132:135], v[20:23]\n"
"buffer_load_dwordx4 a[184:187], v1, s[40:43], s84 offen offset:2048 sc1\n"
"v_mfma_f32_16x16x32_bf16 v[24:27], a[104:107], a[56:59], v[24:27]\n"
"ds_read_b64_tr_b16 a[100:101], v2 offset:20992\n"
"v_mfma_f32_16x16x32_bf16 v[24:27], a[108:111], a[72:75], v[24:27]\n"
"ds_read_b64_tr_b16 a[102:103], v2 offset:23040\n"
"v_mfma_f32_16x16x32_bf16 v[24:27], a[112:115], a[136:139], v[24:27]\n"
"v_mfma_f32_16x16x32_bf16 v[28:31], a[104:107], a[60:63], v[28:31]\n"
"buffer_load_dwordx4 a[188:191], v1, s[40:43], s84 offen offset:3072 sc1\n"
"v_mfma_f32_16x16x32_bf16 v[28:31], a[108:111], a[76:79], v[28:31]\n"
"s_waitcnt lgkmcnt(6)\n"
"v_mfma_f32_16x16x32_bf16 v[28:31], a[112:115], a[140:143], v[28:31]\n"
"v_mfma_f32_16x16x32_bf16 v[32:35], a[80:83], a[48:51], v[32:35]\n"
"ds_read_b64_tr_b16 a[104:105], v3 offset:12800\n"
"v_mfma_f32_16x16x32_bf16 v[32:35], a[84:87], a[64:67], v[32:35]\n"
"ds_read_b64_tr_b16 a[106:107], v3 offset:14848\n"
"v_mfma_f32_16x16x32_bf16 v[32:35], a[88:91], a[128:131], v[32:35]\n"
"s_add_u32 s84, s84, 4096\n"
"buffer_load_dwordx4 v[176:179], v1, s[40:43], s84 offen sc1\n"
"v_mfma_f32_16x16x32_bf16 v[36:39], a[80:83], a[52:55], v[36:39]\n"
"ds_read_b64_tr_b16 a[108:109], v3 offset:16896\n"
"v_mfma_f32_16x16x32_bf16 v[36:39], a[84:87], a[68:71], v[36:39]\n"
"ds_read_b64_tr_b16 a[110:111], v3 offset:18944\n"
"v_mfma_f32_16x16x32_bf16 v[36:39], a[88:91], a[132:135], v[36:39]\n"
"v_mfma_f32_16x16x32_bf16 v[40:43], a[80:83], a[56:59], v[40:43]\n"
"ds_read_b64_tr_b16 a[112:113], v3 offset:20992\n"
"v_mfma_f32_16x16x32_bf16 v[40:43], a[84:87], a[72:75], v[40:43]\n"
"ds_read_b64_tr_b16 a[114:115], v3 offset:23040\n"
"v_mfma_f32_16x16x32_bf16 v[40:43], a[88:91], a[136:139], v[40:43]\n"
"buffer_load_dwordx4 v[180:183], v1, s[40:43], s84 offen offset:1024 sc1\n"
"v_mfma_f32_16x16x32_bf16 v[44:47], a[80:83], a[60:63], v[44:47]\n"
"v_mfma_f32_16x16x32_bf16 v[44:47], a[84:87], a[76:79], v[44:47]\n"
"s_waitcnt lgkmcnt(6)\n"
"v_mfma_f32_16x16x32_bf16 v[44:47], a[88:91], a[140:143], v[44:47]\n"
"buffer_load_dwordx4 v[184:187], v1, s[40:43], s84 offen offset:2048 sc1\n"
"v_mfma_f32_16x16x32_bf16 v[48:51], a[92:95], a[48:51], v[48:51]\n"
"ds_read_b64_tr_b16 a[80:81], v12 offset:12288\n"
"v_mfma_f32_16x16x32_bf16 v[48:51], a[96:99], a[64:67], v[48:51]\n"
"ds_read_b64_tr_b16 a[82:83], v12 offset:14336\n"
"v_mfma_f32_16x16x32_bf16 v[48:51], a[100:103], a[128:131], v[48:51]\n"
"v_mfma_f32_16x16x32_bf16 v[52:55], a[92:95], a[52:55], v[52:55]\n"
"ds_read_b64_tr_b16 a[84:85], v12 offset:16384\n"
"v_mfma_f32_16x16x32_bf16 v[52:55], a[96:99], a[68:71], v[52:55]\n"
"ds_read_b64_tr_b16 a[86:87], v12 offset:18432\n"
"v_mfma_f32_16x16x32_bf16 v[52:55], a[100:103], a[132:135], v[52:55]\n"
"buffer_load_dwordx4 v[188:191], v1, s[40:43], s84 offen offset:3072 sc1\n"
"v_mfma_f32_16x16x32_bf16 v[56:59], a[92:95], a[56:59], v[56:59]\n"
"ds_read_b64_tr_b16 a[88:89], v12 offset:20480\n"
"v_mfma_f32_16x16x32_bf16 v[56:59], a[96:99], a[72:75], v[56:59]\n"
"ds_read_b64_tr_b16 a[90:91], v12 offset:22528\n"
"v_mfma_f32_16x16x32_bf16 v[56:59], a[100:103], a[136:139], v[56:59]\n"
"s_add_u32 s84, s84, 4096\n"
"buffer_load_dwordx4 v[192:195], v1, s[40:43], s84 offen sc1\n"
"v_mfma_f32_16x16x32_bf16 v[60:63], a[92:95], a[60:63], v[60:63]\n"
"v_mfma_f32_16x16x32_bf16 v[60:63], a[96:99], a[76:79], v[60:63]\n"
"s_waitcnt lgkmcnt(6)\n"
"v_mfma_f32_16x16x32_bf16 v[60:63], a[100:103], a[140:143], v[60:63]\n"
"v_mfma_f32_16x16x32_bf16 v[64:67], a[104:107], a[48:51], v[64:67]\n"
"ds_read_b64_tr_b16 a[92:93], v13 offset:12288\n"
"v_mfma_f32_16x16x32_bf16 v[64:67], a[108:111], a[64:67], v[64:67]\n"
"ds_read_b64_tr_b16 a[94:95], v13 offset:14336\n"
"v_mfma_f32_16x16x32_bf16 v[64:67], a[112:115], a[128:131], v[64:67]\n"
"buffer_load_dwordx4 v[196:199], v1, s[40:43], s84 offen offset:1024 sc1\n"
"v_mfma_f32_16x16x32_bf16 v[68:71], a[104:107], a[52:55], v[68:71]\n"
"ds_read_b64_tr_b16 a[96:97], v13 offset:16384\n"
"v_mfma_f32_16x16x32_bf16 v[68:71], a[108:111], a[68:71], v[68:71]\n"
"ds_read_b64_tr_b16 a[98:99], v13 offset:18432\n"
"v_mfma_f32_16x16x32_bf16 v[68:71], a[112:115], a[132:135], v[68:71]\n"
"buffer_load_dwordx4 v[200:203], v1, s[40:43], s84 offen offset:2048 sc1\n"
"v_mfma_f32_16x16x32_bf16 v[72:75], a[104:107], a[56:59], v[72:75]\n"
"ds_read_b64_tr_b16 a[100:101], v13 offset:20480\n"
"v_mfma_f32_16x16x32_bf16 v[72:75], a[108:111], a[72:75], v[72:75]\n"
"ds_read_b64_tr_b16 a[102:103], v13 offset:22528\n"
"v_mfma_f32_16x16x32_bf16 v[72:75], a[112:115], a[136:139], v[72:75]\n"
"v_mfma_f32_16x16x32_bf16 v[76:79], a[104:107], a[60:63], v[76:79]\n"
"buffer_load_dwordx4 v[204:207], v1, s[40:43], s84 offen offset:3072 sc1\n"
"v_mfma_f32_16x16x32_bf16 v[76:79], a[108:111], a[76:79], v[76:79]\n"
"s_waitcnt lgkmcnt(6)\n"
"v_mfma_f32_16x16x32_bf16 v[76:79], a[112:115], a[140:143], v[76:79]\n"
"v_mfma_f32_16x16x32_bf16 v[80:83], a[80:83], a[48:51], v[80:83]\n"
"ds_read_b64_tr_b16 a[104:105], v12 offset:12800\n"
"v_mfma_f32_16x16x32_bf16 v[80:83], a[84:87], a[64:67], v[80:83]\n"
"ds_read_b64_tr_b16 a[106:107], v12 offset:14848\n"
"v_mfma_f32_16x16x32_bf16 v[80:83], a[88:91], a[128:131], v[80:83]\n"
"s_add_u32 s84, s84, 4096\n"
"buffer_load_dwordx4 a[192:195], v1, s[40:43], s84 offen sc1\n"
"v_mfma_f32_16x16x32_bf16 v[84:87], a[80:83], a[52:55], v[84:87]\n"
"ds_read_b64_tr_b16 a[108:109], v12 offset:16896\n"
"v_mfma_f32_16x16x32_bf16 v[84:87], a[84:87], a[68:71], v[84:87]\n"
"ds_read_b64_tr_b16 a[110:111], v12 offset:18944\n"
"v_mfma_f32_16x16x32_bf16 v[84:87], a[88:91], a[132:135], v[84:87]\n"
"v_mfma_f32_16x16x32_bf16 v[88:91], a[80:83], a[56:59], v[88:91]\n"
"ds_read_b64_tr_b16 a[112:113], v12 offset:20992\n"
"v_mfma_f32_16x16x32_bf16 v[88:91], a[84:87], a[72:75], v[88:91]\n"
"ds_read_b64_tr_b16 a[114:115], v12 offset:23040\n"
"v_mfma_f32_16x16x32_bf16 v[88:91], a[88:91], a[136:139], v[88:91]\n"
"buffer_load_dwordx4 a[196:199], v1, s[40:43], s84 offen offset:1024 sc1\n"
"v_mfma_f32_16x16x32_bf16 v[92:95], a[80:83], a[60:63], v[92:95]\n"
"v_mfma_f32_16x16x32_bf16 v[92:95], a[84:87], a[76:79], v[92:95]\n"
"s_waitcnt lgkmcnt(6)\n"
"v_mfma_f32_16x16x32_bf16 v[92:95], a[88:91], a[140:143], v[92:95]\n"
"buffer_load_dwordx4 a[200:203], v1, s[40:43], s84 offen offset:2048 sc1\n"
"v_mfma_f32_16x16x32_bf16 v[96:99], a[92:95], a[48:51], v[96:99]\n"
"ds_read_b64_tr_b16 a[80:81], v13 offset:12800\n"
"v_mfma_f32_16x16x32_bf16 v[96:99], a[96:99], a[64:67], v[96:99]\n"
"ds_read_b64_tr_b16 a[82:83], v13 offset:14848\n"
"v_mfma_f32_16x16x32_bf16 v[96:99], a[100:103], a[128:131], v[96:99]\n"
"v_mfma_f32_16x16x32_bf16 v[100:103], a[92:95], a[52:55], v[100:103]\n"
"ds_read_b64_tr_b16 a[84:85], v13 offset:16896\n"
"v_mfma_f32_16x16x32_bf16 v[100:103], a[96:99], a[68:71], v[100:103]\n"
"ds_read_b64_tr_b16 a[86:87], v13 offset:18944\n"
"v_mfma_f32_16x16x32_bf16 v[100:103], a[100:103], a[132:135], v[100:103]\n"
"buffer_load_dwordx4 a[204:207], v1, s[40:43], s84 offen offset:3072 sc1\n"
"v_mfma_f32_16x16x32_bf16 v[104:107], a[92:95], a[56:59], v[104:107]\n"
"ds_read_b64_tr_b16 a[88:89], v13 offset:20992\n"
"v_mfma_f32_16x16x32_bf16 v[104:107], a[96:99], a[72:75], v[104:107]\n"
"ds_read_b64_tr_b16 a[90:91], v13 offset:23040\n"
"v_mfma_f32_16x16x32_bf16 v[104:107], a[100:103], a[136:139], v[104:107]\n"
"s_add_u32 s84, s84, 4096\n"
"buffer_load_dwordx4 a[208:211], v1, s[40:43], s84 offen sc1\n"
"v_mfma_f32_16x16x32_bf16 v[108:111], a[92:95], a[60:63], v[108:111]\n"
"v_mfma_f32_16x16x32_bf16 v[108:111], a[96:99], a[76:79], v[108:111]\n"
"s_waitcnt lgkmcnt(6)\n"
"v_mfma_f32_16x16x32_bf16 v[108:111], a[100:103], a[140:143], v[108:111]\n"
"v_mfma_f32_16x16x32_bf16 v[112:115], a[104:107], a[48:51], v[112:115]\n"
"ds_read_b64_tr_b16 a[92:93], v2 offset:24576\n"
"v_mfma_f32_16x16x32_bf16 v[112:115], a[108:111], a[64:67], v[112:115]\n"
"ds_read_b64_tr_b16 a[94:95], v2 offset:26624\n"
"v_mfma_f32_16x16x32_bf16 v[112:115], a[112:115], a[128:131], v[112:115]\n"
"buffer_load_dwordx4 a[212:215], v1, s[40:43], s84 offen offset:1024 sc1\n"
"v_mfma_f32_16x16x32_bf16 v[116:119], a[104:107], a[52:55], v[116:119]\n"
"ds_read_b64_tr_b16 a[96:97], v2 offset:28672\n"
"v_mfma_f32_16x16x32_bf16 v[116:119], a[108:111], a[68:71], v[116:119]\n"
"ds_read_b64_tr_b16 a[98:99], v2 offset:30720\n"
"v_mfma_f32_16x16x32_bf16 v[116:119], a[112:115], a[132:135], v[116:119]\n"
"buffer_load_dwordx4 a[216:219], v1, s[40:43], s84 offen offset:2048 sc1\n"
"v_mfma_f32_16x16x32_bf16 v[120:123], a[104:107], a[56:59], v[120:123]\n"
"ds_read_b64_tr_b16 a[100:101], v2 offset:32768\n"
"v_mfma_f32_16x16x32_bf16 v[120:123], a[108:111], a[72:75], v[120:123]\n"
"ds_read_b64_tr_b16 a[102:103], v2 offset:34816\n"
"v_mfma_f32_16x16x32_bf16 v[120:123], a[112:115], a[136:139], v[120:123]\n"
"v_mfma_f32_16x16x32_bf16 v[124:127], a[104:107], a[60:63], v[124:127]\n"
"buffer_load_dwordx4 a[220:223], v1, s[40:43], s84 offen offset:3072 sc1\n"
"v_mfma_f32_16x16x32_bf16 v[124:127], a[108:111], a[76:79], v[124:127]\n"
"s_waitcnt lgkmcnt(6)\n"
"v_mfma_f32_16x16x32_bf16 v[124:127], a[112:115], a[140:143], v[124:127]\n"
"v_mfma_f32_16x16x32_bf16 v[128:131], a[80:83], a[48:51], v[128:131]\n"
"ds_read_b64_tr_b16 a[104:105], v3 offset:24576\n"
"v_mfma_f32_16x16x32_bf16 v[128:131], a[84:87], a[64:67], v[128:131]\n"
"ds_read_b64_tr_b16 a[106:107], v3 offset:26624\n"
"v_mfma_f32_16x16x32_bf16 v[128:131], a[88:91], a[128:131], v[128:131]\n"
"s_add_u32 s84, s84, 4096\n"
"buffer_load_dwordx4 a[224:227], v1, s[40:43], s84 offen sc1\n"
"v_mfma_f32_16x16x32_bf16 v[132:135], a[80:83], a[52:55], v[132:135]\n"
"ds_read_b64_tr_b16 a[108:109], v3 offset:28672\n"
"v_mfma_f32_16x16x32_bf16 v[132:135], a[84:87], a[68:71], v[132:135]\n"
"ds_read_b64_tr_b16 a[110:111], v3 offset:30720\n"
"v_mfma_f32_16x16x32_bf16 v[132:135], a[88:91], a[132:135], v[132:135]\n"
"v_mfma_f32_16x16x32_bf16 v[136:139], a[80:83], a[56:59], v[136:139]\n"
"ds_read_b64_tr_b16 a[112:113], v3 offset:32768\n"
"v_mfma_f32_16x16x32_bf16 v[136:139], a[84:87], a[72:75], v[136:139]\n"
"ds_read_b64_tr_b16 a[114:115], v3 offset:34816\n"
"v_mfma_f32_16x16x32_bf16 v[136:139], a[88:91], a[136:139], v[136:139]\n"
"buffer_load_dwordx4 a[228:231], v1, s[40:43], s84 offen offset:1024 sc1\n"
"v_mfma_f32_16x16x32_bf16 v[140:143], a[80:83], a[60:63], v[140:143]\n"
"v_mfma_f32_16x16x32_bf16 v[140:143], a[84:87], a[76:79], v[140:143]\n"
"s_waitcnt vmcnt(18) lgkmcnt(6)\n"
"v_mfma_f32_16x16x32_bf16 v[140:143], a[88:91], a[140:143], v[140:143]\n"
"buffer_load_dwordx4 a[232:235], v1, s[40:43], s84 offen offset:2048 sc1\n"
"v_mfma_f32_16x16x32_bf16 v[16:19], a[92:95], a[144:147], v[16:19]\n"
"ds_read_b64_tr_b16 a[80:81], v2 offset:25088\n"
"v_mfma_f32_16x16x32_bf16 v[16:19], a[96:99], a[160:163], v[16:19]\n"
"ds_read_b64_tr_b16 a[82:83], v2 offset:27136\n"
"v_mfma_f32_16x16x32_bf16 v[16:19], a[100:103], a[176:179], v[16:19]\n"
"v_mfma_f32_16x16x32_bf16 v[20:23], a[92:95], a[148:151], v[20:23]\n"
"ds_read_b64_tr_b16 a[84:85], v2 offset:29184\n"
"v_mfma_f32_16x16x32_bf16 v[20:23], a[96:99], a[164:167], v[20:23]\n"
"ds_read_b64_tr_b16 a[86:87], v2 offset:31232\n"
"v_mfma_f32_16x16x32_bf16 v[20:23], a[100:103], a[180:183], v[20:23]\n"
"buffer_load_dwordx4 a[236:239], v1, s[40:43], s84 offen offset:3072 sc1\n"
"v_mfma_f32_16x16x32_bf16 v[24:27], a[92:95], a[152:155], v[24:27]\n"
"ds_read_b64_tr_b16 a[88:89], v2 offset:33280\n"
"v_mfma_f32_16x16x32_bf16 v[24:27], a[96:99], a[168:171], v[24:27]\n"
"ds_read_b64_tr_b16 a[90:91], v2 offset:35328\n"
"v_mfma_f32_16x16x32_bf16 v[24:27], a[100:103], a[184:187], v[24:27]\n"
"s_add_u32 s84, s84, 4096\n"
"buffer_load_dwordx4 a[240:243], v1, s[40:43], s84 offen sc1\n"
"v_mfma_f32_16x16x32_bf16 v[28:31], a[92:95], a[156:159], v[28:31]\n"
"v_mfma_f32_16x16x32_bf16 v[28:31], a[96:99], a[172:175], v[28:31]\n"
"s_waitcnt lgkmcnt(6)\n"
"v_mfma_f32_16x16x32_bf16 v[28:31], a[100:103], a[188:191], v[28:31]\n"
"v_mfma_f32_16x16x32_bf16 v[32:35], a[104:107], a[144:147], v[32:35]\n"
"ds_read_b64_tr_b16 a[92:93], v3 offset:25088\n"
"v_mfma_f32_16x16x32_bf16 v[32:35], a[108:111], a[160:163], v[32:35]\n"
"ds_read_b64_tr_b16 a[94:95], v3 offset:27136\n"
"v_mfma_f32_16x16x32_bf16 v[32:35], a[112:115], a[176:179], v[32:35]\n"
"buffer_load_dwordx4 a[244:247], v1, s[40:43], s84 offen offset:1024 sc1\n"
"v_mfma_f32_16x16x32_bf16 v[36:39], a[104:107], a[148:151], v[36:39]\n"
"ds_read_b64_tr_b16 a[96:97], v3 offset:29184\n"
"v_mfma_f32_16x16x32_bf16 v[36:39], a[108:111], a[164:167], v[36:39]\n"
"ds_read_b64_tr_b16 a[98:99], v3 offset:31232\n"
"v_mfma_f32_16x16x32_bf16 v[36:39], a[112:115], a[180:183], v[36:39]\n"
"buffer_load_dwordx4 a[248:251], v1, s[40:43], s84 offen offset:2048 sc1\n"
"v_mfma_f32_16x16x32_bf16 v[40:43], a[104:107], a[152:155], v[40:43]\n"
"ds_read_b64_tr_b16 a[100:101], v3 offset:33280\n"
"v_mfma_f32_16x16x32_bf16 v[40:43], a[108:111], a[168:171], v[40:43]\n"
"ds_read_b64_tr_b16 a[102:103], v3 offset:35328\n"
"v_mfma_f32_16x16x32_bf16 v[40:43], a[112:115], a[184:187], v[40:43]\n"
"v_mfma_f32_16x16x32_bf16 v[44:47], a[104:107], a[156:159], v[44:47]\n"
"buffer_load_dwordx4 a[252:255], v1, s[40:43], s84 offen offset:3072 sc1\n"
"v_mfma_f32_16x16x32_bf16 v[44:47], a[108:111], a[172:175], v[44:47]\n"
"s_waitcnt lgkmcnt(6)\n"
"v_mfma_f32_16x16x32_bf16 v[44:47], a[112:115], a[188:191], v[44:47]\n"
"v_mfma_f32_16x16x32_bf16 v[48:51], a[80:83], a[144:147], v[48:51]\n"
"ds_read_b64_tr_b16 a[104:105], v12 offset:24576\n"
"v_mfma_f32_16x16x32_bf16 v[48:51], a[84:87], a[160:163], v[48:51]\n"
"ds_read_b64_tr_b16 a[106:107], v12 offset:26624\n"
"v_mfma_f32_16x16x32_bf16 v[48:51], a[88:91], a[176:179], v[48:51]\n"
"s_add_u32 s84, s84, 4096\n"
"buffer_load_dwordx4 a[0:3], v1, s[40:43], s84 offen sc1\n"
"v_mfma_f32_16x16x32_bf16 v[52:55], a[80:83], a[148:151], v[52:55]\n"
"ds_read_b64_tr_b16 a[108:109], v12 offset:28672\n"
"v_mfma_f32_16x16x32_bf16 v[52:55], a[84:87], a[164:167], v[52:55]\n"
"ds_read_b64_tr_b16 a[110:111], v12 offset:30720\n"
"v_mfma_f32_16x16x32_bf16 v[52:55], a[88:91], a[180:183], v[52:55]\n"
"v_mfma_f32_16x16x32_bf16 v[56:59], a[80:83], a[152:155], v[56:59]\n"
"ds_read_b64_tr_b16 a[112:113], v12 offset:32768\n"
"v_mfma_f32_16x16x32_bf16 v[56:59], a[84:87], a[168:171], v[56:59]\n"
"ds_read_b64_tr_b16 a[114:115], v12 offset:34816\n"
"v_mfma_f32_16x16x32_bf16 v[56:59], a[88:91], a[184:187], v[56:59]\n"
"buffer_load_dwordx4 a[4:7], v1, s[40:43], s84 offen offset:1024 sc1\n"
"v_mfma_f32_16x16x32_bf16 v[60:63], a[80:83], a[156:159], v[60:63]\n"
"v_mfma_f32_16x16x32_bf16 v[60:63], a[84:87], a[172:175], v[60:63]\n"
"s_waitcnt lgkmcnt(6)\n"
"v_mfma_f32_16x16x32_bf16 v[60:63], a[88:91], a[188:191], v[60:63]\n"
"buffer_load_dwordx4 a[8:11], v1, s[40:43], s84 offen offset:2048 sc1\n"
"v_mfma_f32_16x16x32_bf16 v[64:67], a[92:95], a[144:147], v[64:67]\n"
"ds_read_b64_tr_b16 a[80:81], v13 offset:24576\n"
"v_mfma_f32_16x16x32_bf16 v[64:67], a[96:99], a[160:163], v[64:67]\n"
"ds_read_b64_tr_b16 a[82:83], v13 offset:26624\n"
"v_mfma_f32_16x16x32_bf16 v[64:67], a[100:103], a[176:179], v[64:67]\n"
"v_mfma_f32_16x16x32_bf16 v[68:71], a[92:95], a[148:151], v[68:71]\n"
"ds_read_b64_tr_b16 a[84:85], v13 offset:28672\n"
"v_mfma_f32_16x16x32_bf16 v[68:71], a[96:99], a[164:167], v[68:71]\n"
"ds_read_b64_tr_b16 a[86:87], v13 offset:30720\n"
"v_mfma_f32_16x16x32_bf16 v[68:71], a[100:103], a[180:183], v[68:71]\n"
"buffer_load_dwordx4 a[12:15], v1, s[40:43], s84 offen offset:3072 sc1\n"
"v_mfma_f32_16x16x32_bf16 v[72:75], a[92:95], a[152:155], v[72:75]\n"
"ds_read_b64_tr_b16 a[88:89], v13 offset:32768\n"
"v_mfma_f32_16x16x32_bf16 v[72:75], a[96:99], a[168:171], v[72:75]\n"
"ds_read_b64_tr_b16 a[90:91], v13 offset:34816\n"
"v_mfma_f32_16x16x32_bf16 v[72:75], a[100:103], a[184:187], v[72:75]\n"
"s_add_u32 s84, s84, 4096\n"
"v_mfma_f32_16x16x32_bf16 v[76:79], a[92:95], a[156:159], v[76:79]\n"
"s_add_u32 s82, s77, 2359424\n"
"v_mfma_f32_16x16x32_bf16 v[76:79], a[96:99], a[172:175], v[76:79]\n"
"s_waitcnt lgkmcnt(6)\n"
"v_mfma_f32_16x16x32_bf16 v[76:79], a[100:103], a[188:191], v[76:79]\n"
"s_add_u32 m0, s74, 126976\n"
"v_mfma_f32_16x16x32_bf16 v[80:83], a[104:107], a[144:147], v[80:83]\n"
"ds_read_b64_tr_b16 a[92:93], v12 offset:25088\n"
"v_mfma_f32_16x16x32_bf16 v[80:83], a[108:111], a[160:163], v[80:83]\n"
"buffer_load_dwordx4 v7, s[28:31], s82 offen lds\n"
"v_mfma_f32_16x16x32_bf16 v[80:83], a[112:115], a[176:179], v[80:83]\n"
"s_add_u32 s82, s77, 2621568\n"
"ds_read_b64_tr_b16 a[94:95], v12 offset:27136\n"
"v_mfma_f32_16x16x32_bf16 v[84:87], a[104:107], a[148:151], v[84:87]\n"
"s_add_u32 m0, s74, 131072\n"
"ds_read_b64_tr_b16 a[96:97], v12 offset:29184\n"
"v_mfma_f32_16x16x32_bf16 v[84:87], a[108:111], a[164:167], v[84:87]\n"
"buffer_load_dwordx4 v7, s[28:31], s82 offen lds\n"
"v_mfma_f32_16x16x32_bf16 v[84:87], a[112:115], a[180:183], v[84:87]\n"
"s_add_u32 s82, s77, 2883712\n"
"ds_read_b64_tr_b16 a[98:99], v12 offset:31232\n"
"v_mfma_f32_16x16x32_bf16 v[88:91], a[104:107], a[152:155], v[88:91]\n"
"s_add_u32 m0, s74, 135168\n"
"ds_read_b64_tr_b16 a[100:101], v12 offset:33280\n"
"v_mfma_f32_16x16x32_bf16 v[88:91], a[108:111], a[168:171], v[88:91]\n"
"buffer_load_dwordx4 v7, s[28:31], s82 offen lds\n"
"v_mfma_f32_16x16x32_bf16 v[88:91], a[112:115], a[184:187], v[88:91]\n"
"s_add_u32 s82, s77, 3145856\n"
"ds_read_b64_tr_b16 a[102:103], v12 offset:35328\n"
"v_mfma_f32_16x16x32_bf16 v[92:95], a[104:107], a[156:159], v[92:95]\n"
"s_add_u32 m0, s74, 139264\n"
"v_mfma_f32_16x16x32_bf16 v[92:95], a[108:111], a[172:175], v[92:95]\n"
"s_waitcnt lgkmcnt(6)\n"
"buffer_load_dwordx4 v7, s[28:31], s82 offen lds\n"
"v_mfma_f32_16x16x32_bf16 v[92:95], a[112:115], a[188:191], v[92:95]\n"
"s_add_u32 s82, s77, 3408000\n"
"v_mfma_f32_16x16x32_bf16 v[96:99], a[80:83], a[144:147], v[96:99]\n"
"s_add_u32 m0, s74, 143360\n"
"ds_read_b64_tr_b16 a[104:105], v13 offset:25088\n"
"v_mfma_f32_16x16x32_bf16 v[96:99], a[84:87], a[160:163], v[96:99]\n"
"buffer_load_dwordx4 v7, s[28:31], s82 offen lds\n"
"v_mfma_f32_16x16x32_bf16 v[96:99], a[88:91], a[176:179], v[96:99]\n"
"s_add_u32 s82, s77, 3670144\n"
"ds_read_b64_tr_b16 a[106:107], v13 offset:27136\n"
"v_mfma_f32_16x16x32_bf16 v[100:103], a[80:83], a[148:151], v[100:103]\n"
"s_add_u32 m0, s74, 147456\n"
"ds_read_b64_tr_b16 a[108:109], v13 offset:29184\n"
"v_mfma_f32_16x16x32_bf16 v[100:103], a[84:87], a[164:167], v[100:103]\n"
"buffer_load_dwordx4 v7, s[28:31], s82 offen lds\n"
"v_mfma_f32_16x16x32_bf16 v[100:103], a[88:91], a[180:183], v[100:103]\n"
"s_add_u32 s82, s77, 3932288\n"
"ds_read_b64_tr_b16 a[110:111], v13 offset:31232\n"
"v_mfma_f32_16x16x32_bf16 v[104:107], a[80:83], a[152:155], v[104:107]\n"
"s_add_u32 m0, s74, 151552\n"
"ds_read_b64_tr_b16 a[112:113], v13 offset:33280\n"
"v_mfma_f32_16x16x32_bf16 v[104:107], a[84:87], a[168:171], v[104:107]\n"
"buffer_load_dwordx4 v7, s[28:31], s82 offen lds\n"
"v_mfma_f32_16x16x32_bf16 v[104:107], a[88:91], a[184:187], v[104:107]\n"
"s_add_u32 s82, s77, 4194432\n"
"ds_read_b64_tr_b16 a[114:115], v13 offset:35328\n"
"v_mfma_f32_16x16x32_bf16 v[108:111], a[80:83], a[156:159], v[108:111]\n"
"s_add_u32 m0, s74, 155648\n"
"v_mfma_f32_16x16x32_bf16 v[108:111], a[84:87], a[172:175], v[108:111]\n"
"s_waitcnt lgkmcnt(6)\n"
"buffer_load_dwordx4 v7, s[28:31], s82 offen lds\n"
"v_mfma_f32_16x16x32_bf16 v[108:111], a[88:91], a[188:191], v[108:111]\n"
"s_add_u32 s82, s77, 4456576\n"
"v_mfma_f32_16x16x32_bf16 v[112:115], a[92:95], a[144:147], v[112:115]\n"
"s_barrier\n"
"v_add_u32 v2, 4294930432, v2\n"
"v_add_u32 v3, 4294930432, v3\n"
"v_mfma_f32_16x16x32_bf16 v[112:115], a[96:99], a[160:163], v[112:115]\n"
"s_add_u32 m0, s74, 159744\n"
"ds_read_b64_tr_b16 a[80:81], v2\n"
"v_mfma_f32_16x16x32_bf16 v[112:115], a[100:103], a[176:179], v[112:115]\n"
"buffer_load_dwordx4 v7, s[28:31], s82 offen lds\n"
"v_mfma_f32_16x16x32_bf16 v[116:119], a[92:95], a[148:151], v[116:119]\n"
"ds_read_b64_tr_b16 a[82:83], v2 offset:2048\n"
"v_mfma_f32_16x16x32_bf16 v[116:119], a[96:99], a[164:167], v[116:119]\n"
"buffer_load_dwordx4 a[16:19], v1, s[40:43], s84 offen sc1\n"
"v_mfma_f32_16x16x32_bf16 v[116:119], a[100:103], a[180:183], v[116:119]\n"
"ds_read_b64_tr_b16 a[84:85], v2 offset:4096\n"
"ds_read_b64_tr_b16 a[86:87], v2 offset:6144\n"
"v_mfma_f32_16x16x32_bf16 v[120:123], a[92:95], a[152:155], v[120:123]\n"
"buffer_load_dwordx4 a[20:23], v1, s[40:43], s84 offen offset:1024 sc1\n"
"v_mfma_f32_16x16x32_bf16 v[120:123], a[96:99], a[168:171], v[120:123]\n"
"ds_read_b64_tr_b16 a[88:89], v2 offset:8192\n"
"v_mfma_f32_16x16x32_bf16 v[120:123], a[100:103], a[184:187], v[120:123]\n"
"ds_read_b64_tr_b16 a[90:91], v2 offset:10240\n"
"v_mfma_f32_16x16x32_bf16 v[124:127], a[92:95], a[156:159], v[124:127]\n"
"buffer_load_dwordx4 a[24:27], v1, s[40:43], s84 offen offset:2048 sc1\n"
"v_mfma_f32_16x16x32_bf16 v[124:127], a[96:99], a[172:175], v[124:127]\n"
"s_waitcnt lgkmcnt(6)\n"
"v_mfma_f32_16x16x32_bf16 v[124:127], a[100:103], a[188:191], v[124:127]\n"
"v_mfma_f32_16x16x32_bf16 v[128:131], a[104:107], a[144:147], v[128:131]\n"
"ds_read_b64_tr_b16 a[92:93], v3\n"
"v_mfma_f32_16x16x32_bf16 v[128:131], a[108:111], a[160:163], v[128:131]\n"
"ds_read_b64_tr_b16 a[94:95], v3 offset:2048\n"
"v_mfma_f32_16x16x32_bf16 v[128:131], a[112:115], a[176:179], v[128:131]\n"
"buffer_load_dwordx4 a[28:31], v1, s[40:43], s84 offen offset:3072 sc1\n"
"v_mfma_f32_16x16x32_bf16 v[132:135], a[104:107], a[148:151], v[132:135]\n"
"ds_read_b64_tr_b16 a[96:97], v3 offset:4096\n"
"v_mfma_f32_16x16x32_bf16 v[132:135], a[108:111], a[164:167], v[132:135]\n"
"ds_read_b64_tr_b16 a[98:99], v3 offset:6144\n"
"v_mfma_f32_16x16x32_bf16 v[132:135], a[112:115], a[180:183], v[132:135]\n"
"s_add_u32 s84, s84, 4096\n"
"buffer_load_dwordx4 a[32:35], v1, s[40:43], s84 offen sc1\n"
"v_mfma_f32_16x16x32_bf16 v[136:139], a[104:107], a[152:155], v[136:139]\n"
"ds_read_b64_tr_b16 a[100:101], v3 offset:8192\n"
"v_mfma_f32_16x16x32_bf16 v[136:139], a[108:111], a[168:171], v[136:139]\n"
"ds_read_b64_tr_b16 a[102:103], v3 offset:10240\n"
"v_mfma_f32_16x16x32_bf16 v[136:139], a[112:115], a[184:187], v[136:139]\n"
"v_mfma_f32_16x16x32_bf16 v[140:143], a[104:107], a[156:159], v[140:143]\n"
"buffer_load_dwordx4 a[36:39], v1, s[40:43], s84 offen offset:1024 sc1\n"
"v_mfma_f32_16x16x32_bf16 v[140:143], a[108:111], a[172:175], v[140:143]\n"
"s_waitcnt vmcnt(31) lgkmcnt(6)\n"
"v_mfma_f32_16x16x32_bf16 v[140:143], a[112:115], a[188:191], v[140:143]\n"
"v_mfma_f32_16x16x32_bf16 v[16:19], a[80:83], v[176:179], v[16:19]\n"
"ds_read_b64_tr_b16 a[104:105], v2 offset:512\n"
"v_mfma_f32_16x16x32_bf16 v[16:19], a[84:87], v[192:195], v[16:19]\n"
"ds_read_b64_tr_b16 a[106:107], v2 offset:2560\n"
"v_mfma_f32_16x16x32_bf16 v[16:19], a[88:91], a[192:195], v[16:19]\n"
"buffer_load_dwordx4 a[40:43], v1, s[40:43], s84 offen offset:2048 sc1\n"
"v_mfma_f32_16x16x32_bf16 v[20:23], a[80:83], v[180:183], v[20:23]\n"
"ds_read_b64_tr_b16 a[108:109], v2 offset:4608\n"
"v_mfma_f32_16x16x32_bf16 v[20:23], a[84:87], v[196:199], v[20:23]\n"
"ds_read_b64_tr_b16 a[110:111], v2 offset:6656\n"
"v_mfma_f32_16x16x32_bf16 v[20:23], a[88:91], a[196:199], v[20:23]\n"
"v_mfma_f32_16x16x32_bf16 v[24:27], a[80:83], v[184:187], v[24:27]\n"
"ds_read_b64_tr_b16 a[112:113], v2 offset:8704\n"
"v_mfma_f32_16x16x32_bf16 v[24:27], a[84:87], v[200:203], v[24:27]\n"
"ds_read_b64_tr_b16 a[114:115], v2 offset:10752\n"
"v_mfma_f32_16x16x32_bf16 v[24:27], a[88:91], a[200:203], v[24:27]\n"
"buffer_load_dwordx4 a[44:47], v1, s[40:43], s84 offen offset:3072 sc1\n"
"v_mfma_f32_16x16x32_bf16 v[28:31], a[80:83], v[188:191], v[28:31]\n"
"v_mfma_f32_16x16x32_bf16 v[28:31], a[84:87], v[204:207], v[28:31]\n"
"s_waitcnt lgkmcnt(6)\n"
"v_mfma_f32_16x16x32_bf16 v[28:31], a[88:91], a[204:207], v[28:31]\n"
"s_add_u32 s84, s84, 4096\n"
"v_mfma_f32_16x16x32_bf16 v[32:35], a[92:95], v[176:179], v[32:35]\n"
"s_barrier\n"
"ds_read_b64_tr_b16 a[80:81], v3 offset:512\n"
"v_mfma_f32_16x16x32_bf16 v[32:35], a[96:99], v[192:195], v[32:35]\n"
"ds_read_b64_tr_b16 a[82:83], v3 offset:2560\n"
"v_mfma_f32_16x16x32_bf16 v[32:35], a[100:103], a[192:195], v[32:35]\n"
"s_add_u32 s82, s66, 0\n"
"v_mfma_f32_16x16x32_bf16 v[36:39], a[92:95], v[180:183], v[36:39]\n"
"s_add_u32 m0, s66, 65536\n"
"ds_read_b64_tr_b16 a[84:85], v3 offset:4608\n"
"v_mfma_f32_16x16x32_bf16 v[36:39], a[96:99], v[196:199], v[36:39]\n"
"ds_read_b64_tr_b16 a[86:87], v3 offset:6656\n"
"v_mfma_f32_16x16x32_bf16 v[36:39], a[100:103], a[196:199], v[36:39]\n"
"buffer_load_dwordx4 v244, s[32:35], s82 offen lds\n"
"v_mfma_f32_16x16x32_bf16 v[40:43], a[92:95], v[184:187], v[40:43]\n"
"ds_read_b64_tr_b16 a[88:89], v3 offset:8704\n"
"v_mfma_f32_16x16x32_bf16 v[40:43], a[96:99], v[200:203], v[40:43]\n"
"ds_read_b64_tr_b16 a[90:91], v3 offset:10752\n"
"v_mfma_f32_16x16x32_bf16 v[40:43], a[100:103], a[200:203], v[40:43]\n"
"buffer_load_dwordx4 v245, s[32:35], s82 offen offset:1024 lds\n"
"v_mfma_f32_16x16x32_bf16 v[44:47], a[92:95], v[188:191], v[44:47]\n"
"v_mfma_f32_16x16x32_bf16 v[44:47], a[96:99], v[204:207], v[44:47]\n"
"s_waitcnt lgkmcnt(6)\n"
"v_mfma_f32_16x16x32_bf16 v[44:47], a[100:103], a[204:207], v[44:47]\n"
"v_mfma_f32_16x16x32_bf16 v[48:51], a[104:107], v[176:179], v[48:51]\n"
"ds_read_b64_tr_b16 a[92:93], v2 offset:12288\n"
"v_mfma_f32_16x16x32_bf16 v[48:51], a[108:111], v[192:195], v[48:51]\n"
"ds_read_b64_tr_b16 a[94:95], v2 offset:14336\n"
"v_mfma_f32_16x16x32_bf16 v[48:51], a[112:115], a[192:195], v[48:51]\n"
"buffer_load_dwordx4 v246, s[32:35], s82 offen offset:2048 lds\n"
"v_mfma_f32_16x16x32_bf16 v[52:55], a[104:107], v[180:183], v[52:55]\n"
"ds_read_b64_tr_b16 a[96:97], v2 offset:16384\n"
"v_mfma_f32_16x16x32_bf16 v[52:55], a[108:111], v[196:199], v[52:55]\n"
"ds_read_b64_tr_b16 a[98:99], v2 offset:18432\n"
"v_mfma_f32_16x16x32_bf16 v[52:55], a[112:115], a[196:199], v[52:55]\n"
"buffer_load_dwordx4 v247, s[32:35], s82 offen offset:3072 lds\n"
"v_mfma_f32_16x16x32_bf16 v[56:59], a[104:107], v[184:187], v[56:59]\n"
"ds_read_b64_tr_b16 a[100:101], v2 offset:20480\n"
"v_mfma_f32_16x16x32_bf16 v[56:59], a[108:111], v[200:203], v[56:59]\n"
"ds_read_b64_tr_b16 a[102:103], v2 offset:22528\n"
"v_mfma_f32_16x16x32_bf16 v[56:59], a[112:115], a[200:203], v[56:59]\n"
"v_mfma_f32_16x16x32_bf16 v[60:63], a[104:107], v[188:191], v[60:63]\n"
"v_mfma_f32_16x16x32_bf16 v[60:63], a[108:111], v[204:207], v[60:63]\n"
"s_waitcnt lgkmcnt(6)\n"
"v_mfma_f32_16x16x32_bf16 v[60:63], a[112:115], a[204:207], v[60:63]\n"
"s_add_u32 s82, s66, 16384\n"
"v_mfma_f32_16x16x32_bf16 v[64:67], a[80:83], v[176:179], v[64:67]\n"
"s_add_u32 m0, s66, 98304\n"
"ds_read_b64_tr_b16 a[104:105], v2 offset:24576\n"
"v_mfma_f32_16x16x32_bf16 v[64:67], a[84:87], v[192:195], v[64:67]\n"
"ds_read_b64_tr_b16 a[106:107], v2 offset:26624\n"
"v_mfma_f32_16x16x32_bf16 v[64:67], a[88:91], a[192:195], v[64:67]\n"
"buffer_load_dwordx4 v244, s[32:35], s82 offen lds\n"
"v_mfma_f32_16x16x32_bf16 v[68:71], a[80:83], v[180:183], v[68:71]\n"
"ds_read_b64_tr_b16 a[108:109], v2 offset:28672\n"
"v_mfma_f32_16x16x32_bf16 v[68:71], a[84:87], v[196:199], v[68:71]\n"
"ds_read_b64_tr_b16 a[110:111], v2 offset:30720\n"
"v_mfma_f32_16x16x32_bf16 v[68:71], a[88:91], a[196:199], v[68:71]\n"
"buffer_load_dwordx4 v245, s[32:35], s82 offen offset:1024 lds\n"
"v_mfma_f32_16x16x32_bf16 v[72:75], a[80:83], v[184:187], v[72:75]\n"
"ds_read_b64_tr_b16 a[112:113], v2 offset:32768\n"
"v_mfma_f32_16x16x32_bf16 v[72:75], a[84:87], v[200:203], v[72:75]\n"
"ds_read_b64_tr_b16 a[114:115], v2 offset:34816\n"
"v_mfma_f32_16x16x32_bf16 v[72:75], a[88:91], a[200:203], v[72:75]\n"
"buffer_load_dwordx4 v246, s[32:35], s82 offen offset:2048 lds\n"
"v_mfma_f32_16x16x32_bf16 v[76:79], a[80:83], v[188:191], v[76:79]\n"
"v_mfma_f32_16x16x32_bf16 v[76:79], a[84:87], v[204:207], v[76:79]\n"
"s_waitcnt vmcnt(28) lgkmcnt(6)\n"
"v_mfma_f32_16x16x32_bf16 v[76:79], a[88:91], a[204:207], v[76:79]\n"
"v_mfma_f32_16x16x32_bf16 v[16:19], a[92:95], a[208:211], v[16:19]\n"
"ds_read_b64_tr_b16 a[80:81], v3 offset:12288\n"
"v_mfma_f32_16x16x32_bf16 v[16:19], a[96:99], a[224:227], v[16:19]\n"
"ds_read_b64_tr_b16 a[82:83], v3 offset:14336\n"
"v_mfma_f32_16x16x32_bf16 v[16:19], a[100:103], a[240:243], v[16:19]\n"
"buffer_load_dwordx4 v247, s[32:35], s82 offen offset:3072 lds\n"
"v_mfma_f32_16x16x32_bf16 v[20:23], a[92:95], a[212:215], v[20:23]\n"
"ds_read_b64_tr_b16 a[84:85], v3 offset:16384\n"
"v_mfma_f32_16x16x32_bf16 v[20:23], a[96:99], a[228:231], v[20:23]\n"
"ds_read_b64_tr_b16 a[86:87], v3 offset:18432\n"
"v_mfma_f32_16x16x32_bf16 v[20:23], a[100:103], a[244:247], v[20:23]\n"
"v_mfma_f32_16x16x32_bf16 v[24:27], a[92:95], a[216:219], v[24:27]\n"
"ds_read_b64_tr_b16 a[88:89], v3 offset:20480\n"
"v_mfma_f32_16x16x32_bf16 v[24:27], a[96:99], a[232:235], v[24:27]\n"
"ds_read_b64_tr_b16 a[90:91], v3 offset:22528\n"
"v_mfma_f32_16x16x32_bf16 v[24:27], a[100:103], a[248:251], v[24:27]\n"
"v_mfma_f32_16x16x32_bf16 v[28:31], a[92:95], a[220:223], v[28:31]\n"
"v_mfma_f32_16x16x32_bf16 v[28:31], a[96:99], a[236:239], v[28:31]\n"
"s_waitcnt vmcnt(8) lgkmcnt(6)\n"
"v_mfma_f32_16x16x32_bf16 v[28:31], a[100:103], a[252:255], v[28:31]\n"
"v_mfma_f32_16x16x32_bf16 v[16:19], a[104:107], a[0:3], v[16:19]\n"
"ds_read_b64_tr_b16 a[92:93], v3 offset:24576\n"
"v_mfma_f32_16x16x32_bf16 v[16:19], a[108:111], a[16:19], v[16:19]\n"
"ds_read_b64_tr_b16 a[94:95], v3 offset:26624\n"
"v_mfma_f32_16x16x32_bf16 v[16:19], a[112:115], a[32:35], v[16:19]\n"
"v_mfma_f32_16x16x32_bf16 v[20:23], a[104:107], a[4:7], v[20:23]\n"
"ds_read_b64_tr_b16 a[96:97], v3 offset:28672\n"
"v_mfma_f32_16x16x32_bf16 v[20:23], a[108:111], a[20:23], v[20:23]\n"
"ds_read_b64_tr_b16 a[98:99], v3 offset:30720\n"
"v_mfma_f32_16x16x32_bf16 v[20:23], a[112:115], a[36:39], v[20:23]\n"
"v_mfma_f32_16x16x32_bf16 v[24:27], a[104:107], a[8:11], v[24:27]\n"
"ds_read_b64_tr_b16 a[100:101], v3 offset:32768\n"
"v_mfma_f32_16x16x32_bf16 v[24:27], a[108:111], a[24:27], v[24:27]\n"
"ds_read_b64_tr_b16 a[102:103], v3 offset:34816\n"
"v_mfma_f32_16x16x32_bf16 v[24:27], a[112:115], a[40:43], v[24:27]\n"
"v_mfma_f32_16x16x32_bf16 v[28:31], a[104:107], a[12:15], v[28:31]\n"
"v_mfma_f32_16x16x32_bf16 v[28:31], a[108:111], a[28:31], v[28:31]\n"
"s_waitcnt lgkmcnt(6)\n"
"v_mfma_f32_16x16x32_bf16 v[28:31], a[112:115], a[44:47], v[28:31]\n"
"v_mfma_f32_16x16x32_bf16 v[32:35], a[80:83], a[208:211], v[32:35]\n"
"ds_read_b64_tr_b16 a[104:105], v2 offset:12800\n"
"v_mfma_f32_16x16x32_bf16 v[32:35], a[84:87], a[224:227], v[32:35]\n"
"ds_read_b64_tr_b16 a[106:107], v2 offset:14848\n"
"v_mfma_f32_16x16x32_bf16 v[32:35], a[88:91], a[240:243], v[32:35]\n"
"v_mfma_f32_16x16x32_bf16 v[36:39], a[80:83], a[212:215], v[36:39]\n"
"ds_read_b64_tr_b16 a[108:109], v2 offset:16896\n"
"v_mfma_f32_16x16x32_bf16 v[36:39], a[84:87], a[228:231], v[36:39]\n"
"ds_read_b64_tr_b16 a[110:111], v2 offset:18944\n"
"v_mul_f32 v16, s87, v16\n"
"v_mfma_f32_16x16x32_bf16 v[36:39], a[88:91], a[244:247], v[36:39]\n"
"v_mul_f32 v17, s87, v17\n"
"v_mul_f32 v18, s87, v18\n"
"v_mfma_f32_16x16x32_bf16 v[40:43], a[80:83], a[216:219], v[40:43]\n"
"ds_read_b64_tr_b16 a[112:113], v2 offset:20992\n"
"v_mul_f32 v19, s87, v19\n"
"v_mfma_f32_16x16x32_bf16 v[40:43], a[84:87], a[232:235], v[40:43]\n"
"ds_read_b64_tr_b16 a[114:115], v2 offset:23040\n"
"v_cvt_pk_bf16_f32 v144, v16, v17\n"
"v_mfma_f32_16x16x32_bf16 v[40:43], a[88:91], a[248:251], v[40:43]\n"
"v_cvt_pk_bf16_f32 v145, v18, v19\n"
"v_mfma_f32_16x16x32_bf16 v[44:47], a[80:83], a[220:223], v[44:47]\n"
"v_mul_f32 v20, s87, v20\n"
"v_mfma_f32_16x16x32_bf16 v[44:47], a[84:87], a[236:239], v[44:47]\n"
"s_waitcnt lgkmcnt(6)\n"
"v_mul_f32 v21, s87, v21\n"
"v_mfma_f32_16x16x32_bf16 v[44:47], a[88:91], a[252:255], v[44:47]\n"
"v_mul_f32 v22, s87, v22\n"
"v_mfma_f32_16x16x32_bf16 v[32:35], a[92:95], a[0:3], v[32:35]\n"
"ds_read_b64_tr_b16 a[80:81], v2 offset:25088\n"
"v_mul_f32 v23, s87, v23\n"
"v_mfma_f32_16x16x32_bf16 v[32:35], a[96:99], a[16:19], v[32:35]\n"
"ds_read_b64_tr_b16 a[82:83], v2 offset:27136\n"
"v_cvt_pk_bf16_f32 v152, v20, v21\n"
"v_mfma_f32_16x16x32_bf16 v[32:35], a[100:103], a[32:35], v[32:35]\n"
"v_cvt_pk_bf16_f32 v153, v22, v23\n"
"v_mfma_f32_16x16x32_bf16 v[36:39], a[92:95], a[4:7], v[36:39]\n"
"ds_read_b64_tr_b16 a[84:85], v2 offset:29184\n"
"v_mul_f32 v24, s87, v24\n"
"v_mfma_f32_16x16x32_bf16 v[36:39], a[96:99], a[20:23], v[36:39]\n"
"ds_read_b64_tr_b16 a[86:87], v2 offset:31232\n"
"v_mul_f32 v25, s87, v25\n"
"v_mfma_f32_16x16x32_bf16 v[36:39], a[100:103], a[36:39], v[36:39]\n"
"v_mul_f32 v26, s87, v26\n"
"v_mul_f32 v27, s87, v27\n"
"v_mfma_f32_16x16x32_bf16 v[40:43], a[92:95], a[8:11], v[40:43]\n"
"ds_read_b64_tr_b16 a[88:89], v2 offset:33280\n"
"v_cvt_pk_bf16_f32 v160, v24, v25\n"
"v_mfma_f32_16x16x32_bf16 v[40:43], a[96:99], a[24:27], v[40:43]\n"
"ds_read_b64_tr_b16 a[90:91], v2 offset:35328\n"
"v_cvt_pk_bf16_f32 v161, v26, v27\n"
"v_mfma_f32_16x16x32_bf16 v[40:43], a[100:103], a[40:43], v[40:43]\n"
"v_mul_f32 v28, s87, v28\n"
"v_mfma_f32_16x16x32_bf16 v[44:47], a[92:95], a[12:15], v[44:47]\n"
"v_mul_f32 v29, s87, v29\n"
"v_mfma_f32_16x16x32_bf16 v[44:47], a[96:99], a[28:31], v[44:47]\n"
"s_waitcnt lgkmcnt(6)\n"
"v_mul_f32 v30, s87, v30\n"
"v_mfma_f32_16x16x32_bf16 v[44:47], a[100:103], a[44:47], v[44:47]\n"
"v_mul_f32 v31, s87, v31\n"
"v_mfma_f32_16x16x32_bf16 v[48:51], a[104:107], a[208:211], v[48:51]\n"
"ds_read_b64_tr_b16 a[92:93], v3 offset:12800\n"
"v_cvt_pk_bf16_f32 v168, v28, v29\n"
"v_mfma_f32_16x16x32_bf16 v[48:51], a[108:111], a[224:227], v[48:51]\n"
"ds_read_b64_tr_b16 a[94:95], v3 offset:14848\n"
"v_cvt_pk_bf16_f32 v169, v30, v31\n"
"v_mfma_f32_16x16x32_bf16 v[48:51], a[112:115], a[240:243], v[48:51]\n"
"v_mfma_f32_16x16x32_bf16 v[52:55], a[104:107], a[212:215], v[52:55]\n"
"ds_read_b64_tr_b16 a[96:97], v3 offset:16896\n"
"v_mfma_f32_16x16x32_bf16 v[52:55], a[108:111], a[228:231], v[52:55]\n"
"ds_read_b64_tr_b16 a[98:99], v3 offset:18944\n"
"v_mul_f32 v32, s87, v32\n"
"v_mfma_f32_16x16x32_bf16 v[52:55], a[112:115], a[244:247], v[52:55]\n"
"v_mul_f32 v33, s87, v33\n"
"v_mul_f32 v34, s87, v34\n"
"v_mfma_f32_16x16x32_bf16 v[56:59], a[104:107], a[216:219], v[56:59]\n"
"ds_read_b64_tr_b16 a[100:101], v3 offset:20992\n"
"v_mul_f32 v35, s87, v35\n"
"v_mfma_f32_16x16x32_bf16 v[56:59], a[108:111], a[232:235], v[56:59]\n"
"ds_read_b64_tr_b16 a[102:103], v3 offset:23040\n"
"v_cvt_pk_bf16_f32 v146, v32, v33\n"
"v_mfma_f32_16x16x32_bf16 v[56:59], a[112:115], a[248:251], v[56:59]\n"
"v_cvt_pk_bf16_f32 v147, v34, v35\n"
"v_mul_f32 v36, s87, v36\n"
"v_mfma_f32_16x16x32_bf16 v[60:63], a[104:107], a[220:223], v[60:63]\n"
"v_mul_f32 v37, s87, v37\n"
"v_mfma_f32_16x16x32_bf16 v[60:63], a[108:111], a[236:239], v[60:63]\n"
"s_waitcnt lgkmcnt(6)\n"
"v_mul_f32 v38, s87, v38\n"
"v_mul_f32 v39, s87, v39\n"
"v_mfma_f32_16x16x32_bf16 v[60:63], a[112:115], a[252:255], v[60:63]\n"
"v_cvt_pk_bf16_f32 v154, v36, v37\n"
"v_mfma_f32_16x16x32_bf16 v[48:51], a[80:83], a[0:3], v[48:51]\n"
"ds_read_b64_tr_b16 a[104:105], v3 offset:25088\n"
"v_cvt_pk_bf16_f32 v155, v38, v39\n"
"v_mfma_f32_16x16x32_bf16 v[48:51], a[84:87], a[16:19], v[48:51]\n"
"ds_read_b64_tr_b16 a[106:107], v3 offset:27136\n"
"v_mul_f32 v40, s87, v40\n"
"v_mfma_f32_16x16x32_bf16 v[48:51], a[88:91], a[32:35], v[48:51]\n"
"v_mul_f32 v41, s87, v41\n"
"v_mul_f32 v42, s87, v42\n"
"v_mfma_f32_16x16x32_bf16 v[52:55], a[80:83], a[4:7], v[52:55]\n"
"ds_read_b64_tr_b16 a[108:109], v3 offset:29184\n"
"v_mul_f32 v43, s87, v43\n"
"v_mfma_f32_16x16x32_bf16 v[52:55], a[84:87], a[20:23], v[52:55]\n"
"ds_read_b64_tr_b16 a[110:111], v3 offset:31232\n"
"v_cvt_pk_bf16_f32 v162, v40, v41\n"
"v_mfma_f32_16x16x32_bf16 v[52:55], a[88:91], a[36:39], v[52:55]\n"
"v_cvt_pk_bf16_f32 v163, v42, v43\n"
"v_mul_f32 v44, s87, v44\n"
"v_mfma_f32_16x16x32_bf16 v[56:59], a[80:83], a[8:11], v[56:59]\n"
"ds_read_b64_tr_b16 a[112:113], v3 offset:33280\n"
"v_mul_f32 v45, s87, v45\n"
"v_mfma_f32_16x16x32_bf16 v[56:59], a[84:87], a[24:27], v[56:59]\n"
"ds_read_b64_tr_b16 a[114:115], v3 offset:35328\n"
"v_mul_f32 v46, s87, v46\n"
"v_mfma_f32_16x16x32_bf16 v[56:59], a[88:91], a[40:43], v[56:59]\n"
"v_mul_f32 v47, s87, v47\n"
"v_cvt_pk_bf16_f32 v170, v44, v45\n"
"v_mfma_f32_16x16x32_bf16 v[60:63], a[80:83], a[12:15], v[60:63]\n"
"v_cvt_pk_bf16_f32 v171, v46, v47\n"
"v_mfma_f32_16x16x32_bf16 v[60:63], a[84:87], a[28:31], v[60:63]\n"
"s_waitcnt lgkmcnt(6)\n"
"v_permlane16_swap_b32 v144, v146\n"
"v_permlane16_swap_b32 v145, v147\n"
"v_mfma_f32_16x16x32_bf16 v[60:63], a[88:91], a[44:47], v[60:63]\n"
"v_permlane16_swap_b32 v152, v154\n"
"v_mfma_f32_16x16x32_bf16 v[64:67], a[92:95], a[208:211], v[64:67]\n"
"s_barrier\n"
"v_add_u32 v2, 110592, v2\n"
"v_add_u32 v3, 110592, v3\n"
"v_mfma_f32_16x16x32_bf16 v[64:67], a[96:99], a[224:227], v[64:67]\n"
"ds_read_b64_tr_b16 a[80:81], v2\n"
"v_permlane16_swap_b32 v153, v155\n"
"v_mfma_f32_16x16x32_bf16 v[64:67], a[100:103], a[240:243], v[64:67]\n"
"ds_read_b64_tr_b16 a[82:83], v2 offset:2048\n"
"v_permlane16_swap_b32 v160, v162\n"
"v_mfma_f32_16x16x32_bf16 v[68:71], a[92:95], a[212:215], v[68:71]\n"
"v_permlane16_swap_b32 v161, v163\n"
"v_permlane16_swap_b32 v168, v170\n"
"v_mfma_f32_16x16x32_bf16 v[68:71], a[96:99], a[228:231], v[68:71]\n"
"ds_read_b64_tr_b16 a[84:85], v2 offset:4096\n"
"v_permlane16_swap_b32 v169, v171\n"
"v_mfma_f32_16x16x32_bf16 v[68:71], a[100:103], a[244:247], v[68:71]\n"
"ds_read_b64_tr_b16 a[86:87], v2 offset:6144\n"
"v_mul_f32 v48, s87, v48\n"
"v_mfma_f32_16x16x32_bf16 v[72:75], a[92:95], a[216:219], v[72:75]\n"
"v_mul_f32 v49, s87, v49\n"
"v_mul_f32 v50, s87, v50\n"
"v_mfma_f32_16x16x32_bf16 v[72:75], a[96:99], a[232:235], v[72:75]\n"
"ds_read_b64_tr_b16 a[88:89], v2 offset:8192\n"
"v_mul_f32 v51, s87, v51\n"
"v_mfma_f32_16x16x32_bf16 v[72:75], a[100:103], a[248:251], v[72:75]\n"
"ds_read_b64_tr_b16 a[90:91], v2 offset:10240\n"
"v_cvt_pk_bf16_f32 v148, v48, v49\n"
"v_mfma_f32_16x16x32_bf16 v[76:79], a[92:95], a[220:223], v[76:79]\n"
"v_cvt_pk_bf16_f32 v149, v50, v51\n"
"v_mfma_f32_16x16x32_bf16 v[76:79], a[96:99], a[236:239], v[76:79]\n"
"s_waitcnt lgkmcnt(6)\n"
"v_mul_f32 v52, s87, v52\n"
"v_mfma_f32_16x16x32_bf16 v[76:79], a[100:103], a[252:255], v[76:79]\n"
"v_mul_f32 v53, s87, v53\n"
"v_mfma_f32_16x16x32_bf16 v[64:67], a[104:107], a[0:3], v[64:67]\n"
"ds_read_b64_tr_b16 a[92:93], v2 offset:12288\n"
"v_mul_f32 v54, s87, v54\n"
"v_mfma_f32_16x16x32_bf16 v[64:67], a[108:111], a[16:19], v[64:67]\n"
"ds_read_b64_tr_b16 a[94:95], v2 offset:14336\n"
"v_mul_f32 v55, s87, v55\n"
"v_mfma_f32_16x16x32_bf16 v[64:67], a[112:115], a[32:35], v[64:67]\n"
"v_cvt_pk_bf16_f32 v156, v52, v53\n"
"v_cvt_pk_bf16_f32 v157, v54, v55\n"
"v_mfma_f32_16x16x32_bf16 v[68:71], a[104:107], a[4:7], v[68:71]\n"
"ds_read_b64_tr_b16 a[96:97], v2 offset:16384\n"
"v_mul_f32 v56, s87, v56\n"
"v_mfma_f32_16x16x32_bf16 v[68:71], a[108:111], a[20:23], v[68:71]\n"
"ds_read_b64_tr_b16 a[98:99], v2 offset:18432\n"
"v_mul_f32 v57, s87, v57\n"
"v_mfma_f32_16x16x32_bf16 v[68:71], a[112:115], a[36:39], v[68:71]\n"
"v_mul_f32 v58, s87, v58\n"
"v_mfma_f32_16x16x32_bf16 v[72:75], a[104:107], a[8:11], v[72:75]\n"
"ds_read_b64_tr_b16 a[100:101], v2 offset:20480\n"
"v_mul_f32 v59, s87, v59\n"
"v_mfma_f32_16x16x32_bf16 v[72:75], a[108:111], a[24:27], v[72:75]\n"
"ds_read_b64_tr_b16 a[102:103], v2 offset:22528\n"
"v_cvt_pk_bf16_f32 v164, v56, v57\n"
"v_mfma_f32_16x16x32_bf16 v[72:75], a[112:115], a[40:43], v[72:75]\n"
"v_cvt_pk_bf16_f32 v165, v58, v59\n"
"v_mul_f32 v60, s87, v60\n"
"v_mfma_f32_16x16x32_bf16 v[76:79], a[104:107], a[12:15], v[76:79]\n"
"v_mul_f32 v61, s87, v61\n"
"v_mfma_f32_16x16x32_bf16 v[76:79], a[108:111], a[28:31], v[76:79]\n"
"s_waitcnt lgkmcnt(6)\n"
"v_mul_f32 v62, s87, v62\n"
"v_mfma_f32_16x16x32_bf16 v[76:79], a[112:115], a[44:47], v[76:79]\n"
"v_mul_f32 v63, s87, v63\n"
"v_mfma_f32_16x16x32_bf16 v[80:83], a[80:83], v[176:179], v[80:83]\n"
"ds_read_b64_tr_b16 a[104:105], v2 offset:24576\n"
"v_cvt_pk_bf16_f32 v172, v60, v61\n"
"v_mfma_f32_16x16x32_bf16 v[80:83], a[84:87], v[192:195], v[80:83]\n"
"ds_read_b64_tr_b16 a[106:107], v2 offset:26624\n"
"v_cvt_pk_bf16_f32 v173, v62, v63\n"
"v_mfma_f32_16x16x32_bf16 v[80:83], a[88:91], a[192:195], v[80:83]\n"
"v_mfma_f32_16x16x32_bf16 v[84:87], a[80:83], v[180:183], v[84:87]\n"
"ds_read_b64_tr_b16 a[108:109], v2 offset:28672\n"
"v_mfma_f32_16x16x32_bf16 v[84:87], a[84:87], v[196:199], v[84:87]\n"
"ds_read_b64_tr_b16 a[110:111], v2 offset:30720\n"
"v_mul_f32 v64, s87, v64\n"
"v_mfma_f32_16x16x32_bf16 v[84:87], a[88:91], a[196:199], v[84:87]\n"
"v_mul_f32 v65, s87, v65\n"
"v_mfma_f32_16x16x32_bf16 v[88:91], a[80:83], v[184:187], v[88:91]\n"
"ds_read_b64_tr_b16 a[112:113], v2 offset:32768\n"
"v_mul_f32 v66, s87, v66\n"
"v_mfma_f32_16x16x32_bf16 v[88:91], a[84:87], v[200:203], v[88:91]\n"
"ds_read_b64_tr_b16 a[114:115], v2 offset:34816\n"
"v_mul_f32 v67, s87, v67\n"
"v_mfma_f32_16x16x32_bf16 v[88:91], a[88:91], a[200:203], v[88:91]\n"
"v_cvt_pk_bf16_f32 v150, v64, v65\n"
"v_mfma_f32_16x16x32_bf16 v[92:95], a[80:83], v[188:191], v[92:95]\n"
"v_cvt_pk_bf16_f32 v151, v66, v67\n"
"v_mfma_f32_16x16x32_bf16 v[92:95], a[84:87], v[204:207], v[92:95]\n"
"s_waitcnt lgkmcnt(6)\n"
"v_mul_f32 v68, s87, v68\n"
"v_mfma_f32_16x16x32_bf16 v[92:95], a[88:91], a[204:207], v[92:95]\n"
"v_mul_f32 v69, s87, v69\n"
"v_mfma_f32_16x16x32_bf16 v[80:83], a[92:95], a[208:211], v[80:83]\n"
"ds_read_b64_tr_b16 a[80:81], v3\n"
"v_mul_f32 v70, s87, v70\n"
"v_mfma_f32_16x16x32_bf16 v[80:83], a[96:99], a[224:227], v[80:83]\n"
"s_waitcnt vmcnt(0)\n"
"ds_read_b64_tr_b16 a[82:83], v3 offset:2048\n"
"v_mul_f32 v71, s87, v71\n"
"v_mfma_f32_16x16x32_bf16 v[80:83], a[100:103], a[240:243], v[80:83]\n"
"s_barrier\n"
"v_cvt_pk_bf16_f32 v158, v68, v69\n"
"v_mfma_f32_16x16x32_bf16 v[84:87], a[92:95], a[212:215], v[84:87]\n"
"s_add_u32 s82, s66, 32768\n"
"ds_read_b64_tr_b16 a[84:85], v3 offset:4096\n"
"v_cvt_pk_bf16_f32 v159, v70, v71\n"
"v_mfma_f32_16x16x32_bf16 v[84:87], a[96:99], a[228:231], v[84:87]\n"
"s_add_u32 m0, s66, 0\n"
"ds_read_b64_tr_b16 a[86:87], v3 offset:6144\n"
"v_mul_f32 v72, s87, v72\n"
"v_mfma_f32_16x16x32_bf16 v[84:87], a[100:103], a[244:247], v[84:87]\n"
"v_mul_f32 v73, s87, v73\n"
"v_mfma_f32_16x16x32_bf16 v[88:91], a[92:95], a[216:219], v[88:91]\n"
"ds_read_b64_tr_b16 a[88:89], v3 offset:8192\n"
"v_mul_f32 v74, s87, v74\n"
"v_mfma_f32_16x16x32_bf16 v[88:91], a[96:99], a[232:235], v[88:91]\n"
"ds_read_b64_tr_b16 a[90:91], v3 offset:10240\n"
"v_mul_f32 v75, s87, v75\n"
"v_mfma_f32_16x16x32_bf16 v[88:91], a[100:103], a[248:251], v[88:91]\n"
"buffer_load_dwordx4 v244, s[32:35], s82 offen lds\n"
"v_mfma_f32_16x16x32_bf16 v[92:95], a[92:95], a[220:223], v[92:95]\n"
"v_cvt_pk_bf16_f32 v166, v72, v73\n"
"v_cvt_pk_bf16_f32 v167, v74, v75\n"
"v_mfma_f32_16x16x32_bf16 v[92:95], a[96:99], a[236:239], v[92:95]\n"
"s_waitcnt lgkmcnt(6)\n"
"ds_read_b128 a[128:131], v250\n"
"v_mul_f32 v76, s87, v76\n"
"v_mfma_f32_16x16x32_bf16 v[92:95], a[100:103], a[252:255], v[92:95]\n"
"buffer_load_dwordx4 v245, s[32:35], s82 offen offset:1024 lds\n"
"v_mfma_f32_16x16x32_bf16 v[80:83], a[104:107], a[0:3], v[80:83]\n"
"ds_read_b64_tr_b16 a[92:93], v3 offset:12288\n"
"v_mul_f32 v77, s87, v77\n"
"v_mfma_f32_16x16x32_bf16 v[80:83], a[108:111], a[16:19], v[80:83]\n"
"ds_read_b64_tr_b16 a[94:95], v3 offset:14336\n"
"v_mul_f32 v78, s87, v78\n"
"v_mfma_f32_16x16x32_bf16 v[80:83], a[112:115], a[32:35], v[80:83]\n"
"v_mul_f32 v79, s87, v79\n"
"v_cvt_pk_bf16_f32 v174, v76, v77\n"
"v_mfma_f32_16x16x32_bf16 v[84:87], a[104:107], a[4:7], v[84:87]\n"
"ds_read_b64_tr_b16 a[96:97], v3 offset:16384\n"
"v_cvt_pk_bf16_f32 v175, v78, v79\n"
"v_mfma_f32_16x16x32_bf16 v[84:87], a[108:111], a[20:23], v[84:87]\n"
"ds_read_b64_tr_b16 a[98:99], v3 offset:18432\n"
"ds_read_b128 a[132:135], v251\n"
"v_mfma_f32_16x16x32_bf16 v[84:87], a[112:115], a[36:39], v[84:87]\n"
"v_permlane16_swap_b32 v148, v150\n"
"v_permlane16_swap_b32 v149, v151\n"
"v_mfma_f32_16x16x32_bf16 v[88:91], a[104:107], a[8:11], v[88:91]\n"
"ds_read_b64_tr_b16 a[100:101], v3 offset:20480\n"
"v_permlane16_swap_b32 v156, v158\n"
"v_mfma_f32_16x16x32_bf16 v[88:91], a[108:111], a[24:27], v[88:91]\n"
"buffer_load_dwordx4 v246, s[32:35], s82 offen offset:2048 lds\n"
"v_mfma_f32_16x16x32_bf16 v[88:91], a[112:115], a[40:43], v[88:91]\n"
"ds_read_b64_tr_b16 a[102:103], v3 offset:22528\n"
"v_permlane16_swap_b32 v157, v159\n"
"v_mfma_f32_16x16x32_bf16 v[92:95], a[104:107], a[12:15], v[92:95]\n"
"v_permlane16_swap_b32 v164, v166\n"
"v_permlane16_swap_b32 v165, v167\n"
"v_mfma_f32_16x16x32_bf16 v[92:95], a[108:111], a[28:31], v[92:95]\n"
"s_waitcnt lgkmcnt(8)\n"
"ds_read_b128 a[136:139], v252\n"
"v_permlane16_swap_b32 v172, v174\n"
"v_mfma_f32_16x16x32_bf16 v[92:95], a[112:115], a[44:47], v[92:95]\n"
"buffer_load_dwordx4 v247, s[32:35], s82 offen offset:3072 lds\n"
"v_mfma_f32_16x16x32_bf16 v[96:99], a[80:83], v[176:179], v[96:99]\n"
"ds_read_b64_tr_b16 a[104:105], v3 offset:24576\n"
"v_permlane16_swap_b32 v173, v175\n"
"v_mfma_f32_16x16x32_bf16 v[96:99], a[84:87], v[192:195], v[96:99]\n"
"s_add_u32 s82, s66, 16384\n"
"ds_read_b64_tr_b16 a[106:107], v3 offset:26624\n"
"v_mfma_f32_16x16x32_bf16 v[96:99], a[88:91], a[192:195], v[96:99]\n"
"s_add_u32 m0, s66, 16384\n"
"ds_read_b128 a[140:143], v253\n"
"v_mfma_f32_16x16x32_bf16 v[100:103], a[80:83], v[180:183], v[100:103]\n"
"ds_read_b64_tr_b16 a[108:109], v3 offset:28672\n"
"v_mfma_f32_16x16x32_bf16 v[100:103], a[84:87], v[196:199], v[100:103]\n"
"ds_read_b64_tr_b16 a[110:111], v3 offset:30720\n"
"v_mul_f32 v80, s87, v80\n"
"v_mfma_f32_16x16x32_bf16 v[100:103], a[88:91], a[196:199], v[100:103]\n"
"buffer_load_dwordx4 v248, s[36:39], s82 offen lds\n"
"v_mfma_f32_16x16x32_bf16 v[104:107], a[80:83], v[184:187], v[104:107]\n"
"ds_read_b64_tr_b16 a[112:113], v3 offset:32768\n"
"v_mul_f32 v81, s87, v81\n"
"v_mfma_f32_16x16x32_bf16 v[104:107], a[84:87], v[200:203], v[104:107]\n"
"ds_read_b64_tr_b16 a[114:115], v3 offset:34816\n"
"v_mul_f32 v82, s87, v82\n"
"v_mfma_f32_16x16x32_bf16 v[104:107], a[88:91], a[200:203], v[104:107]\n"
"ds_read_b128 a[144:147], v250 offset:4096\n"
"v_mul_f32 v83, s87, v83\n"
"v_mfma_f32_16x16x32_bf16 v[108:111], a[80:83], v[188:191], v[108:111]\n"
"buffer_load_dwordx4 v249, s[36:39], s82 offen offset:1024 lds\n"
"v_mfma_f32_16x16x32_bf16 v[108:111], a[84:87], v[204:207], v[108:111]\n"
"s_waitcnt lgkmcnt(9)\n"
"v_cvt_pk_bf16_f32 v48, v80, v81\n"
"v_cvt_pk_bf16_f32 v49, v82, v83\n"
"v_mfma_f32_16x16x32_bf16 v[108:111], a[88:91], a[204:207], v[108:111]\n"
"v_mfma_f32_16x16x32_bf16 v[96:99], a[92:95], a[208:211], v[96:99]\n"
"ds_read_b64_tr_b16 a[80:81], v2 offset:512\n"
"v_mul_f32 v84, s87, v84\n"
"v_mfma_f32_16x16x32_bf16 v[96:99], a[96:99], a[224:227], v[96:99]\n"
"ds_read_b64_tr_b16 a[82:83], v2 offset:2560\n"
"v_mul_f32 v85, s87, v85\n"
"v_mfma_f32_16x16x32_bf16 v[96:99], a[100:103], a[240:243], v[96:99]\n"
"ds_read_b128 a[148:151], v251 offset:4096\n"
"v_mul_f32 v86, s87, v86\n"
"v_mfma_f32_16x16x32_bf16 v[100:103], a[92:95], a[212:215], v[100:103]\n"
"ds_read_b64_tr_b16 a[84:85], v2 offset:4608\n"
"v_mfma_f32_16x16x32_bf16 v[100:103], a[96:99], a[228:231], v[100:103]\n"
"ds_read_b64_tr_b16 a[86:87], v2 offset:6656\n"
"v_mul_f32 v87, s87, v87\n"
"v_mfma_f32_16x16x32_bf16 v[100:103], a[100:103], a[244:247], v[100:103]\n"
"buffer_load_dwordx4 v248, s[36:39], s82 offen offset:2048 lds\n"
"v_mfma_f32_16x16x32_bf16 v[104:107], a[92:95], a[216:219], v[104:107]\n"
"ds_read_b64_tr_b16 a[88:89], v2 offset:8704\n"
"v_cvt_pk_bf16_f32 v56, v84, v85\n"
"v_mfma_f32_16x16x32_bf16 v[104:107], a[96:99], a[232:235], v[104:107]\n"
"ds_read_b64_tr_b16 a[90:91], v2 offset:10752\n"
"v_cvt_pk_bf16_f32 v57, v86, v87\n"
"v_mfma_f32_16x16x32_bf16 v[104:107], a[100:103], a[248:251], v[104:107]\n"
"buffer_load_dwordx4 v249, s[36:39], s82 offen offset:3072 lds\n"
"v_mfma_f32_16x16x32_bf16 v[108:111], a[92:95], a[220:223], v[108:111]\n"
"v_mul_f32 v88, s87, v88\n"
"v_mul_f32 v89, s87, v89\n"
"v_mfma_f32_16x16x32_bf16 v[108:111], a[96:99], a[236:239], v[108:111]\n"
"s_waitcnt lgkmcnt(8)\n"
"ds_read_b128 a[152:155], v252 offset:4096\n"
"v_mul_f32 v90, s87, v90\n"
"v_mfma_f32_16x16x32_bf16 v[108:111], a[100:103], a[252:255], v[108:111]\n"
"s_add_u32 s82, s66, 49152\n"
"v_mfma_f32_16x16x32_bf16 v[96:99], a[104:107], a[0:3], v[96:99]\n"
"s_add_u32 m0, s66, 32768\n"
"ds_read_b64_tr_b16 a[92:93], v2 offset:12800\n"
"v_mul_f32 v91, s87, v91\n"
"v_mfma_f32_16x16x32_bf16 v[96:99], a[108:111], a[16:19], v[96:99]\n"
"ds_read_b64_tr_b16 a[94:95], v2 offset:14848\n"
"v_cvt_pk_bf16_f32 v64, v88, v89\n"
"v_mfma_f32_16x16x32_bf16 v[96:99], a[112:115], a[32:35], v[96:99]\n"
"buffer_load_dwordx4 v244, s[32:35], s82 offen lds\n"
"v_mfma_f32_16x16x32_bf16 v[100:103], a[104:107], a[4:7], v[100:103]\n"
"ds_read_b64_tr_b16 a[96:97], v2 offset:16896\n"
"v_cvt_pk_bf16_f32 v65, v90, v91\n"
"v_mfma_f32_16x16x32_bf16 v[100:103], a[108:111], a[20:23], v[100:103]\n"
"ds_read_b64_tr_b16 a[98:99], v2 offset:18944\n"
"ds_read_b128 a[156:159], v253 offset:4096\n"
"v_mfma_f32_16x16x32_bf16 v[100:103], a[112:115], a[36:39], v[100:103]\n"
"v_mul_f32 v92, s87, v92\n"
"v_mul_f32 v93, s87, v93\n"
"v_mfma_f32_16x16x32_bf16 v[104:107], a[104:107], a[8:11], v[104:107]\n"
"ds_read_b64_tr_b16 a[100:101], v2 offset:20992\n"
"v_mul_f32 v94, s87, v94\n"
"v_mfma_f32_16x16x32_bf16 v[104:107], a[108:111], a[24:27], v[104:107]\n"
"ds_read_b64_tr_b16 a[102:103], v2 offset:23040\n"
"v_mfma_f32_16x16x32_bf16 v[104:107], a[112:115], a[40:43], v[104:107]\n"
"buffer_load_dwordx4 v245, s[32:35], s82 offen offset:1024 lds\n"
"v_mfma_f32_16x16x32_bf16 v[108:111], a[104:107], a[12:15], v[108:111]\n"
"v_mul_f32 v95, s87, v95\n"
"v_cvt_pk_bf16_f32 v72, v92, v93\n"
"v_mfma_f32_16x16x32_bf16 v[108:111], a[108:111], a[28:31], v[108:111]\n"
"s_waitcnt lgkmcnt(8)\n"
"ds_read_b128 a[160:163], v250 offset:8192\n"
"v_cvt_pk_bf16_f32 v73, v94, v95\n"
"v_mfma_f32_16x16x32_bf16 v[108:111], a[112:115], a[44:47], v[108:111]\n"
"buffer_load_dwordx4 v246, s[32:35], s82 offen offset:2048 lds\n"
"v_mfma_f32_16x16x32_bf16 v[112:115], a[80:83], v[176:179], v[112:115]\n"
"ds_read_b64_tr_b16 a[104:105], v2 offset:25088\n"
"v_mfma_f32_16x16x32_bf16 v[112:115], a[84:87], v[192:195], v[112:115]\n"
"ds_read_b64_tr_b16 a[106:107], v2 offset:27136\n"
"v_mfma_f32_16x16x32_bf16 v[112:115], a[88:91], a[192:195], v[112:115]\n"
"ds_read_b128 a[164:167], v251 offset:8192\n"
"v_mfma_f32_16x16x32_bf16 v[116:119], a[80:83], v[180:183], v[116:119]\n"
"ds_read_b64_tr_b16 a[108:109], v2 offset:29184\n"
"v_mfma_f32_16x16x32_bf16 v[116:119], a[84:87], v[196:199], v[116:119]\n"
"ds_read_b64_tr_b16 a[110:111], v2 offset:31232\n"
"v_mul_f32 v96, s87, v96\n"
"v_mfma_f32_16x16x32_bf16 v[116:119], a[88:91], a[196:199], v[116:119]\n"
"v_mul_f32 v97, s87, v97\n"
"v_mfma_f32_16x16x32_bf16 v[120:123], a[80:83], v[184:187], v[120:123]\n"
"ds_read_b64_tr_b16 a[112:113], v2 offset:33280\n"
"v_mul_f32 v98, s87, v98\n"
"v_mfma_f32_16x16x32_bf16 v[120:123], a[84:87], v[200:203], v[120:123]\n"
"ds_read_b64_tr_b16 a[114:115], v2 offset:35328\n"
"v_mul_f32 v99, s87, v99\n"
"v_mfma_f32_16x16x32_bf16 v[120:123], a[88:91], a[200:203], v[120:123]\n"
"buffer_load_dwordx4 v247, s[32:35], s82 offen offset:3072 lds\n"
"v_mfma_f32_16x16x32_bf16 v[124:127], a[80:83], v[188:191], v[124:127]\n"
"s_add_u32 s82, s66, 32768\n"
"v_cvt_pk_bf16_f32 v50, v96, v97\n"
"v_cvt_pk_bf16_f32 v51, v98, v99\n"
"v_mfma_f32_16x16x32_bf16 v[124:127], a[84:87], v[204:207], v[124:127]\n"
"s_waitcnt lgkmcnt(8)\n"
"ds_read_b128 a[168:171], v252 offset:8192\n"
"v_mul_f32 v100, s87, v100\n"
"v_mfma_f32_16x16x32_bf16 v[124:127], a[88:91], a[204:207], v[124:127]\n"
"s_add_u32 m0, s66, 49152\n"
"v_mul_f32 v101, s87, v101\n"
"v_mfma_f32_16x16x32_bf16 v[112:115], a[92:95], a[208:211], v[112:115]\n"
"ds_read_b64_tr_b16 a[80:81], v3 offset:512\n"
"v_mul_f32 v102, s87, v102\n"
"v_mfma_f32_16x16x32_bf16 v[112:115], a[96:99], a[224:227], v[112:115]\n"
"ds_read_b64_tr_b16 a[82:83], v3 offset:2560\n"
"v_mul_f32 v103, s87, v103\n"
"v_mfma_f32_16x16x32_bf16 v[112:115], a[100:103], a[240:243], v[112:115]\n"
"buffer_load_dwordx4 v248, s[36:39], s82 offen lds\n"
"v_mfma_f32_16x16x32_bf16 v[116:119], a[92:95], a[212:215], v[116:119]\n"
"ds_read_b64_tr_b16 a[84:85], v3 offset:4608\n"
"v_cvt_pk_bf16_f32 v58, v100, v101\n"
"v_mfma_f32_16x16x32_bf16 v[116:119], a[96:99], a[228:231], v[116:119]\n"
"ds_read_b64_tr_b16 a[86:87], v3 offset:6656\n"
"v_cvt_pk_bf16_f32 v59, v102, v103\n"
"v_mfma_f32_16x16x32_bf16 v[116:119], a[100:103], a[244:247], v[116:119]\n"
"ds_read_b128 a[172:175], v253 offset:8192\n"
"v_mul_f32 v104, s87, v104\n"
"v_mfma_f32_16x16x32_bf16 v[120:123], a[92:95], a[216:219], v[120:123]\n"
"ds_read_b64_tr_b16 a[88:89], v3 offset:8704\n"
"v_mul_f32 v105, s87, v105\n"
"v_mfma_f32_16x16x32_bf16 v[120:123], a[96:99], a[232:235], v[120:123]\n"
"buffer_load_dwordx4 v249, s[36:39], s82 offen offset:1024 lds\n"
"v_mfma_f32_16x16x32_bf16 v[120:123], a[100:103], a[248:251], v[120:123]\n"
"ds_read_b64_tr_b16 a[90:91], v3 offset:10752\n"
"v_mul_f32 v106, s87, v106\n"
"v_mfma_f32_16x16x32_bf16 v[124:127], a[92:95], a[220:223], v[124:127]\n"
"v_mul_f32 v107, s87, v107\n"
"v_cvt_pk_bf16_f32 v66, v104, v105\n"
"v_mfma_f32_16x16x32_bf16 v[124:127], a[96:99], a[236:239], v[124:127]\n"
"s_waitcnt lgkmcnt(8)\n"
"ds_read_b128 a[176:179], v250 offset:12288\n"
"v_cvt_pk_bf16_f32 v67, v106, v107\n"
"v_mfma_f32_16x16x32_bf16 v[124:127], a[100:103], a[252:255], v[124:127]\n"
"buffer_load_dwordx4 v248, s[36:39], s82 offen offset:2048 lds\n"
"v_mfma_f32_16x16x32_bf16 v[112:115], a[104:107], a[0:3], v[112:115]\n"
"ds_read_b64_tr_b16 a[92:93], v3 offset:12800\n"
"v_mul_f32 v108, s87, v108\n"
"v_mfma_f32_16x16x32_bf16 v[112:115], a[108:111], a[16:19], v[112:115]\n"
"ds_read_b64_tr_b16 a[94:95], v3 offset:14848\n"
"v_mul_f32 v109, s87, v109\n"
"v_mfma_f32_16x16x32_bf16 v[112:115], a[112:115], a[32:35], v[112:115]\n"
"v_mul_f32 v110, s87, v110\n"
"v_mul_f32 v111, s87, v111\n"
"v_mfma_f32_16x16x32_bf16 v[116:119], a[104:107], a[4:7], v[116:119]\n"
"ds_read_b64_tr_b16 a[96:97], v3 offset:16896\n"
"v_cvt_pk_bf16_f32 v74, v108, v109\n"
"v_mfma_f32_16x16x32_bf16 v[116:119], a[108:111], a[20:23], v[116:119]\n"
"ds_read_b64_tr_b16 a[98:99], v3 offset:18944\n"
"ds_read_b128 a[180:183], v251 offset:12288\n"
"v_mfma_f32_16x16x32_bf16 v[116:119], a[112:115], a[36:39], v[116:119]\n"
"buffer_load_dwordx4 v249, s[36:39], s82 offen offset:3072 lds\n"
"v_mfma_f32_16x16x32_bf16 v[120:123], a[104:107], a[8:11], v[120:123]\n"
"ds_read_b64_tr_b16 a[100:101], v3 offset:20992\n"
"v_cvt_pk_bf16_f32 v75, v110, v111\n"
"v_mfma_f32_16x16x32_bf16 v[120:123], a[108:111], a[24:27], v[120:123]\n"
"ds_read_b64_tr_b16 a[102:103], v3 offset:23040\n"
"v_permlane16_swap_b32 v48, v50\n"
"v_mfma_f32_16x16x32_bf16 v[120:123], a[112:115], a[40:43], v[120:123]\n"
"v_permlane16_swap_b32 v49, v51\n"
"v_permlane16_swap_b32 v56, v58\n"
"v_mfma_f32_16x16x32_bf16 v[124:127], a[104:107], a[12:15], v[124:127]\n"
"v_permlane16_swap_b32 v57, v59\n"
"v_permlane16_swap_b32 v64, v66\n"
"v_mfma_f32_16x16x32_bf16 v[124:127], a[108:111], a[28:31], v[124:127]\n"
"s_waitcnt lgkmcnt(8)\n"
"ds_read_b128 a[184:187], v252 offset:12288\n"
"v_permlane16_swap_b32 v65, v67\n"
"v_mfma_f32_16x16x32_bf16 v[124:127], a[112:115], a[44:47], v[124:127]\n"
"v_permlane16_swap_b32 v72, v74\n"
"v_mfma_f32_16x16x32_bf16 v[128:131], a[80:83], v[176:179], v[128:131]\n"
"ds_read_b64_tr_b16 a[104:105], v3 offset:25088\n"
"v_permlane16_swap_b32 v73, v75\n"
"v_mfma_f32_16x16x32_bf16 v[128:131], a[84:87], v[192:195], v[128:131]\n"
"s_barrier\n"
"ds_read_b64_tr_b16 a[106:107], v3 offset:27136\n"
"v_mfma_f32_16x16x32_bf16 v[128:131], a[88:91], a[192:195], v[128:131]\n"
"s_add_u32 s82, s66, 0\n"
"ds_read_b64_tr_b16 a[108:109], v3 offset:29184\n"
"ds_read_b128 a[188:191], v253 offset:12288\n"
"v_mfma_f32_16x16x32_bf16 v[132:135], a[80:83], v[180:183], v[132:135]\n"
"s_add_u32 m0, s66, 114688\n"
"ds_read_b64_tr_b16 a[110:111], v3 offset:31232\n"
"v_mul_f32 v112, s87, v112\n"
"v_mfma_f32_16x16x32_bf16 v[132:135], a[84:87], v[196:199], v[132:135]\n"
"ds_read_b64_tr_b16 a[112:113], v3 offset:33280\n"
"v_mul_f32 v113, s87, v113\n"
"v_mfma_f32_16x16x32_bf16 v[132:135], a[88:91], a[196:199], v[132:135]\n"
"v_mul_f32 v114, s87, v114\n"
"v_mul_f32 v115, s87, v115\n"
"v_mfma_f32_16x16x32_bf16 v[136:139], a[80:83], v[184:187], v[136:139]\n"
"ds_read_b64_tr_b16 a[114:115], v3 offset:35328\n"
"v_cvt_pk_bf16_f32 v52, v112, v113\n"
"v_mfma_f32_16x16x32_bf16 v[136:139], a[84:87], v[200:203], v[136:139]\n"
"buffer_load_dwordx4 v248, s[36:39], s82 offen lds\n"
"v_mfma_f32_16x16x32_bf16 v[136:139], a[88:91], a[200:203], v[136:139]\n"
"v_cvt_pk_bf16_f32 v53, v114, v115\n"
"v_mul_f32 v116, s87, v116\n"
"v_mfma_f32_16x16x32_bf16 v[140:143], a[80:83], v[188:191], v[140:143]\n"
"v_mul_f32 v117, s87, v117\n"
"v_mul_f32 v118, s87, v118\n"
"v_mfma_f32_16x16x32_bf16 v[140:143], a[84:87], v[204:207], v[140:143]\n"
"s_waitcnt lgkmcnt(8)\n"
"buffer_load_dwordx4 v249, s[36:39], s82 offen offset:1024 lds\n"
"v_mul_f32 v119, s87, v119\n"
"v_mfma_f32_16x16x32_bf16 v[140:143], a[88:91], a[204:207], v[140:143]\n"
"v_cvt_pk_bf16_f32 v60, v116, v117\n"
"v_cvt_pk_bf16_f32 v61, v118, v119\n"
"v_mfma_f32_16x16x32_bf16 v[128:131], a[92:95], a[208:211], v[128:131]\n"
"buffer_load_dwordx4 v248, s[36:39], s82 offen offset:2048 lds\n"
"v_mul_f32 v120, s87, v120\n"
"v_mfma_f32_16x16x32_bf16 v[128:131], a[96:99], a[224:227], v[128:131]\n"
"v_mul_f32 v121, s87, v121\n"
"v_mul_f32 v122, s87, v122\n"
"v_mfma_f32_16x16x32_bf16 v[128:131], a[100:103], a[240:243], v[128:131]\n"
"s_waitcnt lgkmcnt(5)\n"
"buffer_load_dwordx4 v249, s[36:39], s82 offen offset:3072 lds\n"
"v_mul_f32 v123, s87, v123\n"
"v_mfma_f32_16x16x32_bf16 v[128:131], a[104:107], a[0:3], v[128:131]\n"
"s_waitcnt lgkmcnt(2)\n"
"v_cvt_pk_bf16_f32 v68, v120, v121\n"
"v_cvt_pk_bf16_f32 v69, v122, v123\n"
"v_mfma_f32_16x16x32_bf16 v[128:131], a[108:111], a[16:19], v[128:131]\n"
"s_waitcnt lgkmcnt(0)\n"
"v_mul_f32 v124, s87, v124\n"
"v_mul_f32 v125, s87, v125\n"
"v_mfma_f32_16x16x32_bf16 v[128:131], a[112:115], a[32:35], v[128:131]\n"
"v_mul_f32 v126, s87, v126\n"
"v_mul_f32 v127, s87, v127\n"
"v_mfma_f32_16x16x32_bf16 v[132:135], a[92:95], a[212:215], v[132:135]\n"
"v_cvt_pk_bf16_f32 v76, v124, v125\n"
"v_cvt_pk_bf16_f32 v77, v126, v127\n"
"v_mfma_f32_16x16x32_bf16 v[132:135], a[96:99], a[228:231], v[132:135]\n"
"v_mfma_f32_16x16x32_bf16 v[132:135], a[100:103], a[244:247], v[132:135]\n"
"s_nop 0\n"
"v_mul_f32 v128, s87, v128\n"
"v_mul_f32 v129, s87, v129\n"
"v_mfma_f32_16x16x32_bf16 v[132:135], a[104:107], a[4:7], v[132:135]\n"
"v_mul_f32 v130, s87, v130\n"
"v_mul_f32 v131, s87, v131\n"
"v_mfma_f32_16x16x32_bf16 v[132:135], a[108:111], a[20:23], v[132:135]\n"
"v_cvt_pk_bf16_f32 v54, v128, v129\n"
"v_cvt_pk_bf16_f32 v55, v130, v131\n"
"v_mfma_f32_16x16x32_bf16 v[132:135], a[112:115], a[36:39], v[132:135]\n"
"v_mfma_f32_16x16x32_bf16 v[136:139], a[92:95], a[216:219], v[136:139]\n"
"v_permlane16_swap_b32 v52, v54\n"
"v_permlane16_swap_b32 v53, v55\n"
"v_mfma_f32_16x16x32_bf16 v[136:139], a[96:99], a[232:235], v[136:139]\n"
"v_mfma_f32_16x16x32_bf16 v[136:139], a[100:103], a[248:251], v[136:139]\n"
"s_nop 2\n"
"v_mul_f32 v132, s87, v132\n"
"v_mul_f32 v133, s87, v133\n"
"v_mfma_f32_16x16x32_bf16 v[136:139], a[104:107], a[8:11], v[136:139]\n"
"v_mul_f32 v134, s87, v134\n"
"v_mul_f32 v135, s87, v135\n"
"v_mfma_f32_16x16x32_bf16 v[136:139], a[108:111], a[24:27], v[136:139]\n"
"v_cvt_pk_bf16_f32 v62, v132, v133\n"
"v_cvt_pk_bf16_f32 v63, v134, v135\n"
"v_mfma_f32_16x16x32_bf16 v[136:139], a[112:115], a[40:43], v[136:139]\n"
"v_mfma_f32_16x16x32_bf16 v[140:143], a[92:95], a[220:223], v[140:143]\n"
"v_permlane16_swap_b32 v60, v62\n"
"v_permlane16_swap_b32 v61, v63\n"
"v_mfma_f32_16x16x32_bf16 v[140:143], a[96:99], a[236:239], v[140:143]\n"
"v_mfma_f32_16x16x32_bf16 v[140:143], a[100:103], a[252:255], v[140:143]\n"
"s_nop 2\n"
"v_mul_f32 v136, s87, v136\n"
"v_mul_f32 v137, s87, v137\n"
"v_mfma_f32_16x16x32_bf16 v[140:143], a[104:107], a[12:15], v[140:143]\n"
"v_mul_f32 v138, s87, v138\n"
"v_mul_f32 v139, s87, v139\n"
"v_mfma_f32_16x16x32_bf16 v[140:143], a[108:111], a[28:31], v[140:143]\n"
"v_cvt_pk_bf16_f32 v70, v136, v137\n"
"v_cvt_pk_bf16_f32 v71, v138, v139\n"
"v_mfma_f32_16x16x32_bf16 v[140:143], a[112:115], a[44:47], v[140:143]\n"
"v_mfma_f32_16x16x32_bf16 v[32:35], a[128:131], v[144:147], 0\n"
"v_permlane16_swap_b32 v68, v70\n"
"v_permlane16_swap_b32 v69, v71\n"
"v_mfma_f32_16x16x32_bf16 v[32:35], a[132:135], v[148:151], v[32:35]\n"
"v_mfma_f32_16x16x32_bf16 v[32:35], a[136:139], v[48:51], v[32:35]\n"
"s_nop 2\n"
"v_mul_f32 v140, s87, v140\n"
"v_mul_f32 v141, s87, v141\n"
"v_mfma_f32_16x16x32_bf16 v[32:35], a[140:143], v[52:55], v[32:35]\n"
"v_mul_f32 v142, s87, v142\n"
"v_mul_f32 v143, s87, v143\n"
"v_mfma_f32_16x16x32_bf16 v[36:39], a[128:131], v[152:155], 0\n"
"v_cvt_pk_bf16_f32 v78, v140, v141\n"
"v_cvt_pk_bf16_f32 v79, v142, v143\n"
"v_mfma_f32_16x16x32_bf16 v[36:39], a[132:135], v[156:159], v[36:39]\n"
"v_mfma_f32_16x16x32_bf16 v[36:39], a[136:139], v[56:59], v[36:39]\n"
"v_permlane16_swap_b32 v76, v78\n"
"v_permlane16_swap_b32 v77, v79\n"
"v_mfma_f32_16x16x32_bf16 v[36:39], a[140:143], v[60:63], v[36:39]\n"
"v_mfma_f32_16x16x32_bf16 v[40:43], a[128:131], v[160:163], 0\n"
"v_max3_f32 v112, v32, v33, v34\n"
"v_max_f32 v112, v112, v35\n"
"v_mfma_f32_16x16x32_bf16 v[40:43], a[132:135], v[164:167], v[40:43]\n"
"v_mov_b32 v113, v112\n"
"s_nop 1\n"
"v_permlane16_swap_b32 v112, v113\n"
"v_mfma_f32_16x16x32_bf16 v[40:43], a[136:139], v[64:67], v[40:43]\n"
"v_max_f32 v112, v112, v113\n"
"v_mov_b32 v113, v112\n"
"v_mfma_f32_16x16x32_bf16 v[40:43], a[140:143], v[68:71], v[40:43]\n"
"s_nop 0\n"
"v_permlane32_swap_b32 v112, v113\n"
"v_max_f32 v112, v112, v113\n"
"v_mfma_f32_16x16x32_bf16 v[44:47], a[128:131], v[168:171], 0\n"
"v_sub_f32 v113, 0, v112\n"
"v_sub_f32 v114, 0, v112\n"
"v_mfma_f32_16x16x32_bf16 v[44:47], a[132:135], v[172:175], v[44:47]\n"
"v_sub_f32 v115, 0, v112\n"
"v_sub_f32 v112, 0, v112\n"
"v_mfma_f32_16x16x32_bf16 v[44:47], a[136:139], v[72:75], v[44:47]\n"
"v_max3_f32 v116, v36, v37, v38\n"
"v_max_f32 v116, v116, v39\n"
"v_mfma_f32_16x16x32_bf16 v[44:47], a[140:143], v[76:79], v[44:47]\n"
"v_mov_b32 v117, v116\n"
"s_nop 1\n"
"v_permlane16_swap_b32 v116, v117\n"
"v_max_f32 v116, v116, v117\n"
"v_mov_b32 v117, v116\n"
"s_nop 1\n"
"v_permlane32_swap_b32 v116, v117\n"
"v_max_f32 v116, v116, v117\n"
"v_sub_f32 v117, 0, v116\n"
"v_sub_f32 v118, 0, v116\n"
"v_sub_f32 v119, 0, v116\n"
"v_sub_f32 v116, 0, v116\n"
"v_max3_f32 v120, v40, v41, v42\n"
"v_max_f32 v120, v120, v43\n"
"v_mov_b32 v121, v120\n"
"s_nop 1\n"
"v_permlane16_swap_b32 v120, v121\n"
"v_max_f32 v120, v120, v121\n"
"v_mov_b32 v121, v120\n"
"s_nop 1\n"
"v_permlane32_swap_b32 v120, v121\n"
"v_max_f32 v120, v120, v121\n"
"v_sub_f32 v121, 0, v120\n"
"v_sub_f32 v122, 0, v120\n"
"v_sub_f32 v123, 0, v120\n"
"v_sub_f32 v120, 0, v120\n"
"v_max3_f32 v124, v44, v45, v46\n"
"v_max_f32 v124, v124, v47\n"
"v_mov_b32 v125, v124\n"
"s_nop 1\n"
"v_permlane16_swap_b32 v124, v125\n"
"v_max_f32 v124, v124, v125\n"
"v_mov_b32 v125, v124\n"
"s_nop 1\n"
"v_permlane32_swap_b32 v124, v125\n"
"v_max_f32 v124, v124, v125\n"
"v_sub_f32 v125, 0, v124\n"
"v_sub_f32 v126, 0, v124\n"
"v_sub_f32 v127, 0, v124\n"
"v_sub_f32 v124, 0, v124\n"
"s_waitcnt vmcnt(0)\n"
"L_attn_4_%=:\n"
"s_add_u32 s89, s66, 65536\n"
"s_add_u32 s90, s66, 65536\n"
"s_mov_b32 s55, 0\n"
"s_mov_b32 s56, 0\n"
"s_mov_b64 s[28:29], s[24:25]\n"
"s_mov_b64 s[30:31], s[26:27]\n"
"v_mov_b32 v234, v6\n"
"v_mov_b32 v128, 0\n"
"v_mov_b32 v129, 0\n"
"v_mov_b32 v130, 0\n"
"v_mov_b32 v131, 0\n"
"v_mfma_f32_16x16x32_bf16 v[16:19], a[128:131], v[144:147], v[112:115]\n"
"s_waitcnt vmcnt(16) lgkmcnt(0)\n"
"s_barrier\n"
"v_mfma_f32_16x16x32_bf16 v[16:19], a[132:135], v[148:151], v[16:19]\n"
"ds_read_b64_tr_b16 a[192:193], v254 offset:49152\n"
"ds_read_b64_tr_b16 a[194:195], v254 offset:53248\n"
"v_readlane_b32 s52, v232, s55\n"
"v_mfma_f32_16x16x32_bf16 v[16:19], a[136:139], v[48:51], v[16:19]\n"
"ds_read_b64_tr_b16 a[196:197], v254 offset:57344\n"
"ds_read_b64_tr_b16 a[198:199], v254 offset:61440\n"
"v_mfma_f32_16x16x32_bf16 v[16:19], a[140:143], v[52:55], v[16:19]\n"
"ds_read_b64_tr_b16 a[200:201], v255 offset:49152\n"
"ds_read_b64_tr_b16 a[202:203], v255 offset:53248\n"
"v_mfma_f32_16x16x32_bf16 v[20:23], a[144:147], v[144:147], v[112:115]\n"
"ds_read_b64_tr_b16 a[204:205], v255 offset:57344\n"
"ds_read_b64_tr_b16 a[206:207], v255 offset:61440\n"
"v_mfma_f32_16x16x32_bf16 v[20:23], a[148:151], v[148:151], v[20:23]\n"
"ds_read_b64_tr_b16 a[208:209], v254 offset:49664\n"
"ds_read_b64_tr_b16 a[210:211], v254 offset:53760\n"
"v_mfma_f32_16x16x32_bf16 v[20:23], a[152:155], v[48:51], v[20:23]\n"
"ds_read_b64_tr_b16 a[212:213], v254 offset:57856\n"
"ds_read_b64_tr_b16 a[214:215], v254 offset:61952\n"
"v_mfma_f32_16x16x32_bf16 v[20:23], a[156:159], v[52:55], v[20:23]\n"
"ds_read_b64_tr_b16 a[216:217], v255 offset:49664\n"
"ds_read_b64_tr_b16 a[218:219], v255 offset:53760\n"
"v_mfma_f32_16x16x32_bf16 v[24:27], a[160:163], v[144:147], v[112:115]\n"
"ds_read_b64_tr_b16 a[220:221], v255 offset:57856\n"
"ds_read_b64_tr_b16 a[222:223], v255 offset:61952\n"
"v_mfma_f32_16x16x32_bf16 v[24:27], a[164:167], v[148:151], v[24:27]\n"
"ds_read_b64_tr_b16 a[224:225], v254 offset:50176\n"
"ds_read_b64_tr_b16 a[226:227], v254 offset:54272\n"
"v_mfma_f32_16x16x32_bf16 v[24:27], a[168:171], v[48:51], v[24:27]\n"
"ds_read_b64_tr_b16 a[228:229], v254 offset:58368\n"
"ds_read_b64_tr_b16 a[230:231], v254 offset:62464\n"
"v_mfma_f32_16x16x32_bf16 v[24:27], a[172:175], v[52:55], v[24:27]\n"
"ds_read_b64_tr_b16 a[232:233], v255 offset:50176\n"
"ds_read_b64_tr_b16 a[234:235], v255 offset:54272\n"
"v_mfma_f32_16x16x32_bf16 v[28:31], a[176:179], v[144:147], v[112:115]\n"
"ds_read_b64_tr_b16 a[236:237], v255 offset:58368\n"
"ds_read_b64_tr_b16 a[238:239], v255 offset:62464\n"
"v_mfma_f32_16x16x32_bf16 v[28:31], a[180:183], v[148:151], v[28:31]\n"
"s_and_b32 s78, s56, 7168\n"
"ds_read_b64_tr_b16 a[240:241], v254 offset:50688\n"
"ds_read_b64_tr_b16 a[242:243], v254 offset:54784\n"
"v_mfma_f32_16x16x32_bf16 v[28:31], a[184:187], v[48:51], v[28:31]\n"
"ds_read_b64_tr_b16 a[244:245], v254 offset:58880\n"
"ds_read_b64_tr_b16 a[246:247], v254 offset:62976\n"
"v_mfma_f32_16x16x32_bf16 v[28:31], a[188:191], v[52:55], v[28:31]\n"
"ds_read_b64_tr_b16 a[248:249], v255 offset:50688\n"
"ds_read_b64_tr_b16 a[250:251], v255 offset:54784\n"
"v_mfma_f32_16x16x32_bf16 v[32:35], a[128:131], v[152:155], v[116:119]\n"
"s_add_u32 m0, s54, s78\n"
"ds_read_b64_tr_b16 a[252:253], v255 offset:58880\n"
"ds_read_b64_tr_b16 a[254:255], v255 offset:62976\n"
"v_mfma_f32_16x16x32_bf16 v[32:35], a[132:135], v[156:159], v[32:35]\n"
"v_exp_f32 v16, v16\n"
"v_mfma_f32_16x16x32_bf16 v[32:35], a[136:139], v[56:59], v[32:35]\n"
"buffer_load_dwordx4 v234, s[28:31], s52 offen nt lds\n"
"v_mfma_f32_16x16x32_bf16 v[32:35], a[140:143], v[60:63], v[32:35]\n"
"v_exp_f32 v17, v17\n"
"v_mfma_f32_16x16x32_bf16 v[36:39], a[144:147], v[152:155], v[116:119]\n"
"v_exp_f32 v18, v18\n"
"v_mfma_f32_16x16x32_bf16 v[36:39], a[148:151], v[156:159], v[36:39]\n"
"v_exp_f32 v19, v19\n"
"v_mfma_f32_16x16x32_bf16 v[36:39], a[152:155], v[56:59], v[36:39]\n"
"v_cvt_pk_bf16_f32 v80, v16, v17\n"
"v_mfma_f32_16x16x32_bf16 v[36:39], a[156:159], v[60:63], v[36:39]\n"
"v_exp_f32 v20, v20\n"
"v_mfma_f32_16x16x32_bf16 v[40:43], a[160:163], v[152:155], v[116:119]\n"
"v_exp_f32 v21, v21\n"
"v_mfma_f32_16x16x32_bf16 v[40:43], a[164:167], v[156:159], v[40:43]\n"
"v_cvt_pk_bf16_f32 v81, v18, v19\n"
"v_exp_f32 v22, v22\n"
"v_mfma_f32_16x16x32_bf16 v[40:43], a[168:171], v[56:59], v[40:43]\n"
"v_exp_f32 v23, v23\n"
"v_mfma_f32_16x16x32_bf16 v[40:43], a[172:175], v[60:63], v[40:43]\n"
"s_add_u32 s80, s55, 4\n"
"v_cvt_pk_bf16_f32 v82, v20, v21\n"
"v_mfma_f32_16x16x32_bf16 v[44:47], a[176:179], v[152:155], v[116:119]\n"
"s_and_b32 s79, s80, 1\n"
"v_exp_f32 v24, v24\n"
"v_mfma_f32_16x16x32_bf16 v[44:47], a[180:183], v[156:159], v[44:47]\n"
"v_exp_f32 v25, v25\n"
"v_mfma_f32_16x16x32_bf16 v[44:47], a[184:187], v[56:59], v[44:47]\n"
"s_lshl_b32 s76, s79, 5\n"
"v_cvt_pk_bf16_f32 v83, v22, v23\n"
"v_mfma_f32_16x16x32_bf16 v[44:47], a[188:191], v[60:63], v[44:47]\n"
"s_and_b32 s79, s80, 2\n"
"v_exp_f32 v26, v26\n"
"v_mfma_f32_16x16x32_bf16 a[12:15], v[128:131], v[128:131], 0\n"
"s_lshl_b32 s79, s79, 8\n"
"v_exp_f32 v27, v27\n"
"v_mfma_f32_16x16x32_bf16 a[28:31], v[128:131], v[128:131], 0\n"
"s_or_b32 s76, s76, s79\n"
"v_cvt_pk_bf16_f32 v84, v24, v25\n"
"v_mfma_f32_16x16x32_bf16 a[44:47], v[128:131], v[128:131], 0\n"
"v_exp_f32 v28, v28\n"
"v_mfma_f32_16x16x32_bf16 a[60:63], v[128:131], v[128:131], 0\n"
"s_and_b32 s79, s80, 4\n"
"v_exp_f32 v29, v29\n"
"v_mfma_f32_16x16x32_bf16 a[76:79], v[128:131], v[128:131], 0\n"
"s_lshl_b32 s79, s79, 10\n"
"v_cvt_pk_bf16_f32 v85, v26, v27\n"
"v_mfma_f32_16x16x32_bf16 a[92:95], v[128:131], v[128:131], 0\n"
"v_exp_f32 v30, v30\n"
"v_mfma_f32_16x16x32_bf16 a[108:111], v[128:131], v[128:131], 0\n"
"s_or_b32 s76, s76, s79\n"
"v_exp_f32 v31, v31\n"
"v_mfma_f32_16x16x32_bf16 a[124:127], v[128:131], v[128:131], 0\n"
"v_cvt_pk_bf16_f32 v86, v28, v29\n"
"v_mfma_f32_16x16x32_bf16 v[16:19], a[128:131], v[160:163], v[120:123]\n"
"v_exp_f32 v32, v32\n"
"v_mfma_f32_16x16x32_bf16 v[16:19], a[132:135], v[164:167], v[16:19]\n"
"v_cvt_pk_bf16_f32 v87, v30, v31\n"
"v_mfma_f32_16x16x32_bf16 v[16:19], a[136:139], v[64:67], v[16:19]\n"
"v_exp_f32 v33, v33\n"
"v_mfma_f32_16x16x32_bf16 v[16:19], a[140:143], v[68:71], v[16:19]\n"
"v_mfma_f32_16x16x32_bf16 v[20:23], a[144:147], v[160:163], v[120:123]\n"
"v_exp_f32 v34, v34\n"
"v_mfma_f32_16x16x32_bf16 v[20:23], a[148:151], v[164:167], v[20:23]\n"
"v_cvt_pk_bf16_f32 v88, v32, v33\n"
"v_mfma_f32_16x16x32_bf16 v[20:23], a[152:155], v[64:67], v[20:23]\n"
"v_exp_f32 v35, v35\n"
"v_mfma_f32_16x16x32_bf16 v[20:23], a[156:159], v[68:71], v[20:23]\n"
"v_mfma_f32_16x16x32_bf16 v[24:27], a[160:163], v[160:163], v[120:123]\n"
"v_exp_f32 v36, v36\n"
"v_mfma_f32_16x16x32_bf16 v[24:27], a[164:167], v[164:167], v[24:27]\n"
"v_cvt_pk_bf16_f32 v89, v34, v35\n"
"v_mov_b32 v132, 0\n"
"v_mfma_f32_16x16x32_bf16 v[24:27], a[168:171], v[64:67], v[24:27]\n"
"v_exp_f32 v37, v37\n"
"v_mfma_f32_16x16x32_bf16 v[24:27], a[172:175], v[68:71], v[24:27]\n"
"v_mov_b32 v133, 0\n"
"v_smfmac_f32_16x16x64_bf16 v[128:131], v[228:231], v[80:87], v239\n"
"v_mfma_f32_16x16x32_bf16 v[28:31], a[176:179], v[160:163], v[120:123]\n"
"v_exp_f32 v38, v38\n"
"v_mfma_f32_16x16x32_bf16 v[28:31], a[180:183], v[164:167], v[28:31]\n"
"v_cvt_pk_bf16_f32 v90, v36, v37\n"
"v_mov_b32 v134, 0\n"
"v_mfma_f32_16x16x32_bf16 v[28:31], a[184:187], v[64:67], v[28:31]\n"
"s_waitcnt lgkmcnt(15)\n"
"v_exp_f32 v39, v39\n"
"v_mfma_f32_16x16x32_bf16 v[28:31], a[188:191], v[68:71], v[28:31]\n"
"v_mfma_f32_16x16x32_bf16 a[0:3], a[192:195], v[80:83], 0\n"
"v_exp_f32 v40, v40\n"
"v_mfma_f32_16x16x32_bf16 a[0:3], a[196:199], v[84:87], a[0:3]\n"
"v_cvt_pk_bf16_f32 v91, v38, v39\n"
"v_mov_b32 v135, 0\n"
"v_mfma_f32_16x16x32_bf16 a[16:19], a[200:203], v[80:83], 0\n"
"v_exp_f32 v41, v41\n"
"v_mfma_f32_16x16x32_bf16 a[16:19], a[204:207], v[84:87], a[16:19]\n"
"v_mov_b32 v136, 0\n"
"v_mfma_f32_16x16x32_bf16 a[32:35], a[208:211], v[80:83], 0\n"
"v_exp_f32 v42, v42\n"
"v_mfma_f32_16x16x32_bf16 a[32:35], a[212:215], v[84:87], a[32:35]\n"
"v_cvt_pk_bf16_f32 v92, v40, v41\n"
"v_mov_b32 v137, 0\n"
"v_mfma_f32_16x16x32_bf16 a[48:51], a[216:219], v[80:83], 0\n"
"s_waitcnt lgkmcnt(8)\n"
"v_exp_f32 v43, v43\n"
"v_mfma_f32_16x16x32_bf16 a[48:51], a[220:223], v[84:87], a[48:51]\n"
"v_mfma_f32_16x16x32_bf16 a[64:67], a[224:227], v[80:83], 0\n"
"v_exp_f32 v44, v44\n"
"v_mfma_f32_16x16x32_bf16 a[64:67], a[228:231], v[84:87], a[64:67]\n"
"s_add_u32 m0, s90, 0\n"
"v_cvt_pk_bf16_f32 v93, v42, v43\n"
"v_mov_b32 v138, 0\n"
"v_mfma_f32_16x16x32_bf16 a[80:83], a[232:235], v[80:83], 0\n"
"s_waitcnt lgkmcnt(4)\n"
"v_exp_f32 v45, v45\n"
"v_mfma_f32_16x16x32_bf16 a[80:83], a[236:239], v[84:87], a[80:83]\n"
"buffer_load_dwordx4 v244, s[32:35], s89 offen nt lds\n"
"v_mfma_f32_16x16x32_bf16 a[96:99], a[240:243], v[80:83], 0\n"
"s_waitcnt lgkmcnt(0)\n"
"v_exp_f32 v46, v46\n"
"v_mfma_f32_16x16x32_bf16 a[96:99], a[244:247], v[84:87], a[96:99]\n"
"v_cvt_pk_bf16_f32 v94, v44, v45\n"
"v_xor_b32 v235, s76, v4\n"
"v_mfma_f32_16x16x32_bf16 a[112:115], a[248:251], v[80:83], 0\n"
"v_exp_f32 v47, v47\n"
"v_mfma_f32_16x16x32_bf16 a[112:115], a[252:255], v[84:87], a[112:115]\n"
"buffer_load_dwordx4 v245, s[32:35], s89 offen offset:1024 nt lds\n"
"v_mfma_f32_16x16x32_bf16 v[32:35], a[128:131], v[168:171], v[124:127]\n"
"v_exp_f32 v16, v16\n"
"v_mov_b32 v139, 0\n"
"v_mfma_f32_16x16x32_bf16 v[32:35], a[132:135], v[172:175], v[32:35]\n"
"ds_read_b128 a[128:131], v250 offset:32768\n"
"v_cvt_pk_bf16_f32 v95, v46, v47\n"
"v_mfma_f32_16x16x32_bf16 v[32:35], a[136:139], v[72:75], v[32:35]\n"
"v_exp_f32 v17, v17\n"
"v_mfma_f32_16x16x32_bf16 v[32:35], a[140:143], v[76:79], v[32:35]\n"
"ds_read_b128 a[132:135], v251 offset:32768\n"
"ds_read_b128 a[136:139], v252 offset:32768\n"
"v_mov_b32 v140, 0\n"
"v_mfma_f32_16x16x32_bf16 v[36:39], a[144:147], v[168:171], v[124:127]\n"
"v_exp_f32 v18, v18\n"
"v_mfma_f32_16x16x32_bf16 v[36:39], a[148:151], v[172:175], v[36:39]\n"
"ds_read_b128 a[140:143], v253 offset:32768\n"
"v_cvt_pk_bf16_f32 v80, v16, v17\n"
"v_mfma_f32_16x16x32_bf16 v[36:39], a[152:155], v[72:75], v[36:39]\n"
"v_exp_f32 v19, v19\n"
"v_mov_b32 v141, 0\n"
"v_mfma_f32_16x16x32_bf16 v[36:39], a[156:159], v[76:79], v[36:39]\n"
"ds_read_b128 a[144:147], v250 offset:36864\n"
"ds_read_b128 a[148:151], v251 offset:36864\n"
"v_mfma_f32_16x16x32_bf16 v[40:43], a[160:163], v[168:171], v[124:127]\n"
"v_exp_f32 v20, v20\n"
"v_mov_b32 v142, 0\n"
"v_mfma_f32_16x16x32_bf16 v[40:43], a[164:167], v[172:175], v[40:43]\n"
"ds_read_b128 a[152:155], v252 offset:36864\n"
"v_cvt_pk_bf16_f32 v81, v18, v19\n"
"v_mfma_f32_16x16x32_bf16 v[40:43], a[168:171], v[72:75], v[40:43]\n"
"v_exp_f32 v21, v21\n"
"v_mfma_f32_16x16x32_bf16 v[40:43], a[172:175], v[76:79], v[40:43]\n"
"ds_read_b128 a[156:159], v253 offset:36864\n"
"ds_read_b128 a[160:163], v250 offset:40960\n"
"v_mov_b32 v143, 0\n"
"v_mfma_f32_16x16x32_bf16 v[44:47], a[176:179], v[168:171], v[124:127]\n"
"v_exp_f32 v22, v22\n"
"v_mfma_f32_16x16x32_bf16 v[44:47], a[180:183], v[172:175], v[44:47]\n"
"ds_read_b128 a[164:167], v251 offset:40960\n"
"v_cvt_pk_bf16_f32 v82, v20, v21\n"
"v_mfma_f32_16x16x32_bf16 v[44:47], a[184:187], v[72:75], v[44:47]\n"
"v_exp_f32 v23, v23\n"
"v_mfma_f32_16x16x32_bf16 v[44:47], a[188:191], v[76:79], v[44:47]\n"
"ds_read_b128 a[168:171], v252 offset:40960\n"
"ds_read_b128 a[172:175], v253 offset:40960\n"
"v_mfma_f32_16x16x32_bf16 a[4:7], a[192:195], v[88:91], 0\n"
"v_exp_f32 v24, v24\n"
"v_mfma_f32_16x16x32_bf16 a[4:7], a[196:199], v[92:95], a[4:7]\n"
"ds_read_b128 a[176:179], v250 offset:45056\n"
"v_cvt_pk_bf16_f32 v83, v22, v23\n"
"v_mfma_f32_16x16x32_bf16 a[20:23], a[200:203], v[88:91], 0\n"
"v_exp_f32 v25, v25\n"
"v_mfma_f32_16x16x32_bf16 a[20:23], a[204:207], v[92:95], a[20:23]\n"
"ds_read_b128 a[180:183], v251 offset:45056\n"
"ds_read_b128 a[184:187], v252 offset:45056\n"
"v_mfma_f32_16x16x32_bf16 a[36:39], a[208:211], v[88:91], 0\n"
"v_exp_f32 v26, v26\n"
"v_mfma_f32_16x16x32_bf16 a[36:39], a[212:215], v[92:95], a[36:39]\n"
"ds_read_b128 a[188:191], v253 offset:45056\n"
"v_cvt_pk_bf16_f32 v84, v24, v25\n"
"v_mfma_f32_16x16x32_bf16 a[52:55], a[216:219], v[88:91], 0\n"
"v_exp_f32 v27, v27\n"
"v_mfma_f32_16x16x32_bf16 a[52:55], a[220:223], v[92:95], a[52:55]\n"
"buffer_load_dwordx4 v246, s[32:35], s89 offen offset:2048 nt lds\n"
"v_mfma_f32_16x16x32_bf16 a[68:71], a[224:227], v[88:91], 0\n"
"v_exp_f32 v28, v28\n"
"v_mfma_f32_16x16x32_bf16 a[68:71], a[228:231], v[92:95], a[68:71]\n"
"v_cvt_pk_bf16_f32 v85, v26, v27\n"
"v_mfma_f32_16x16x32_bf16 a[84:87], a[232:235], v[88:91], 0\n"
"v_exp_f32 v29, v29\n"
"v_mfma_f32_16x16x32_bf16 a[84:87], a[236:239], v[92:95], a[84:87]\n"
"buffer_load_dwordx4 v247, s[32:35], s89 offen offset:3072 nt lds\n"
"v_smfmac_f32_16x16x64_bf16 v[132:135], v[228:231], v[88:95], v239\n"
"v_mfma_f32_16x16x32_bf16 a[100:103], a[240:243], v[88:91], 0\n"
"s_sub_u32 s82, s89, 16384\n"
"v_exp_f32 v30, v30\n"
"v_mfma_f32_16x16x32_bf16 a[100:103], a[244:247], v[92:95], a[100:103]\n"
"s_add_u32 m0, s90, 16384\n"
"v_cvt_pk_bf16_f32 v86, v28, v29\n"
"v_mfma_f32_16x16x32_bf16 a[116:119], a[248:251], v[88:91], 0\n"
"s_waitcnt lgkmcnt(0)\n"
"v_exp_f32 v31, v31\n"
"v_mfma_f32_16x16x32_bf16 a[116:119], a[252:255], v[92:95], a[116:119]\n"
"buffer_load_dwordx4 v248, s[36:39], s82 offen nt lds\n"
"v_mfma_f32_16x16x32_bf16 v[16:19], a[128:131], v[144:147], v[112:115]\n"
"v_exp_f32 v32, v32\n"
"v_mfma_f32_16x16x32_bf16 v[16:19], a[132:135], v[148:151], v[16:19]\n"
"v_cvt_pk_bf16_f32 v87, v30, v31\n"
"v_xor_b32 v254, 0x10000, v254\n"
"v_mfma_f32_16x16x32_bf16 v[16:19], a[136:139], v[48:51], v[16:19]\n"
"v_exp_f32 v33, v33\n"
"v_mfma_f32_16x16x32_bf16 v[16:19], a[140:143], v[52:55], v[16:19]\n"
"buffer_load_dwordx4 v249, s[36:39], s82 offen offset:1024 nt lds\n"
"v_mfma_f32_16x16x32_bf16 v[20:23], a[144:147], v[144:147], v[112:115]\n"
"v_exp_f32 v34, v34\n"
"v_mfma_f32_16x16x32_bf16 v[20:23], a[148:151], v[148:151], v[20:23]\n"
"v_cvt_pk_bf16_f32 v88, v32, v33\n"
"v_xor_b32 v255, 0x10000, v255\n"
"v_mfma_f32_16x16x32_bf16 v[20:23], a[152:155], v[48:51], v[20:23]\n"
"v_exp_f32 v35, v35\n"
"v_mfma_f32_16x16x32_bf16 v[20:23], a[156:159], v[52:55], v[20:23]\n"
"buffer_load_dwordx4 v248, s[36:39], s82 offen offset:2048 nt lds\n"
"v_mfma_f32_16x16x32_bf16 v[24:27], a[160:163], v[144:147], v[112:115]\n"
"v_exp_f32 v36, v36\n"
"v_mfma_f32_16x16x32_bf16 v[24:27], a[164:167], v[148:151], v[24:27]\n"
"v_cvt_pk_bf16_f32 v89, v34, v35\n"
"v_xor_b32 v250, 0x10000, v250\n"
"v_mfma_f32_16x16x32_bf16 v[24:27], a[168:171], v[48:51], v[24:27]\n"
"v_exp_f32 v37, v37\n"
"v_mfma_f32_16x16x32_bf16 v[24:27], a[172:175], v[52:55], v[24:27]\n"
"buffer_load_dwordx4 v249, s[36:39], s82 offen offset:3072 nt lds\n"
"v_mfma_f32_16x16x32_bf16 v[28:31], a[176:179], v[144:147], v[112:115]\n"
"v_exp_f32 v38, v38\n"
"v_mfma_f32_16x16x32_bf16 v[28:31], a[180:183], v[148:151], v[28:31]\n"
"v_cvt_pk_bf16_f32 v90, v36, v37\n"
"v_mfma_f32_16x16x32_bf16 v[28:31], a[184:187], v[48:51], v[28:31]\n"
"v_exp_f32 v39, v39\n"
"v_mfma_f32_16x16x32_bf16 v[28:31], a[188:191], v[52:55], v[28:31]\n"
"v_mfma_f32_16x16x32_bf16 a[8:11], a[192:195], v[80:83], 0\n"
"v_exp_f32 v40, v40\n"
"v_mfma_f32_16x16x32_bf16 a[8:11], a[196:199], v[84:87], a[8:11]\n"
"v_cvt_pk_bf16_f32 v91, v38, v39\n"
"v_xor_b32 v251, 0x10000, v251\n"
"v_mfma_f32_16x16x32_bf16 a[24:27], a[200:203], v[80:83], 0\n"
"v_exp_f32 v41, v41\n"
"v_mfma_f32_16x16x32_bf16 a[24:27], a[204:207], v[84:87], a[24:27]\n"
"v_mfma_f32_16x16x32_bf16 a[40:43], a[208:211], v[80:83], 0\n"
"v_exp_f32 v42, v42\n"
"v_mfma_f32_16x16x32_bf16 a[40:43], a[212:215], v[84:87], a[40:43]\n"
"s_add_u32 s89, s89, 16384\n"
"v_cvt_pk_bf16_f32 v92, v40, v41\n"
"v_xor_b32 v252, 0x10000, v252\n"
"v_mfma_f32_16x16x32_bf16 a[56:59], a[216:219], v[80:83], 0\n"
"s_add_u32 s55, s55, 1\n"
"v_exp_f32 v43, v43\n"
"v_mfma_f32_16x16x32_bf16 a[56:59], a[220:223], v[84:87], a[56:59]\n"
"v_mfma_f32_16x16x32_bf16 a[72:75], a[224:227], v[80:83], 0\n"
"s_add_u32 s56, s56, 1024\n"
"v_exp_f32 v44, v44\n"
"v_mfma_f32_16x16x32_bf16 a[72:75], a[228:231], v[84:87], a[72:75]\n"
"v_cvt_pk_bf16_f32 v93, v42, v43\n"
"v_xor_b32 v253, 0x10000, v253\n"
"v_mfma_f32_16x16x32_bf16 a[88:91], a[232:235], v[80:83], 0\n"
"v_exp_f32 v45, v45\n"
"v_mfma_f32_16x16x32_bf16 a[88:91], a[236:239], v[84:87], a[88:91]\n"
"v_smfmac_f32_16x16x64_bf16 v[136:139], v[228:231], v[80:87], v239\n"
"v_mfma_f32_16x16x32_bf16 a[104:107], a[240:243], v[80:83], 0\n"
"v_exp_f32 v46, v46\n"
"v_mfma_f32_16x16x32_bf16 a[104:107], a[244:247], v[84:87], a[104:107]\n"
"v_cvt_pk_bf16_f32 v94, v44, v45\n"
"v_mfma_f32_16x16x32_bf16 a[120:123], a[248:251], v[80:83], 0\n"
"v_exp_f32 v47, v47\n"
"v_mfma_f32_16x16x32_bf16 a[120:123], a[252:255], v[84:87], a[120:123]\n"
"s_waitcnt vmcnt(9)\n"
"s_cmp_eq_u32 s72, 1\n"
"s_cbranch_scc1 L_att1_5_%=\n"
"s_cmp_eq_u32 s72, 3\n"
"s_cbranch_scc1 L_att1_5_%=\n"
".p2align 6\n"
"L_att_5_%=:\n"
"v_mfma_f32_16x16x32_bf16 v[32:35], a[128:131], v[152:155], v[116:119]\n"
"v_exp_f32 v16, v16\n"
"v_mfma_f32_16x16x32_bf16 v[32:35], a[132:135], v[156:159], v[32:35]\n"
"v_cvt_pk_bf16_f32 v95, v46, v47\n"
"v_readlane_b32 s52, v232, s55\n"
"v_mfma_f32_16x16x32_bf16 v[32:35], a[136:139], v[56:59], v[32:35]\n"
"s_waitcnt vmcnt(20) lgkmcnt(0)\n"
"v_exp_f32 v17, v17\n"
"v_mfma_f32_16x16x32_bf16 v[32:35], a[140:143], v[60:63], v[32:35]\n"
"s_barrier\n"
"v_mfma_f32_16x16x32_bf16 v[36:39], a[144:147], v[152:155], v[116:119]\n"
"v_exp_f32 v18, v18\n"
"v_mfma_f32_16x16x32_bf16 v[36:39], a[148:151], v[156:159], v[36:39]\n"
"v_cvt_pk_bf16_f32 v80, v16, v17\n"
"v_mfma_f32_16x16x32_bf16 v[36:39], a[152:155], v[56:59], v[36:39]\n"
"v_exp_f32 v19, v19\n"
"v_mfma_f32_16x16x32_bf16 v[36:39], a[156:159], v[60:63], v[36:39]\n"
"ds_read_b64_tr_b16 v[240:241], v235\n"
"ds_read_b64_tr_b16 v[242:243], v235 offset:2048\n"
"v_mfma_f32_16x16x32_bf16 v[40:43], a[160:163], v[152:155], v[116:119]\n"
"v_exp_f32 v20, v20\n"
"v_mfma_f32_16x16x32_bf16 v[40:43], a[164:167], v[156:159], v[40:43]\n"
"s_cmp_eq_u32 s55, 21\n"
"s_cselect_b32 s59, s57, 0x7fff0000\n"
"v_cvt_pk_bf16_f32 v81, v18, v19\n"
"v_mfma_f32_16x16x32_bf16 v[40:43], a[168:171], v[56:59], v[40:43]\n"
"v_exp_f32 v21, v21\n"
"v_mfma_f32_16x16x32_bf16 v[40:43], a[172:175], v[60:63], v[40:43]\n"
"buffer_store_dwordx2 v[236:237], v14, s[40:43], s59 offen sc1\n"
"v_mfma_f32_16x16x32_bf16 v[44:47], a[176:179], v[152:155], v[116:119]\n"
"s_and_b32 s78, s56, 7168\n"
"v_exp_f32 v22, v22\n"
"v_mfma_f32_16x16x32_bf16 v[44:47], a[180:183], v[156:159], v[44:47]\n"
"s_waitcnt lgkmcnt(0)\n"
"s_add_u32 m0, s54, s78\n"
"v_cvt_pk_bf16_f32 v82, v20, v21\n"
"v_mfma_f32_16x16x32_bf16 v[44:47], a[184:187], v[56:59], v[44:47]\n"
"v_exp_f32 v23, v23\n"
"v_mfma_f32_16x16x32_bf16 v[44:47], a[188:191], v[60:63], v[44:47]\n"
"buffer_load_dwordx4 v234, s[28:31], s52 offen nt lds\n"
"v_mfma_f32_16x16x32_bf16 a[12:15], a[192:195], v[88:91], a[12:15]\n"
"v_exp_f32 v24, v24\n"
"v_mfma_f32_16x16x32_bf16 a[12:15], a[196:199], v[92:95], a[12:15]\n"
"ds_read_b64_tr_b16 a[192:193], v254 offset:16384\n"
"v_cvt_pk_bf16_f32 v83, v22, v23\n"
"v_mfma_f32_16x16x32_bf16 a[28:31], a[200:203], v[88:91], a[28:31]\n"
"v_exp_f32 v25, v25\n"
"v_mfma_f32_16x16x32_bf16 a[28:31], a[204:207], v[92:95], a[28:31]\n"
"ds_read_b64_tr_b16 a[194:195], v254 offset:20480\n"
"ds_read_b64_tr_b16 a[196:197], v254 offset:24576\n"
"v_mfma_f32_16x16x32_bf16 a[44:47], a[208:211], v[88:91], a[44:47]\n"
"v_exp_f32 v26, v26\n"
"v_mfma_f32_16x16x32_bf16 a[44:47], a[212:215], v[92:95], a[44:47]\n"
"ds_read_b64_tr_b16 a[198:199], v254 offset:28672\n"
"v_cvt_pk_bf16_f32 v84, v24, v25\n"
"v_mfma_f32_16x16x32_bf16 a[60:63], a[216:219], v[88:91], a[60:63]\n"
"s_add_u32 s80, s55, 4\n"
"v_exp_f32 v27, v27\n"
"v_mfma_f32_16x16x32_bf16 a[60:63], a[220:223], v[92:95], a[60:63]\n"
"s_and_b32 s79, s80, 1\n"
"ds_read_b64_tr_b16 a[200:201], v255 offset:16384\n"
"ds_read_b64_tr_b16 a[202:203], v255 offset:20480\n"
"v_mfma_f32_16x16x32_bf16 a[76:79], a[224:227], v[88:91], a[76:79]\n"
"s_lshl_b32 s76, s79, 5\n"
"v_exp_f32 v28, v28\n"
"v_mfma_f32_16x16x32_bf16 a[76:79], a[228:231], v[92:95], a[76:79]\n"
"s_and_b32 s79, s80, 2\n"
"ds_read_b64_tr_b16 a[204:205], v255 offset:24576\n"
"v_cvt_pk_bf16_f32 v85, v26, v27\n"
"v_smfmac_f32_16x16x64_bf16 v[140:143], v[228:231], v[88:95], v239\n"
"ds_read_b64_tr_b16 a[206:207], v255 offset:28672\n"
"v_mfma_f32_16x16x32_bf16 a[92:95], a[232:235], v[88:91], a[92:95]\n"
"s_lshl_b32 s79, s79, 8\n"
"v_exp_f32 v29, v29\n"
"v_mfma_f32_16x16x32_bf16 a[92:95], a[236:239], v[92:95], a[92:95]\n"
"s_or_b32 s76, s76, s79\n"
"ds_read_b64_tr_b16 a[208:209], v254 offset:16896\n"
"ds_read_b64_tr_b16 a[210:211], v254 offset:20992\n"
"v_mfma_f32_16x16x32_bf16 a[108:111], a[240:243], v[88:91], a[108:111]\n"
"s_and_b32 s79, s80, 4\n"
"v_exp_f32 v30, v30\n"
"v_mfma_f32_16x16x32_bf16 a[108:111], a[244:247], v[92:95], a[108:111]\n"
"s_lshl_b32 s79, s79, 10\n"
"ds_read_b64_tr_b16 a[212:213], v254 offset:25088\n"
"v_cvt_pk_bf16_f32 v86, v28, v29\n"
"v_mfma_f32_16x16x32_bf16 a[124:127], a[248:251], v[88:91], a[124:127]\n"
"v_exp_f32 v31, v31\n"
"v_mfma_f32_16x16x32_bf16 a[124:127], a[252:255], v[92:95], a[124:127]\n"
"s_or_b32 s76, s76, s79\n"
"ds_read_b64_tr_b16 a[214:215], v254 offset:29184\n"
"ds_read_b64_tr_b16 a[216:217], v255 offset:16896\n"
"v_mfma_f32_16x16x32_bf16 v[16:19], a[128:131], v[160:163], v[120:123]\n"
"v_exp_f32 v32, v32\n"
"v_mfma_f32_16x16x32_bf16 v[16:19], a[132:135], v[164:167], v[16:19]\n"
"ds_read_b64_tr_b16 a[218:219], v255 offset:20992\n"
"v_cvt_pk_bf16_f32 v87, v30, v31\n"
"v_mfma_f32_16x16x32_bf16 v[16:19], a[136:139], v[64:67], v[16:19]\n"
"v_exp_f32 v33, v33\n"
"v_mfma_f32_16x16x32_bf16 v[16:19], a[140:143], v[68:71], v[16:19]\n"
"ds_read_b64_tr_b16 a[220:221], v255 offset:25088\n"
"ds_read_b64_tr_b16 a[222:223], v255 offset:29184\n"
"v_mfma_f32_16x16x32_bf16 v[20:23], a[144:147], v[160:163], v[120:123]\n"
"v_exp_f32 v34, v34\n"
"v_mfma_f32_16x16x32_bf16 v[20:23], a[148:151], v[164:167], v[20:23]\n"
"ds_read_b64_tr_b16 a[224:225], v254 offset:17408\n"
"v_cvt_pk_bf16_f32 v88, v32, v33\n"
"v_mfma_f32_16x16x32_bf16 v[20:23], a[152:155], v[64:67], v[20:23]\n"
"v_exp_f32 v35, v35\n"
"v_mfma_f32_16x16x32_bf16 v[20:23], a[156:159], v[68:71], v[20:23]\n"
"ds_read_b64_tr_b16 a[226:227], v254 offset:21504\n"
"ds_read_b64_tr_b16 a[228:229], v254 offset:25600\n"
"v_mfma_f32_16x16x32_bf16 v[24:27], a[160:163], v[160:163], v[120:123]\n"
"v_exp_f32 v36, v36\n"
"v_mfma_f32_16x16x32_bf16 v[24:27], a[164:167], v[164:167], v[24:27]\n"
"ds_read_b64_tr_b16 a[230:231], v254 offset:29696\n"
"v_cvt_pk_bf16_f32 v89, v34, v35\n"
"v_mfma_f32_16x16x32_bf16 v[24:27], a[168:171], v[64:67], v[24:27]\n"
"v_exp_f32 v37, v37\n"
"v_mfma_f32_16x16x32_bf16 v[24:27], a[172:175], v[68:71], v[24:27]\n"
"ds_read_b64_tr_b16 a[232:233], v255 offset:17408\n"
"ds_read_b64_tr_b16 a[234:235], v255 offset:21504\n"
"v_smfmac_f32_16x16x64_bf16 v[128:131], v[228:231], v[80:87], v239\n"
"ds_read_b64_tr_b16 a[236:237], v255 offset:25600\n"
"v_mfma_f32_16x16x32_bf16 v[28:31], a[176:179], v[160:163], v[120:123]\n"
"v_exp_f32 v38, v38\n"
"v_mfma_f32_16x16x32_bf16 v[28:31], a[180:183], v[164:167], v[28:31]\n"
"ds_read_b64_tr_b16 a[238:239], v255 offset:29696\n"
"v_cvt_pk_bf16_f32 v90, v36, v37\n"
"v_mfma_f32_16x16x32_bf16 v[28:31], a[184:187], v[64:67], v[28:31]\n"
"s_waitcnt lgkmcnt(15)\n"
"v_exp_f32 v39, v39\n"
"v_mfma_f32_16x16x32_bf16 v[28:31], a[188:191], v[68:71], v[28:31]\n"
"ds_read_b64_tr_b16 a[240:241], v254 offset:17920\n"
"ds_read_b64_tr_b16 a[242:243], v254 offset:22016\n"
"v_mfma_f32_16x16x32_bf16 a[0:3], a[192:195], v[80:83], a[0:3]\n"
"v_exp_f32 v40, v40\n"
"v_mfma_f32_16x16x32_bf16 a[0:3], a[196:199], v[84:87], a[0:3]\n"
"ds_read_b64_tr_b16 a[244:245], v254 offset:26112\n"
"v_cvt_pk_bf16_f32 v91, v38, v39\n"
"v_mfma_f32_16x16x32_bf16 a[16:19], a[200:203], v[80:83], a[16:19]\n"
"s_waitcnt lgkmcnt(11)\n"
"v_exp_f32 v41, v41\n"
"v_mfma_f32_16x16x32_bf16 a[16:19], a[204:207], v[84:87], a[16:19]\n"
"ds_read_b64_tr_b16 a[246:247], v254 offset:30208\n"
"ds_read_b64_tr_b16 a[248:249], v255 offset:17920\n"
"v_mfma_f32_16x16x32_bf16 a[32:35], a[208:211], v[80:83], a[32:35]\n"
"v_exp_f32 v42, v42\n"
"v_mfma_f32_16x16x32_bf16 a[32:35], a[212:215], v[84:87], a[32:35]\n"
"ds_read_b64_tr_b16 a[250:251], v255 offset:22016\n"
"v_cvt_pk_bf16_f32 v92, v40, v41\n"
"v_mfma_f32_16x16x32_bf16 a[48:51], a[216:219], v[80:83], a[48:51]\n"
"s_waitcnt lgkmcnt(6)\n"
"v_exp_f32 v43, v43\n"
"v_mfma_f32_16x16x32_bf16 a[48:51], a[220:223], v[84:87], a[48:51]\n"
"ds_read_b64_tr_b16 a[252:253], v255 offset:26112\n"
"ds_read_b64_tr_b16 a[254:255], v255 offset:30208\n"
"v_mfma_f32_16x16x32_bf16 a[64:67], a[224:227], v[80:83], a[64:67]\n"
"v_exp_f32 v44, v44\n"
"v_mfma_f32_16x16x32_bf16 a[64:67], a[228:231], v[84:87], a[64:67]\n"
"s_add_u32 m0, s90, 32768\n"
"v_cvt_pk_bf16_f32 v93, v42, v43\n"
"v_mfma_f32_16x16x32_bf16 a[80:83], a[232:235], v[80:83], a[80:83]\n"
"s_waitcnt lgkmcnt(4)\n"
"v_exp_f32 v45, v45\n"
"v_mfma_f32_16x16x32_bf16 a[80:83], a[236:239], v[84:87], a[80:83]\n"
"buffer_load_dwordx4 v244, s[32:35], s89 offen nt lds\n"
"v_mfma_f32_16x16x32_bf16 a[96:99], a[240:243], v[80:83], a[96:99]\n"
"s_waitcnt lgkmcnt(0)\n"
"v_exp_f32 v46, v46\n"
"v_mfma_f32_16x16x32_bf16 a[96:99], a[244:247], v[84:87], a[96:99]\n"
"v_cvt_pk_bf16_f32 v94, v44, v45\n"
"v_xor_b32 v235, s76, v4\n"
"v_mfma_f32_16x16x32_bf16 a[112:115], a[248:251], v[80:83], a[112:115]\n"
"v_exp_f32 v47, v47\n"
"v_mfma_f32_16x16x32_bf16 a[112:115], a[252:255], v[84:87], a[112:115]\n"
"buffer_load_dwordx4 v245, s[32:35], s89 offen offset:1024 nt lds\n"
"v_mfma_f32_16x16x32_bf16 v[32:35], a[128:131], v[168:171], v[124:127]\n"
"v_exp_f32 v16, v16\n"
"v_mfma_f32_16x16x32_bf16 v[32:35], a[132:135], v[172:175], v[32:35]\n"
"ds_read_b128 a[128:131], v250\n"
"v_cvt_pk_bf16_f32 v95, v46, v47\n"
"v_mfma_f32_16x16x32_bf16 v[32:35], a[136:139], v[72:75], v[32:35]\n"
"v_exp_f32 v17, v17\n"
"v_mfma_f32_16x16x32_bf16 v[32:35], a[140:143], v[76:79], v[32:35]\n"
"ds_read_b128 a[132:135], v251\n"
"ds_read_b128 a[136:139], v252\n"
"v_mfma_f32_16x16x32_bf16 v[36:39], a[144:147], v[168:171], v[124:127]\n"
"v_exp_f32 v18, v18\n"
"v_mfma_f32_16x16x32_bf16 v[36:39], a[148:151], v[172:175], v[36:39]\n"
"ds_read_b128 a[140:143], v253\n"
"v_cvt_pk_bf16_f32 v80, v16, v17\n"
"v_mfma_f32_16x16x32_bf16 v[36:39], a[152:155], v[72:75], v[36:39]\n"
"v_exp_f32 v19, v19\n"
"v_mfma_f32_16x16x32_bf16 v[36:39], a[156:159], v[76:79], v[36:39]\n"
"ds_read_b128 a[144:147], v250 offset:4096\n"
"ds_read_b128 a[148:151], v251 offset:4096\n"
"v_mfma_f32_16x16x32_bf16 v[40:43], a[160:163], v[168:171], v[124:127]\n"
"v_exp_f32 v20, v20\n"
"v_mfma_f32_16x16x32_bf16 v[40:43], a[164:167], v[172:175], v[40:43]\n"
"ds_read_b128 a[152:155], v252 offset:4096\n"
"v_cvt_pk_bf16_f32 v81, v18, v19\n"
"v_mfma_f32_16x16x32_bf16 v[40:43], a[168:171], v[72:75], v[40:43]\n"
"v_exp_f32 v21, v21\n"
"v_mfma_f32_16x16x32_bf16 v[40:43], a[172:175], v[76:79], v[40:43]\n"
"ds_read_b128 a[156:159], v253 offset:4096\n"
"ds_read_b128 a[160:163], v250 offset:8192\n"
"v_mfma_f32_16x16x32_bf16 v[44:47], a[176:179], v[168:171], v[124:127]\n"
"v_exp_f32 v22, v22\n"
"v_mfma_f32_16x16x32_bf16 v[44:47], a[180:183], v[172:175], v[44:47]\n"
"ds_read_b128 a[164:167], v251 offset:8192\n"
"v_cvt_pk_bf16_f32 v82, v20, v21\n"
"v_mfma_f32_16x16x32_bf16 v[44:47], a[184:187], v[72:75], v[44:47]\n"
"v_exp_f32 v23, v23\n"
"v_mfma_f32_16x16x32_bf16 v[44:47], a[188:191], v[76:79], v[44:47]\n"
"ds_read_b128 a[168:171], v252 offset:8192\n"
"ds_read_b128 a[172:175], v253 offset:8192\n"
"v_mfma_f32_16x16x32_bf16 a[4:7], a[192:195], v[88:91], a[4:7]\n"
"v_exp_f32 v24, v24\n"
"v_mfma_f32_16x16x32_bf16 a[4:7], a[196:199], v[92:95], a[4:7]\n"
"ds_read_b128 a[176:179], v250 offset:12288\n"
"v_cvt_pk_bf16_f32 v83, v22, v23\n"
"v_mfma_f32_16x16x32_bf16 a[20:23], a[200:203], v[88:91], a[20:23]\n"
"v_exp_f32 v25, v25\n"
"v_mfma_f32_16x16x32_bf16 a[20:23], a[204:207], v[92:95], a[20:23]\n"
"ds_read_b128 a[180:183], v251 offset:12288\n"
"ds_read_b128 a[184:187], v252 offset:12288\n"
"v_mfma_f32_16x16x32_bf16 a[36:39], a[208:211], v[88:91], a[36:39]\n"
"v_exp_f32 v26, v26\n"
"v_mfma_f32_16x16x32_bf16 a[36:39], a[212:215], v[92:95], a[36:39]\n"
"ds_read_b128 a[188:191], v253 offset:12288\n"
"v_cvt_pk_bf16_f32 v84, v24, v25\n"
"v_mfma_f32_16x16x32_bf16 a[52:55], a[216:219], v[88:91], a[52:55]\n"
"v_exp_f32 v27, v27\n"
"v_mfma_f32_16x16x32_bf16 a[52:55], a[220:223], v[92:95], a[52:55]\n"
"buffer_load_dwordx4 v246, s[32:35], s89 offen offset:2048 nt lds\n"
"v_mfma_f32_16x16x32_bf16 a[68:71], a[224:227], v[88:91], a[68:71]\n"
"v_exp_f32 v28, v28\n"
"v_mfma_f32_16x16x32_bf16 a[68:71], a[228:231], v[92:95], a[68:71]\n"
"v_cvt_pk_bf16_f32 v85, v26, v27\n"
"v_mfma_f32_16x16x32_bf16 a[84:87], a[232:235], v[88:91], a[84:87]\n"
"v_exp_f32 v29, v29\n"
"v_mfma_f32_16x16x32_bf16 a[84:87], a[236:239], v[92:95], a[84:87]\n"
"buffer_load_dwordx4 v247, s[32:35], s89 offen offset:3072 nt lds\n"
"v_smfmac_f32_16x16x64_bf16 v[132:135], v[228:231], v[88:95], v239\n"
"v_mfma_f32_16x16x32_bf16 a[100:103], a[240:243], v[88:91], a[100:103]\n"
"s_sub_u32 s82, s89, 16384\n"
"v_exp_f32 v30, v30\n"
"v_mfma_f32_16x16x32_bf16 a[100:103], a[244:247], v[92:95], a[100:103]\n"
"s_add_u32 m0, s90, 49152\n"
"v_cvt_pk_bf16_f32 v86, v28, v29\n"
"v_mfma_f32_16x16x32_bf16 a[116:119], a[248:251], v[88:91], a[116:119]\n"
"s_waitcnt lgkmcnt(0)\n"
"v_exp_f32 v31, v31\n"
"v_mfma_f32_16x16x32_bf16 a[116:119], a[252:255], v[92:95], a[116:119]\n"
"buffer_load_dwordx4 v248, s[36:39], s82 offen nt lds\n"
"v_mfma_f32_16x16x32_bf16 v[16:19], a[128:131], v[144:147], v[112:115]\n"
"v_exp_f32 v32, v32\n"
"v_mfma_f32_16x16x32_bf16 v[16:19], a[132:135], v[148:151], v[16:19]\n"
"v_cvt_pk_bf16_f32 v87, v30, v31\n"
"v_mfma_f32_16x16x32_bf16 v[16:19], a[136:139], v[48:51], v[16:19]\n"
"v_exp_f32 v33, v33\n"
"v_mfma_f32_16x16x32_bf16 v[16:19], a[140:143], v[52:55], v[16:19]\n"
"buffer_load_dwordx4 v249, s[36:39], s82 offen offset:1024 nt lds\n"
"v_mfma_f32_16x16x32_bf16 v[20:23], a[144:147], v[144:147], v[112:115]\n"
"v_exp_f32 v34, v34\n"
"v_mfma_f32_16x16x32_bf16 v[20:23], a[148:151], v[148:151], v[20:23]\n"
"v_cvt_pk_bf16_f32 v88, v32, v33\n"
"v_mfma_f32_16x16x32_bf16 v[20:23], a[152:155], v[48:51], v[20:23]\n"
"v_exp_f32 v35, v35\n"
"v_mfma_f32_16x16x32_bf16 v[20:23], a[156:159], v[52:55], v[20:23]\n"
"buffer_load_dwordx4 v248, s[36:39], s82 offen offset:2048 nt lds\n"
"v_mfma_f32_16x16x32_bf16 v[24:27], a[160:163], v[144:147], v[112:115]\n"
"v_exp_f32 v36, v36\n"
"v_mfma_f32_16x16x32_bf16 v[24:27], a[164:167], v[148:151], v[24:27]\n"
"v_cvt_pk_bf16_f32 v89, v34, v35\n"
"v_mfma_f32_16x16x32_bf16 v[24:27], a[168:171], v[48:51], v[24:27]\n"
"v_exp_f32 v37, v37\n"
"v_mfma_f32_16x16x32_bf16 v[24:27], a[172:175], v[52:55], v[24:27]\n"
"buffer_load_dwordx4 v249, s[36:39], s82 offen offset:3072 nt lds\n"
"v_mfma_f32_16x16x32_bf16 v[28:31], a[176:179], v[144:147], v[112:115]\n"
"v_exp_f32 v38, v38\n"
"v_mfma_f32_16x16x32_bf16 v[28:31], a[180:183], v[148:151], v[28:31]\n"
"v_cvt_pk_bf16_f32 v90, v36, v37\n"
"v_readlane_b32 s53, v233, s55\n"
"v_mfma_f32_16x16x32_bf16 v[28:31], a[184:187], v[48:51], v[28:31]\n"
"v_exp_f32 v39, v39\n"
"v_mfma_f32_16x16x32_bf16 v[28:31], a[188:191], v[52:55], v[28:31]\n"
"v_mfma_f32_16x16x32_bf16 a[8:11], a[192:195], v[80:83], a[8:11]\n"
"v_exp_f32 v40, v40\n"
"v_mfma_f32_16x16x32_bf16 a[8:11], a[196:199], v[84:87], a[8:11]\n"
"v_cvt_pk_bf16_f32 v91, v38, v39\n"
"v_mfma_f32_16x16x32_bf16 a[24:27], a[200:203], v[80:83], a[24:27]\n"
"v_exp_f32 v41, v41\n"
"v_mfma_f32_16x16x32_bf16 a[24:27], a[204:207], v[84:87], a[24:27]\n"
"buffer_store_dwordx4 v[240:243], v1, s[40:43], s53 offen sc1\n"
"v_mfma_f32_16x16x32_bf16 a[40:43], a[208:211], v[80:83], a[40:43]\n"
"v_exp_f32 v42, v42\n"
"v_mfma_f32_16x16x32_bf16 a[40:43], a[212:215], v[84:87], a[40:43]\n"
"s_add_u32 s89, s89, 16384\n"
"v_cvt_pk_bf16_f32 v92, v40, v41\n"
"v_mfma_f32_16x16x32_bf16 a[56:59], a[216:219], v[80:83], a[56:59]\n"
"s_add_u32 s55, s55, 1\n"
"v_exp_f32 v43, v43\n"
"v_mfma_f32_16x16x32_bf16 a[56:59], a[220:223], v[84:87], a[56:59]\n"
"v_mfma_f32_16x16x32_bf16 a[72:75], a[224:227], v[80:83], a[72:75]\n"
"s_add_u32 s56, s56, 1024\n"
"v_exp_f32 v44, v44\n"
"v_mfma_f32_16x16x32_bf16 a[72:75], a[228:231], v[84:87], a[72:75]\n"
"v_cvt_pk_bf16_f32 v93, v42, v43\n"
"v_mfma_f32_16x16x32_bf16 a[88:91], a[232:235], v[80:83], a[88:91]\n"
"v_exp_f32 v45, v45\n"
"v_mfma_f32_16x16x32_bf16 a[88:91], a[236:239], v[84:87], a[88:91]\n"
"v_smfmac_f32_16x16x64_bf16 v[136:139], v[228:231], v[80:87], v239\n"
"v_mfma_f32_16x16x32_bf16 a[104:107], a[240:243], v[80:83], a[104:107]\n"
"v_exp_f32 v46, v46\n"
"v_mfma_f32_16x16x32_bf16 a[104:107], a[244:247], v[84:87], a[104:107]\n"
"v_cvt_pk_bf16_f32 v94, v44, v45\n"
"v_mfma_f32_16x16x32_bf16 a[120:123], a[248:251], v[80:83], a[120:123]\n"
"v_exp_f32 v47, v47\n"
"v_mfma_f32_16x16x32_bf16 a[120:123], a[252:255], v[84:87], a[120:123]\n"
"v_mfma_f32_16x16x32_bf16 v[32:35], a[128:131], v[152:155], v[116:119]\n"
"v_exp_f32 v16, v16\n"
"v_mfma_f32_16x16x32_bf16 v[32:35], a[132:135], v[156:159], v[32:35]\n"
"v_cvt_pk_bf16_f32 v95, v46, v47\n"
"v_readlane_b32 s52, v232, s55\n"
"v_mfma_f32_16x16x32_bf16 v[32:35], a[136:139], v[56:59], v[32:35]\n"
"s_waitcnt vmcnt(20) lgkmcnt(0)\n"
"v_exp_f32 v17, v17\n"
"v_mfma_f32_16x16x32_bf16 v[32:35], a[140:143], v[60:63], v[32:35]\n"
"s_barrier\n"
"v_mfma_f32_16x16x32_bf16 v[36:39], a[144:147], v[152:155], v[116:119]\n"
"v_exp_f32 v18, v18\n"
"v_mfma_f32_16x16x32_bf16 v[36:39], a[148:151], v[156:159], v[36:39]\n"
"v_cvt_pk_bf16_f32 v80, v16, v17\n"
"v_mfma_f32_16x16x32_bf16 v[36:39], a[152:155], v[56:59], v[36:39]\n"
"v_exp_f32 v19, v19\n"
"v_mfma_f32_16x16x32_bf16 v[36:39], a[156:159], v[60:63], v[36:39]\n"
"ds_read_b64_tr_b16 v[240:241], v235\n"
"ds_read_b64_tr_b16 v[242:243], v235 offset:2048\n"
"v_mfma_f32_16x16x32_bf16 v[40:43], a[160:163], v[152:155], v[116:119]\n"
"v_exp_f32 v20, v20\n"
"v_mfma_f32_16x16x32_bf16 v[40:43], a[164:167], v[156:159], v[40:43]\n"
"s_cmp_eq_u32 s55, 12\n"
"s_cbranch_scc1 L_retile2_6_%=\n"
"L_retile2b_7_%=:\n"
"s_cmp_eq_u32 s55, 24\n"
"s_cselect_b32 s59, s96, 0x7fff0000\n"
"v_cvt_pk_bf16_f32 v81, v18, v19\n"
"v_mfma_f32_16x16x32_bf16 v[40:43], a[168:171], v[56:59], v[40:43]\n"
"v_exp_f32 v21, v21\n"
"v_mfma_f32_16x16x32_bf16 v[40:43], a[172:175], v[60:63], v[40:43]\n"
"buffer_load_dword v227, v1, s[16:19], s59 offen\n"
"v_mfma_f32_16x16x32_bf16 v[44:47], a[176:179], v[152:155], v[116:119]\n"
"s_and_b32 s78, s56, 7168\n"
"v_exp_f32 v22, v22\n"
"v_mfma_f32_16x16x32_bf16 v[44:47], a[180:183], v[156:159], v[44:47]\n"
"s_waitcnt lgkmcnt(0)\n"
"s_add_u32 m0, s54, s78\n"
"v_cvt_pk_bf16_f32 v82, v20, v21\n"
"v_mfma_f32_16x16x32_bf16 v[44:47], a[184:187], v[56:59], v[44:47]\n"
"v_exp_f32 v23, v23\n"
"v_mfma_f32_16x16x32_bf16 v[44:47], a[188:191], v[60:63], v[44:47]\n"
"buffer_load_dwordx4 v234, s[28:31], s52 offen nt lds\n"
"v_mfma_f32_16x16x32_bf16 a[12:15], a[192:195], v[88:91], a[12:15]\n"
"v_exp_f32 v24, v24\n"
"v_mfma_f32_16x16x32_bf16 a[12:15], a[196:199], v[92:95], a[12:15]\n"
"ds_read_b64_tr_b16 a[192:193], v254 offset:49152\n"
"v_cvt_pk_bf16_f32 v83, v22, v23\n"
"v_mfma_f32_16x16x32_bf16 a[28:31], a[200:203], v[88:91], a[28:31]\n"
"v_exp_f32 v25, v25\n"
"v_mfma_f32_16x16x32_bf16 a[28:31], a[204:207], v[92:95], a[28:31]\n"
"ds_read_b64_tr_b16 a[194:195], v254 offset:53248\n"
"ds_read_b64_tr_b16 a[196:197], v254 offset:57344\n"
"v_mfma_f32_16x16x32_bf16 a[44:47], a[208:211], v[88:91], a[44:47]\n"
"v_exp_f32 v26, v26\n"
"v_mfma_f32_16x16x32_bf16 a[44:47], a[212:215], v[92:95], a[44:47]\n"
"ds_read_b64_tr_b16 a[198:199], v254 offset:61440\n"
"v_cvt_pk_bf16_f32 v84, v24, v25\n"
"v_mfma_f32_16x16x32_bf16 a[60:63], a[216:219], v[88:91], a[60:63]\n"
"s_add_u32 s80, s55, 4\n"
"v_exp_f32 v27, v27\n"
"v_mfma_f32_16x16x32_bf16 a[60:63], a[220:223], v[92:95], a[60:63]\n"
"s_and_b32 s79, s80, 1\n"
"ds_read_b64_tr_b16 a[200:201], v255 offset:49152\n"
"ds_read_b64_tr_b16 a[202:203], v255 offset:53248\n"
"v_mfma_f32_16x16x32_bf16 a[76:79], a[224:227], v[88:91], a[76:79]\n"
"s_lshl_b32 s76, s79, 5\n"
"v_exp_f32 v28, v28\n"
"v_mfma_f32_16x16x32_bf16 a[76:79], a[228:231], v[92:95], a[76:79]\n"
"s_and_b32 s79, s80, 2\n"
"ds_read_b64_tr_b16 a[204:205], v255 offset:57344\n"
"v_cvt_pk_bf16_f32 v85, v26, v27\n"
"v_smfmac_f32_16x16x64_bf16 v[140:143], v[228:231], v[88:95], v239\n"
"ds_read_b64_tr_b16 a[206:207], v255 offset:61440\n"
"v_mfma_f32_16x16x32_bf16 a[92:95], a[232:235], v[88:91], a[92:95]\n"
"s_lshl_b32 s79, s79, 8\n"
"v_exp_f32 v29, v29\n"
"v_mfma_f32_16x16x32_bf16 a[92:95], a[236:239], v[92:95], a[92:95]\n"
"s_or_b32 s76, s76, s79\n"
"ds_read_b64_tr_b16 a[208:209], v254 offset:49664\n"
"ds_read_b64_tr_b16 a[210:211], v254 offset:53760\n"
"v_mfma_f32_16x16x32_bf16 a[108:111], a[240:243], v[88:91], a[108:111]\n"
"s_and_b32 s79, s80, 4\n"
"v_exp_f32 v30, v30\n"
"v_mfma_f32_16x16x32_bf16 a[108:111], a[244:247], v[92:95], a[108:111]\n"
"s_lshl_b32 s79, s79, 10\n"
"ds_read_b64_tr_b16 a[212:213], v254 offset:57856\n"
"v_cvt_pk_bf16_f32 v86, v28, v29\n"
"v_mfma_f32_16x16x32_bf16 a[124:127], a[248:251], v[88:91], a[124:127]\n"
"v_exp_f32 v31, v31\n"
"v_mfma_f32_16x16x32_bf16 a[124:127], a[252:255], v[92:95], a[124:127]\n"
"s_or_b32 s76, s76, s79\n"
"ds_read_b64_tr_b16 a[214:215], v254 offset:61952\n"
"ds_read_b64_tr_b16 a[216:217], v255 offset:49664\n"
"v_mfma_f32_16x16x32_bf16 v[16:19], a[128:131], v[160:163], v[120:123]\n"
"v_exp_f32 v32, v32\n"
"v_mfma_f32_16x16x32_bf16 v[16:19], a[132:135], v[164:167], v[16:19]\n"
"ds_read_b64_tr_b16 a[218:219], v255 offset:53760\n"
"v_cvt_pk_bf16_f32 v87, v30, v31\n"
"v_mfma_f32_16x16x32_bf16 v[16:19], a[136:139], v[64:67], v[16:19]\n"
"v_exp_f32 v33, v33\n"
"v_mfma_f32_16x16x32_bf16 v[16:19], a[140:143], v[68:71], v[16:19]\n"
"ds_read_b64_tr_b16 a[220:221], v255 offset:57856\n"
"ds_read_b64_tr_b16 a[222:223], v255 offset:61952\n"
"v_mfma_f32_16x16x32_bf16 v[20:23], a[144:147], v[160:163], v[120:123]\n"
"v_exp_f32 v34, v34\n"
"v_mfma_f32_16x16x32_bf16 v[20:23], a[148:151], v[164:167], v[20:23]\n"
"ds_read_b64_tr_b16 a[224:225], v254 offset:50176\n"
"v_cvt_pk_bf16_f32 v88, v32, v33\n"
"v_mfma_f32_16x16x32_bf16 v[20:23], a[152:155], v[64:67], v[20:23]\n"
"v_exp_f32 v35, v35\n"
"v_mfma_f32_16x16x32_bf16 v[20:23], a[156:159], v[68:71], v[20:23]\n"
"ds_read_b64_tr_b16 a[226:227], v254 offset:54272\n"
"ds_read_b64_tr_b16 a[228:229], v254 offset:58368\n"
"v_mfma_f32_16x16x32_bf16 v[24:27], a[160:163], v[160:163], v[120:123]\n"
"v_exp_f32 v36, v36\n"
"v_mfma_f32_16x16x32_bf16 v[24:27], a[164:167], v[164:167], v[24:27]\n"
"ds_read_b64_tr_b16 a[230:231], v254 offset:62464\n"
"v_cvt_pk_bf16_f32 v89, v34, v35\n"
"v_mfma_f32_16x16x32_bf16 v[24:27], a[168:171], v[64:67], v[24:27]\n"
"v_exp_f32 v37, v37\n"
"v_mfma_f32_16x16x32_bf16 v[24:27], a[172:175], v[68:71], v[24:27]\n"
"ds_read_b64_tr_b16 a[232:233], v255 offset:50176\n"
"ds_read_b64_tr_b16 a[234:235], v255 offset:54272\n"
"v_smfmac_f32_16x16x64_bf16 v[128:131], v[228:231], v[80:87], v239\n"
"ds_read_b64_tr_b16 a[236:237], v255 offset:58368\n"
"v_mfma_f32_16x16x32_bf16 v[28:31], a[176:179], v[160:163], v[120:123]\n"
"v_exp_f32 v38, v38\n"
"v_mfma_f32_16x16x32_bf16 v[28:31], a[180:183], v[164:167], v[28:31]\n"
"ds_read_b64_tr_b16 a[238:239], v255 offset:62464\n"
"v_cvt_pk_bf16_f32 v90, v36, v37\n"
"v_mfma_f32_16x16x32_bf16 v[28:31], a[184:187], v[64:67], v[28:31]\n"
"s_waitcnt lgkmcnt(15)\n"
"v_exp_f32 v39, v39\n"
"v_mfma_f32_16x16x32_bf16 v[28:31], a[188:191], v[68:71], v[28:31]\n"
"ds_read_b64_tr_b16 a[240:241], v254 offset:50688\n"
"ds_read_b64_tr_b16 a[242:243], v254 offset:54784\n"
"v_mfma_f32_16x16x32_bf16 a[0:3], a[192:195], v[80:83], a[0:3]\n"
"v_exp_f32 v40, v40\n"
"v_mfma_f32_16x16x32_bf16 a[0:3], a[196:199], v[84:87], a[0:3]\n"
"ds_read_b64_tr_b16 a[244:245], v254 offset:58880\n"
"v_cvt_pk_bf16_f32 v91, v38, v39\n"
"v_mfma_f32_16x16x32_bf16 a[16:19], a[200:203], v[80:83], a[16:19]\n"
"s_waitcnt lgkmcnt(11)\n"
"v_exp_f32 v41, v41\n"
"v_mfma_f32_16x16x32_bf16 a[16:19], a[204:207], v[84:87], a[16:19]\n"
"ds_read_b64_tr_b16 a[246:247], v254 offset:62976\n"
"ds_read_b64_tr_b16 a[248:249], v255 offset:50688\n"
"v_mfma_f32_16x16x32_bf16 a[32:35], a[208:211], v[80:83], a[32:35]\n"
"v_exp_f32 v42, v42\n"
"v_mfma_f32_16x16x32_bf16 a[32:35], a[212:215], v[84:87], a[32:35]\n"
"ds_read_b64_tr_b16 a[250:251], v255 offset:54784\n"
"v_cvt_pk_bf16_f32 v92, v40, v41\n"
"v_mfma_f32_16x16x32_bf16 a[48:51], a[216:219], v[80:83], a[48:51]\n"
"s_waitcnt lgkmcnt(6)\n"
"v_exp_f32 v43, v43\n"
"v_mfma_f32_16x16x32_bf16 a[48:51], a[220:223], v[84:87], a[48:51]\n"
"ds_read_b64_tr_b16 a[252:253], v255 offset:58880\n"
"ds_read_b64_tr_b16 a[254:255], v255 offset:62976\n"
"v_mfma_f32_16x16x32_bf16 a[64:67], a[224:227], v[80:83], a[64:67]\n"
"s_xor_b32 s90, s90, 0x10000\n"
"v_exp_f32 v44, v44\n"
"v_mfma_f32_16x16x32_bf16 a[64:67], a[228:231], v[84:87], a[64:67]\n"
"s_add_u32 m0, s90, 0\n"
"v_cvt_pk_bf16_f32 v93, v42, v43\n"
"v_mfma_f32_16x16x32_bf16 a[80:83], a[232:235], v[80:83], a[80:83]\n"
"s_waitcnt lgkmcnt(4)\n"
"v_exp_f32 v45, v45\n"
"v_mfma_f32_16x16x32_bf16 a[80:83], a[236:239], v[84:87], a[80:83]\n"
"buffer_load_dwordx4 v244, s[32:35], s89 offen nt lds\n"
"v_mfma_f32_16x16x32_bf16 a[96:99], a[240:243], v[80:83], a[96:99]\n"
"s_waitcnt lgkmcnt(0)\n"
"v_exp_f32 v46, v46\n"
"v_mfma_f32_16x16x32_bf16 a[96:99], a[244:247], v[84:87], a[96:99]\n"
"v_cvt_pk_bf16_f32 v94, v44, v45\n"
"v_xor_b32 v235, s76, v4\n"
"v_mfma_f32_16x16x32_bf16 a[112:115], a[248:251], v[80:83], a[112:115]\n"
"v_exp_f32 v47, v47\n"
"v_mfma_f32_16x16x32_bf16 a[112:115], a[252:255], v[84:87], a[112:115]\n"
"buffer_load_dwordx4 v245, s[32:35], s89 offen offset:1024 nt lds\n"
"v_mfma_f32_16x16x32_bf16 v[32:35], a[128:131], v[168:171], v[124:127]\n"
"v_exp_f32 v16, v16\n"
"v_mfma_f32_16x16x32_bf16 v[32:35], a[132:135], v[172:175], v[32:35]\n"
"ds_read_b128 a[128:131], v250 offset:32768\n"
"v_cvt_pk_bf16_f32 v95, v46, v47\n"
"v_mfma_f32_16x16x32_bf16 v[32:35], a[136:139], v[72:75], v[32:35]\n"
"v_exp_f32 v17, v17\n"
"v_mfma_f32_16x16x32_bf16 v[32:35], a[140:143], v[76:79], v[32:35]\n"
"ds_read_b128 a[132:135], v251 offset:32768\n"
"ds_read_b128 a[136:139], v252 offset:32768\n"
"v_mfma_f32_16x16x32_bf16 v[36:39], a[144:147], v[168:171], v[124:127]\n"
"v_exp_f32 v18, v18\n"
"v_mfma_f32_16x16x32_bf16 v[36:39], a[148:151], v[172:175], v[36:39]\n"
"ds_read_b128 a[140:143], v253 offset:32768\n"
"v_cvt_pk_bf16_f32 v80, v16, v17\n"
"v_mfma_f32_16x16x32_bf16 v[36:39], a[152:155], v[72:75], v[36:39]\n"
"v_exp_f32 v19, v19\n"
"v_mfma_f32_16x16x32_bf16 v[36:39], a[156:159], v[76:79], v[36:39]\n"
"ds_read_b128 a[144:147], v250 offset:36864\n"
"ds_read_b128 a[148:151], v251 offset:36864\n"
"v_mfma_f32_16x16x32_bf16 v[40:43], a[160:163], v[168:171], v[124:127]\n"
"v_exp_f32 v20, v20\n"
"v_mfma_f32_16x16x32_bf16 v[40:43], a[164:167], v[172:175], v[40:43]\n"
"ds_read_b128 a[152:155], v252 offset:36864\n"
"v_cvt_pk_bf16_f32 v81, v18, v19\n"
"v_mfma_f32_16x16x32_bf16 v[40:43], a[168:171], v[72:75], v[40:43]\n"
"v_exp_f32 v21, v21\n"
"v_mfma_f32_16x16x32_bf16 v[40:43], a[172:175], v[76:79], v[40:43]\n"
"ds_read_b128 a[156:159], v253 offset:36864\n"
"ds_read_b128 a[160:163], v250 offset:40960\n"
"v_mfma_f32_16x16x32_bf16 v[44:47], a[176:179], v[168:171], v[124:127]\n"
"v_exp_f32 v22, v22\n"
"v_mfma_f32_16x16x32_bf16 v[44:47], a[180:183], v[172:175], v[44:47]\n"
"ds_read_b128 a[164:167], v251 offset:40960\n"
"v_cvt_pk_bf16_f32 v82, v20, v21\n"
"v_mfma_f32_16x16x32_bf16 v[44:47], a[184:187], v[72:75], v[44:47]\n"
"v_exp_f32 v23, v23\n"
"v_mfma_f32_16x16x32_bf16 v[44:47], a[188:191], v[76:79], v[44:47]\n"
"ds_read_b128 a[168:171], v252 offset:40960\n"
"ds_read_b128 a[172:175], v253 offset:40960\n"
"v_mfma_f32_16x16x32_bf16 a[4:7], a[192:195], v[88:91], a[4:7]\n"
"v_exp_f32 v24, v24\n"
"v_mfma_f32_16x16x32_bf16 a[4:7], a[196:199], v[92:95], a[4:7]\n"
"ds_read_b128 a[176:179], v250 offset:45056\n"
"v_cvt_pk_bf16_f32 v83, v22, v23\n"
"v_mfma_f32_16x16x32_bf16 a[20:23], a[200:203], v[88:91], a[20:23]\n"
"v_exp_f32 v25, v25\n"
"v_mfma_f32_16x16x32_bf16 a[20:23], a[204:207], v[92:95], a[20:23]\n"
"ds_read_b128 a[180:183], v251 offset:45056\n"
"ds_read_b128 a[184:187], v252 offset:45056\n"
"v_mfma_f32_16x16x32_bf16 a[36:39], a[208:211], v[88:91], a[36:39]\n"
"v_exp_f32 v26, v26\n"
"v_mfma_f32_16x16x32_bf16 a[36:39], a[212:215], v[92:95], a[36:39]\n"
"ds_read_b128 a[188:191], v253 offset:45056\n"
"v_cvt_pk_bf16_f32 v84, v24, v25\n"
"v_mfma_f32_16x16x32_bf16 a[52:55], a[216:219], v[88:91], a[52:55]\n"
"v_exp_f32 v27, v27\n"
"v_mfma_f32_16x16x32_bf16 a[52:55], a[220:223], v[92:95], a[52:55]\n"
"buffer_load_dwordx4 v246, s[32:35], s89 offen offset:2048 nt lds\n"
"v_mfma_f32_16x16x32_bf16 a[68:71], a[224:227], v[88:91], a[68:71]\n"
"v_exp_f32 v28, v28\n"
"v_mfma_f32_16x16x32_bf16 a[68:71], a[228:231], v[92:95], a[68:71]\n"
"v_cvt_pk_bf16_f32 v85, v26, v27\n"
"v_mfma_f32_16x16x32_bf16 a[84:87], a[232:235], v[88:91], a[84:87]\n"
"v_exp_f32 v29, v29\n"
"v_mfma_f32_16x16x32_bf16 a[84:87], a[236:239], v[92:95], a[84:87]\n"
"buffer_load_dwordx4 v247, s[32:35], s89 offen offset:3072 nt lds\n"
"v_smfmac_f32_16x16x64_bf16 v[132:135], v[228:231], v[88:95], v239\n"
"v_mfma_f32_16x16x32_bf16 a[100:103], a[240:243], v[88:91], a[100:103]\n"
"s_sub_u32 s82, s89, 16384\n"
"v_exp_f32 v30, v30\n"
"v_mfma_f32_16x16x32_bf16 a[100:103], a[244:247], v[92:95], a[100:103]\n"
"s_add_u32 m0, s90, 16384\n"
"v_cvt_pk_bf16_f32 v86, v28, v29\n"
"v_mfma_f32_16x16x32_bf16 a[116:119], a[248:251], v[88:91], a[116:119]\n"
"s_waitcnt lgkmcnt(0)\n"
"v_exp_f32 v31, v31\n"
"v_mfma_f32_16x16x32_bf16 a[116:119], a[252:255], v[92:95], a[116:119]\n"
"buffer_load_dwordx4 v248, s[36:39], s82 offen nt lds\n"
"v_mfma_f32_16x16x32_bf16 v[16:19], a[128:131], v[144:147], v[112:115]\n"
"v_exp_f32 v32, v32\n"
"v_mfma_f32_16x16x32_bf16 v[16:19], a[132:135], v[148:151], v[16:19]\n"
"v_cvt_pk_bf16_f32 v87, v30, v31\n"
"v_xor_b32 v254, 0x10000, v254\n"
"v_mfma_f32_16x16x32_bf16 v[16:19], a[136:139], v[48:51], v[16:19]\n"
"v_exp_f32 v33, v33\n"
"v_mfma_f32_16x16x32_bf16 v[16:19], a[140:143], v[52:55], v[16:19]\n"
"buffer_load_dwordx4 v249, s[36:39], s82 offen offset:1024 nt lds\n"
"v_mfma_f32_16x16x32_bf16 v[20:23], a[144:147], v[144:147], v[112:115]\n"
"v_exp_f32 v34, v34\n"
"v_mfma_f32_16x16x32_bf16 v[20:23], a[148:151], v[148:151], v[20:23]\n"
"v_cvt_pk_bf16_f32 v88, v32, v33\n"
"v_xor_b32 v255, 0x10000, v255\n"
"v_mfma_f32_16x16x32_bf16 v[20:23], a[152:155], v[48:51], v[20:23]\n"
"v_exp_f32 v35, v35\n"
"v_mfma_f32_16x16x32_bf16 v[20:23], a[156:159], v[52:55], v[20:23]\n"
"buffer_load_dwordx4 v248, s[36:39], s82 offen offset:2048 nt lds\n"
"v_mfma_f32_16x16x32_bf16 v[24:27], a[160:163], v[144:147], v[112:115]\n"
"v_exp_f32 v36, v36\n"
"v_mfma_f32_16x16x32_bf16 v[24:27], a[164:167], v[148:151], v[24:27]\n"
"v_cvt_pk_bf16_f32 v89, v34, v35\n"
"v_xor_b32 v250, 0x10000, v250\n"
"v_mfma_f32_16x16x32_bf16 v[24:27], a[168:171], v[48:51], v[24:27]\n"
"v_exp_f32 v37, v37\n"
"v_mfma_f32_16x16x32_bf16 v[24:27], a[172:175], v[52:55], v[24:27]\n"
"buffer_load_dwordx4 v249, s[36:39], s82 offen offset:3072 nt lds\n"
"v_mfma_f32_16x16x32_bf16 v[28:31], a[176:179], v[144:147], v[112:115]\n"
"v_exp_f32 v38, v38\n"
"v_mfma_f32_16x16x32_bf16 v[28:31], a[180:183], v[148:151], v[28:31]\n"
"v_cvt_pk_bf16_f32 v90, v36, v37\n"
"v_readlane_b32 s53, v233, s55\n"
"v_mfma_f32_16x16x32_bf16 v[28:31], a[184:187], v[48:51], v[28:31]\n"
"v_exp_f32 v39, v39\n"
"v_mfma_f32_16x16x32_bf16 v[28:31], a[188:191], v[52:55], v[28:31]\n"
"v_mfma_f32_16x16x32_bf16 a[8:11], a[192:195], v[80:83], a[8:11]\n"
"v_exp_f32 v40, v40\n"
"v_mfma_f32_16x16x32_bf16 a[8:11], a[196:199], v[84:87], a[8:11]\n"
"v_cvt_pk_bf16_f32 v91, v38, v39\n"
"v_xor_b32 v251, 0x10000, v251\n"
"v_mfma_f32_16x16x32_bf16 a[24:27], a[200:203], v[80:83], a[24:27]\n"
"v_exp_f32 v41, v41\n"
"v_mfma_f32_16x16x32_bf16 a[24:27], a[204:207], v[84:87], a[24:27]\n"
"buffer_store_dwordx4 v[240:243], v1, s[40:43], s53 offen sc1\n"
"v_mfma_f32_16x16x32_bf16 a[40:43], a[208:211], v[80:83], a[40:43]\n"
"v_exp_f32 v42, v42\n"
"v_mfma_f32_16x16x32_bf16 a[40:43], a[212:215], v[84:87], a[40:43]\n"
"s_add_u32 s89, s89, 16384\n"
"v_cvt_pk_bf16_f32 v92, v40, v41\n"
"v_xor_b32 v252, 0x10000, v252\n"
"v_mfma_f32_16x16x32_bf16 a[56:59], a[216:219], v[80:83], a[56:59]\n"
"s_add_u32 s55, s55, 1\n"
"v_exp_f32 v43, v43\n"
"v_mfma_f32_16x16x32_bf16 a[56:59], a[220:223], v[84:87], a[56:59]\n"
"v_mfma_f32_16x16x32_bf16 a[72:75], a[224:227], v[80:83], a[72:75]\n"
"s_add_u32 s56, s56, 1024\n"
"v_exp_f32 v44, v44\n"
"v_mfma_f32_16x16x32_bf16 a[72:75], a[228:231], v[84:87], a[72:75]\n"
"v_cvt_pk_bf16_f32 v93, v42, v43\n"
"v_xor_b32 v253, 0x10000, v253\n"
"v_mfma_f32_16x16x32_bf16 a[88:91], a[232:235], v[80:83], a[88:91]\n"
"v_exp_f32 v45, v45\n"
"v_mfma_f32_16x16x32_bf16 a[88:91], a[236:239], v[84:87], a[88:91]\n"
"v_smfmac_f32_16x16x64_bf16 v[136:139], v[228:231], v[80:87], v239\n"
"v_mfma_f32_16x16x32_bf16 a[104:107], a[240:243], v[80:83], a[104:107]\n"
"v_exp_f32 v46, v46\n"
"v_mfma_f32_16x16x32_bf16 a[104:107], a[244:247], v[84:87], a[104:107]\n"
"v_cvt_pk_bf16_f32 v94, v44, v45\n"
"v_mfma_f32_16x16x32_bf16 a[120:123], a[248:251], v[80:83], a[120:123]\n"
"v_exp_f32 v47, v47\n"
"v_mfma_f32_16x16x32_bf16 a[120:123], a[252:255], v[84:87], a[120:123]\n"
"s_cmp_lg_u32 s55, 29\n"
"s_cbranch_scc1 L_att_5_%=\n"
"L_attdone_50_%=:\n"
"v_mfma_f32_16x16x32_bf16 v[32:35], a[128:131], v[152:155], v[116:119]\n"
"v_exp_f32 v16, v16\n"
"v_mfma_f32_16x16x32_bf16 v[32:35], a[132:135], v[156:159], v[32:35]\n"
"v_cvt_pk_bf16_f32 v95, v46, v47\n"
"v_mfma_f32_16x16x32_bf16 v[32:35], a[136:139], v[56:59], v[32:35]\n"
"s_waitcnt vmcnt(22) lgkmcnt(0)\n"
"v_exp_f32 v17, v17\n"
"v_mfma_f32_16x16x32_bf16 v[32:35], a[140:143], v[60:63], v[32:35]\n"
"s_barrier\n"
"s_lshl_b32 s23, s70, 11\n"
"s_add_u32 s23, s23, 79560704\n"
"v_mfma_f32_16x16x32_bf16 v[36:39], a[144:147], v[152:155], v[116:119]\n"
"v_exp_f32 v18, v18\n"
"v_mfma_f32_16x16x32_bf16 v[36:39], a[148:151], v[156:159], v[36:39]\n"
"v_cvt_pk_bf16_f32 v80, v16, v17\n"
"v_and_b32 v15, 31, v0\n"
"v_mfma_f32_16x16x32_bf16 v[36:39], a[152:155], v[56:59], v[36:39]\n"
"v_exp_f32 v19, v19\n"
"v_mfma_f32_16x16x32_bf16 v[36:39], a[156:159], v[60:63], v[36:39]\n"
"v_mfma_f32_16x16x32_bf16 v[40:43], a[160:163], v[152:155], v[116:119]\n"
"v_exp_f32 v20, v20\n"
"v_mfma_f32_16x16x32_bf16 v[40:43], a[164:167], v[156:159], v[40:43]\n"
"v_cvt_pk_bf16_f32 v81, v18, v19\n"
"v_lshlrev_b32 v15, 6, v15\n"
"v_mfma_f32_16x16x32_bf16 v[40:43], a[168:171], v[56:59], v[40:43]\n"
"v_exp_f32 v21, v21\n"
"v_mfma_f32_16x16x32_bf16 v[40:43], a[172:175], v[60:63], v[40:43]\n"
"buffer_load_dwordx2 v[10:11], v15, s[40:43], s23 offen sc1\n"
"v_mfma_f32_16x16x32_bf16 v[44:47], a[176:179], v[152:155], v[116:119]\n"
"v_exp_f32 v22, v22\n"
"v_mfma_f32_16x16x32_bf16 v[44:47], a[180:183], v[156:159], v[44:47]\n"
"v_cvt_pk_bf16_f32 v82, v20, v21\n"
"v_mfma_f32_16x16x32_bf16 v[44:47], a[184:187], v[56:59], v[44:47]\n"
"v_exp_f32 v23, v23\n"
"v_mfma_f32_16x16x32_bf16 v[44:47], a[188:191], v[60:63], v[44:47]\n"
"v_mfma_f32_16x16x32_bf16 a[12:15], a[192:195], v[88:91], a[12:15]\n"
"v_exp_f32 v24, v24\n"
"v_mfma_f32_16x16x32_bf16 a[12:15], a[196:199], v[92:95], a[12:15]\n"
"ds_read_b64_tr_b16 a[192:193], v254 offset:16384\n"
"v_cvt_pk_bf16_f32 v83, v22, v23\n"
"v_mfma_f32_16x16x32_bf16 a[28:31], a[200:203], v[88:91], a[28:31]\n"
"v_exp_f32 v25, v25\n"
"v_mfma_f32_16x16x32_bf16 a[28:31], a[204:207], v[92:95], a[28:31]\n"
"ds_read_b64_tr_b16 a[194:195], v254 offset:20480\n"
"ds_read_b64_tr_b16 a[196:197], v254 offset:24576\n"
"v_mfma_f32_16x16x32_bf16 a[44:47], a[208:211], v[88:91], a[44:47]\n"
"v_exp_f32 v26, v26\n"
"v_mfma_f32_16x16x32_bf16 a[44:47], a[212:215], v[92:95], a[44:47]\n"
"ds_read_b64_tr_b16 a[198:199], v254 offset:28672\n"
"v_cvt_pk_bf16_f32 v84, v24, v25\n"
"v_mfma_f32_16x16x32_bf16 a[60:63], a[216:219], v[88:91], a[60:63]\n"
"v_exp_f32 v27, v27\n"
"v_mfma_f32_16x16x32_bf16 a[60:63], a[220:223], v[92:95], a[60:63]\n"
"ds_read_b64_tr_b16 a[200:201], v255 offset:16384\n"
"ds_read_b64_tr_b16 a[202:203], v255 offset:20480\n"
"v_mfma_f32_16x16x32_bf16 a[76:79], a[224:227], v[88:91], a[76:79]\n"
"v_exp_f32 v28, v28\n"
"v_mfma_f32_16x16x32_bf16 a[76:79], a[228:231], v[92:95], a[76:79]\n"
"ds_read_b64_tr_b16 a[204:205], v255 offset:24576\n"
"v_cvt_pk_bf16_f32 v85, v26, v27\n"
"v_smfmac_f32_16x16x64_bf16 v[140:143], v[228:231], v[88:95], v239\n"
"ds_read_b64_tr_b16 a[206:207], v255 offset:28672\n"
"v_mfma_f32_16x16x32_bf16 a[92:95], a[232:235], v[88:91], a[92:95]\n"
"v_exp_f32 v29, v29\n"
"v_mfma_f32_16x16x32_bf16 a[92:95], a[236:239], v[92:95], a[92:95]\n"
"ds_read_b64_tr_b16 a[208:209], v254 offset:16896\n"
"ds_read_b64_tr_b16 a[210:211], v254 offset:20992\n"
"v_mfma_f32_16x16x32_bf16 a[108:111], a[240:243], v[88:91], a[108:111]\n"
"s_mul_i32 s84, s73, 73728\n"
"v_exp_f32 v30, v30\n"
"v_mfma_f32_16x16x32_bf16 a[108:111], a[244:247], v[92:95], a[108:111]\n"
"ds_read_b64_tr_b16 a[212:213], v254 offset:25088\n"
"v_cvt_pk_bf16_f32 v86, v28, v29\n"
"v_mfma_f32_16x16x32_bf16 a[124:127], a[248:251], v[88:91], a[124:127]\n"
"s_add_u32 s84, s84, 45056\n"
"v_exp_f32 v31, v31\n"
"v_mfma_f32_16x16x32_bf16 a[124:127], a[252:255], v[92:95], a[124:127]\n"
"ds_read_b64_tr_b16 a[214:215], v254 offset:29184\n"
"ds_read_b64_tr_b16 a[216:217], v255 offset:16896\n"
"v_mfma_f32_16x16x32_bf16 v[16:19], a[128:131], v[160:163], v[120:123]\n"
"v_exp_f32 v32, v32\n"
"v_mfma_f32_16x16x32_bf16 v[16:19], a[132:135], v[164:167], v[16:19]\n"
"ds_read_b64_tr_b16 a[218:219], v255 offset:20992\n"
"v_cvt_pk_bf16_f32 v87, v30, v31\n"
"v_mfma_f32_16x16x32_bf16 v[16:19], a[136:139], v[64:67], v[16:19]\n"
"v_exp_f32 v33, v33\n"
"v_mfma_f32_16x16x32_bf16 v[16:19], a[140:143], v[68:71], v[16:19]\n"
"ds_read_b64_tr_b16 a[220:221], v255 offset:25088\n"
"ds_read_b64_tr_b16 a[222:223], v255 offset:29184\n"
"v_mfma_f32_16x16x32_bf16 v[20:23], a[144:147], v[160:163], v[120:123]\n"
"v_exp_f32 v34, v34\n"
"v_mfma_f32_16x16x32_bf16 v[20:23], a[148:151], v[164:167], v[20:23]\n"
"ds_read_b64_tr_b16 a[224:225], v254 offset:17408\n"
"v_cvt_pk_bf16_f32 v88, v32, v33\n"
"v_mfma_f32_16x16x32_bf16 v[20:23], a[152:155], v[64:67], v[20:23]\n"
"v_exp_f32 v35, v35\n"
"v_mfma_f32_16x16x32_bf16 v[20:23], a[156:159], v[68:71], v[20:23]\n"
"ds_read_b64_tr_b16 a[226:227], v254 offset:21504\n"
"ds_read_b64_tr_b16 a[228:229], v254 offset:25600\n"
"v_mfma_f32_16x16x32_bf16 v[24:27], a[160:163], v[160:163], v[120:123]\n"
"v_exp_f32 v36, v36\n"
"v_mfma_f32_16x16x32_bf16 v[24:27], a[164:167], v[164:167], v[24:27]\n"
"ds_read_b64_tr_b16 a[230:231], v254 offset:29696\n"
"v_cvt_pk_bf16_f32 v89, v34, v35\n"
"v_mfma_f32_16x16x32_bf16 v[24:27], a[168:171], v[64:67], v[24:27]\n"
"v_exp_f32 v37, v37\n"
"v_mfma_f32_16x16x32_bf16 v[24:27], a[172:175], v[68:71], v[24:27]\n"
"ds_read_b64_tr_b16 a[232:233], v255 offset:17408\n"
"ds_read_b64_tr_b16 a[234:235], v255 offset:21504\n"
"v_smfmac_f32_16x16x64_bf16 v[128:131], v[228:231], v[80:87], v239\n"
"ds_read_b64_tr_b16 a[236:237], v255 offset:25600\n"
"v_mfma_f32_16x16x32_bf16 v[28:31], a[176:179], v[160:163], v[120:123]\n"
"v_exp_f32 v38, v38\n"
"v_mfma_f32_16x16x32_bf16 v[28:31], a[180:183], v[164:167], v[28:31]\n"
"ds_read_b64_tr_b16 a[238:239], v255 offset:29696\n"
"v_cvt_pk_bf16_f32 v90, v36, v37\n"
"v_mfma_f32_16x16x32_bf16 v[28:31], a[184:187], v[64:67], v[28:31]\n"
"s_waitcnt lgkmcnt(15)\n"
"v_exp_f32 v39, v39\n"
"v_mfma_f32_16x16x32_bf16 v[28:31], a[188:191], v[68:71], v[28:31]\n"
"ds_read_b64_tr_b16 a[240:241], v254 offset:17920\n"
"ds_read_b64_tr_b16 a[242:243], v254 offset:22016\n"
"v_mfma_f32_16x16x32_bf16 a[0:3], a[192:195], v[80:83], a[0:3]\n"
"v_exp_f32 v40, v40\n"
"v_mfma_f32_16x16x32_bf16 a[0:3], a[196:199], v[84:87], a[0:3]\n"
"ds_read_b64_tr_b16 a[244:245], v254 offset:26112\n"
"v_cvt_pk_bf16_f32 v91, v38, v39\n"
"v_mfma_f32_16x16x32_bf16 a[16:19], a[200:203], v[80:83], a[16:19]\n"
"s_waitcnt lgkmcnt(11)\n"
"v_exp_f32 v41, v41\n"
"v_mfma_f32_16x16x32_bf16 a[16:19], a[204:207], v[84:87], a[16:19]\n"
"ds_read_b64_tr_b16 a[246:247], v254 offset:30208\n"
"ds_read_b64_tr_b16 a[248:249], v255 offset:17920\n"
"v_mfma_f32_16x16x32_bf16 a[32:35], a[208:211], v[80:83], a[32:35]\n"
"v_exp_f32 v42, v42\n"
"v_mfma_f32_16x16x32_bf16 a[32:35], a[212:215], v[84:87], a[32:35]\n"
"ds_read_b64_tr_b16 a[250:251], v255 offset:22016\n"
"v_cvt_pk_bf16_f32 v92, v40, v41\n"
"v_mfma_f32_16x16x32_bf16 a[48:51], a[216:219], v[80:83], a[48:51]\n"
"s_waitcnt lgkmcnt(6)\n"
"v_exp_f32 v43, v43\n"
"v_mfma_f32_16x16x32_bf16 a[48:51], a[220:223], v[84:87], a[48:51]\n"
"ds_read_b64_tr_b16 a[252:253], v255 offset:26112\n"
"ds_read_b64_tr_b16 a[254:255], v255 offset:30208\n"
"v_mfma_f32_16x16x32_bf16 a[64:67], a[224:227], v[80:83], a[64:67]\n"
"v_exp_f32 v44, v44\n"
"v_mfma_f32_16x16x32_bf16 a[64:67], a[228:231], v[84:87], a[64:67]\n"
"v_cvt_pk_bf16_f32 v93, v42, v43\n"
"v_mfma_f32_16x16x32_bf16 a[80:83], a[232:235], v[80:83], a[80:83]\n"
"s_waitcnt lgkmcnt(4)\n"
"v_exp_f32 v45, v45\n"
"v_mfma_f32_16x16x32_bf16 a[80:83], a[236:239], v[84:87], a[80:83]\n"
"v_mfma_f32_16x16x32_bf16 a[96:99], a[240:243], v[80:83], a[96:99]\n"
"s_waitcnt lgkmcnt(0)\n"
"v_exp_f32 v46, v46\n"
"v_mfma_f32_16x16x32_bf16 a[96:99], a[244:247], v[84:87], a[96:99]\n"
"v_cvt_pk_bf16_f32 v94, v44, v45\n"
"v_mfma_f32_16x16x32_bf16 a[112:115], a[248:251], v[80:83], a[112:115]\n"
"v_exp_f32 v47, v47\n"
"v_mfma_f32_16x16x32_bf16 a[112:115], a[252:255], v[84:87], a[112:115]\n"
"v_mfma_f32_16x16x32_bf16 v[32:35], a[128:131], v[168:171], v[124:127]\n"
"v_exp_f32 v16, v16\n"
"v_mfma_f32_16x16x32_bf16 v[32:35], a[132:135], v[172:175], v[32:35]\n"
"ds_read_b128 a[128:131], v250\n"
"v_cvt_pk_bf16_f32 v95, v46, v47\n"
"v_mfma_f32_16x16x32_bf16 v[32:35], a[136:139], v[72:75], v[32:35]\n"
"v_exp_f32 v17, v17\n"
"v_mfma_f32_16x16x32_bf16 v[32:35], a[140:143], v[76:79], v[32:35]\n"
"ds_read_b128 a[132:135], v251\n"
"ds_read_b128 a[136:139], v252\n"
"v_mfma_f32_16x16x32_bf16 v[36:39], a[144:147], v[168:171], v[124:127]\n"
"v_exp_f32 v18, v18\n"
"v_mfma_f32_16x16x32_bf16 v[36:39], a[148:151], v[172:175], v[36:39]\n"
"ds_read_b128 a[140:143], v253\n"
"v_cvt_pk_bf16_f32 v80, v16, v17\n"
"v_mfma_f32_16x16x32_bf16 v[36:39], a[152:155], v[72:75], v[36:39]\n"
"v_exp_f32 v19, v19\n"
"v_mfma_f32_16x16x32_bf16 v[36:39], a[156:159], v[76:79], v[36:39]\n"
"ds_read_b128 a[144:147], v250 offset:4096\n"
"ds_read_b128 a[148:151], v251 offset:4096\n"
"v_mfma_f32_16x16x32_bf16 v[40:43], a[160:163], v[168:171], v[124:127]\n"
"v_exp_f32 v20, v20\n"
"v_mfma_f32_16x16x32_bf16 v[40:43], a[164:167], v[172:175], v[40:43]\n"
"ds_read_b128 a[152:155], v252 offset:4096\n"
"v_cvt_pk_bf16_f32 v81, v18, v19\n"
"v_mfma_f32_16x16x32_bf16 v[40:43], a[168:171], v[72:75], v[40:43]\n"
"v_exp_f32 v21, v21\n"
"v_mfma_f32_16x16x32_bf16 v[40:43], a[172:175], v[76:79], v[40:43]\n"
"ds_read_b128 a[156:159], v253 offset:4096\n"
"ds_read_b128 a[160:163], v250 offset:8192\n"
"v_mfma_f32_16x16x32_bf16 v[44:47], a[176:179], v[168:171], v[124:127]\n"
"v_exp_f32 v22, v22\n"
"v_mfma_f32_16x16x32_bf16 v[44:47], a[180:183], v[172:175], v[44:47]\n"
"ds_read_b128 a[164:167], v251 offset:8192\n"
"v_cvt_pk_bf16_f32 v82, v20, v21\n"
"v_mfma_f32_16x16x32_bf16 v[44:47], a[184:187], v[72:75], v[44:47]\n"
"v_exp_f32 v23, v23\n"
"v_mfma_f32_16x16x32_bf16 v[44:47], a[188:191], v[76:79], v[44:47]\n"
"ds_read_b128 a[168:171], v252 offset:8192\n"
"ds_read_b128 a[172:175], v253 offset:8192\n"
"v_mfma_f32_16x16x32_bf16 a[4:7], a[192:195], v[88:91], a[4:7]\n"
"v_exp_f32 v24, v24\n"
"v_mfma_f32_16x16x32_bf16 a[4:7], a[196:199], v[92:95], a[4:7]\n"
"ds_read_b128 a[176:179], v250 offset:12288\n"
"v_cvt_pk_bf16_f32 v83, v22, v23\n"
"v_mfma_f32_16x16x32_bf16 a[20:23], a[200:203], v[88:91], a[20:23]\n"
"v_exp_f32 v25, v25\n"
"v_mfma_f32_16x16x32_bf16 a[20:23], a[204:207], v[92:95], a[20:23]\n"
"ds_read_b128 a[180:183], v251 offset:12288\n"
"ds_read_b128 a[184:187], v252 offset:12288\n"
"v_mfma_f32_16x16x32_bf16 a[36:39], a[208:211], v[88:91], a[36:39]\n"
"v_exp_f32 v26, v26\n"
"v_mfma_f32_16x16x32_bf16 a[36:39], a[212:215], v[92:95], a[36:39]\n"
"ds_read_b128 a[188:191], v253 offset:12288\n"
"v_cvt_pk_bf16_f32 v84, v24, v25\n"
"v_mfma_f32_16x16x32_bf16 a[52:55], a[216:219], v[88:91], a[52:55]\n"
"v_exp_f32 v27, v27\n"
"v_mfma_f32_16x16x32_bf16 a[52:55], a[220:223], v[92:95], a[52:55]\n"
"v_mfma_f32_16x16x32_bf16 a[68:71], a[224:227], v[88:91], a[68:71]\n"
"v_exp_f32 v28, v28\n"
"v_mfma_f32_16x16x32_bf16 a[68:71], a[228:231], v[92:95], a[68:71]\n"
"s_waitcnt vmcnt(0)\n"
"v_cvt_pk_bf16_f32 v85, v26, v27\n"
"v_cmp_ne_u64 vcc, s[94:95], v[10:11]\n"
"v_mfma_f32_16x16x32_bf16 a[84:87], a[232:235], v[88:91], a[84:87]\n"
"s_cbranch_vccnz L_slow_8_%=\n"
"L_back_9_%=:\n"
"v_exp_f32 v29, v29\n"
"v_mfma_f32_16x16x32_bf16 a[84:87], a[236:239], v[92:95], a[84:87]\n"
"v_smfmac_f32_16x16x64_bf16 v[132:135], v[228:231], v[88:95], v239\n"
"v_mfma_f32_16x16x32_bf16 a[100:103], a[240:243], v[88:91], a[100:103]\n"
"s_add_u32 s82, s58, 0\n"
"v_exp_f32 v30, v30\n"
"v_mfma_f32_16x16x32_bf16 a[100:103], a[244:247], v[92:95], a[100:103]\n"
"s_add_u32 m0, s66, 98304\n"
"v_cvt_pk_bf16_f32 v86, v28, v29\n"
"v_mfma_f32_16x16x32_bf16 a[116:119], a[248:251], v[88:91], a[116:119]\n"
"s_waitcnt lgkmcnt(0)\n"
"v_exp_f32 v31, v31\n"
"v_mfma_f32_16x16x32_bf16 a[116:119], a[252:255], v[92:95], a[116:119]\n"
"buffer_load_dwordx4 v1, s[40:43], s82 offen sc1 lds\n"
"v_mfma_f32_16x16x32_bf16 v[16:19], a[128:131], v[144:147], v[112:115]\n"
"v_exp_f32 v32, v32\n"
"v_mfma_f32_16x16x32_bf16 v[16:19], a[132:135], v[148:151], v[16:19]\n"
"v_cvt_pk_bf16_f32 v87, v30, v31\n"
"v_mfma_f32_16x16x32_bf16 v[16:19], a[136:139], v[48:51], v[16:19]\n"
"v_exp_f32 v33, v33\n"
"v_mfma_f32_16x16x32_bf16 v[16:19], a[140:143], v[52:55], v[16:19]\n"
"buffer_load_dwordx4 v1, s[40:43], s82 offen offset:1024 sc1 lds\n"
"v_mfma_f32_16x16x32_bf16 v[20:23], a[144:147], v[144:147], v[112:115]\n"
"v_exp_f32 v34, v34\n"
"v_mfma_f32_16x16x32_bf16 v[20:23], a[148:151], v[148:151], v[20:23]\n"
"v_cvt_pk_bf16_f32 v88, v32, v33\n"
"v_mfma_f32_16x16x32_bf16 v[20:23], a[152:155], v[48:51], v[20:23]\n"
"v_exp_f32 v35, v35\n"
"v_mfma_f32_16x16x32_bf16 v[20:23], a[156:159], v[52:55], v[20:23]\n"
"buffer_load_dwordx4 v1, s[40:43], s82 offen offset:2048 sc1 lds\n"
"v_mfma_f32_16x16x32_bf16 v[24:27], a[160:163], v[144:147], v[112:115]\n"
"v_exp_f32 v36, v36\n"
"v_mfma_f32_16x16x32_bf16 v[24:27], a[164:167], v[148:151], v[24:27]\n"
"v_cvt_pk_bf16_f32 v89, v34, v35\n"
"v_mfma_f32_16x16x32_bf16 v[24:27], a[168:171], v[48:51], v[24:27]\n"
"v_exp_f32 v37, v37\n"
"v_mfma_f32_16x16x32_bf16 v[24:27], a[172:175], v[52:55], v[24:27]\n"
"buffer_load_dwordx4 v1, s[40:43], s82 offen offset:3072 sc1 lds\n"
"v_mfma_f32_16x16x32_bf16 v[28:31], a[176:179], v[144:147], v[112:115]\n"
"s_add_u32 s82, s58, 16384\n"
"v_exp_f32 v38, v38\n"
"v_mfma_f32_16x16x32_bf16 v[28:31], a[180:183], v[148:151], v[28:31]\n"
"s_add_u32 m0, s66, 114688\n"
"v_cvt_pk_bf16_f32 v90, v36, v37\n"
"v_mfma_f32_16x16x32_bf16 v[28:31], a[184:187], v[48:51], v[28:31]\n"
"v_exp_f32 v39, v39\n"
"v_mfma_f32_16x16x32_bf16 v[28:31], a[188:191], v[52:55], v[28:31]\n"
"buffer_load_dwordx4 v1, s[40:43], s82 offen sc1 lds\n"
"v_mfma_f32_16x16x32_bf16 a[8:11], a[192:195], v[80:83], a[8:11]\n"
"v_exp_f32 v40, v40\n"
"v_mfma_f32_16x16x32_bf16 a[8:11], a[196:199], v[84:87], a[8:11]\n"
"v_cvt_pk_bf16_f32 v91, v38, v39\n"
"v_mfma_f32_16x16x32_bf16 a[24:27], a[200:203], v[80:83], a[24:27]\n"
"v_exp_f32 v41, v41\n"
"v_mfma_f32_16x16x32_bf16 a[24:27], a[204:207], v[84:87], a[24:27]\n"
"buffer_load_dwordx4 v1, s[40:43], s82 offen offset:1024 sc1 lds\n"
"v_mfma_f32_16x16x32_bf16 a[40:43], a[208:211], v[80:83], a[40:43]\n"
"v_exp_f32 v42, v42\n"
"v_mfma_f32_16x16x32_bf16 a[40:43], a[212:215], v[84:87], a[40:43]\n"
"v_cvt_pk_bf16_f32 v92, v40, v41\n"
"v_mfma_f32_16x16x32_bf16 a[56:59], a[216:219], v[80:83], a[56:59]\n"
"v_exp_f32 v43, v43\n"
"v_mfma_f32_16x16x32_bf16 a[56:59], a[220:223], v[84:87], a[56:59]\n"
"buffer_load_dwordx4 v1, s[40:43], s82 offen offset:2048 sc1 lds\n"
"v_mfma_f32_16x16x32_bf16 a[72:75], a[224:227], v[80:83], a[72:75]\n"
"v_exp_f32 v44, v44\n"
"v_mfma_f32_16x16x32_bf16 a[72:75], a[228:231], v[84:87], a[72:75]\n"
"v_cvt_pk_bf16_f32 v93, v42, v43\n"
"v_mfma_f32_16x16x32_bf16 a[88:91], a[232:235], v[80:83], a[88:91]\n"
"v_exp_f32 v45, v45\n"
"v_mfma_f32_16x16x32_bf16 a[88:91], a[236:239], v[84:87], a[88:91]\n"
"buffer_load_dwordx4 v1, s[40:43], s82 offen offset:3072 sc1 lds\n"
"v_smfmac_f32_16x16x64_bf16 v[136:139], v[228:231], v[80:87], v239\n"
"v_mfma_f32_16x16x32_bf16 a[104:107], a[240:243], v[80:83], a[104:107]\n"
"v_exp_f32 v46, v46\n"
"v_mfma_f32_16x16x32_bf16 a[104:107], a[244:247], v[84:87], a[104:107]\n"
"v_cvt_pk_bf16_f32 v94, v44, v45\n"
"v_mfma_f32_16x16x32_bf16 a[120:123], a[248:251], v[80:83], a[120:123]\n"
"v_exp_f32 v47, v47\n"
"v_mfma_f32_16x16x32_bf16 a[120:123], a[252:255], v[84:87], a[120:123]\n"
"v_mfma_f32_16x16x32_bf16 v[32:35], a[128:131], v[152:155], v[116:119]\n"
"v_exp_f32 v16, v16\n"
"v_mfma_f32_16x16x32_bf16 v[32:35], a[132:135], v[156:159], v[32:35]\n"
"v_cvt_pk_bf16_f32 v95, v46, v47\n"
"v_mfma_f32_16x16x32_bf16 v[32:35], a[136:139], v[56:59], v[32:35]\n"
"s_waitcnt vmcnt(15) lgkmcnt(0)\n"
"v_exp_f32 v17, v17\n"
"v_mfma_f32_16x16x32_bf16 v[32:35], a[140:143], v[60:63], v[32:35]\n"
"s_barrier\n"
"v_mfma_f32_16x16x32_bf16 v[36:39], a[144:147], v[152:155], v[116:119]\n"
"s_add_u32 s82, s58, 32768\n"
"v_exp_f32 v18, v18\n"
"v_mfma_f32_16x16x32_bf16 v[36:39], a[148:151], v[156:159], v[36:39]\n"
"s_add_u32 m0, s66, 131072\n"
"v_cvt_pk_bf16_f32 v80, v16, v17\n"
"v_mfma_f32_16x16x32_bf16 v[36:39], a[152:155], v[56:59], v[36:39]\n"
"v_exp_f32 v19, v19\n"
"v_mfma_f32_16x16x32_bf16 v[36:39], a[156:159], v[60:63], v[36:39]\n"
"buffer_load_dwordx4 v1, s[40:43], s82 offen sc1 lds\n"
"v_mfma_f32_16x16x32_bf16 v[40:43], a[160:163], v[152:155], v[116:119]\n"
"v_exp_f32 v20, v20\n"
"v_mfma_f32_16x16x32_bf16 v[40:43], a[164:167], v[156:159], v[40:43]\n"
"v_cvt_pk_bf16_f32 v81, v18, v19\n"
"v_mfma_f32_16x16x32_bf16 v[40:43], a[168:171], v[56:59], v[40:43]\n"
"v_exp_f32 v21, v21\n"
"v_mfma_f32_16x16x32_bf16 v[40:43], a[172:175], v[60:63], v[40:43]\n"
"buffer_load_dwordx4 v1, s[40:43], s82 offen offset:1024 sc1 lds\n"
"v_mfma_f32_16x16x32_bf16 v[44:47], a[176:179], v[152:155], v[116:119]\n"
"v_exp_f32 v22, v22\n"
"v_mfma_f32_16x16x32_bf16 v[44:47], a[180:183], v[156:159], v[44:47]\n"
"v_cvt_pk_bf16_f32 v82, v20, v21\n"
"v_mfma_f32_16x16x32_bf16 v[44:47], a[184:187], v[56:59], v[44:47]\n"
"v_exp_f32 v23, v23\n"
"v_mfma_f32_16x16x32_bf16 v[44:47], a[188:191], v[60:63], v[44:47]\n"
"buffer_load_dwordx4 v1, s[40:43], s82 offen offset:2048 sc1 lds\n"
"v_mfma_f32_16x16x32_bf16 a[12:15], a[192:195], v[88:91], a[12:15]\n"
"v_exp_f32 v24, v24\n"
"v_mfma_f32_16x16x32_bf16 a[12:15], a[196:199], v[92:95], a[12:15]\n"
"ds_read_b64_tr_b16 a[192:193], v254 offset:49152\n"
"v_cvt_pk_bf16_f32 v83, v22, v23\n"
"v_mfma_f32_16x16x32_bf16 a[28:31], a[200:203], v[88:91], a[28:31]\n"
"v_exp_f32 v25, v25\n"
"v_mfma_f32_16x16x32_bf16 a[28:31], a[204:207], v[92:95], a[28:31]\n"
"ds_read_b64_tr_b16 a[194:195], v254 offset:53248\n"
"ds_read_b64_tr_b16 a[196:197], v254 offset:57344\n"
"v_mfma_f32_16x16x32_bf16 a[44:47], a[208:211], v[88:91], a[44:47]\n"
"v_exp_f32 v26, v26\n"
"v_mfma_f32_16x16x32_bf16 a[44:47], a[212:215], v[92:95], a[44:47]\n"
"ds_read_b64_tr_b16 a[198:199], v254 offset:61440\n"
"v_cvt_pk_bf16_f32 v84, v24, v25\n"
"v_mfma_f32_16x16x32_bf16 a[60:63], a[216:219], v[88:91], a[60:63]\n"
"v_exp_f32 v27, v27\n"
"v_mfma_f32_16x16x32_bf16 a[60:63], a[220:223], v[92:95], a[60:63]\n"
"ds_read_b64_tr_b16 a[200:201], v255 offset:49152\n"
"ds_read_b64_tr_b16 a[202:203], v255 offset:53248\n"
"v_mfma_f32_16x16x32_bf16 a[76:79], a[224:227], v[88:91], a[76:79]\n"
"v_exp_f32 v28, v28\n"
"v_mfma_f32_16x16x32_bf16 a[76:79], a[228:231], v[92:95], a[76:79]\n"
"ds_read_b64_tr_b16 a[204:205], v255 offset:57344\n"
"v_cvt_pk_bf16_f32 v85, v26, v27\n"
"v_smfmac_f32_16x16x64_bf16 v[140:143], v[228:231], v[88:95], v239\n"
"ds_read_b64_tr_b16 a[206:207], v255 offset:61440\n"
"v_mfma_f32_16x16x32_bf16 a[92:95], a[232:235], v[88:91], a[92:95]\n"
"v_exp_f32 v29, v29\n"
"v_mfma_f32_16x16x32_bf16 a[92:95], a[236:239], v[92:95], a[92:95]\n"
"ds_read_b64_tr_b16 a[208:209], v254 offset:49664\n"
"ds_read_b64_tr_b16 a[210:211], v254 offset:53760\n"
"v_mfma_f32_16x16x32_bf16 a[108:111], a[240:243], v[88:91], a[108:111]\n"
"v_exp_f32 v30, v30\n"
"v_mfma_f32_16x16x32_bf16 a[108:111], a[244:247], v[92:95], a[108:111]\n"
"ds_read_b64_tr_b16 a[212:213], v254 offset:57856\n"
"v_cvt_pk_bf16_f32 v86, v28, v29\n"
"v_mfma_f32_16x16x32_bf16 a[124:127], a[248:251], v[88:91], a[124:127]\n"
"v_exp_f32 v31, v31\n"
"v_mfma_f32_16x16x32_bf16 a[124:127], a[252:255], v[92:95], a[124:127]\n"
"ds_read_b64_tr_b16 a[214:215], v254 offset:61952\n"
"ds_read_b64_tr_b16 a[216:217], v255 offset:49664\n"
"v_mfma_f32_16x16x32_bf16 v[16:19], a[128:131], v[160:163], v[120:123]\n"
"v_exp_f32 v32, v32\n"
"v_mfma_f32_16x16x32_bf16 v[16:19], a[132:135], v[164:167], v[16:19]\n"
"ds_read_b64_tr_b16 a[218:219], v255 offset:53760\n"
"v_cvt_pk_bf16_f32 v87, v30, v31\n"
"v_mfma_f32_16x16x32_bf16 v[16:19], a[136:139], v[64:67], v[16:19]\n"
"v_exp_f32 v33, v33\n"
"v_mfma_f32_16x16x32_bf16 v[16:19], a[140:143], v[68:71], v[16:19]\n"
"ds_read_b64_tr_b16 a[220:221], v255 offset:57856\n"
"ds_read_b64_tr_b16 a[222:223], v255 offset:61952\n"
"v_mfma_f32_16x16x32_bf16 v[20:23], a[144:147], v[160:163], v[120:123]\n"
"v_exp_f32 v34, v34\n"
"v_mfma_f32_16x16x32_bf16 v[20:23], a[148:151], v[164:167], v[20:23]\n"
"ds_read_b64_tr_b16 a[224:225], v254 offset:50176\n"
"v_cvt_pk_bf16_f32 v88, v32, v33\n"
"v_mfma_f32_16x16x32_bf16 v[20:23], a[152:155], v[64:67], v[20:23]\n"
"v_exp_f32 v35, v35\n"
"v_mfma_f32_16x16x32_bf16 v[20:23], a[156:159], v[68:71], v[20:23]\n"
"ds_read_b64_tr_b16 a[226:227], v254 offset:54272\n"
"ds_read_b64_tr_b16 a[228:229], v254 offset:58368\n"
"v_mfma_f32_16x16x32_bf16 v[24:27], a[160:163], v[160:163], v[120:123]\n"
"v_exp_f32 v36, v36\n"
"v_mfma_f32_16x16x32_bf16 v[24:27], a[164:167], v[164:167], v[24:27]\n"
"ds_read_b64_tr_b16 a[230:231], v254 offset:62464\n"
"v_cvt_pk_bf16_f32 v89, v34, v35\n"
"v_mfma_f32_16x16x32_bf16 v[24:27], a[168:171], v[64:67], v[24:27]\n"
"v_exp_f32 v37, v37\n"
"v_mfma_f32_16x16x32_bf16 v[24:27], a[172:175], v[68:71], v[24:27]\n"
"ds_read_b64_tr_b16 a[232:233], v255 offset:50176\n"
"ds_read_b64_tr_b16 a[234:235], v255 offset:54272\n"
"v_smfmac_f32_16x16x64_bf16 v[128:131], v[228:231], v[80:87], v239\n"
"ds_read_b64_tr_b16 a[236:237], v255 offset:58368\n"
"v_mfma_f32_16x16x32_bf16 v[28:31], a[176:179], v[160:163], v[120:123]\n"
"v_exp_f32 v38, v38\n"
"v_mfma_f32_16x16x32_bf16 v[28:31], a[180:183], v[164:167], v[28:31]\n"
"ds_read_b64_tr_b16 a[238:239], v255 offset:62464\n"
"v_cvt_pk_bf16_f32 v90, v36, v37\n"
"v_mfma_f32_16x16x32_bf16 v[28:31], a[184:187], v[64:67], v[28:31]\n"
"s_waitcnt lgkmcnt(15)\n"
"v_exp_f32 v39, v39\n"
"v_mfma_f32_16x16x32_bf16 v[28:31], a[188:191], v[68:71], v[28:31]\n"
"ds_read_b64_tr_b16 a[240:241], v254 offset:50688\n"
"ds_read_b64_tr_b16 a[242:243], v254 offset:54784\n"
"v_mfma_f32_16x16x32_bf16 a[0:3], a[192:195], v[80:83], a[0:3]\n"
"v_exp_f32 v40, v40\n"
"v_mfma_f32_16x16x32_bf16 a[0:3], a[196:199], v[84:87], a[0:3]\n"
"ds_read_b64_tr_b16 a[244:245], v254 offset:58880\n"
"v_cvt_pk_bf16_f32 v91, v38, v39\n"
"v_mfma_f32_16x16x32_bf16 a[16:19], a[200:203], v[80:83], a[16:19]\n"
"s_waitcnt lgkmcnt(11)\n"
"v_exp_f32 v41, v41\n"
"v_mfma_f32_16x16x32_bf16 a[16:19], a[204:207], v[84:87], a[16:19]\n"
"ds_read_b64_tr_b16 a[246:247], v254 offset:62976\n"
"ds_read_b64_tr_b16 a[248:249], v255 offset:50688\n"
"v_mfma_f32_16x16x32_bf16 a[32:35], a[208:211], v[80:83], a[32:35]\n"
"v_exp_f32 v42, v42\n"
"v_mfma_f32_16x16x32_bf16 a[32:35], a[212:215], v[84:87], a[32:35]\n"
"ds_read_b64_tr_b16 a[250:251], v255 offset:54784\n"
"v_cvt_pk_bf16_f32 v92, v40, v41\n"
"v_mfma_f32_16x16x32_bf16 a[48:51], a[216:219], v[80:83], a[48:51]\n"
"s_waitcnt lgkmcnt(6)\n"
"v_exp_f32 v43, v43\n"
"v_mfma_f32_16x16x32_bf16 a[48:51], a[220:223], v[84:87], a[48:51]\n"
"ds_read_b64_tr_b16 a[252:253], v255 offset:58880\n"
"ds_read_b64_tr_b16 a[254:255], v255 offset:62976\n"
"v_mfma_f32_16x16x32_bf16 a[64:67], a[224:227], v[80:83], a[64:67]\n"
"v_exp_f32 v44, v44\n"
"v_mfma_f32_16x16x32_bf16 a[64:67], a[228:231], v[84:87], a[64:67]\n"
"v_cvt_pk_bf16_f32 v93, v42, v43\n"
"v_mfma_f32_16x16x32_bf16 a[80:83], a[232:235], v[80:83], a[80:83]\n"
"s_waitcnt lgkmcnt(4)\n"
"v_exp_f32 v45, v45\n"
"v_mfma_f32_16x16x32_bf16 a[80:83], a[236:239], v[84:87], a[80:83]\n"
"buffer_load_dwordx4 v1, s[40:43], s82 offen offset:3072 sc1 lds\n"
"v_mfma_f32_16x16x32_bf16 a[96:99], a[240:243], v[80:83], a[96:99]\n"
"s_waitcnt lgkmcnt(0)\n"
"v_exp_f32 v46, v46\n"
"v_mfma_f32_16x16x32_bf16 a[96:99], a[244:247], v[84:87], a[96:99]\n"
"s_add_u32 s82, s58, 49152\n"
"s_add_u32 m0, s66, 147456\n"
"v_cvt_pk_bf16_f32 v94, v44, v45\n"
"v_mfma_f32_16x16x32_bf16 a[112:115], a[248:251], v[80:83], a[112:115]\n"
"v_exp_f32 v47, v47\n"
"v_mfma_f32_16x16x32_bf16 a[112:115], a[252:255], v[84:87], a[112:115]\n"
"buffer_load_dwordx4 v1, s[40:43], s82 offen sc1 lds\n"
"v_mfma_f32_16x16x32_bf16 v[32:35], a[128:131], v[168:171], v[124:127]\n"
"v_exp_f32 v16, v16\n"
"v_mfma_f32_16x16x32_bf16 v[32:35], a[132:135], v[172:175], v[32:35]\n"
"ds_read_b128 a[128:131], v250 offset:32768\n"
"v_cvt_pk_bf16_f32 v95, v46, v47\n"
"v_mfma_f32_16x16x32_bf16 v[32:35], a[136:139], v[72:75], v[32:35]\n"
"v_exp_f32 v17, v17\n"
"v_mfma_f32_16x16x32_bf16 v[32:35], a[140:143], v[76:79], v[32:35]\n"
"ds_read_b128 a[132:135], v251 offset:32768\n"
"ds_read_b128 a[136:139], v252 offset:32768\n"
"v_mfma_f32_16x16x32_bf16 v[36:39], a[144:147], v[168:171], v[124:127]\n"
"v_exp_f32 v18, v18\n"
"v_mfma_f32_16x16x32_bf16 v[36:39], a[148:151], v[172:175], v[36:39]\n"
"ds_read_b128 a[140:143], v253 offset:32768\n"
"v_cvt_pk_bf16_f32 v80, v16, v17\n"
"v_mfma_f32_16x16x32_bf16 v[36:39], a[152:155], v[72:75], v[36:39]\n"
"v_exp_f32 v19, v19\n"
"v_mfma_f32_16x16x32_bf16 v[36:39], a[156:159], v[76:79], v[36:39]\n"
"ds_read_b128 a[144:147], v250 offset:36864\n"
"ds_read_b128 a[148:151], v251 offset:36864\n"
"v_mfma_f32_16x16x32_bf16 v[40:43], a[160:163], v[168:171], v[124:127]\n"
"v_exp_f32 v20, v20\n"
"v_mfma_f32_16x16x32_bf16 v[40:43], a[164:167], v[172:175], v[40:43]\n"
"ds_read_b128 a[152:155], v252 offset:36864\n"
"v_cvt_pk_bf16_f32 v81, v18, v19\n"
"v_mfma_f32_16x16x32_bf16 v[40:43], a[168:171], v[72:75], v[40:43]\n"
"v_exp_f32 v21, v21\n"
"v_mfma_f32_16x16x32_bf16 v[40:43], a[172:175], v[76:79], v[40:43]\n"
"ds_read_b128 a[156:159], v253 offset:36864\n"
"ds_read_b128 a[160:163], v250 offset:40960\n"
"v_mfma_f32_16x16x32_bf16 v[44:47], a[176:179], v[168:171], v[124:127]\n"
"v_exp_f32 v22, v22\n"
"v_mfma_f32_16x16x32_bf16 v[44:47], a[180:183], v[172:175], v[44:47]\n"
"ds_read_b128 a[164:167], v251 offset:40960\n"
"v_cvt_pk_bf16_f32 v82, v20, v21\n"
"v_mfma_f32_16x16x32_bf16 v[44:47], a[184:187], v[72:75], v[44:47]\n"
"v_exp_f32 v23, v23\n"
"v_mfma_f32_16x16x32_bf16 v[44:47], a[188:191], v[76:79], v[44:47]\n"
"ds_read_b128 a[168:171], v252 offset:40960\n"
"ds_read_b128 a[172:175], v253 offset:40960\n"
"v_mfma_f32_16x16x32_bf16 a[4:7], a[192:195], v[88:91], a[4:7]\n"
"v_exp_f32 v24, v24\n"
"v_mfma_f32_16x16x32_bf16 a[4:7], a[196:199], v[92:95], a[4:7]\n"
"ds_read_b128 a[176:179], v250 offset:45056\n"
"v_cvt_pk_bf16_f32 v83, v22, v23\n"
"v_mfma_f32_16x16x32_bf16 a[20:23], a[200:203], v[88:91], a[20:23]\n"
"v_exp_f32 v25, v25\n"
"v_mfma_f32_16x16x32_bf16 a[20:23], a[204:207], v[92:95], a[20:23]\n"
"ds_read_b128 a[180:183], v251 offset:45056\n"
"ds_read_b128 a[184:187], v252 offset:45056\n"
"v_mfma_f32_16x16x32_bf16 a[36:39], a[208:211], v[88:91], a[36:39]\n"
"v_exp_f32 v26, v26\n"
"v_mfma_f32_16x16x32_bf16 a[36:39], a[212:215], v[92:95], a[36:39]\n"
"ds_read_b128 a[188:191], v253 offset:45056\n"
"v_cvt_pk_bf16_f32 v84, v24, v25\n"
"v_mfma_f32_16x16x32_bf16 a[52:55], a[216:219], v[88:91], a[52:55]\n"
"v_exp_f32 v27, v27\n"
"v_mfma_f32_16x16x32_bf16 a[52:55], a[220:223], v[92:95], a[52:55]\n"
"buffer_load_dwordx4 v1, s[40:43], s82 offen offset:1024 sc1 lds\n"
"v_mfma_f32_16x16x32_bf16 a[68:71], a[224:227], v[88:91], a[68:71]\n"
"v_exp_f32 v28, v28\n"
"v_mfma_f32_16x16x32_bf16 a[68:71], a[228:231], v[92:95], a[68:71]\n"
"v_cvt_pk_bf16_f32 v85, v26, v27\n"
"v_mfma_f32_16x16x32_bf16 a[84:87], a[232:235], v[88:91], a[84:87]\n"
"v_exp_f32 v29, v29\n"
"v_mfma_f32_16x16x32_bf16 a[84:87], a[236:239], v[92:95], a[84:87]\n"
"buffer_load_dwordx4 v1, s[40:43], s82 offen offset:2048 sc1 lds\n"
"v_smfmac_f32_16x16x64_bf16 v[132:135], v[228:231], v[88:95], v239\n"
"v_mfma_f32_16x16x32_bf16 a[100:103], a[240:243], v[88:91], a[100:103]\n"
"v_exp_f32 v30, v30\n"
"v_mfma_f32_16x16x32_bf16 a[100:103], a[244:247], v[92:95], a[100:103]\n"
"v_cvt_pk_bf16_f32 v86, v28, v29\n"
"v_mfma_f32_16x16x32_bf16 a[116:119], a[248:251], v[88:91], a[116:119]\n"
"s_waitcnt lgkmcnt(0)\n"
"v_exp_f32 v31, v31\n"
"v_mfma_f32_16x16x32_bf16 a[116:119], a[252:255], v[92:95], a[116:119]\n"
"buffer_load_dwordx4 v1, s[40:43], s82 offen offset:3072 sc1 lds\n"
"v_mfma_f32_16x16x32_bf16 v[16:19], a[128:131], v[144:147], v[112:115]\n"
"s_add_u32 s82, s58, 65536\n"
"v_exp_f32 v32, v32\n"
"v_mfma_f32_16x16x32_bf16 v[16:19], a[132:135], v[148:151], v[16:19]\n"
"s_add_u32 m0, s66, 0\n"
"v_cvt_pk_bf16_f32 v87, v30, v31\n"
"v_xor_b32 v254, 0x10000, v254\n"
"v_mfma_f32_16x16x32_bf16 v[16:19], a[136:139], v[48:51], v[16:19]\n"
"v_exp_f32 v33, v33\n"
"v_mfma_f32_16x16x32_bf16 v[16:19], a[140:143], v[52:55], v[16:19]\n"
"buffer_load_dwordx4 v1, s[40:43], s82 offen sc1 lds\n"
"v_mfma_f32_16x16x32_bf16 v[20:23], a[144:147], v[144:147], v[112:115]\n"
"v_exp_f32 v34, v34\n"
"v_mfma_f32_16x16x32_bf16 v[20:23], a[148:151], v[148:151], v[20:23]\n"
"v_cvt_pk_bf16_f32 v88, v32, v33\n"
"v_xor_b32 v255, 0x10000, v255\n"
"v_mfma_f32_16x16x32_bf16 v[20:23], a[152:155], v[48:51], v[20:23]\n"
"v_exp_f32 v35, v35\n"
"v_mfma_f32_16x16x32_bf16 v[20:23], a[156:159], v[52:55], v[20:23]\n"
"buffer_load_dwordx4 v1, s[40:43], s82 offen offset:1024 sc1 lds\n"
"v_mfma_f32_16x16x32_bf16 v[24:27], a[160:163], v[144:147], v[112:115]\n"
"v_exp_f32 v36, v36\n"
"v_mfma_f32_16x16x32_bf16 v[24:27], a[164:167], v[148:151], v[24:27]\n"
"v_cvt_pk_bf16_f32 v89, v34, v35\n"
"v_xor_b32 v250, 0x10000, v250\n"
"v_mfma_f32_16x16x32_bf16 v[24:27], a[168:171], v[48:51], v[24:27]\n"
"v_exp_f32 v37, v37\n"
"v_mfma_f32_16x16x32_bf16 v[24:27], a[172:175], v[52:55], v[24:27]\n"
"buffer_load_dwordx4 v1, s[40:43], s82 offen offset:2048 sc1 lds\n"
"v_mfma_f32_16x16x32_bf16 v[28:31], a[176:179], v[144:147], v[112:115]\n"
"v_exp_f32 v38, v38\n"
"v_mfma_f32_16x16x32_bf16 v[28:31], a[180:183], v[148:151], v[28:31]\n"
"v_cvt_pk_bf16_f32 v90, v36, v37\n"
"v_mfma_f32_16x16x32_bf16 v[28:31], a[184:187], v[48:51], v[28:31]\n"
"v_exp_f32 v39, v39\n"
"v_mfma_f32_16x16x32_bf16 v[28:31], a[188:191], v[52:55], v[28:31]\n"
"buffer_load_dwordx4 v1, s[40:43], s82 offen offset:3072 sc1 lds\n"
"v_mfma_f32_16x16x32_bf16 a[8:11], a[192:195], v[80:83], a[8:11]\n"
"s_add_u32 s82, s58, 81920\n"
"v_exp_f32 v40, v40\n"
"v_mfma_f32_16x16x32_bf16 a[8:11], a[196:199], v[84:87], a[8:11]\n"
"s_add_u32 m0, s66, 16384\n"
"v_cvt_pk_bf16_f32 v91, v38, v39\n"
"v_xor_b32 v251, 0x10000, v251\n"
"v_mfma_f32_16x16x32_bf16 a[24:27], a[200:203], v[80:83], a[24:27]\n"
"v_exp_f32 v41, v41\n"
"v_mfma_f32_16x16x32_bf16 a[24:27], a[204:207], v[84:87], a[24:27]\n"
"buffer_load_dwordx4 v1, s[40:43], s82 offen sc1 lds\n"
"v_mfma_f32_16x16x32_bf16 a[40:43], a[208:211], v[80:83], a[40:43]\n"
"v_exp_f32 v42, v42\n"
"v_mfma_f32_16x16x32_bf16 a[40:43], a[212:215], v[84:87], a[40:43]\n"
"v_cvt_pk_bf16_f32 v92, v40, v41\n"
"v_xor_b32 v252, 0x10000, v252\n"
"v_mfma_f32_16x16x32_bf16 a[56:59], a[216:219], v[80:83], a[56:59]\n"
"v_exp_f32 v43, v43\n"
"v_mfma_f32_16x16x32_bf16 a[56:59], a[220:223], v[84:87], a[56:59]\n"
"buffer_load_dwordx4 v1, s[40:43], s82 offen offset:1024 sc1 lds\n"
"v_mfma_f32_16x16x32_bf16 a[72:75], a[224:227], v[80:83], a[72:75]\n"
"v_exp_f32 v44, v44\n"
"v_mfma_f32_16x16x32_bf16 a[72:75], a[228:231], v[84:87], a[72:75]\n"
"v_cvt_pk_bf16_f32 v93, v42, v43\n"
"v_xor_b32 v253, 0x10000, v253\n"
"v_mfma_f32_16x16x32_bf16 a[88:91], a[232:235], v[80:83], a[88:91]\n"
"v_exp_f32 v45, v45\n"
"v_mfma_f32_16x16x32_bf16 a[88:91], a[236:239], v[84:87], a[88:91]\n"
"buffer_load_dwordx4 v1, s[40:43], s82 offen offset:2048 sc1 lds\n"
"v_smfmac_f32_16x16x64_bf16 v[136:139], v[228:231], v[80:87], v239\n"
"v_mfma_f32_16x16x32_bf16 a[104:107], a[240:243], v[80:83], a[104:107]\n"
"v_exp_f32 v46, v46\n"
"v_mfma_f32_16x16x32_bf16 a[104:107], a[244:247], v[84:87], a[104:107]\n"
"v_cvt_pk_bf16_f32 v94, v44, v45\n"
"v_mfma_f32_16x16x32_bf16 a[120:123], a[248:251], v[80:83], a[120:123]\n"
"v_exp_f32 v47, v47\n"
"v_mfma_f32_16x16x32_bf16 a[120:123], a[252:255], v[84:87], a[120:123]\n"
"buffer_load_dwordx4 v1, s[40:43], s82 offen offset:3072 sc1 lds\n"
"v_mfma_f32_16x16x32_bf16 v[32:35], a[128:131], v[152:155], v[116:119]\n"
"v_exp_f32 v16, v16\n"
"v_mfma_f32_16x16x32_bf16 v[32:35], a[132:135], v[156:159], v[32:35]\n"
"v_cvt_pk_bf16_f32 v95, v46, v47\n"
"v_mov_b32 v15, 65536\n"
"v_mfma_f32_16x16x32_bf16 v[32:35], a[136:139], v[56:59], v[32:35]\n"
"s_waitcnt vmcnt(16) lgkmcnt(0)\n"
"v_exp_f32 v17, v17\n"
"v_mfma_f32_16x16x32_bf16 v[32:35], a[140:143], v[60:63], v[32:35]\n"
"s_barrier\n"
"v_mfma_f32_16x16x32_bf16 v[36:39], a[144:147], v[152:155], v[116:119]\n"
"s_add_u32 s82, s58, 98304\n"
"v_exp_f32 v18, v18\n"
"v_mfma_f32_16x16x32_bf16 v[36:39], a[148:151], v[156:159], v[36:39]\n"
"s_add_u32 m0, s66, 32768\n"
"v_cvt_pk_bf16_f32 v80, v16, v17\n"
"v_mov_b32 v11, s72\n"
"v_mfma_f32_16x16x32_bf16 v[36:39], a[152:155], v[56:59], v[36:39]\n"
"v_exp_f32 v19, v19\n"
"v_mfma_f32_16x16x32_bf16 v[36:39], a[156:159], v[60:63], v[36:39]\n"
"buffer_load_dwordx4 v1, s[40:43], s82 offen sc1 lds\n"
"v_mfma_f32_16x16x32_bf16 v[40:43], a[160:163], v[152:155], v[116:119]\n"
"v_exp_f32 v20, v20\n"
"v_mfma_f32_16x16x32_bf16 v[40:43], a[164:167], v[156:159], v[40:43]\n"
"v_cvt_pk_bf16_f32 v81, v18, v19\n"
"v_lshl_add_u32 v11, v11, 2, v15\n"
"v_mfma_f32_16x16x32_bf16 v[40:43], a[168:171], v[56:59], v[40:43]\n"
"v_exp_f32 v21, v21\n"
"v_mfma_f32_16x16x32_bf16 v[40:43], a[172:175], v[60:63], v[40:43]\n"
"buffer_load_dwordx4 v1, s[40:43], s82 offen offset:1024 sc1 lds\n"
"v_mfma_f32_16x16x32_bf16 v[44:47], a[176:179], v[152:155], v[116:119]\n"
"v_exp_f32 v22, v22\n"
"v_mfma_f32_16x16x32_bf16 v[44:47], a[180:183], v[156:159], v[44:47]\n"
"v_cvt_pk_bf16_f32 v82, v20, v21\n"
"v_add_u32 v2, 65536, v1\n"
"v_mfma_f32_16x16x32_bf16 v[44:47], a[184:187], v[56:59], v[44:47]\n"
"v_exp_f32 v23, v23\n"
"v_mfma_f32_16x16x32_bf16 v[44:47], a[188:191], v[60:63], v[44:47]\n"
"buffer_load_dwordx4 v1, s[40:43], s82 offen offset:2048 sc1 lds\n"
"v_mfma_f32_16x16x32_bf16 a[12:15], a[192:195], v[88:91], a[12:15]\n"
"v_exp_f32 v24, v24\n"
"v_mfma_f32_16x16x32_bf16 a[12:15], a[196:199], v[92:95], a[12:15]\n"
"ds_read_b64_tr_b16 a[192:193], v254 offset:16384\n"
"v_cvt_pk_bf16_f32 v83, v22, v23\n"
"v_mfma_f32_16x16x32_bf16 a[28:31], a[200:203], v[88:91], a[28:31]\n"
"v_exp_f32 v25, v25\n"
"v_mfma_f32_16x16x32_bf16 a[28:31], a[204:207], v[92:95], a[28:31]\n"
"ds_read_b64_tr_b16 a[194:195], v254 offset:20480\n"
"ds_read_b64_tr_b16 a[196:197], v254 offset:24576\n"
"v_mfma_f32_16x16x32_bf16 a[44:47], a[208:211], v[88:91], a[44:47]\n"
"v_exp_f32 v26, v26\n"
"v_mfma_f32_16x16x32_bf16 a[44:47], a[212:215], v[92:95], a[44:47]\n"
"ds_read_b64_tr_b16 a[198:199], v254 offset:28672\n"
"v_cvt_pk_bf16_f32 v84, v24, v25\n"
"v_mfma_f32_16x16x32_bf16 a[60:63], a[216:219], v[88:91], a[60:63]\n"
"v_exp_f32 v27, v27\n"
"v_mfma_f32_16x16x32_bf16 a[60:63], a[220:223], v[92:95], a[60:63]\n"
"ds_read_b64_tr_b16 a[200:201], v255 offset:16384\n"
"ds_read_b64_tr_b16 a[202:203], v255 offset:20480\n"
"v_mfma_f32_16x16x32_bf16 a[76:79], a[224:227], v[88:91], a[76:79]\n"
"v_exp_f32 v28, v28\n"
"v_mfma_f32_16x16x32_bf16 a[76:79], a[228:231], v[92:95], a[76:79]\n"
"ds_read_b64_tr_b16 a[204:205], v255 offset:24576\n"
"v_cvt_pk_bf16_f32 v85, v26, v27\n"
"v_smfmac_f32_16x16x64_bf16 v[140:143], v[228:231], v[88:95], v239\n"
"ds_read_b64_tr_b16 a[206:207], v255 offset:28672\n"
"v_mfma_f32_16x16x32_bf16 a[92:95], a[232:235], v[88:91], a[92:95]\n"
"v_exp_f32 v29, v29\n"
"v_mfma_f32_16x16x32_bf16 a[92:95], a[236:239], v[92:95], a[92:95]\n"
"ds_read_b64_tr_b16 a[208:209], v254 offset:16896\n"
"ds_read_b64_tr_b16 a[210:211], v254 offset:20992\n"
"v_mfma_f32_16x16x32_bf16 a[108:111], a[240:243], v[88:91], a[108:111]\n"
"v_exp_f32 v30, v30\n"
"v_mfma_f32_16x16x32_bf16 a[108:111], a[244:247], v[92:95], a[108:111]\n"
"ds_read_b64_tr_b16 a[212:213], v254 offset:25088\n"
"v_cvt_pk_bf16_f32 v86, v28, v29\n"
"v_mfma_f32_16x16x32_bf16 a[124:127], a[248:251], v[88:91], a[124:127]\n"
"v_exp_f32 v31, v31\n"
"v_mfma_f32_16x16x32_bf16 a[124:127], a[252:255], v[92:95], a[124:127]\n"
"ds_read_b64_tr_b16 a[214:215], v254 offset:29184\n"
"ds_read_b64_tr_b16 a[216:217], v255 offset:16896\n"
"v_mfma_f32_16x16x32_bf16 v[16:19], a[128:131], v[160:163], v[120:123]\n"
"v_exp_f32 v32, v32\n"
"v_mfma_f32_16x16x32_bf16 v[16:19], a[132:135], v[164:167], v[16:19]\n"
"ds_read_b64_tr_b16 a[218:219], v255 offset:20992\n"
"v_cvt_pk_bf16_f32 v87, v30, v31\n"
"v_mfma_f32_16x16x32_bf16 v[16:19], a[136:139], v[64:67], v[16:19]\n"
"v_exp_f32 v33, v33\n"
"v_mfma_f32_16x16x32_bf16 v[16:19], a[140:143], v[68:71], v[16:19]\n"
"ds_read_b64_tr_b16 a[220:221], v255 offset:25088\n"
"ds_read_b64_tr_b16 a[222:223], v255 offset:29184\n"
"v_mfma_f32_16x16x32_bf16 v[20:23], a[144:147], v[160:163], v[120:123]\n"
"v_exp_f32 v34, v34\n"
"v_mfma_f32_16x16x32_bf16 v[20:23], a[148:151], v[164:167], v[20:23]\n"
"ds_read_b64_tr_b16 a[224:225], v254 offset:17408\n"
"v_cvt_pk_bf16_f32 v88, v32, v33\n"
"v_mfma_f32_16x16x32_bf16 v[20:23], a[152:155], v[64:67], v[20:23]\n"
"v_exp_f32 v35, v35\n"
"v_mfma_f32_16x16x32_bf16 v[20:23], a[156:159], v[68:71], v[20:23]\n"
"ds_read_b64_tr_b16 a[226:227], v254 offset:21504\n"
"ds_read_b64_tr_b16 a[228:229], v254 offset:25600\n"
"v_mfma_f32_16x16x32_bf16 v[24:27], a[160:163], v[160:163], v[120:123]\n"
"v_exp_f32 v36, v36\n"
"v_mfma_f32_16x16x32_bf16 v[24:27], a[164:167], v[164:167], v[24:27]\n"
"ds_read_b64_tr_b16 a[230:231], v254 offset:29696\n"
"v_cvt_pk_bf16_f32 v89, v34, v35\n"
"v_mfma_f32_16x16x32_bf16 v[24:27], a[168:171], v[64:67], v[24:27]\n"
"v_exp_f32 v37, v37\n"
"v_mfma_f32_16x16x32_bf16 v[24:27], a[172:175], v[68:71], v[24:27]\n"
"ds_read_b64_tr_b16 a[232:233], v255 offset:17408\n"
"ds_read_b64_tr_b16 a[234:235], v255 offset:21504\n"
"v_smfmac_f32_16x16x64_bf16 v[128:131], v[228:231], v[80:87], v239\n"
"ds_read_b64_tr_b16 a[236:237], v255 offset:25600\n"
"v_mfma_f32_16x16x32_bf16 v[28:31], a[176:179], v[160:163], v[120:123]\n"
"v_exp_f32 v38, v38\n"
"v_mfma_f32_16x16x32_bf16 v[28:31], a[180:183], v[164:167], v[28:31]\n"
"ds_read_b64_tr_b16 a[238:239], v255 offset:29696\n"
"v_cvt_pk_bf16_f32 v90, v36, v37\n"
"v_mfma_f32_16x16x32_bf16 v[28:31], a[184:187], v[64:67], v[28:31]\n"
"s_waitcnt lgkmcnt(15)\n"
"v_exp_f32 v39, v39\n"
"v_mfma_f32_16x16x32_bf16 v[28:31], a[188:191], v[68:71], v[28:31]\n"
"ds_read_b64_tr_b16 a[240:241], v254 offset:17920\n"
"ds_read_b64_tr_b16 a[242:243], v254 offset:22016\n"
"v_mfma_f32_16x16x32_bf16 a[0:3], a[192:195], v[80:83], a[0:3]\n"
"v_exp_f32 v40, v40\n"
"v_mfma_f32_16x16x32_bf16 a[0:3], a[196:199], v[84:87], a[0:3]\n"
"ds_read_b64_tr_b16 a[244:245], v254 offset:26112\n"
"v_cvt_pk_bf16_f32 v91, v38, v39\n"
"v_mfma_f32_16x16x32_bf16 a[16:19], a[200:203], v[80:83], a[16:19]\n"
"s_waitcnt lgkmcnt(11)\n"
"v_exp_f32 v41, v41\n"
"v_mfma_f32_16x16x32_bf16 a[16:19], a[204:207], v[84:87], a[16:19]\n"
"ds_read_b64_tr_b16 a[246:247], v254 offset:30208\n"
"ds_read_b64_tr_b16 a[248:249], v255 offset:17920\n"
"v_mfma_f32_16x16x32_bf16 a[32:35], a[208:211], v[80:83], a[32:35]\n"
"v_exp_f32 v42, v42\n"
"v_mfma_f32_16x16x32_bf16 a[32:35], a[212:215], v[84:87], a[32:35]\n"
"ds_read_b64_tr_b16 a[250:251], v255 offset:22016\n"
"v_cvt_pk_bf16_f32 v92, v40, v41\n"
"v_mfma_f32_16x16x32_bf16 a[48:51], a[216:219], v[80:83], a[48:51]\n"
"s_waitcnt lgkmcnt(6)\n"
"v_exp_f32 v43, v43\n"
"v_mfma_f32_16x16x32_bf16 a[48:51], a[220:223], v[84:87], a[48:51]\n"
"ds_read_b64_tr_b16 a[252:253], v255 offset:26112\n"
"ds_read_b64_tr_b16 a[254:255], v255 offset:30208\n"
"v_mfma_f32_16x16x32_bf16 a[64:67], a[224:227], v[80:83], a[64:67]\n"
"v_exp_f32 v44, v44\n"
"v_mfma_f32_16x16x32_bf16 a[64:67], a[228:231], v[84:87], a[64:67]\n"
"v_cvt_pk_bf16_f32 v93, v42, v43\n"
"v_add_u32 v3, 131072, v1\n"
"v_mfma_f32_16x16x32_bf16 a[80:83], a[232:235], v[80:83], a[80:83]\n"
"s_waitcnt lgkmcnt(4)\n"
"v_exp_f32 v45, v45\n"
"v_mfma_f32_16x16x32_bf16 a[80:83], a[236:239], v[84:87], a[80:83]\n"
"buffer_load_dwordx4 v1, s[40:43], s82 offen offset:3072 sc1 lds\n"
"v_mfma_f32_16x16x32_bf16 a[96:99], a[240:243], v[80:83], a[96:99]\n"
"s_waitcnt lgkmcnt(0)\n"
"v_exp_f32 v46, v46\n"
"v_mfma_f32_16x16x32_bf16 a[96:99], a[244:247], v[84:87], a[96:99]\n"
"s_add_u32 s59, s57, 65536\n"
"v_cvt_pk_bf16_f32 v94, v44, v45\n"
"v_mfma_f32_16x16x32_bf16 a[112:115], a[248:251], v[80:83], a[112:115]\n"
"v_exp_f32 v47, v47\n"
"v_mfma_f32_16x16x32_bf16 a[112:115], a[252:255], v[84:87], a[112:115]\n"
"buffer_store_dwordx2 v[236:237], v14, s[40:43], s59 offen sc1\n"
"v_mfma_f32_16x16x32_bf16 v[32:35], a[128:131], v[168:171], v[124:127]\n"
"v_cvt_pk_bf16_f32 v95, v46, v47\n"
"v_mfma_f32_16x16x32_bf16 v[32:35], a[132:135], v[172:175], v[32:35]\n"
"v_exp_f32 v16, v16\n"
"v_mfma_f32_16x16x32_bf16 v[32:35], a[136:139], v[72:75], v[32:35]\n"
"v_exp_f32 v17, v17\n"
"v_mfma_f32_16x16x32_bf16 v[32:35], a[140:143], v[76:79], v[32:35]\n"
"v_exp_f32 v18, v18\n"
"v_mfma_f32_16x16x32_bf16 v[36:39], a[144:147], v[168:171], v[124:127]\n"
"v_exp_f32 v19, v19\n"
"v_mfma_f32_16x16x32_bf16 v[36:39], a[148:151], v[172:175], v[36:39]\n"
"v_cvt_pk_bf16_f32 v80, v16, v17\n"
"v_mfma_f32_16x16x32_bf16 v[36:39], a[152:155], v[72:75], v[36:39]\n"
"v_exp_f32 v20, v20\n"
"v_mfma_f32_16x16x32_bf16 v[36:39], a[156:159], v[76:79], v[36:39]\n"
"v_exp_f32 v21, v21\n"
"v_mfma_f32_16x16x32_bf16 v[40:43], a[160:163], v[168:171], v[124:127]\n"
"v_cvt_pk_bf16_f32 v81, v18, v19\n"
"v_mfma_f32_16x16x32_bf16 v[40:43], a[164:167], v[172:175], v[40:43]\n"
"v_exp_f32 v22, v22\n"
"v_mfma_f32_16x16x32_bf16 v[40:43], a[168:171], v[72:75], v[40:43]\n"
"v_exp_f32 v23, v23\n"
"v_mfma_f32_16x16x32_bf16 v[40:43], a[172:175], v[76:79], v[40:43]\n"
"v_cvt_pk_bf16_f32 v82, v20, v21\n"
"v_exp_f32 v24, v24\n"
"v_mfma_f32_16x16x32_bf16 v[44:47], a[176:179], v[168:171], v[124:127]\n"
"v_exp_f32 v25, v25\n"
"v_mfma_f32_16x16x32_bf16 v[44:47], a[180:183], v[172:175], v[44:47]\n"
"v_cvt_pk_bf16_f32 v83, v22, v23\n"
"v_mfma_f32_16x16x32_bf16 v[44:47], a[184:187], v[72:75], v[44:47]\n"
"v_exp_f32 v26, v26\n"
"v_mfma_f32_16x16x32_bf16 v[44:47], a[188:191], v[76:79], v[44:47]\n"
"v_exp_f32 v27, v27\n"
"v_mfma_f32_16x16x32_bf16 a[4:7], a[192:195], v[88:91], a[4:7]\n"
"v_cvt_pk_bf16_f32 v84, v24, v25\n"
"v_mfma_f32_16x16x32_bf16 a[4:7], a[196:199], v[92:95], a[4:7]\n"
"v_exp_f32 v28, v28\n"
"v_mfma_f32_16x16x32_bf16 a[20:23], a[200:203], v[88:91], a[20:23]\n"
"v_exp_f32 v29, v29\n"
"v_mfma_f32_16x16x32_bf16 a[20:23], a[204:207], v[92:95], a[20:23]\n"
"v_cvt_pk_bf16_f32 v85, v26, v27\n"
"v_exp_f32 v30, v30\n"
"v_mfma_f32_16x16x32_bf16 a[36:39], a[208:211], v[88:91], a[36:39]\n"
"v_exp_f32 v31, v31\n"
"v_mfma_f32_16x16x32_bf16 a[36:39], a[212:215], v[92:95], a[36:39]\n"
"v_cvt_pk_bf16_f32 v86, v28, v29\n"
"v_cvt_pk_bf16_f32 v87, v30, v31\n"
"v_mfma_f32_16x16x32_bf16 a[52:55], a[216:219], v[88:91], a[52:55]\n"
"buffer_load_dwordx4 a[128:131], v1, s[40:43], s84 offen sc1\n"
"v_mfma_f32_16x16x32_bf16 a[52:55], a[220:223], v[92:95], a[52:55]\n"
"buffer_load_dwordx4 a[132:135], v1, s[40:43], s84 offen offset:1024 sc1\n"
"v_smfmac_f32_16x16x64_bf16 v[132:135], v[228:231], v[88:95], v239\n"
"v_mfma_f32_16x16x32_bf16 a[68:71], a[224:227], v[88:91], a[68:71]\n"
"buffer_load_dwordx4 a[136:139], v1, s[40:43], s84 offen offset:2048 sc1\n"
"v_mfma_f32_16x16x32_bf16 a[68:71], a[228:231], v[92:95], a[68:71]\n"
"buffer_load_dwordx4 a[140:143], v1, s[40:43], s84 offen offset:3072 sc1\n"
"v_mfma_f32_16x16x32_bf16 a[84:87], a[232:235], v[88:91], a[84:87]\n"
"s_add_u32 s84, s84, 4096\n"
"v_exp_f32 v32, v32\n"
"v_mfma_f32_16x16x32_bf16 a[84:87], a[236:239], v[92:95], a[84:87]\n"
"v_exp_f32 v33, v33\n"
"v_mfma_f32_16x16x32_bf16 a[100:103], a[240:243], v[88:91], a[100:103]\n"
"v_exp_f32 v34, v34\n"
"v_mfma_f32_16x16x32_bf16 a[100:103], a[244:247], v[92:95], a[100:103]\n"
"v_exp_f32 v35, v35\n"
"v_mfma_f32_16x16x32_bf16 a[116:119], a[248:251], v[88:91], a[116:119]\n"
"v_cvt_pk_bf16_f32 v96, v32, v33\n"
"v_exp_f32 v36, v36\n"
"v_mfma_f32_16x16x32_bf16 a[116:119], a[252:255], v[92:95], a[116:119]\n"
"v_exp_f32 v37, v37\n"
"v_mfma_f32_16x16x32_bf16 a[8:11], a[192:195], v[80:83], a[8:11]\n"
"v_cvt_pk_bf16_f32 v97, v34, v35\n"
"v_mfma_f32_16x16x32_bf16 a[8:11], a[196:199], v[84:87], a[8:11]\n"
"v_exp_f32 v38, v38\n"
"v_mfma_f32_16x16x32_bf16 a[24:27], a[200:203], v[80:83], a[24:27]\n"
"v_exp_f32 v39, v39\n"
"v_mfma_f32_16x16x32_bf16 a[24:27], a[204:207], v[84:87], a[24:27]\n"
"v_cvt_pk_bf16_f32 v98, v36, v37\n"
"v_exp_f32 v40, v40\n"
"v_mfma_f32_16x16x32_bf16 a[40:43], a[208:211], v[80:83], a[40:43]\n"
"v_mfma_f32_16x16x32_bf16 a[40:43], a[212:215], v[84:87], a[40:43]\n"
"v_exp_f32 v41, v41\n"
"v_mfma_f32_16x16x32_bf16 a[56:59], a[216:219], v[80:83], a[56:59]\n"
"v_cvt_pk_bf16_f32 v99, v38, v39\n"
"v_exp_f32 v42, v42\n"
"v_mfma_f32_16x16x32_bf16 a[56:59], a[220:223], v[84:87], a[56:59]\n"
"v_exp_f32 v43, v43\n"
"v_smfmac_f32_16x16x64_bf16 v[136:139], v[228:231], v[80:87], v239\n"
"v_cvt_pk_bf16_f32 v100, v40, v41\n"
"v_mfma_f32_16x16x32_bf16 a[72:75], a[224:227], v[80:83], a[72:75]\n"
"v_exp_f32 v44, v44\n"
"v_mfma_f32_16x16x32_bf16 a[72:75], a[228:231], v[84:87], a[72:75]\n"
"v_exp_f32 v45, v45\n"
"v_mfma_f32_16x16x32_bf16 a[88:91], a[232:235], v[80:83], a[88:91]\n"
"v_cvt_pk_bf16_f32 v101, v42, v43\n"
"v_exp_f32 v46, v46\n"
"v_mfma_f32_16x16x32_bf16 a[88:91], a[236:239], v[84:87], a[88:91]\n"
"v_exp_f32 v47, v47\n"
"v_mfma_f32_16x16x32_bf16 a[104:107], a[240:243], v[80:83], a[104:107]\n"
"v_cvt_pk_bf16_f32 v102, v44, v45\n"
"v_mfma_f32_16x16x32_bf16 a[104:107], a[244:247], v[84:87], a[104:107]\n"
"v_cvt_pk_bf16_f32 v103, v46, v47\n"
"v_mfma_f32_16x16x32_bf16 a[120:123], a[248:251], v[80:83], a[120:123]\n"
"v_mfma_f32_16x16x32_bf16 a[120:123], a[252:255], v[84:87], a[120:123]\n"
"buffer_load_dwordx4 a[144:147], v1, s[40:43], s84 offen sc1\n"
"v_mfma_f32_16x16x32_bf16 a[12:15], a[192:195], v[96:99], a[12:15]\n"
"v_mfma_f32_16x16x32_bf16 a[12:15], a[196:199], v[100:103], a[12:15]\n"
"v_smfmac_f32_16x16x64_bf16 v[140:143], v[228:231], v[96:103], v239\n"
"v_mfma_f32_16x16x32_bf16 a[28:31], a[200:203], v[96:99], a[28:31]\n"
"ds_read_b128 v[208:211], v2 offset:32768\n"
"v_mfma_f32_16x16x32_bf16 a[28:31], a[204:207], v[100:103], a[28:31]\n"
"buffer_load_dwordx4 a[148:151], v1, s[40:43], s84 offen offset:1024 sc1\n"
"v_mfma_f32_16x16x32_bf16 a[44:47], a[208:211], v[96:99], a[44:47]\n"
"ds_read_b128 v[212:215], v2 offset:33792\n"
"v_mfma_f32_16x16x32_bf16 a[44:47], a[212:215], v[100:103], a[44:47]\n"
"buffer_load_dwordx4 a[152:155], v1, s[40:43], s84 offen offset:2048 sc1\n"
"v_mfma_f32_16x16x32_bf16 a[60:63], a[216:219], v[96:99], a[60:63]\n"
"ds_read_b128 v[216:219], v2 offset:34816\n"
"v_mfma_f32_16x16x32_bf16 a[60:63], a[220:223], v[100:103], a[60:63]\n"
"v_mfma_f32_16x16x32_bf16 a[76:79], a[224:227], v[96:99], a[76:79]\n"
"ds_read_b128 v[220:223], v2 offset:35840\n"
"v_mfma_f32_16x16x32_bf16 a[76:79], a[228:231], v[100:103], a[76:79]\n"
"buffer_load_dwordx4 a[156:159], v1, s[40:43], s84 offen offset:3072 sc1\n"
"v_mfma_f32_16x16x32_bf16 a[92:95], a[232:235], v[96:99], a[92:95]\n"
"ds_read_b128 v[224:227], v2 offset:36864\n"
"v_mfma_f32_16x16x32_bf16 a[92:95], a[236:239], v[100:103], a[92:95]\n"
"s_mov_b32 s78, 0x5f800000\n"
"v_add_f32 v128, v128, v129\n"
"v_mfma_f32_16x16x32_bf16 a[108:111], a[240:243], v[96:99], a[108:111]\n"
"s_add_u32 s84, s84, 4096\n"
"ds_read_b128 v[228:231], v2 offset:37888\n"
"v_add_f32 v132, v132, v133\n"
"v_mfma_f32_16x16x32_bf16 a[108:111], a[244:247], v[100:103], a[108:111]\n"
"v_add_f32 v136, v136, v137\n"
"v_add_f32 v140, v140, v141\n"
"v_mfma_f32_16x16x32_bf16 a[124:127], a[248:251], v[96:99], a[124:127]\n"
"v_add_f32 v9, v128, v132\n"
"v_mfma_f32_16x16x32_bf16 a[124:127], a[252:255], v[100:103], a[124:127]\n"
"s_waitcnt lgkmcnt(5)\n"
"v_add_f32 v10, v136, v140\n"
"v_add_f32 v8, v9, v10\n"
"v_mfma_f32_16x16x32_bf16 v[16:19], v[208:211], v[176:179], 0\n"
"s_waitcnt lgkmcnt(4)\n"
"buffer_load_dwordx4 a[160:163], v1, s[40:43], s84 offen sc1\n"
"v_mfma_f32_16x16x32_bf16 v[16:19], v[212:215], v[192:195], v[16:19]\n"
"s_waitcnt vmcnt(8) lgkmcnt(3)\n"
"v_cmp_nge_f32 vcc, s78, v8\n"
"v_mfma_f32_16x16x32_bf16 v[16:19], v[216:219], a[128:131], v[16:19]\n"
"s_cmp_lg_u64 vcc, 0\n"
"s_cselect_b32 s78, 1, 0\n"
"v_mov_b32 v8, s78\n"
"v_mfma_f32_16x16x32_bf16 v[20:23], v[208:211], v[180:183], 0\n"
"ds_write_b32 v11, v8\n"
"v_mfma_f32_16x16x32_bf16 v[20:23], v[212:215], v[196:199], v[20:23]\n"
"s_waitcnt vmcnt(7)\n"
"buffer_load_dwordx4 a[164:167], v1, s[40:43], s84 offen offset:1024 sc1\n"
"v_mfma_f32_16x16x32_bf16 v[20:23], v[216:219], a[132:135], v[20:23]\n"
"buffer_load_dwordx4 a[168:171], v1, s[40:43], s84 offen offset:2048 sc1\n"
"v_mfma_f32_16x16x32_bf16 v[24:27], v[208:211], v[184:187], 0\n"
"s_waitcnt lgkmcnt(0)\n"
"s_barrier\n"
"ds_read_b128 v[8:11], v15\n"
"v_mfma_f32_16x16x32_bf16 v[24:27], v[212:215], v[200:203], v[24:27]\n"
"s_waitcnt vmcnt(8)\n"
"buffer_load_dwordx4 a[172:175], v1, s[40:43], s84 offen offset:3072 sc1\n"
"v_mfma_f32_16x16x32_bf16 v[24:27], v[216:219], a[136:139], v[24:27]\n"
"s_add_u32 s84, s84, 4096\n"
"buffer_load_dwordx4 a[176:179], v1, s[40:43], s84 offen sc1\n"
"v_mfma_f32_16x16x32_bf16 v[28:31], v[208:211], v[188:191], 0\n"
"v_mfma_f32_16x16x32_bf16 v[28:31], v[212:215], v[204:207], v[28:31]\n"
"s_waitcnt vmcnt(9)\n"
"v_mfma_f32_16x16x32_bf16 v[28:31], v[216:219], a[140:143], v[28:31]\n"
"v_mfma_f32_16x16x32_bf16 v[32:35], v[220:223], v[176:179], 0\n"
"s_waitcnt lgkmcnt(0)\n"
"ds_read_b128 v[208:211], v2 offset:41984\n"
"v_or3_b32 v8, v8, v9, v10\n"
"v_mfma_f32_16x16x32_bf16 v[32:35], v[224:227], v[192:195], v[32:35]\n"
"v_or_b32 v8, v8, v11\n"
"v_mfma_f32_16x16x32_bf16 v[32:35], v[228:231], a[128:131], v[32:35]\n"
"v_readfirstlane_b32 s78, v8\n"
"v_mfma_f32_16x16x32_bf16 v[36:39], v[220:223], v[180:183], 0\n"
"s_andn2_b32 s78, s78, s22\n"
"s_cbranch_scc1 L_fallback_11_%=\n"
"ds_read_b128 v[212:215], v2 offset:43008\n"
"v_mfma_f32_16x16x32_bf16 v[36:39], v[224:227], v[196:199], v[36:39]\n"
"ds_read_b128 v[232:235], v2 offset:38912\n"
"ds_read_b128 v[236:239], v2 offset:39936\n"
"v_mfma_f32_16x16x32_bf16 v[36:39], v[228:231], a[132:135], v[36:39]\n"
"ds_read_b128 v[240:243], v2 offset:40960\n"
"v_mfma_f32_16x16x32_bf16 v[40:43], v[220:223], v[184:187], 0\n"
"buffer_load_dwordx4 a[180:183], v1, s[40:43], s84 offen offset:1024 sc1\n"
"v_rcp_f32 v244, v128\n"
"v_rcp_f32 v245, v132\n"
"v_rcp_f32 v246, v136\n"
"v_rcp_f32 v247, v140\n"
"v_mfma_f32_16x16x32_bf16 v[40:43], v[224:227], v[200:203], v[40:43]\n"
"s_lshl_b32 s85, s73, 5\n"
"s_add_u32 s85, s85, s68\n"
"ds_read_b128 v[216:219], v2 offset:44032\n"
"v_mfma_f32_16x16x32_bf16 v[40:43], v[228:231], a[136:139], v[40:43]\n"
"s_lshl_b32 s85, s85, 14\n"
"buffer_load_dwordx4 a[184:187], v1, s[40:43], s84 offen offset:2048 sc1\n"
"v_mfma_f32_16x16x32_bf16 v[44:47], v[220:223], v[188:191], 0\n"
"s_add_u32 s85, s85, 21233664\n"
"v_accvgpr_read_b32 v252, a16\n"
"v_accvgpr_read_b32 v253, a17\n"
"v_mfma_f32_16x16x32_bf16 v[44:47], v[224:227], v[204:207], v[44:47]\n"
"s_waitcnt lgkmcnt(1)\n"
"v_accvgpr_read_b32 v254, a18\n"
"v_accvgpr_read_b32 v255, a19\n"
"v_mfma_f32_16x16x32_bf16 v[44:47], v[228:231], a[140:143], v[44:47]\n"
"buffer_load_dwordx4 a[188:191], v1, s[40:43], s84 offen offset:3072 sc1\n"
"v_mfma_f32_16x16x32_bf16 v[48:51], v[232:235], v[176:179], 0\n"
"s_add_u32 s84, s84, 4096\n"
"v_accvgpr_read_b32 v248, a0\n"
"v_accvgpr_read_b32 v249, a1\n"
"v_mfma_f32_16x16x32_bf16 v[48:51], v[236:239], v[192:195], v[48:51]\n"
"ds_read_b128 v[220:223], v2 offset:45056\n"
"v_accvgpr_read_b32 v250, a2\n"
"v_mfma_f32_16x16x32_bf16 v[48:51], v[240:243], a[128:131], v[48:51]\n"
"v_accvgpr_read_b32 v251, a3\n"
"v_mul_f32 v252, v252, v244\n"
"v_mfma_f32_16x16x32_bf16 v[52:55], v[232:235], v[180:183], 0\n"
"ds_read_b128 v[224:227], v2 offset:46080\n"
"v_mul_f32 v253, v253, v244\n"
"v_mfma_f32_16x16x32_bf16 v[52:55], v[236:239], v[196:199], v[52:55]\n"
"v_mul_f32 v254, v254, v244\n"
"v_mul_f32 v255, v255, v244\n"
"v_mfma_f32_16x16x32_bf16 v[52:55], v[240:243], a[132:135], v[52:55]\n"
"buffer_load_dwordx4 a[192:195], v1, s[40:43], s84 offen sc1\n"
"v_mfma_f32_16x16x32_bf16 v[56:59], v[232:235], v[184:187], 0\n"
"v_mul_f32 v248, v248, v244\n"
"v_mul_f32 v249, v249, v244\n"
"v_mfma_f32_16x16x32_bf16 v[56:59], v[236:239], v[200:203], v[56:59]\n"
"ds_read_b128 v[228:231], v2 offset:47104\n"
"v_mul_f32 v250, v250, v244\n"
"v_mfma_f32_16x16x32_bf16 v[56:59], v[240:243], a[136:139], v[56:59]\n"
"buffer_load_dwordx4 a[196:199], v1, s[40:43], s84 offen offset:1024 sc1\n"
"v_mfma_f32_16x16x32_bf16 v[60:63], v[232:235], v[188:191], 0\n"
"v_mul_f32 v251, v251, v244\n"
"v_cvt_pk_bf16_f32 v248, v248, v249\n"
"v_mfma_f32_16x16x32_bf16 v[60:63], v[236:239], v[204:207], v[60:63]\n"
"s_waitcnt lgkmcnt(3)\n"
"v_cvt_pk_bf16_f32 v249, v250, v251\n"
"v_cvt_pk_bf16_f32 v250, v252, v253\n"
"v_mfma_f32_16x16x32_bf16 v[60:63], v[240:243], a[140:143], v[60:63]\n"
"buffer_load_dwordx4 a[200:203], v1, s[40:43], s84 offen offset:2048 sc1\n"
"v_mfma_f32_16x16x32_bf16 v[64:67], v[208:211], v[176:179], 0\n"
"ds_read_b128 v[232:235], v2 offset:48128\n"
"v_cvt_pk_bf16_f32 v251, v254, v255\n"
"v_mfma_f32_16x16x32_bf16 v[64:67], v[212:215], v[192:195], v[64:67]\n"
"buffer_store_dwordx4 v[248:251], v1, s[40:43], s85 offen sc1\n"
"v_mfma_f32_16x16x32_bf16 v[64:67], v[216:219], a[128:131], v[64:67]\n"
"buffer_load_dwordx4 a[204:207], v1, s[40:43], s84 offen offset:3072 sc1\n"
"v_mfma_f32_16x16x32_bf16 v[68:71], v[208:211], v[180:183], 0\n"
"s_add_u32 s84, s84, 4096\n"
"v_accvgpr_read_b32 v252, a20\n"
"v_accvgpr_read_b32 v253, a21\n"
"v_mfma_f32_16x16x32_bf16 v[68:71], v[212:215], v[196:199], v[68:71]\n"
"ds_read_b128 v[236:239], v2 offset:49152\n"
"v_accvgpr_read_b32 v254, a22\n"
"v_mfma_f32_16x16x32_bf16 v[68:71], v[216:219], a[132:135], v[68:71]\n"
"v_accvgpr_read_b32 v255, a23\n"
"v_accvgpr_read_b32 v248, a4\n"
"v_mfma_f32_16x16x32_bf16 v[72:75], v[208:211], v[184:187], 0\n"
"buffer_load_dwordx4 a[208:211], v1, s[40:43], s84 offen sc1\n"
"v_mfma_f32_16x16x32_bf16 v[72:75], v[212:215], v[200:203], v[72:75]\n"
"ds_read_b128 v[240:243], v2 offset:50176\n"
"v_accvgpr_read_b32 v249, a5\n"
"v_mfma_f32_16x16x32_bf16 v[72:75], v[216:219], a[136:139], v[72:75]\n"
"v_accvgpr_read_b32 v250, a6\n"
"v_accvgpr_read_b32 v251, a7\n"
"v_mfma_f32_16x16x32_bf16 v[76:79], v[208:211], v[188:191], 0\n"
"buffer_load_dwordx4 a[212:215], v1, s[40:43], s84 offen offset:1024 sc1\n"
"v_mfma_f32_16x16x32_bf16 v[76:79], v[212:215], v[204:207], v[76:79]\n"
"s_waitcnt lgkmcnt(3)\n"
"v_mul_f32 v252, v252, v245\n"
"v_mul_f32 v253, v253, v245\n"
"v_mfma_f32_16x16x32_bf16 v[76:79], v[216:219], a[140:143], v[76:79]\n"
"v_mul_f32 v254, v254, v245\n"
"v_mul_f32 v255, v255, v245\n"
"v_mfma_f32_16x16x32_bf16 v[80:83], v[220:223], v[176:179], 0\n"
"buffer_load_dwordx4 a[216:219], v1, s[40:43], s84 offen offset:2048 sc1\n"
"v_mfma_f32_16x16x32_bf16 v[80:83], v[224:227], v[192:195], v[80:83]\n"
"ds_read_b128 v[208:211], v2 offset:51200\n"
"v_mul_f32 v248, v248, v245\n"
"v_mfma_f32_16x16x32_bf16 v[80:83], v[228:231], a[128:131], v[80:83]\n"
"v_mul_f32 v249, v249, v245\n"
"v_mul_f32 v250, v250, v245\n"
"v_mfma_f32_16x16x32_bf16 v[84:87], v[220:223], v[180:183], 0\n"
"v_mul_f32 v251, v251, v245\n"
"v_cvt_pk_bf16_f32 v144, v248, v249\n"
"v_mfma_f32_16x16x32_bf16 v[84:87], v[224:227], v[196:199], v[84:87]\n"
"buffer_load_dwordx4 a[220:223], v1, s[40:43], s84 offen offset:3072 sc1\n"
"v_mfma_f32_16x16x32_bf16 v[84:87], v[228:231], a[132:135], v[84:87]\n"
"s_add_u32 s84, s84, 4096\n"
"ds_read_b128 v[212:215], v2 offset:52224\n"
"v_cvt_pk_bf16_f32 v145, v250, v251\n"
"v_mfma_f32_16x16x32_bf16 v[88:91], v[220:223], v[184:187], 0\n"
"v_cvt_pk_bf16_f32 v146, v252, v253\n"
"v_cvt_pk_bf16_f32 v147, v254, v255\n"
"v_mfma_f32_16x16x32_bf16 v[88:91], v[224:227], v[200:203], v[88:91]\n"
"ds_read_b128 v[216:219], v2 offset:53248\n"
"v_accvgpr_read_b32 v252, a24\n"
"v_mfma_f32_16x16x32_bf16 v[88:91], v[228:231], a[136:139], v[88:91]\n"
"buffer_load_dwordx4 a[224:227], v1, s[40:43], s84 offen sc1\n"
"v_accvgpr_read_b32 v253, a25\n"
"v_mfma_f32_16x16x32_bf16 v[92:95], v[220:223], v[188:191], 0\n"
"v_accvgpr_read_b32 v254, a26\n"
"v_accvgpr_read_b32 v255, a27\n"
"v_mfma_f32_16x16x32_bf16 v[92:95], v[224:227], v[204:207], v[92:95]\n"
"s_waitcnt lgkmcnt(3)\n"
"buffer_load_dwordx4 a[228:231], v1, s[40:43], s84 offen offset:1024 sc1\n"
"v_mfma_f32_16x16x32_bf16 v[92:95], v[228:231], a[140:143], v[92:95]\n"
"v_accvgpr_read_b32 v248, a8\n"
"v_accvgpr_read_b32 v249, a9\n"
"v_mfma_f32_16x16x32_bf16 v[96:99], v[232:235], v[176:179], 0\n"
"v_accvgpr_read_b32 v250, a10\n"
"v_accvgpr_read_b32 v251, a11\n"
"v_mfma_f32_16x16x32_bf16 v[96:99], v[236:239], v[192:195], v[96:99]\n"
"ds_read_b128 v[220:223], v2 offset:54272\n"
"v_mul_f32 v252, v252, v246\n"
"v_mfma_f32_16x16x32_bf16 v[96:99], v[240:243], a[128:131], v[96:99]\n"
"buffer_load_dwordx4 a[232:235], v1, s[40:43], s84 offen offset:2048 sc1\n"
"v_mfma_f32_16x16x32_bf16 v[100:103], v[232:235], v[180:183], 0\n"
"v_mul_f32 v253, v253, v246\n"
"v_mul_f32 v254, v254, v246\n"
"v_mfma_f32_16x16x32_bf16 v[100:103], v[236:239], v[196:199], v[100:103]\n"
"ds_read_b128 v[224:227], v2 offset:55296\n"
"v_mul_f32 v255, v255, v246\n"
"v_mfma_f32_16x16x32_bf16 v[100:103], v[240:243], a[132:135], v[100:103]\n"
"buffer_load_dwordx4 a[236:239], v1, s[40:43], s84 offen offset:3072 sc1\n"
"v_mfma_f32_16x16x32_bf16 v[104:107], v[232:235], v[184:187], 0\n"
"s_sub_u32 s84, s84, 69632\n"
"v_mul_f32 v248, v248, v246\n"
"v_mul_f32 v249, v249, v246\n"
"v_mfma_f32_16x16x32_bf16 v[104:107], v[236:239], v[200:203], v[104:107]\n"
"ds_read_b128 v[228:231], v2 offset:56320\n"
"v_mul_f32 v250, v250, v246\n"
"v_mfma_f32_16x16x32_bf16 v[104:107], v[240:243], a[136:139], v[104:107]\n"
"v_mul_f32 v251, v251, v246\n"
"v_cvt_pk_bf16_f32 v248, v248, v249\n"
"v_mfma_f32_16x16x32_bf16 v[108:111], v[232:235], v[188:191], 0\n"
"buffer_load_dwordx4 a[240:243], v1, s[40:43], s84 offen sc1\n"
"v_mfma_f32_16x16x32_bf16 v[108:111], v[236:239], v[204:207], v[108:111]\n"
"s_waitcnt lgkmcnt(3)\n"
"v_cvt_pk_bf16_f32 v249, v250, v251\n"
"v_cvt_pk_bf16_f32 v250, v252, v253\n"
"v_mfma_f32_16x16x32_bf16 v[108:111], v[240:243], a[140:143], v[108:111]\n"
"v_cvt_pk_bf16_f32 v251, v254, v255\n"
"v_mfma_f32_16x16x32_bf16 v[112:115], v[208:211], v[176:179], 0\n"
"buffer_load_dwordx4 a[244:247], v1, s[40:43], s84 offen offset:1024 sc1\n"
"buffer_store_dwordx4 v[248:251], v1, s[40:43], s85 offen offset:2048 sc1\n"
"v_mfma_f32_16x16x32_bf16 v[112:115], v[212:215], v[192:195], v[112:115]\n"
"ds_read_b128 v[232:235], v2 offset:57344\n"
"v_accvgpr_read_b32 v252, a28\n"
"v_mfma_f32_16x16x32_bf16 v[112:115], v[216:219], a[128:131], v[112:115]\n"
"buffer_load_dwordx4 a[248:251], v1, s[40:43], s84 offen offset:2048 sc1\n"
"v_mfma_f32_16x16x32_bf16 v[116:119], v[208:211], v[180:183], 0\n"
"v_accvgpr_read_b32 v253, a29\n"
"v_accvgpr_read_b32 v254, a30\n"
"v_mfma_f32_16x16x32_bf16 v[116:119], v[212:215], v[196:199], v[116:119]\n"
"ds_read_b128 v[236:239], v2 offset:58368\n"
"v_accvgpr_read_b32 v255, a31\n"
"v_mfma_f32_16x16x32_bf16 v[116:119], v[216:219], a[132:135], v[116:119]\n"
"v_accvgpr_read_b32 v248, a12\n"
"v_accvgpr_read_b32 v249, a13\n"
"v_mfma_f32_16x16x32_bf16 v[120:123], v[208:211], v[184:187], 0\n"
"buffer_load_dwordx4 a[252:255], v1, s[40:43], s84 offen offset:3072 sc1\n"
"v_mfma_f32_16x16x32_bf16 v[120:123], v[212:215], v[200:203], v[120:123]\n"
"s_add_u32 s84, s84, 4096\n"
"ds_read_b128 v[240:243], v2 offset:59392\n"
"v_accvgpr_read_b32 v250, a14\n"
"v_mfma_f32_16x16x32_bf16 v[120:123], v[216:219], a[136:139], v[120:123]\n"
"v_accvgpr_read_b32 v251, a15\n"
"v_mul_f32 v252, v252, v247\n"
"v_mfma_f32_16x16x32_bf16 v[124:127], v[208:211], v[188:191], 0\n"
"v_mul_f32 v253, v253, v247\n"
"v_mul_f32 v254, v254, v247\n"
"v_mfma_f32_16x16x32_bf16 v[124:127], v[212:215], v[204:207], v[124:127]\n"
"s_waitcnt lgkmcnt(3)\n"
"v_mul_f32 v255, v255, v247\n"
"v_mul_f32 v248, v248, v247\n"
"v_mfma_f32_16x16x32_bf16 v[124:127], v[216:219], a[140:143], v[124:127]\n"
"v_mul_f32 v249, v249, v247\n"
"v_mul_f32 v250, v250, v247\n"
"v_mfma_f32_16x16x32_bf16 v[128:131], v[220:223], v[176:179], 0\n"
"ds_read_b128 v[208:211], v2 offset:60416\n"
"v_mul_f32 v251, v251, v247\n"
"v_mfma_f32_16x16x32_bf16 v[128:131], v[224:227], v[192:195], v[128:131]\n"
"v_cvt_pk_bf16_f32 v148, v248, v249\n"
"v_cvt_pk_bf16_f32 v149, v250, v251\n"
"v_mfma_f32_16x16x32_bf16 v[128:131], v[228:231], a[128:131], v[128:131]\n"
"v_cvt_pk_bf16_f32 v150, v252, v253\n"
"v_mfma_f32_16x16x32_bf16 v[132:135], v[220:223], v[180:183], 0\n"
"ds_read_b128 v[212:215], v2 offset:61440\n"
"v_cvt_pk_bf16_f32 v151, v254, v255\n"
"v_mfma_f32_16x16x32_bf16 v[132:135], v[224:227], v[196:199], v[132:135]\n"
"s_add_u32 s85, s85, 4096\n"
"v_accvgpr_read_b32 v252, a48\n"
"v_accvgpr_read_b32 v253, a49\n"
"v_mfma_f32_16x16x32_bf16 v[132:135], v[228:231], a[132:135], v[132:135]\n"
"v_accvgpr_read_b32 v254, a50\n"
"v_mfma_f32_16x16x32_bf16 v[136:139], v[220:223], v[184:187], 0\n"
"ds_read_b128 v[216:219], v2 offset:62464\n"
"v_accvgpr_read_b32 v255, a51\n"
"v_mfma_f32_16x16x32_bf16 v[136:139], v[224:227], v[200:203], v[136:139]\n"
"v_accvgpr_read_b32 v248, a32\n"
"v_mfma_f32_16x16x32_bf16 v[136:139], v[228:231], a[136:139], v[136:139]\n"
"v_accvgpr_read_b32 v249, a33\n"
"v_mfma_f32_16x16x32_bf16 v[140:143], v[220:223], v[188:191], 0\n"
"v_accvgpr_read_b32 v250, a34\n"
"v_accvgpr_read_b32 v251, a35\n"
"v_mfma_f32_16x16x32_bf16 v[140:143], v[224:227], v[204:207], v[140:143]\n"
"s_waitcnt vmcnt(18) lgkmcnt(3)\n"
"v_mul_f32 v252, v252, v244\n"
"v_mfma_f32_16x16x32_bf16 v[140:143], v[228:231], a[140:143], v[140:143]\n"
"v_mul_f32 v253, v253, v244\n"
"v_mfma_f32_16x16x32_bf16 v[16:19], v[232:235], a[144:147], v[16:19]\n"
"ds_read_b128 v[220:223], v2 offset:63488\n"
"v_mul_f32 v254, v254, v244\n"
"v_mfma_f32_16x16x32_bf16 v[16:19], v[236:239], a[160:163], v[16:19]\n"
"buffer_load_dwordx4 a[0:3], v1, s[40:43], s84 offen sc1\n"
"v_mfma_f32_16x16x32_bf16 v[16:19], v[240:243], a[176:179], v[16:19]\n"
"v_mul_f32 v255, v255, v244\n"
"v_mul_f32 v248, v248, v244\n"
"v_mfma_f32_16x16x32_bf16 v[20:23], v[232:235], a[148:151], v[20:23]\n"
"ds_read_b128 v[224:227], v2 offset:64512\n"
"v_mul_f32 v249, v249, v244\n"
"v_mfma_f32_16x16x32_bf16 v[20:23], v[236:239], a[164:167], v[20:23]\n"
"v_mul_f32 v250, v250, v244\n"
"v_mul_f32 v251, v251, v244\n"
"v_mfma_f32_16x16x32_bf16 v[20:23], v[240:243], a[180:183], v[20:23]\n"
"s_barrier\n"
"v_cvt_pk_bf16_f32 v248, v248, v249\n"
"v_mfma_f32_16x16x32_bf16 v[24:27], v[232:235], a[152:155], v[24:27]\n"
"buffer_load_dwordx4 a[4:7], v1, s[40:43], s84 offen offset:1024 sc1\n"
"v_mfma_f32_16x16x32_bf16 v[24:27], v[236:239], a[168:171], v[24:27]\n"
"ds_read_b128 v[228:231], v3\n"
"v_cvt_pk_bf16_f32 v249, v250, v251\n"
"v_mfma_f32_16x16x32_bf16 v[24:27], v[240:243], a[184:187], v[24:27]\n"
"v_cvt_pk_bf16_f32 v250, v252, v253\n"
"v_cvt_pk_bf16_f32 v251, v254, v255\n"
"v_mfma_f32_16x16x32_bf16 v[28:31], v[232:235], a[156:159], v[28:31]\n"
"buffer_load_dwordx4 a[8:11], v1, s[40:43], s84 offen offset:2048 sc1\n"
"v_mfma_f32_16x16x32_bf16 v[28:31], v[236:239], a[172:175], v[28:31]\n"
"s_waitcnt lgkmcnt(3)\n"
"buffer_store_dwordx4 v[248:251], v1, s[40:43], s85 offen sc1\n"
"v_mfma_f32_16x16x32_bf16 v[28:31], v[240:243], a[188:191], v[28:31]\n"
"v_accvgpr_read_b32 v252, a52\n"
"v_accvgpr_read_b32 v253, a53\n"
"v_mfma_f32_16x16x32_bf16 v[32:35], v[208:211], a[144:147], v[32:35]\n"
"ds_read_b128 v[232:235], v3 offset:1024\n"
"v_accvgpr_read_b32 v254, a54\n"
"v_mfma_f32_16x16x32_bf16 v[32:35], v[212:215], a[160:163], v[32:35]\n"
"buffer_load_dwordx4 a[12:15], v1, s[40:43], s84 offen offset:3072 sc1\n"
"v_mfma_f32_16x16x32_bf16 v[32:35], v[216:219], a[176:179], v[32:35]\n"
"s_add_u32 s84, s84, 4096\n"
"v_accvgpr_read_b32 v255, a55\n"
"v_accvgpr_read_b32 v248, a36\n"
"v_mfma_f32_16x16x32_bf16 v[36:39], v[208:211], a[148:151], v[36:39]\n"
"ds_read_b128 v[236:239], v3 offset:2048\n"
"v_accvgpr_read_b32 v249, a37\n"
"v_mfma_f32_16x16x32_bf16 v[36:39], v[212:215], a[164:167], v[36:39]\n"
"v_accvgpr_read_b32 v250, a38\n"
"v_accvgpr_read_b32 v251, a39\n"
"v_mfma_f32_16x16x32_bf16 v[36:39], v[216:219], a[180:183], v[36:39]\n"
"buffer_load_dwordx4 a[16:19], v1, s[40:43], s84 offen sc1\n"
"v_mfma_f32_16x16x32_bf16 v[40:43], v[208:211], a[152:155], v[40:43]\n"
"ds_read_b128 v[240:243], v3 offset:3072\n"
"v_mul_f32 v252, v252, v245\n"
"v_mfma_f32_16x16x32_bf16 v[40:43], v[212:215], a[168:171], v[40:43]\n"
"v_mul_f32 v253, v253, v245\n"
"v_mul_f32 v254, v254, v245\n"
"v_mfma_f32_16x16x32_bf16 v[40:43], v[216:219], a[184:187], v[40:43]\n"
"buffer_load_dwordx4 a[20:23], v1, s[40:43], s84 offen offset:1024 sc1\n"
"v_mfma_f32_16x16x32_bf16 v[44:47], v[208:211], a[156:159], v[44:47]\n"
"v_mul_f32 v255, v255, v245\n"
"v_mul_f32 v248, v248, v245\n"
"v_mfma_f32_16x16x32_bf16 v[44:47], v[212:215], a[172:175], v[44:47]\n"
"s_waitcnt lgkmcnt(3)\n"
"v_mul_f32 v249, v249, v245\n"
"v_mul_f32 v250, v250, v245\n"
"v_mfma_f32_16x16x32_bf16 v[44:47], v[216:219], a[188:191], v[44:47]\n"
"v_mul_f32 v251, v251, v245\n"
"v_cvt_pk_bf16_f32 v152, v248, v249\n"
"v_mfma_f32_16x16x32_bf16 v[48:51], v[220:223], a[144:147], v[48:51]\n"
"ds_read_b128 v[208:211], v3 offset:4096\n"
"v_cvt_pk_bf16_f32 v153, v250, v251\n"
"v_mfma_f32_16x16x32_bf16 v[48:51], v[224:227], a[160:163], v[48:51]\n"
"buffer_load_dwordx4 a[24:27], v1, s[40:43], s84 offen offset:2048 sc1\n"
"v_mfma_f32_16x16x32_bf16 v[48:51], v[228:231], a[176:179], v[48:51]\n"
"v_cvt_pk_bf16_f32 v154, v252, v253\n"
"v_cvt_pk_bf16_f32 v155, v254, v255\n"
"v_mfma_f32_16x16x32_bf16 v[52:55], v[220:223], a[148:151], v[52:55]\n"
"ds_read_b128 v[212:215], v3 offset:5120\n"
"v_accvgpr_read_b32 v252, a56\n"
"v_mfma_f32_16x16x32_bf16 v[52:55], v[224:227], a[164:167], v[52:55]\n"
"buffer_load_dwordx4 a[28:31], v1, s[40:43], s84 offen offset:3072 sc1\n"
"v_mfma_f32_16x16x32_bf16 v[52:55], v[228:231], a[180:183], v[52:55]\n"
"s_add_u32 s84, s84, 4096\n"
"v_accvgpr_read_b32 v253, a57\n"
"v_accvgpr_read_b32 v254, a58\n"
"v_mfma_f32_16x16x32_bf16 v[56:59], v[220:223], a[152:155], v[56:59]\n"
"ds_read_b128 v[216:219], v3 offset:6144\n"
"v_accvgpr_read_b32 v255, a59\n"
"v_mfma_f32_16x16x32_bf16 v[56:59], v[224:227], a[168:171], v[56:59]\n"
"v_accvgpr_read_b32 v248, a40\n"
"v_accvgpr_read_b32 v249, a41\n"
"v_mfma_f32_16x16x32_bf16 v[56:59], v[228:231], a[184:187], v[56:59]\n"
"v_accvgpr_read_b32 v250, a42\n"
"v_accvgpr_read_b32 v251, a43\n"
"v_mfma_f32_16x16x32_bf16 v[60:63], v[220:223], a[156:159], v[60:63]\n"
"v_mul_f32 v252, v252, v246\n"
"v_mfma_f32_16x16x32_bf16 v[60:63], v[224:227], a[172:175], v[60:63]\n"
"s_waitcnt lgkmcnt(3)\n"
"v_mul_f32 v253, v253, v246\n"
"v_mfma_f32_16x16x32_bf16 v[60:63], v[228:231], a[188:191], v[60:63]\n"
"v_mul_f32 v254, v254, v246\n"
"v_mfma_f32_16x16x32_bf16 v[64:67], v[232:235], a[144:147], v[64:67]\n"
"ds_read_b128 v[220:223], v3 offset:7168\n"
"v_mul_f32 v255, v255, v246\n"
"v_mfma_f32_16x16x32_bf16 v[64:67], v[236:239], a[160:163], v[64:67]\n"
"v_mul_f32 v248, v248, v246\n"
"v_mul_f32 v249, v249, v246\n"
"v_mfma_f32_16x16x32_bf16 v[64:67], v[240:243], a[176:179], v[64:67]\n"
"v_mul_f32 v250, v250, v246\n"
"v_mfma_f32_16x16x32_bf16 v[68:71], v[232:235], a[148:151], v[68:71]\n"
"ds_read_b128 v[224:227], v3 offset:8192\n"
"v_mul_f32 v251, v251, v246\n"
"v_mfma_f32_16x16x32_bf16 v[68:71], v[236:239], a[164:167], v[68:71]\n"
"v_cvt_pk_bf16_f32 v248, v248, v249\n"
"v_mfma_f32_16x16x32_bf16 v[68:71], v[240:243], a[180:183], v[68:71]\n"
"v_cvt_pk_bf16_f32 v249, v250, v251\n"
"v_mfma_f32_16x16x32_bf16 v[72:75], v[232:235], a[152:155], v[72:75]\n"
"ds_read_b128 v[228:231], v3 offset:9216\n"
"v_cvt_pk_bf16_f32 v250, v252, v253\n"
"v_mfma_f32_16x16x32_bf16 v[72:75], v[236:239], a[168:171], v[72:75]\n"
"v_cvt_pk_bf16_f32 v251, v254, v255\n"
"v_mfma_f32_16x16x32_bf16 v[72:75], v[240:243], a[184:187], v[72:75]\n"
"buffer_store_dwordx4 v[248:251], v1, s[40:43], s85 offen offset:2048 sc1\n"
"v_mfma_f32_16x16x32_bf16 v[76:79], v[232:235], a[156:159], v[76:79]\n"
"v_accvgpr_read_b32 v252, a60\n"
"v_mfma_f32_16x16x32_bf16 v[76:79], v[236:239], a[172:175], v[76:79]\n"
"s_waitcnt lgkmcnt(3)\n"
"v_accvgpr_read_b32 v253, a61\n"
"v_mfma_f32_16x16x32_bf16 v[76:79], v[240:243], a[188:191], v[76:79]\n"
"v_accvgpr_read_b32 v254, a62\n"
"v_accvgpr_read_b32 v255, a63\n"
"v_mfma_f32_16x16x32_bf16 v[80:83], v[208:211], a[144:147], v[80:83]\n"
"ds_read_b128 v[232:235], v3 offset:10240\n"
"v_accvgpr_read_b32 v248, a44\n"
"v_mfma_f32_16x16x32_bf16 v[80:83], v[212:215], a[160:163], v[80:83]\n"
"v_accvgpr_read_b32 v249, a45\n"
"v_mfma_f32_16x16x32_bf16 v[80:83], v[216:219], a[176:179], v[80:83]\n"
"v_accvgpr_read_b32 v250, a46\n"
"v_mfma_f32_16x16x32_bf16 v[84:87], v[208:211], a[148:151], v[84:87]\n"
"ds_read_b128 v[236:239], v3 offset:11264\n"
"v_accvgpr_read_b32 v251, a47\n"
"v_mfma_f32_16x16x32_bf16 v[84:87], v[212:215], a[164:167], v[84:87]\n"
"v_mul_f32 v252, v252, v247\n"
"v_mul_f32 v253, v253, v247\n"
"v_mfma_f32_16x16x32_bf16 v[84:87], v[216:219], a[180:183], v[84:87]\n"
"v_mul_f32 v254, v254, v247\n"
"v_mfma_f32_16x16x32_bf16 v[88:91], v[208:211], a[152:155], v[88:91]\n"
"ds_read_b128 v[240:243], v3 offset:12288\n"
"v_mul_f32 v255, v255, v247\n"
"v_mfma_f32_16x16x32_bf16 v[88:91], v[212:215], a[168:171], v[88:91]\n"
"v_mul_f32 v248, v248, v247\n"
"v_mfma_f32_16x16x32_bf16 v[88:91], v[216:219], a[184:187], v[88:91]\n"
"v_mul_f32 v249, v249, v247\n"
"v_mfma_f32_16x16x32_bf16 v[92:95], v[208:211], a[156:159], v[92:95]\n"
"v_mul_f32 v250, v250, v247\n"
"v_mul_f32 v251, v251, v247\n"
"v_mfma_f32_16x16x32_bf16 v[92:95], v[212:215], a[172:175], v[92:95]\n"
"s_waitcnt lgkmcnt(3)\n"
"v_cvt_pk_bf16_f32 v156, v248, v249\n"
"v_mfma_f32_16x16x32_bf16 v[92:95], v[216:219], a[188:191], v[92:95]\n"
"v_cvt_pk_bf16_f32 v157, v250, v251\n"
"v_mfma_f32_16x16x32_bf16 v[96:99], v[220:223], a[144:147], v[96:99]\n"
"ds_read_b128 v[208:211], v3 offset:13312\n"
"v_cvt_pk_bf16_f32 v158, v252, v253\n"
"v_mfma_f32_16x16x32_bf16 v[96:99], v[224:227], a[160:163], v[96:99]\n"
"v_cvt_pk_bf16_f32 v159, v254, v255\n"
"v_mfma_f32_16x16x32_bf16 v[96:99], v[228:231], a[176:179], v[96:99]\n"
"s_add_u32 s85, s85, 4096\n"
"v_accvgpr_read_b32 v252, a80\n"
"v_accvgpr_read_b32 v253, a81\n"
"v_mfma_f32_16x16x32_bf16 v[100:103], v[220:223], a[148:151], v[100:103]\n"
"ds_read_b128 v[212:215], v3 offset:14336\n"
"v_accvgpr_read_b32 v254, a82\n"
"v_mfma_f32_16x16x32_bf16 v[100:103], v[224:227], a[164:167], v[100:103]\n"
"v_accvgpr_read_b32 v255, a83\n"
"v_mfma_f32_16x16x32_bf16 v[100:103], v[228:231], a[180:183], v[100:103]\n"
"v_accvgpr_read_b32 v248, a64\n"
"v_mfma_f32_16x16x32_bf16 v[104:107], v[220:223], a[152:155], v[104:107]\n"
"ds_read_b128 v[216:219], v3 offset:15360\n"
"v_accvgpr_read_b32 v249, a65\n"
"v_mfma_f32_16x16x32_bf16 v[104:107], v[224:227], a[168:171], v[104:107]\n"
"v_accvgpr_read_b32 v250, a66\n"
"v_accvgpr_read_b32 v251, a67\n"
"v_mfma_f32_16x16x32_bf16 v[104:107], v[228:231], a[184:187], v[104:107]\n"
"v_mul_f32 v252, v252, v244\n"
"v_mfma_f32_16x16x32_bf16 v[108:111], v[220:223], a[156:159], v[108:111]\n"
"v_mul_f32 v253, v253, v244\n"
"v_mfma_f32_16x16x32_bf16 v[108:111], v[224:227], a[172:175], v[108:111]\n"
"s_waitcnt lgkmcnt(3)\n"
"v_mul_f32 v254, v254, v244\n"
"v_mfma_f32_16x16x32_bf16 v[108:111], v[228:231], a[188:191], v[108:111]\n"
"buffer_load_dwordx4 a[32:35], v1, s[40:43], s84 offen sc1\n"
"v_mfma_f32_16x16x32_bf16 v[112:115], v[232:235], a[144:147], v[112:115]\n"
"ds_read_b128 v[220:223], v3 offset:16384\n"
"v_mul_f32 v255, v255, v244\n"
"v_mfma_f32_16x16x32_bf16 v[112:115], v[236:239], a[160:163], v[112:115]\n"
"v_mul_f32 v248, v248, v244\n"
"v_mul_f32 v249, v249, v244\n"
"v_mfma_f32_16x16x32_bf16 v[112:115], v[240:243], a[176:179], v[112:115]\n"
"v_mul_f32 v250, v250, v244\n"
"v_mul_f32 v251, v251, v244\n"
"v_mfma_f32_16x16x32_bf16 v[116:119], v[232:235], a[148:151], v[116:119]\n"
"ds_read_b128 v[224:227], v3 offset:17408\n"
"v_cvt_pk_bf16_f32 v248, v248, v249\n"
"v_mfma_f32_16x16x32_bf16 v[116:119], v[236:239], a[164:167], v[116:119]\n"
"buffer_load_dwordx4 a[36:39], v1, s[40:43], s84 offen offset:1024 sc1\n"
"v_mfma_f32_16x16x32_bf16 v[116:119], v[240:243], a[180:183], v[116:119]\n"
"v_cvt_pk_bf16_f32 v249, v250, v251\n"
"v_cvt_pk_bf16_f32 v250, v252, v253\n"
"v_mfma_f32_16x16x32_bf16 v[120:123], v[232:235], a[152:155], v[120:123]\n"
"ds_read_b128 v[228:231], v3 offset:18432\n"
"v_cvt_pk_bf16_f32 v251, v254, v255\n"
"v_mfma_f32_16x16x32_bf16 v[120:123], v[236:239], a[168:171], v[120:123]\n"
"buffer_load_dwordx4 a[40:43], v1, s[40:43], s84 offen offset:2048 sc1\n"
"v_mfma_f32_16x16x32_bf16 v[120:123], v[240:243], a[184:187], v[120:123]\n"
"buffer_store_dwordx4 v[248:251], v1, s[40:43], s85 offen sc1\n"
"v_mfma_f32_16x16x32_bf16 v[124:127], v[232:235], a[156:159], v[124:127]\n"
"v_accvgpr_read_b32 v252, a84\n"
"v_accvgpr_read_b32 v253, a85\n"
"v_mfma_f32_16x16x32_bf16 v[124:127], v[236:239], a[172:175], v[124:127]\n"
"s_waitcnt lgkmcnt(3)\n"
"buffer_load_dwordx4 a[44:47], v1, s[40:43], s84 offen offset:3072 sc1\n"
"v_mfma_f32_16x16x32_bf16 v[124:127], v[240:243], a[188:191], v[124:127]\n"
"s_add_u32 s84, s84, 4096\n"
"v_accvgpr_read_b32 v254, a86\n"
"v_accvgpr_read_b32 v255, a87\n"
"v_mfma_f32_16x16x32_bf16 v[128:131], v[208:211], a[144:147], v[128:131]\n"
"ds_read_b128 v[232:235], v3 offset:19456\n"
"v_accvgpr_read_b32 v248, a68\n"
"v_mfma_f32_16x16x32_bf16 v[128:131], v[212:215], a[160:163], v[128:131]\n"
"v_accvgpr_read_b32 v249, a69\n"
"v_accvgpr_read_b32 v250, a70\n"
"v_mfma_f32_16x16x32_bf16 v[128:131], v[216:219], a[176:179], v[128:131]\n"
"buffer_load_dwordx4 a[48:51], v1, s[40:43], s84 offen sc1\n"
"v_mfma_f32_16x16x32_bf16 v[132:135], v[208:211], a[148:151], v[132:135]\n"
"ds_read_b128 v[236:239], v3 offset:20480\n"
"v_accvgpr_read_b32 v251, a71\n"
"v_mfma_f32_16x16x32_bf16 v[132:135], v[212:215], a[164:167], v[132:135]\n"
"v_mul_f32 v252, v252, v245\n"
"v_mul_f32 v253, v253, v245\n"
"v_mfma_f32_16x16x32_bf16 v[132:135], v[216:219], a[180:183], v[132:135]\n"
"v_mul_f32 v254, v254, v245\n"
"v_mul_f32 v255, v255, v245\n"
"v_mfma_f32_16x16x32_bf16 v[136:139], v[208:211], a[152:155], v[136:139]\n"
"ds_read_b128 v[240:243], v3 offset:21504\n"
"v_mul_f32 v248, v248, v245\n"
"v_mfma_f32_16x16x32_bf16 v[136:139], v[212:215], a[168:171], v[136:139]\n"
"buffer_load_dwordx4 a[52:55], v1, s[40:43], s84 offen offset:1024 sc1\n"
"v_mfma_f32_16x16x32_bf16 v[136:139], v[216:219], a[184:187], v[136:139]\n"
"v_mul_f32 v249, v249, v245\n"
"v_mul_f32 v250, v250, v245\n"
"v_mfma_f32_16x16x32_bf16 v[140:143], v[208:211], a[156:159], v[140:143]\n"
"v_mul_f32 v251, v251, v245\n"
"v_cvt_pk_bf16_f32 v160, v248, v249\n"
"v_mfma_f32_16x16x32_bf16 v[140:143], v[212:215], a[172:175], v[140:143]\n"
"s_waitcnt vmcnt(22) lgkmcnt(3)\n"
"buffer_load_dwordx4 a[56:59], v1, s[40:43], s84 offen offset:2048 sc1\n"
"v_mfma_f32_16x16x32_bf16 v[140:143], v[216:219], a[188:191], v[140:143]\n"
"v_cvt_pk_bf16_f32 v161, v250, v251\n"
"v_cvt_pk_bf16_f32 v162, v252, v253\n"
"v_mfma_f32_16x16x32_bf16 v[16:19], v[220:223], a[192:195], v[16:19]\n"
"ds_read_b128 v[208:211], v3 offset:22528\n"
"v_cvt_pk_bf16_f32 v163, v254, v255\n"
"v_mfma_f32_16x16x32_bf16 v[16:19], v[224:227], a[208:211], v[16:19]\n"
"buffer_load_dwordx4 a[60:63], v1, s[40:43], s84 offen offset:3072 sc1\n"
"v_mfma_f32_16x16x32_bf16 v[16:19], v[228:231], a[224:227], v[16:19]\n"
"s_add_u32 s84, s84, 4096\n"
"v_accvgpr_read_b32 v252, a88\n"
"v_accvgpr_read_b32 v253, a89\n"
"v_mfma_f32_16x16x32_bf16 v[20:23], v[220:223], a[196:199], v[20:23]\n"
"ds_read_b128 v[212:215], v3 offset:23552\n"
"v_accvgpr_read_b32 v254, a90\n"
"v_mfma_f32_16x16x32_bf16 v[20:23], v[224:227], a[212:215], v[20:23]\n"
"v_accvgpr_read_b32 v255, a91\n"
"v_accvgpr_read_b32 v248, a72\n"
"v_mfma_f32_16x16x32_bf16 v[20:23], v[228:231], a[228:231], v[20:23]\n"
"v_accvgpr_read_b32 v249, a73\n"
"v_mfma_f32_16x16x32_bf16 v[24:27], v[220:223], a[200:203], v[24:27]\n"
"ds_read_b128 v[216:219], v3 offset:24576\n"
"v_accvgpr_read_b32 v250, a74\n"
"v_mfma_f32_16x16x32_bf16 v[24:27], v[224:227], a[216:219], v[24:27]\n"
"v_accvgpr_read_b32 v251, a75\n"
"v_mul_f32 v252, v252, v246\n"
"v_mfma_f32_16x16x32_bf16 v[24:27], v[228:231], a[232:235], v[24:27]\n"
"v_mul_f32 v253, v253, v246\n"
"v_mfma_f32_16x16x32_bf16 v[28:31], v[220:223], a[204:207], v[28:31]\n"
"v_mul_f32 v254, v254, v246\n"
"v_mfma_f32_16x16x32_bf16 v[28:31], v[224:227], a[220:223], v[28:31]\n"
"s_waitcnt lgkmcnt(3)\n"
"v_mul_f32 v255, v255, v246\n"
"v_mfma_f32_16x16x32_bf16 v[28:31], v[228:231], a[236:239], v[28:31]\n"
"v_mul_f32 v248, v248, v246\n"
"v_mul_f32 v249, v249, v246\n"
"v_mfma_f32_16x16x32_bf16 v[32:35], v[232:235], a[192:195], v[32:35]\n"
"ds_read_b128 v[220:223], v3 offset:25600\n"
"v_mul_f32 v250, v250, v246\n"
"v_mfma_f32_16x16x32_bf16 v[32:35], v[236:239], a[208:211], v[32:35]\n"
"v_mul_f32 v251, v251, v246\n"
"v_mfma_f32_16x16x32_bf16 v[32:35], v[240:243], a[224:227], v[32:35]\n"
"v_cvt_pk_bf16_f32 v248, v248, v249\n"
"v_mfma_f32_16x16x32_bf16 v[36:39], v[232:235], a[196:199], v[36:39]\n"
"ds_read_b128 v[224:227], v3 offset:26624\n"
"v_cvt_pk_bf16_f32 v249, v250, v251\n"
"v_mfma_f32_16x16x32_bf16 v[36:39], v[236:239], a[212:215], v[36:39]\n"
"v_cvt_pk_bf16_f32 v250, v252, v253\n"
"v_cvt_pk_bf16_f32 v251, v254, v255\n"
"v_mfma_f32_16x16x32_bf16 v[36:39], v[240:243], a[228:231], v[36:39]\n"
"s_add_u32 s82, s58, 114688\n"
"buffer_store_dwordx4 v[248:251], v1, s[40:43], s85 offen offset:2048 sc1\n"
"v_mfma_f32_16x16x32_bf16 v[40:43], v[232:235], a[200:203], v[40:43]\n"
"s_add_u32 m0, s66, 49152\n"
"ds_read_b128 v[228:231], v3 offset:27648\n"
"v_mfma_f32_16x16x32_bf16 v[40:43], v[236:239], a[216:219], v[40:43]\n"
"v_accvgpr_read_b32 v252, a92\n"
"v_mfma_f32_16x16x32_bf16 v[40:43], v[240:243], a[232:235], v[40:43]\n"
"buffer_load_dwordx4 v1, s[40:43], s82 offen sc1 lds\n"
"v_mfma_f32_16x16x32_bf16 v[44:47], v[232:235], a[204:207], v[44:47]\n"
"v_accvgpr_read_b32 v253, a93\n"
"v_accvgpr_read_b32 v254, a94\n"
"v_mfma_f32_16x16x32_bf16 v[44:47], v[236:239], a[220:223], v[44:47]\n"
"s_waitcnt lgkmcnt(3)\n"
"v_accvgpr_read_b32 v255, a95\n"
"v_accvgpr_read_b32 v248, a76\n"
"v_mfma_f32_16x16x32_bf16 v[44:47], v[240:243], a[236:239], v[44:47]\n"
"v_accvgpr_read_b32 v249, a77\n"
"v_mfma_f32_16x16x32_bf16 v[48:51], v[208:211], a[192:195], v[48:51]\n"
"ds_read_b128 v[232:235], v3 offset:28672\n"
"v_accvgpr_read_b32 v250, a78\n"
"v_mfma_f32_16x16x32_bf16 v[48:51], v[212:215], a[208:211], v[48:51]\n"
"buffer_load_dwordx4 v1, s[40:43], s82 offen offset:1024 sc1 lds\n"
"v_mfma_f32_16x16x32_bf16 v[48:51], v[216:219], a[224:227], v[48:51]\n"
"v_accvgpr_read_b32 v251, a79\n"
"v_mul_f32 v252, v252, v247\n"
"v_mfma_f32_16x16x32_bf16 v[52:55], v[208:211], a[196:199], v[52:55]\n"
"ds_read_b128 v[236:239], v3 offset:29696\n"
"v_mul_f32 v253, v253, v247\n"
"v_mfma_f32_16x16x32_bf16 v[52:55], v[212:215], a[212:215], v[52:55]\n"
"v_mul_f32 v254, v254, v247\n"
"v_mul_f32 v255, v255, v247\n"
"v_mfma_f32_16x16x32_bf16 v[52:55], v[216:219], a[228:231], v[52:55]\n"
"buffer_load_dwordx4 v1, s[40:43], s82 offen offset:2048 sc1 lds\n"
"v_mfma_f32_16x16x32_bf16 v[56:59], v[208:211], a[200:203], v[56:59]\n"
"ds_read_b128 v[240:243], v3 offset:30720\n"
"v_mul_f32 v248, v248, v247\n"
"v_mfma_f32_16x16x32_bf16 v[56:59], v[212:215], a[216:219], v[56:59]\n"
"v_mul_f32 v249, v249, v247\n"
"v_mul_f32 v250, v250, v247\n"
"v_mfma_f32_16x16x32_bf16 v[56:59], v[216:219], a[232:235], v[56:59]\n"
"v_mul_f32 v251, v251, v247\n"
"v_cvt_pk_bf16_f32 v164, v248, v249\n"
"v_mfma_f32_16x16x32_bf16 v[60:63], v[208:211], a[204:207], v[60:63]\n"
"buffer_load_dwordx4 v1, s[40:43], s82 offen offset:3072 sc1 lds\n"
"v_mfma_f32_16x16x32_bf16 v[60:63], v[212:215], a[220:223], v[60:63]\n"
"s_waitcnt lgkmcnt(3)\n"
"v_cvt_pk_bf16_f32 v165, v250, v251\n"
"v_cvt_pk_bf16_f32 v166, v252, v253\n"
"v_mfma_f32_16x16x32_bf16 v[60:63], v[216:219], a[236:239], v[60:63]\n"
"v_cvt_pk_bf16_f32 v167, v254, v255\n"
"v_mfma_f32_16x16x32_bf16 v[64:67], v[220:223], a[192:195], v[64:67]\n"
"s_add_u32 s85, s85, 4096\n"
"ds_read_b128 v[208:211], v3 offset:31744\n"
"v_accvgpr_read_b32 v252, a112\n"
"v_mfma_f32_16x16x32_bf16 v[64:67], v[224:227], a[208:211], v[64:67]\n"
"v_accvgpr_read_b32 v253, a113\n"
"v_accvgpr_read_b32 v254, a114\n"
"v_mfma_f32_16x16x32_bf16 v[64:67], v[228:231], a[224:227], v[64:67]\n"
"s_barrier\n"
"v_accvgpr_read_b32 v255, a115\n"
"v_mfma_f32_16x16x32_bf16 v[68:71], v[220:223], a[196:199], v[68:71]\n"
"ds_read_b128 v[212:215], v1\n"
"v_accvgpr_read_b32 v248, a96\n"
"v_mfma_f32_16x16x32_bf16 v[68:71], v[224:227], a[212:215], v[68:71]\n"
"v_accvgpr_read_b32 v249, a97\n"
"v_mfma_f32_16x16x32_bf16 v[68:71], v[228:231], a[228:231], v[68:71]\n"
"v_accvgpr_read_b32 v250, a98\n"
"v_accvgpr_read_b32 v251, a99\n"
"v_mfma_f32_16x16x32_bf16 v[72:75], v[220:223], a[200:203], v[72:75]\n"
"ds_read_b128 v[216:219], v1 offset:1024\n"
"v_mul_f32 v252, v252, v244\n"
"v_mfma_f32_16x16x32_bf16 v[72:75], v[224:227], a[216:219], v[72:75]\n"
"v_mul_f32 v253, v253, v244\n"
"v_mfma_f32_16x16x32_bf16 v[72:75], v[228:231], a[232:235], v[72:75]\n"
"v_mul_f32 v254, v254, v244\n"
"v_mfma_f32_16x16x32_bf16 v[76:79], v[220:223], a[204:207], v[76:79]\n"
"buffer_load_dwordx4 a[64:67], v1, s[40:43], s84 offen sc1\n"
"v_mfma_f32_16x16x32_bf16 v[76:79], v[224:227], a[220:223], v[76:79]\n"
"s_waitcnt lgkmcnt(3)\n"
"v_mul_f32 v255, v255, v244\n"
"v_mul_f32 v248, v248, v244\n"
"v_mfma_f32_16x16x32_bf16 v[76:79], v[228:231], a[236:239], v[76:79]\n"
"v_mul_f32 v249, v249, v244\n"
"v_mul_f32 v250, v250, v244\n"
"v_mfma_f32_16x16x32_bf16 v[80:83], v[232:235], a[192:195], v[80:83]\n"
"ds_read_b128 v[220:223], v1 offset:2048\n"
"v_mul_f32 v251, v251, v244\n"
"v_mfma_f32_16x16x32_bf16 v[80:83], v[236:239], a[208:211], v[80:83]\n"
"buffer_load_dwordx4 a[68:71], v1, s[40:43], s84 offen offset:1024 sc1\n"
"v_mfma_f32_16x16x32_bf16 v[80:83], v[240:243], a[224:227], v[80:83]\n"
"v_cvt_pk_bf16_f32 v248, v248, v249\n"
"v_cvt_pk_bf16_f32 v249, v250, v251\n"
"v_mfma_f32_16x16x32_bf16 v[84:87], v[232:235], a[196:199], v[84:87]\n"
"ds_read_b128 v[224:227], v1 offset:3072\n"
"v_cvt_pk_bf16_f32 v250, v252, v253\n"
"v_mfma_f32_16x16x32_bf16 v[84:87], v[236:239], a[212:215], v[84:87]\n"
"v_cvt_pk_bf16_f32 v251, v254, v255\n"
"v_mfma_f32_16x16x32_bf16 v[84:87], v[240:243], a[228:231], v[84:87]\n"
"buffer_load_dwordx4 a[72:75], v1, s[40:43], s84 offen offset:2048 sc1\n"
"v_mfma_f32_16x16x32_bf16 v[88:91], v[232:235], a[200:203], v[88:91]\n"
"buffer_store_dwordx4 v[248:251], v1, s[40:43], s85 offen sc1\n"
"v_mfma_f32_16x16x32_bf16 v[88:91], v[236:239], a[216:219], v[88:91]\n"
"ds_read_b128 v[228:231], v1 offset:4096\n"
"v_accvgpr_read_b32 v252, a116\n"
"v_mfma_f32_16x16x32_bf16 v[88:91], v[240:243], a[232:235], v[88:91]\n"
"v_accvgpr_read_b32 v253, a117\n"
"v_accvgpr_read_b32 v254, a118\n"
"v_mfma_f32_16x16x32_bf16 v[92:95], v[232:235], a[204:207], v[92:95]\n"
"buffer_load_dwordx4 a[76:79], v1, s[40:43], s84 offen offset:3072 sc1\n"
"v_mfma_f32_16x16x32_bf16 v[92:95], v[236:239], a[220:223], v[92:95]\n"
"s_waitcnt lgkmcnt(3)\n"
"v_accvgpr_read_b32 v255, a119\n"
"v_accvgpr_read_b32 v248, a100\n"
"v_mfma_f32_16x16x32_bf16 v[92:95], v[240:243], a[236:239], v[92:95]\n"
"s_add_u32 s84, s84, 4096\n"
"v_accvgpr_read_b32 v249, a101\n"
"v_accvgpr_read_b32 v250, a102\n"
"v_mfma_f32_16x16x32_bf16 v[96:99], v[208:211], a[192:195], v[96:99]\n"
"ds_read_b128 v[232:235], v1 offset:5120\n"
"v_accvgpr_read_b32 v251, a103\n"
"v_mfma_f32_16x16x32_bf16 v[96:99], v[212:215], a[208:211], v[96:99]\n"
"buffer_load_dwordx4 a[80:83], v1, s[40:43], s84 offen sc1\n"
"v_mfma_f32_16x16x32_bf16 v[96:99], v[216:219], a[224:227], v[96:99]\n"
"v_mul_f32 v252, v252, v245\n"
"v_mul_f32 v253, v253, v245\n"
"v_mfma_f32_16x16x32_bf16 v[100:103], v[208:211], a[196:199], v[100:103]\n"
"ds_read_b128 v[236:239], v1 offset:6144\n"
"v_mul_f32 v254, v254, v245\n"
"v_mfma_f32_16x16x32_bf16 v[100:103], v[212:215], a[212:215], v[100:103]\n"
"buffer_load_dwordx4 a[84:87], v1, s[40:43], s84 offen offset:1024 sc1\n"
"v_mfma_f32_16x16x32_bf16 v[100:103], v[216:219], a[228:231], v[100:103]\n"
"v_mul_f32 v255, v255, v245\n"
"v_mul_f32 v248, v248, v245\n"
"v_mfma_f32_16x16x32_bf16 v[104:107], v[208:211], a[200:203], v[104:107]\n"
"ds_read_b128 v[240:243], v1 offset:7168\n"
"v_mul_f32 v249, v249, v245\n"
"v_mfma_f32_16x16x32_bf16 v[104:107], v[212:215], a[216:219], v[104:107]\n"
"v_mul_f32 v250, v250, v245\n"
"v_mul_f32 v251, v251, v245\n"
"v_mfma_f32_16x16x32_bf16 v[104:107], v[216:219], a[232:235], v[104:107]\n"
"buffer_load_dwordx4 a[88:91], v1, s[40:43], s84 offen offset:2048 sc1\n"
"v_mfma_f32_16x16x32_bf16 v[108:111], v[208:211], a[204:207], v[108:111]\n"
"v_cvt_pk_bf16_f32 v168, v248, v249\n"
"v_cvt_pk_bf16_f32 v169, v250, v251\n"
"v_mfma_f32_16x16x32_bf16 v[108:111], v[212:215], a[220:223], v[108:111]\n"
"s_waitcnt lgkmcnt(3)\n"
"v_cvt_pk_bf16_f32 v170, v252, v253\n"
"v_cvt_pk_bf16_f32 v171, v254, v255\n"
"v_mfma_f32_16x16x32_bf16 v[108:111], v[216:219], a[236:239], v[108:111]\n"
"buffer_load_dwordx4 a[92:95], v1, s[40:43], s84 offen offset:3072 sc1\n"
"v_mfma_f32_16x16x32_bf16 v[112:115], v[220:223], a[192:195], v[112:115]\n"
"s_add_u32 s84, s84, 4096\n"
"ds_read_b128 v[208:211], v1 offset:8192\n"
"v_accvgpr_read_b32 v252, a120\n"
"v_mfma_f32_16x16x32_bf16 v[112:115], v[224:227], a[208:211], v[112:115]\n"
"v_accvgpr_read_b32 v253, a121\n"
"v_accvgpr_read_b32 v254, a122\n"
"v_mfma_f32_16x16x32_bf16 v[112:115], v[228:231], a[224:227], v[112:115]\n"
"v_accvgpr_read_b32 v255, a123\n"
"v_accvgpr_read_b32 v248, a104\n"
"v_mfma_f32_16x16x32_bf16 v[116:119], v[220:223], a[196:199], v[116:119]\n"
"ds_read_b128 v[212:215], v1 offset:9216\n"
"v_accvgpr_read_b32 v249, a105\n"
"v_mfma_f32_16x16x32_bf16 v[116:119], v[224:227], a[212:215], v[116:119]\n"
"v_accvgpr_read_b32 v250, a106\n"
"v_accvgpr_read_b32 v251, a107\n"
"v_mfma_f32_16x16x32_bf16 v[116:119], v[228:231], a[228:231], v[116:119]\n"
"v_mul_f32 v252, v252, v246\n"
"v_mfma_f32_16x16x32_bf16 v[120:123], v[220:223], a[200:203], v[120:123]\n"
"ds_read_b128 v[216:219], v1 offset:10240\n"
"v_mul_f32 v253, v253, v246\n"
"v_mfma_f32_16x16x32_bf16 v[120:123], v[224:227], a[216:219], v[120:123]\n"
"v_mul_f32 v254, v254, v246\n"
"v_mfma_f32_16x16x32_bf16 v[120:123], v[228:231], a[232:235], v[120:123]\n"
"v_mul_f32 v255, v255, v246\n"
"v_mfma_f32_16x16x32_bf16 v[124:127], v[220:223], a[204:207], v[124:127]\n"
"v_mul_f32 v248, v248, v246\n"
"v_mul_f32 v249, v249, v246\n"
"v_mfma_f32_16x16x32_bf16 v[124:127], v[224:227], a[220:223], v[124:127]\n"
"s_waitcnt lgkmcnt(3)\n"
"v_mul_f32 v250, v250, v246\n"
"v_mfma_f32_16x16x32_bf16 v[124:127], v[228:231], a[236:239], v[124:127]\n"
"v_mul_f32 v251, v251, v246\n"
"v_mfma_f32_16x16x32_bf16 v[128:131], v[232:235], a[192:195], v[128:131]\n"
"ds_read_b128 v[220:223], v1 offset:11264\n"
"v_cvt_pk_bf16_f32 v248, v248, v249\n"
"v_mfma_f32_16x16x32_bf16 v[128:131], v[236:239], a[208:211], v[128:131]\n"
"v_cvt_pk_bf16_f32 v249, v250, v251\n"
"v_mfma_f32_16x16x32_bf16 v[128:131], v[240:243], a[224:227], v[128:131]\n"
"v_cvt_pk_bf16_f32 v250, v252, v253\n"
"v_cvt_pk_bf16_f32 v251, v254, v255\n"
"v_mfma_f32_16x16x32_bf16 v[132:135], v[232:235], a[196:199], v[132:135]\n"
"ds_read_b128 v[224:227], v1 offset:12288\n"
"v_mfma_f32_16x16x32_bf16 v[132:135], v[236:239], a[212:215], v[132:135]\n"
"buffer_store_dwordx4 v[248:251], v1, s[40:43], s85 offen offset:2048 sc1\n"
"v_mfma_f32_16x16x32_bf16 v[132:135], v[240:243], a[228:231], v[132:135]\n"
"v_accvgpr_read_b32 v252, a124\n"
"v_mfma_f32_16x16x32_bf16 v[136:139], v[232:235], a[200:203], v[136:139]\n"
"ds_read_b128 v[228:231], v1 offset:13312\n"
"v_accvgpr_read_b32 v253, a125\n"
"v_mfma_f32_16x16x32_bf16 v[136:139], v[236:239], a[216:219], v[136:139]\n"
"v_accvgpr_read_b32 v254, a126\n"
"v_accvgpr_read_b32 v255, a127\n"
"v_mfma_f32_16x16x32_bf16 v[136:139], v[240:243], a[232:235], v[136:139]\n"
"v_accvgpr_read_b32 v248, a108\n"
"v_mfma_f32_16x16x32_bf16 v[140:143], v[232:235], a[204:207], v[140:143]\n"
"v_accvgpr_read_b32 v249, a109\n"
"v_mfma_f32_16x16x32_bf16 v[140:143], v[236:239], a[220:223], v[140:143]\n"
"s_waitcnt vmcnt(25) lgkmcnt(3)\n"
"v_accvgpr_read_b32 v250, a110\n"
"v_mfma_f32_16x16x32_bf16 v[140:143], v[240:243], a[236:239], v[140:143]\n"
"v_accvgpr_read_b32 v251, a111\n"
"v_mfma_f32_16x16x32_bf16 v[16:19], v[208:211], a[240:243], v[16:19]\n"
"ds_read_b128 v[232:235], v1 offset:14336\n"
"v_mul_f32 v252, v252, v247\n"
"v_mfma_f32_16x16x32_bf16 v[16:19], v[212:215], a[0:3], v[16:19]\n"
"v_mul_f32 v253, v253, v247\n"
"v_mul_f32 v254, v254, v247\n"
"v_mfma_f32_16x16x32_bf16 v[16:19], v[216:219], a[16:19], v[16:19]\n"
"v_mul_f32 v255, v255, v247\n"
"v_mfma_f32_16x16x32_bf16 v[20:23], v[208:211], a[244:247], v[20:23]\n"
"ds_read_b128 v[236:239], v1 offset:15360\n"
"v_mul_f32 v248, v248, v247\n"
"v_mfma_f32_16x16x32_bf16 v[20:23], v[212:215], a[4:7], v[20:23]\n"
"v_mul_f32 v249, v249, v247\n"
"v_mfma_f32_16x16x32_bf16 v[20:23], v[216:219], a[20:23], v[20:23]\n"
"v_mul_f32 v250, v250, v247\n"
"v_mul_f32 v251, v251, v247\n"
"v_mfma_f32_16x16x32_bf16 v[24:27], v[208:211], a[248:251], v[24:27]\n"
"ds_read_b128 v[240:243], v1 offset:16384\n"
"v_cvt_pk_bf16_f32 v172, v248, v249\n"
"v_mfma_f32_16x16x32_bf16 v[24:27], v[212:215], a[8:11], v[24:27]\n"
"v_cvt_pk_bf16_f32 v173, v250, v251\n"
"v_mfma_f32_16x16x32_bf16 v[24:27], v[216:219], a[24:27], v[24:27]\n"
"v_cvt_pk_bf16_f32 v174, v252, v253\n"
"v_mfma_f32_16x16x32_bf16 v[28:31], v[208:211], a[252:255], v[28:31]\n"
"v_cvt_pk_bf16_f32 v175, v254, v255\n"
"v_mfma_f32_16x16x32_bf16 v[28:31], v[212:215], a[12:15], v[28:31]\n"
"s_waitcnt lgkmcnt(3)\n"
"v_mfma_f32_16x16x32_bf16 v[28:31], v[216:219], a[28:31], v[28:31]\n"
"s_add_u32 s85, s85, 4096\n"
"v_mfma_f32_16x16x32_bf16 v[32:35], v[220:223], a[240:243], v[32:35]\n"
"s_add_u32 s82, s58, 131072\n"
"ds_read_b128 v[208:211], v1 offset:17408\n"
"v_mfma_f32_16x16x32_bf16 v[32:35], v[224:227], a[0:3], v[32:35]\n"
"s_add_u32 m0, s66, 65536\n"
"v_mfma_f32_16x16x32_bf16 v[32:35], v[228:231], a[16:19], v[32:35]\n"
"buffer_load_dwordx4 v1, s[40:43], s82 offen sc1 lds\n"
"v_mfma_f32_16x16x32_bf16 v[36:39], v[220:223], a[244:247], v[36:39]\n"
"ds_read_b128 v[212:215], v1 offset:18432\n"
"v_mfma_f32_16x16x32_bf16 v[36:39], v[224:227], a[4:7], v[36:39]\n"
"buffer_load_dwordx4 v1, s[40:43], s82 offen offset:1024 sc1 lds\n"
"v_mfma_f32_16x16x32_bf16 v[36:39], v[228:231], a[20:23], v[36:39]\n"
"v_mfma_f32_16x16x32_bf16 v[40:43], v[220:223], a[248:251], v[40:43]\n"
"ds_read_b128 v[216:219], v1 offset:19456\n"
"v_mfma_f32_16x16x32_bf16 v[40:43], v[224:227], a[8:11], v[40:43]\n"
"v_mfma_f32_16x16x32_bf16 v[40:43], v[228:231], a[24:27], v[40:43]\n"
"v_mfma_f32_16x16x32_bf16 v[44:47], v[220:223], a[252:255], v[44:47]\n"
"buffer_load_dwordx4 v1, s[40:43], s82 offen offset:2048 sc1 lds\n"
"v_mfma_f32_16x16x32_bf16 v[44:47], v[224:227], a[12:15], v[44:47]\n"
"s_waitcnt lgkmcnt(3)\n"
"v_mfma_f32_16x16x32_bf16 v[44:47], v[228:231], a[28:31], v[44:47]\n"
"v_mfma_f32_16x16x32_bf16 v[48:51], v[232:235], a[240:243], v[48:51]\n"
"ds_read_b128 v[220:223], v1 offset:20480\n"
"v_mfma_f32_16x16x32_bf16 v[48:51], v[236:239], a[0:3], v[48:51]\n"
"buffer_load_dwordx4 v1, s[40:43], s82 offen offset:3072 sc1 lds\n"
"v_mfma_f32_16x16x32_bf16 v[48:51], v[240:243], a[16:19], v[48:51]\n"
"s_sub_u32 s59, s85, 16384\n"
"buffer_store_dwordx4 v[144:147], v1, s[40:43], s59 offen offset:1024 sc1\n"
"v_mfma_f32_16x16x32_bf16 v[52:55], v[232:235], a[244:247], v[52:55]\n"
"ds_read_b128 v[224:227], v1 offset:21504\n"
"v_mfma_f32_16x16x32_bf16 v[52:55], v[236:239], a[4:7], v[52:55]\n"
"v_mfma_f32_16x16x32_bf16 v[52:55], v[240:243], a[20:23], v[52:55]\n"
"v_mfma_f32_16x16x32_bf16 v[56:59], v[232:235], a[248:251], v[56:59]\n"
"ds_read_b128 v[228:231], v1 offset:22528\n"
"v_mfma_f32_16x16x32_bf16 v[56:59], v[236:239], a[8:11], v[56:59]\n"
"buffer_load_dwordx4 a[96:99], v1, s[40:43], s84 offen sc1\n"
"v_mfma_f32_16x16x32_bf16 v[56:59], v[240:243], a[24:27], v[56:59]\n"
"v_mfma_f32_16x16x32_bf16 v[60:63], v[232:235], a[252:255], v[60:63]\n"
"buffer_load_dwordx4 a[100:103], v1, s[40:43], s84 offen offset:1024 sc1\n"
"v_mfma_f32_16x16x32_bf16 v[60:63], v[236:239], a[12:15], v[60:63]\n"
"s_waitcnt lgkmcnt(3)\n"
"v_mfma_f32_16x16x32_bf16 v[60:63], v[240:243], a[28:31], v[60:63]\n"
"v_mfma_f32_16x16x32_bf16 v[64:67], v[208:211], a[240:243], v[64:67]\n"
"ds_read_b128 v[232:235], v1 offset:23552\n"
"v_mfma_f32_16x16x32_bf16 v[64:67], v[212:215], a[0:3], v[64:67]\n"
"buffer_load_dwordx4 a[104:107], v1, s[40:43], s84 offen offset:2048 sc1\n"
"v_mfma_f32_16x16x32_bf16 v[64:67], v[216:219], a[16:19], v[64:67]\n"
"v_mfma_f32_16x16x32_bf16 v[68:71], v[208:211], a[244:247], v[68:71]\n"
"ds_read_b128 v[236:239], v1 offset:24576\n"
"v_mfma_f32_16x16x32_bf16 v[68:71], v[212:215], a[4:7], v[68:71]\n"
"v_mfma_f32_16x16x32_bf16 v[68:71], v[216:219], a[20:23], v[68:71]\n"
"buffer_load_dwordx4 a[108:111], v1, s[40:43], s84 offen offset:3072 sc1\n"
"v_mfma_f32_16x16x32_bf16 v[72:75], v[208:211], a[248:251], v[72:75]\n"
"ds_read_b128 v[240:243], v1 offset:25600\n"
"v_mfma_f32_16x16x32_bf16 v[72:75], v[212:215], a[8:11], v[72:75]\n"
"v_mfma_f32_16x16x32_bf16 v[72:75], v[216:219], a[24:27], v[72:75]\n"
"v_mfma_f32_16x16x32_bf16 v[76:79], v[208:211], a[252:255], v[76:79]\n"
"s_add_u32 s84, s84, 4096\n"
"buffer_load_dwordx4 a[112:115], v1, s[40:43], s84 offen sc1\n"
"v_mfma_f32_16x16x32_bf16 v[76:79], v[212:215], a[12:15], v[76:79]\n"
"s_waitcnt vmcnt(15) lgkmcnt(3)\n"
"v_mfma_f32_16x16x32_bf16 v[76:79], v[216:219], a[28:31], v[76:79]\n"
"v_mfma_f32_16x16x32_bf16 v[16:19], v[220:223], a[32:35], v[16:19]\n"
"ds_read_b128 v[208:211], v1 offset:26624\n"
"v_mfma_f32_16x16x32_bf16 v[16:19], v[224:227], a[48:51], v[16:19]\n"
"buffer_load_dwordx4 a[116:119], v1, s[40:43], s84 offen offset:1024 sc1\n"
"v_mfma_f32_16x16x32_bf16 v[16:19], v[228:231], a[64:67], v[16:19]\n"
"v_mfma_f32_16x16x32_bf16 v[20:23], v[220:223], a[36:39], v[20:23]\n"
"ds_read_b128 v[212:215], v1 offset:27648\n"
"v_mfma_f32_16x16x32_bf16 v[20:23], v[224:227], a[52:55], v[20:23]\n"
"buffer_load_dwordx4 a[120:123], v1, s[40:43], s84 offen offset:2048 sc1\n"
"v_mfma_f32_16x16x32_bf16 v[20:23], v[228:231], a[68:71], v[20:23]\n"
"v_mfma_f32_16x16x32_bf16 v[24:27], v[220:223], a[40:43], v[24:27]\n"
"ds_read_b128 v[216:219], v1 offset:28672\n"
"v_mfma_f32_16x16x32_bf16 v[24:27], v[224:227], a[56:59], v[24:27]\n"
"v_mfma_f32_16x16x32_bf16 v[24:27], v[228:231], a[72:75], v[24:27]\n"
"buffer_load_dwordx4 a[124:127], v1, s[40:43], s84 offen offset:3072 sc1\n"
"v_mfma_f32_16x16x32_bf16 v[28:31], v[220:223], a[44:47], v[28:31]\n"
"buffer_store_dwordx4 v[148:151], v1, s[40:43], s59 offen offset:3072 sc1\n"
"v_mfma_f32_16x16x32_bf16 v[28:31], v[224:227], a[60:63], v[28:31]\n"
"s_waitcnt lgkmcnt(3)\n"
"v_mfma_f32_16x16x32_bf16 v[28:31], v[228:231], a[76:79], v[28:31]\n"
"v_mfma_f32_16x16x32_bf16 v[32:35], v[232:235], a[32:35], v[32:35]\n"
"s_add_u32 s84, s84, 4096\n"
"ds_read_b128 v[220:223], v1 offset:29696\n"
"v_mfma_f32_16x16x32_bf16 v[32:35], v[236:239], a[48:51], v[32:35]\n"
"s_add_u32 s82, s58, 147456\n"
"v_mfma_f32_16x16x32_bf16 v[32:35], v[240:243], a[64:67], v[32:35]\n"
"s_add_u32 m0, s66, 81920\n"
"v_mfma_f32_16x16x32_bf16 v[36:39], v[232:235], a[36:39], v[36:39]\n"
"ds_read_b128 v[224:227], v1 offset:30720\n"
"v_mfma_f32_16x16x32_bf16 v[36:39], v[236:239], a[52:55], v[36:39]\n"
"buffer_load_dwordx4 v1, s[40:43], s82 offen sc1 lds\n"
"v_mfma_f32_16x16x32_bf16 v[36:39], v[240:243], a[68:71], v[36:39]\n"
"buffer_load_dwordx4 v1, s[40:43], s82 offen offset:1024 sc1 lds\n"
"v_mfma_f32_16x16x32_bf16 v[40:43], v[232:235], a[40:43], v[40:43]\n"
"ds_read_b128 v[228:231], v1 offset:31744\n"
"v_mfma_f32_16x16x32_bf16 v[40:43], v[236:239], a[56:59], v[40:43]\n"
"v_mfma_f32_16x16x32_bf16 v[40:43], v[240:243], a[72:75], v[40:43]\n"
"v_mfma_f32_16x16x32_bf16 v[44:47], v[232:235], a[44:47], v[44:47]\n"
"buffer_load_dwordx4 v1, s[40:43], s82 offen offset:2048 sc1 lds\n"
"v_mfma_f32_16x16x32_bf16 v[44:47], v[236:239], a[60:63], v[44:47]\n"
"s_waitcnt lgkmcnt(3)\n"
"v_mfma_f32_16x16x32_bf16 v[44:47], v[240:243], a[76:79], v[44:47]\n"
"s_barrier\n"
"v_mfma_f32_16x16x32_bf16 v[48:51], v[208:211], a[32:35], v[48:51]\n"
"ds_read_b128 v[232:235], v1 offset:32768\n"
"v_mfma_f32_16x16x32_bf16 v[48:51], v[212:215], a[48:51], v[48:51]\n"
"buffer_load_dwordx4 v1, s[40:43], s82 offen offset:3072 sc1 lds\n"
"v_mfma_f32_16x16x32_bf16 v[48:51], v[216:219], a[64:67], v[48:51]\n"
"v_mfma_f32_16x16x32_bf16 v[52:55], v[208:211], a[36:39], v[52:55]\n"
"ds_read_b128 v[236:239], v1 offset:33792\n"
"v_mfma_f32_16x16x32_bf16 v[52:55], v[212:215], a[52:55], v[52:55]\n"
"v_mfma_f32_16x16x32_bf16 v[52:55], v[216:219], a[68:71], v[52:55]\n"
"v_mfma_f32_16x16x32_bf16 v[56:59], v[208:211], a[40:43], v[56:59]\n"
"s_add_u32 s82, s58, 163840\n"
"ds_read_b128 v[240:243], v1 offset:34816\n"
"v_mfma_f32_16x16x32_bf16 v[56:59], v[212:215], a[56:59], v[56:59]\n"
"s_add_u32 m0, s66, 98304\n"
"v_mfma_f32_16x16x32_bf16 v[56:59], v[216:219], a[72:75], v[56:59]\n"
"buffer_load_dwordx4 v1, s[40:43], s82 offen sc1 lds\n"
"v_mfma_f32_16x16x32_bf16 v[60:63], v[208:211], a[44:47], v[60:63]\n"
"v_mfma_f32_16x16x32_bf16 v[60:63], v[212:215], a[60:63], v[60:63]\n"
"s_waitcnt lgkmcnt(3)\n"
"buffer_load_dwordx4 v1, s[40:43], s82 offen offset:1024 sc1 lds\n"
"v_mfma_f32_16x16x32_bf16 v[60:63], v[216:219], a[76:79], v[60:63]\n"
"v_mfma_f32_16x16x32_bf16 v[64:67], v[220:223], a[32:35], v[64:67]\n"
"ds_read_b128 v[208:211], v1 offset:35840\n"
"v_mfma_f32_16x16x32_bf16 v[64:67], v[224:227], a[48:51], v[64:67]\n"
"v_mfma_f32_16x16x32_bf16 v[64:67], v[228:231], a[64:67], v[64:67]\n"
"v_mfma_f32_16x16x32_bf16 v[68:71], v[220:223], a[36:39], v[68:71]\n"
"s_add_u32 s59, s59, 4096\n"
"ds_read_b128 v[212:215], v1 offset:36864\n"
"v_mfma_f32_16x16x32_bf16 v[68:71], v[224:227], a[52:55], v[68:71]\n"
"buffer_load_dwordx4 v1, s[40:43], s82 offen offset:2048 sc1 lds\n"
"v_mfma_f32_16x16x32_bf16 v[68:71], v[228:231], a[68:71], v[68:71]\n"
"buffer_store_dwordx4 v[152:155], v1, s[40:43], s59 offen offset:1024 sc1\n"
"v_mfma_f32_16x16x32_bf16 v[72:75], v[220:223], a[40:43], v[72:75]\n"
"ds_read_b128 v[216:219], v1 offset:37888\n"
"v_mfma_f32_16x16x32_bf16 v[72:75], v[224:227], a[56:59], v[72:75]\n"
"buffer_load_dwordx4 v1, s[40:43], s82 offen offset:3072 sc1 lds\n"
"v_mfma_f32_16x16x32_bf16 v[72:75], v[228:231], a[72:75], v[72:75]\n"
"v_mfma_f32_16x16x32_bf16 v[76:79], v[220:223], a[44:47], v[76:79]\n"
"v_mfma_f32_16x16x32_bf16 v[76:79], v[224:227], a[60:63], v[76:79]\n"
"s_waitcnt vmcnt(10) lgkmcnt(3)\n"
"v_mfma_f32_16x16x32_bf16 v[76:79], v[228:231], a[76:79], v[76:79]\n"
"v_mfma_f32_16x16x32_bf16 v[16:19], v[232:235], a[80:83], v[16:19]\n"
"s_add_u32 s82, s58, 180224\n"
"ds_read_b128 v[220:223], v1 offset:38912\n"
"v_mfma_f32_16x16x32_bf16 v[16:19], v[236:239], a[96:99], v[16:19]\n"
"s_add_u32 m0, s66, 114688\n"
"v_mfma_f32_16x16x32_bf16 v[16:19], v[240:243], a[112:115], v[16:19]\n"
"buffer_load_dwordx4 v1, s[40:43], s82 offen sc1 lds\n"
"v_mfma_f32_16x16x32_bf16 v[20:23], v[232:235], a[84:87], v[20:23]\n"
"ds_read_b128 v[224:227], v1 offset:39936\n"
"v_mfma_f32_16x16x32_bf16 v[20:23], v[236:239], a[100:103], v[20:23]\n"
"buffer_load_dwordx4 v1, s[40:43], s82 offen offset:1024 sc1 lds\n"
"v_mfma_f32_16x16x32_bf16 v[20:23], v[240:243], a[116:119], v[20:23]\n"
"v_mfma_f32_16x16x32_bf16 v[24:27], v[232:235], a[88:91], v[24:27]\n"
"ds_read_b128 v[228:231], v1 offset:40960\n"
"v_mfma_f32_16x16x32_bf16 v[24:27], v[236:239], a[104:107], v[24:27]\n"
"v_mfma_f32_16x16x32_bf16 v[24:27], v[240:243], a[120:123], v[24:27]\n"
"v_mfma_f32_16x16x32_bf16 v[28:31], v[232:235], a[92:95], v[28:31]\n"
"buffer_load_dwordx4 v1, s[40:43], s82 offen offset:2048 sc1 lds\n"
"v_mfma_f32_16x16x32_bf16 v[28:31], v[236:239], a[108:111], v[28:31]\n"
"s_waitcnt lgkmcnt(3)\n"
"v_mfma_f32_16x16x32_bf16 v[28:31], v[240:243], a[124:127], v[28:31]\n"
"v_mfma_f32_16x16x32_bf16 v[32:35], v[208:211], a[80:83], v[32:35]\n"
"ds_read_b128 v[232:235], v1 offset:41984\n"
"v_mfma_f32_16x16x32_bf16 v[32:35], v[212:215], a[96:99], v[32:35]\n"
"buffer_load_dwordx4 v1, s[40:43], s82 offen offset:3072 sc1 lds\n"
"v_mfma_f32_16x16x32_bf16 v[32:35], v[216:219], a[112:115], v[32:35]\n"
"v_mfma_f32_16x16x32_bf16 v[36:39], v[208:211], a[84:87], v[36:39]\n"
"ds_read_b128 v[236:239], v1 offset:43008\n"
"v_mfma_f32_16x16x32_bf16 v[36:39], v[212:215], a[100:103], v[36:39]\n"
"v_mfma_f32_16x16x32_bf16 v[36:39], v[216:219], a[116:119], v[36:39]\n"
"v_mfma_f32_16x16x32_bf16 v[40:43], v[208:211], a[88:91], v[40:43]\n"
"ds_read_b128 v[240:243], v1 offset:44032\n"
"v_mfma_f32_16x16x32_bf16 v[40:43], v[212:215], a[104:107], v[40:43]\n"
"v_mfma_f32_16x16x32_bf16 v[40:43], v[216:219], a[120:123], v[40:43]\n"
"v_mfma_f32_16x16x32_bf16 v[44:47], v[208:211], a[92:95], v[44:47]\n"
"v_mfma_f32_16x16x32_bf16 v[44:47], v[212:215], a[108:111], v[44:47]\n"
"s_waitcnt lgkmcnt(3)\n"
"buffer_store_dwordx4 v[156:159], v1, s[40:43], s59 offen offset:3072 sc1\n"
"v_mfma_f32_16x16x32_bf16 v[44:47], v[216:219], a[124:127], v[44:47]\n"
"v_mfma_f32_16x16x32_bf16 v[48:51], v[220:223], a[80:83], v[48:51]\n"
"ds_read_b128 v[208:211], v1 offset:45056\n"
"v_mfma_f32_16x16x32_bf16 v[48:51], v[224:227], a[96:99], v[48:51]\n"
"v_mfma_f32_16x16x32_bf16 v[48:51], v[228:231], a[112:115], v[48:51]\n"
"v_mfma_f32_16x16x32_bf16 v[52:55], v[220:223], a[84:87], v[52:55]\n"
"ds_read_b128 v[212:215], v1 offset:46080\n"
"v_mfma_f32_16x16x32_bf16 v[52:55], v[224:227], a[100:103], v[52:55]\n"
"v_mfma_f32_16x16x32_bf16 v[52:55], v[228:231], a[116:119], v[52:55]\n"
"s_add_u32 s82, s58, 196608\n"
"v_mfma_f32_16x16x32_bf16 v[56:59], v[220:223], a[88:91], v[56:59]\n"
"s_add_u32 m0, s66, 131072\n"
"ds_read_b128 v[216:219], v1 offset:47104\n"
"v_mfma_f32_16x16x32_bf16 v[56:59], v[224:227], a[104:107], v[56:59]\n"
"buffer_load_dwordx4 v1, s[40:43], s82 offen sc1 lds\n"
"v_mfma_f32_16x16x32_bf16 v[56:59], v[228:231], a[120:123], v[56:59]\n"
"v_mfma_f32_16x16x32_bf16 v[60:63], v[220:223], a[92:95], v[60:63]\n"
"buffer_load_dwordx4 v1, s[40:43], s82 offen offset:1024 sc1 lds\n"
"v_mfma_f32_16x16x32_bf16 v[60:63], v[224:227], a[108:111], v[60:63]\n"
"s_waitcnt lgkmcnt(3)\n"
"v_mfma_f32_16x16x32_bf16 v[60:63], v[228:231], a[124:127], v[60:63]\n"
"v_mfma_f32_16x16x32_bf16 v[64:67], v[232:235], a[80:83], v[64:67]\n"
"ds_read_b128 v[220:223], v1 offset:48128\n"
"v_mfma_f32_16x16x32_bf16 v[64:67], v[236:239], a[96:99], v[64:67]\n"
"v_mfma_f32_16x16x32_bf16 v[64:67], v[240:243], a[112:115], v[64:67]\n"
"buffer_load_dwordx4 v1, s[40:43], s82 offen offset:2048 sc1 lds\n"
"v_mfma_f32_16x16x32_bf16 v[68:71], v[232:235], a[84:87], v[68:71]\n"
"ds_read_b128 v[224:227], v1 offset:49152\n"
"v_mfma_f32_16x16x32_bf16 v[68:71], v[236:239], a[100:103], v[68:71]\n"
"v_mfma_f32_16x16x32_bf16 v[68:71], v[240:243], a[116:119], v[68:71]\n"
"v_mfma_f32_16x16x32_bf16 v[72:75], v[232:235], a[88:91], v[72:75]\n"
"ds_read_b128 v[228:231], v1 offset:50176\n"
"v_mfma_f32_16x16x32_bf16 v[72:75], v[236:239], a[104:107], v[72:75]\n"
"buffer_load_dwordx4 v1, s[40:43], s82 offen offset:3072 sc1 lds\n"
"v_mfma_f32_16x16x32_bf16 v[72:75], v[240:243], a[120:123], v[72:75]\n"
"v_mfma_f32_16x16x32_bf16 v[76:79], v[232:235], a[92:95], v[76:79]\n"
"v_mfma_f32_16x16x32_bf16 v[76:79], v[236:239], a[108:111], v[76:79]\n"
"s_waitcnt lgkmcnt(3)\n"
"v_mfma_f32_16x16x32_bf16 v[76:79], v[240:243], a[124:127], v[76:79]\n"
"v_mfma_f32_16x16x32_bf16 v[80:83], v[208:211], a[240:243], v[80:83]\n"
"ds_read_b128 v[232:235], v1 offset:51200\n"
"v_mfma_f32_16x16x32_bf16 v[80:83], v[212:215], a[0:3], v[80:83]\n"
"v_mfma_f32_16x16x32_bf16 v[80:83], v[216:219], a[16:19], v[80:83]\n"
"s_add_u32 s85, s83, 0\n"
"v_mul_f32 v48, v16, v48\n"
"v_mul_f32 v49, v17, v49\n"
"v_mfma_f32_16x16x32_bf16 v[84:87], v[208:211], a[244:247], v[84:87]\n"
"ds_read_b128 v[236:239], v1 offset:52224\n"
"v_mul_f32 v50, v18, v50\n"
"v_mfma_f32_16x16x32_bf16 v[84:87], v[212:215], a[4:7], v[84:87]\n"
"v_mul_f32 v51, v19, v51\n"
"v_mfma_f32_16x16x32_bf16 v[84:87], v[216:219], a[20:23], v[84:87]\n"
"s_add_u32 s59, s59, 4096\n"
"v_mul_f32 v16, s86, v16\n"
"v_mfma_f32_16x16x32_bf16 v[88:91], v[208:211], a[248:251], v[88:91]\n"
"ds_read_b128 v[240:243], v1 offset:53248\n"
"v_mul_f32 v17, s86, v17\n"
"v_mfma_f32_16x16x32_bf16 v[88:91], v[212:215], a[8:11], v[88:91]\n"
"v_mul_f32 v18, s86, v18\n"
"v_mfma_f32_16x16x32_bf16 v[88:91], v[216:219], a[24:27], v[88:91]\n"
"buffer_store_dwordx4 v[160:163], v1, s[40:43], s59 offen offset:1024 sc1\n"
"v_mfma_f32_16x16x32_bf16 v[92:95], v[208:211], a[252:255], v[92:95]\n"
"v_mul_f32 v19, s86, v19\n"
"v_mfma_f32_16x16x32_bf16 v[92:95], v[212:215], a[12:15], v[92:95]\n"
"s_waitcnt lgkmcnt(3)\n"
"v_exp_f32 v16, v16\n"
"v_mfma_f32_16x16x32_bf16 v[92:95], v[216:219], a[28:31], v[92:95]\n"
"v_exp_f32 v17, v17\n"
"v_mfma_f32_16x16x32_bf16 v[96:99], v[220:223], a[240:243], v[96:99]\n"
"ds_read_b128 v[208:211], v1 offset:54272\n"
"v_mfma_f32_16x16x32_bf16 v[96:99], v[224:227], a[0:3], v[96:99]\n"
"v_exp_f32 v18, v18\n"
"v_mfma_f32_16x16x32_bf16 v[96:99], v[228:231], a[16:19], v[96:99]\n"
"v_exp_f32 v19, v19\n"
"v_mfma_f32_16x16x32_bf16 v[100:103], v[220:223], a[244:247], v[100:103]\n"
"ds_read_b128 v[212:215], v1 offset:55296\n"
"v_mfma_f32_16x16x32_bf16 v[100:103], v[224:227], a[4:7], v[100:103]\n"
"v_add_f32 v16, 1.0, v16\n"
"v_add_f32 v17, 1.0, v17\n"
"v_mfma_f32_16x16x32_bf16 v[100:103], v[228:231], a[20:23], v[100:103]\n"
"v_add_f32 v18, 1.0, v18\n"
"v_mfma_f32_16x16x32_bf16 v[104:107], v[220:223], a[248:251], v[104:107]\n"
"ds_read_b128 v[216:219], v1 offset:56320\n"
"v_add_f32 v19, 1.0, v19\n"
"v_mfma_f32_16x16x32_bf16 v[104:107], v[224:227], a[8:11], v[104:107]\n"
"v_rcp_f32 v16, v16\n"
"v_mfma_f32_16x16x32_bf16 v[104:107], v[228:231], a[24:27], v[104:107]\n"
"v_mfma_f32_16x16x32_bf16 v[108:111], v[220:223], a[252:255], v[108:111]\n"
"v_rcp_f32 v17, v17\n"
"v_mfma_f32_16x16x32_bf16 v[108:111], v[224:227], a[12:15], v[108:111]\n"
"s_waitcnt lgkmcnt(3)\n"
"v_mfma_f32_16x16x32_bf16 v[108:111], v[228:231], a[28:31], v[108:111]\n"
"v_rcp_f32 v18, v18\n"
"v_mfma_f32_16x16x32_bf16 v[112:115], v[232:235], a[240:243], v[112:115]\n"
"ds_read_b128 v[220:223], v1 offset:57344\n"
"v_mfma_f32_16x16x32_bf16 v[112:115], v[236:239], a[0:3], v[112:115]\n"
"v_rcp_f32 v19, v19\n"
"v_mfma_f32_16x16x32_bf16 v[112:115], v[240:243], a[16:19], v[112:115]\n"
"v_mul_f32 v48, v48, v16\n"
"v_mfma_f32_16x16x32_bf16 v[116:119], v[232:235], a[244:247], v[116:119]\n"
"ds_read_b128 v[224:227], v1 offset:58368\n"
"v_mul_f32 v49, v49, v17\n"
"v_mfma_f32_16x16x32_bf16 v[116:119], v[236:239], a[4:7], v[116:119]\n"
"v_mul_f32 v50, v50, v18\n"
"v_mfma_f32_16x16x32_bf16 v[116:119], v[240:243], a[20:23], v[116:119]\n"
"v_mul_f32 v51, v51, v19\n"
"v_mfma_f32_16x16x32_bf16 v[120:123], v[232:235], a[248:251], v[120:123]\n"
"ds_read_b128 v[228:231], v1 offset:59392\n"
"v_mul_f32 v64, v32, v64\n"
"v_mfma_f32_16x16x32_bf16 v[120:123], v[236:239], a[8:11], v[120:123]\n"
"v_mul_f32 v65, v33, v65\n"
"v_mul_f32 v66, v34, v66\n"
"v_mfma_f32_16x16x32_bf16 v[120:123], v[240:243], a[24:27], v[120:123]\n"
"v_mul_f32 v67, v35, v67\n"
"v_mfma_f32_16x16x32_bf16 v[124:127], v[232:235], a[252:255], v[124:127]\n"
"v_mul_f32 v32, s86, v32\n"
"v_mfma_f32_16x16x32_bf16 v[124:127], v[236:239], a[12:15], v[124:127]\n"
"s_waitcnt lgkmcnt(3)\n"
"v_mul_f32 v33, s86, v33\n"
"v_mfma_f32_16x16x32_bf16 v[124:127], v[240:243], a[28:31], v[124:127]\n"
"v_mul_f32 v34, s86, v34\n"
"v_mfma_f32_16x16x32_bf16 v[128:131], v[208:211], a[240:243], v[128:131]\n"
"ds_read_b128 v[232:235], v1 offset:60416\n"
"v_mul_f32 v35, s86, v35\n"
"v_mfma_f32_16x16x32_bf16 v[128:131], v[212:215], a[0:3], v[128:131]\n"
"v_exp_f32 v32, v32\n"
"v_mfma_f32_16x16x32_bf16 v[128:131], v[216:219], a[16:19], v[128:131]\n"
"buffer_store_dwordx4 v[164:167], v1, s[40:43], s59 offen offset:3072 sc1\n"
"v_mfma_f32_16x16x32_bf16 v[132:135], v[208:211], a[244:247], v[132:135]\n"
"ds_read_b128 v[236:239], v1 offset:61440\n"
"v_mfma_f32_16x16x32_bf16 v[132:135], v[212:215], a[4:7], v[132:135]\n"
"v_exp_f32 v33, v33\n"
"v_mfma_f32_16x16x32_bf16 v[132:135], v[216:219], a[20:23], v[132:135]\n"
"v_exp_f32 v34, v34\n"
"v_mfma_f32_16x16x32_bf16 v[136:139], v[208:211], a[248:251], v[136:139]\n"
"ds_read_b128 v[240:243], v1 offset:62464\n"
"v_mfma_f32_16x16x32_bf16 v[136:139], v[212:215], a[8:11], v[136:139]\n"
"v_exp_f32 v35, v35\n"
"v_mfma_f32_16x16x32_bf16 v[136:139], v[216:219], a[24:27], v[136:139]\n"
"v_add_f32 v32, 1.0, v32\n"
"v_mfma_f32_16x16x32_bf16 v[140:143], v[208:211], a[252:255], v[140:143]\n"
"s_add_u32 s82, s58, 212992\n"
"v_add_f32 v33, 1.0, v33\n"
"v_add_f32 v34, 1.0, v34\n"
"v_mfma_f32_16x16x32_bf16 v[140:143], v[212:215], a[12:15], v[140:143]\n"
"s_waitcnt lgkmcnt(3)\n"
"v_add_f32 v35, 1.0, v35\n"
"v_mfma_f32_16x16x32_bf16 v[140:143], v[216:219], a[28:31], v[140:143]\n"
"s_add_u32 m0, s66, 147456\n"
"v_rcp_f32 v32, v32\n"
"v_mfma_f32_16x16x32_bf16 v[80:83], v[220:223], a[32:35], v[80:83]\n"
"ds_read_b128 v[208:211], v1 offset:63488\n"
"v_mfma_f32_16x16x32_bf16 v[80:83], v[224:227], a[48:51], v[80:83]\n"
"buffer_load_dwordx4 v1, s[40:43], s82 offen sc1 lds\n"
"v_mfma_f32_16x16x32_bf16 v[80:83], v[228:231], a[64:67], v[80:83]\n"
"v_rcp_f32 v33, v33\n"
"v_mfma_f32_16x16x32_bf16 v[84:87], v[220:223], a[36:39], v[84:87]\n"
"ds_read_b128 v[212:215], v1 offset:64512\n"
"v_mfma_f32_16x16x32_bf16 v[84:87], v[224:227], a[52:55], v[84:87]\n"
"buffer_load_dwordx4 v1, s[40:43], s82 offen offset:1024 sc1 lds\n"
"v_mfma_f32_16x16x32_bf16 v[84:87], v[228:231], a[68:71], v[84:87]\n"
"s_waitcnt vmcnt(18)\n"
"s_barrier\n"
"v_mfma_f32_16x16x32_bf16 v[88:91], v[220:223], a[40:43], v[88:91]\n"
"v_rcp_f32 v34, v34\n"
"v_mfma_f32_16x16x32_bf16 v[88:91], v[224:227], a[56:59], v[88:91]\n"
"ds_read_b128 v[216:219], v2\n"
"v_mfma_f32_16x16x32_bf16 v[88:91], v[228:231], a[72:75], v[88:91]\n"
"v_rcp_f32 v35, v35\n"
"v_mfma_f32_16x16x32_bf16 v[92:95], v[220:223], a[44:47], v[92:95]\n"
"v_mul_f32 v64, v64, v32\n"
"v_mul_f32 v65, v65, v33\n"
"v_mfma_f32_16x16x32_bf16 v[92:95], v[224:227], a[60:63], v[92:95]\n"
"s_waitcnt lgkmcnt(3)\n"
"buffer_load_dwordx4 v1, s[40:43], s82 offen offset:2048 sc1 lds\n"
"v_mfma_f32_16x16x32_bf16 v[92:95], v[228:231], a[76:79], v[92:95]\n"
"v_mul_f32 v66, v66, v34\n"
"v_mul_f32 v67, v67, v35\n"
"v_mfma_f32_16x16x32_bf16 v[96:99], v[232:235], a[32:35], v[96:99]\n"
"ds_read_b128 v[220:223], v2 offset:1024\n"
"v_cvt_pk_bf16_f32 v16, v48, v49\n"
"v_mfma_f32_16x16x32_bf16 v[96:99], v[236:239], a[48:51], v[96:99]\n"
"v_cvt_pk_bf16_f32 v17, v50, v51\n"
"v_cvt_pk_bf16_f32 v18, v64, v65\n"
"v_mfma_f32_16x16x32_bf16 v[96:99], v[240:243], a[64:67], v[96:99]\n"
"buffer_load_dwordx4 v1, s[40:43], s82 offen offset:3072 sc1 lds\n"
"v_mfma_f32_16x16x32_bf16 v[100:103], v[232:235], a[36:39], v[100:103]\n"
"ds_read_b128 v[224:227], v2 offset:2048\n"
"v_cvt_pk_bf16_f32 v19, v66, v67\n"
"v_mfma_f32_16x16x32_bf16 v[100:103], v[236:239], a[52:55], v[100:103]\n"
"buffer_store_dwordx4 v[16:19], v1, s[40:43], s85 offen sc1\n"
"v_mfma_f32_16x16x32_bf16 v[100:103], v[240:243], a[68:71], v[100:103]\n"
"v_mul_f32 v52, v20, v52\n"
"v_mul_f32 v53, v21, v53\n"
"v_mfma_f32_16x16x32_bf16 v[104:107], v[232:235], a[40:43], v[104:107]\n"
"s_add_u32 s59, s59, 4096\n"
"ds_read_b128 v[228:231], v2 offset:3072\n"
"v_mul_f32 v54, v22, v54\n"
"v_mfma_f32_16x16x32_bf16 v[104:107], v[236:239], a[56:59], v[104:107]\n"
"v_mul_f32 v55, v23, v55\n"
"v_mul_f32 v20, s86, v20\n"
"v_mfma_f32_16x16x32_bf16 v[104:107], v[240:243], a[72:75], v[104:107]\n"
"v_mul_f32 v21, s86, v21\n"
"v_mul_f32 v22, s86, v22\n"
"v_mfma_f32_16x16x32_bf16 v[108:111], v[232:235], a[44:47], v[108:111]\n"
"buffer_store_dwordx4 v[168:171], v1, s[40:43], s59 offen offset:1024 sc1\n"
"v_mfma_f32_16x16x32_bf16 v[108:111], v[236:239], a[60:63], v[108:111]\n"
"s_waitcnt lgkmcnt(3)\n"
"v_mul_f32 v23, s86, v23\n"
"v_mfma_f32_16x16x32_bf16 v[108:111], v[240:243], a[76:79], v[108:111]\n"
"v_exp_f32 v20, v20\n"
"v_mfma_f32_16x16x32_bf16 v[112:115], v[208:211], a[32:35], v[112:115]\n"
"ds_read_b128 v[232:235], v2 offset:4096\n"
"v_mfma_f32_16x16x32_bf16 v[112:115], v[212:215], a[48:51], v[112:115]\n"
"v_exp_f32 v21, v21\n"
"v_mfma_f32_16x16x32_bf16 v[112:115], v[216:219], a[64:67], v[112:115]\n"
"v_exp_f32 v22, v22\n"
"v_mfma_f32_16x16x32_bf16 v[116:119], v[208:211], a[36:39], v[116:119]\n"
"ds_read_b128 v[236:239], v2 offset:5120\n"
"v_mfma_f32_16x16x32_bf16 v[116:119], v[212:215], a[52:55], v[116:119]\n"
"v_exp_f32 v23, v23\n"
"v_mfma_f32_16x16x32_bf16 v[116:119], v[216:219], a[68:71], v[116:119]\n"
"v_add_f32 v20, 1.0, v20\n"
"v_mfma_f32_16x16x32_bf16 v[120:123], v[208:211], a[40:43], v[120:123]\n"
"ds_read_b128 v[240:243], v2 offset:6144\n"
"v_add_f32 v21, 1.0, v21\n"
"v_mfma_f32_16x16x32_bf16 v[120:123], v[212:215], a[56:59], v[120:123]\n"
"v_add_f32 v22, 1.0, v22\n"
"v_mfma_f32_16x16x32_bf16 v[120:123], v[216:219], a[72:75], v[120:123]\n"
"v_add_f32 v23, 1.0, v23\n"
"v_mfma_f32_16x16x32_bf16 v[124:127], v[208:211], a[44:47], v[124:127]\n"
"v_rcp_f32 v20, v20\n"
"v_mfma_f32_16x16x32_bf16 v[124:127], v[212:215], a[60:63], v[124:127]\n"
"s_waitcnt lgkmcnt(3)\n"
"v_rcp_f32 v21, v21\n"
"v_mfma_f32_16x16x32_bf16 v[124:127], v[216:219], a[76:79], v[124:127]\n"
"v_mfma_f32_16x16x32_bf16 v[128:131], v[220:223], a[32:35], v[128:131]\n"
"ds_read_b128 v[208:211], v2 offset:7168\n"
"v_mfma_f32_16x16x32_bf16 v[128:131], v[224:227], a[48:51], v[128:131]\n"
"v_rcp_f32 v22, v22\n"
"v_mfma_f32_16x16x32_bf16 v[128:131], v[228:231], a[64:67], v[128:131]\n"
"v_rcp_f32 v23, v23\n"
"v_mfma_f32_16x16x32_bf16 v[132:135], v[220:223], a[36:39], v[132:135]\n"
"ds_read_b128 v[212:215], v2 offset:8192\n"
"v_mfma_f32_16x16x32_bf16 v[132:135], v[224:227], a[52:55], v[132:135]\n"
"v_mul_f32 v52, v52, v20\n"
"v_mul_f32 v53, v53, v21\n"
"v_mfma_f32_16x16x32_bf16 v[132:135], v[228:231], a[68:71], v[132:135]\n"
"v_mul_f32 v54, v54, v22\n"
"v_mfma_f32_16x16x32_bf16 v[136:139], v[220:223], a[40:43], v[136:139]\n"
"ds_read_b128 v[216:219], v2 offset:9216\n"
"v_mul_f32 v55, v55, v23\n"
"v_mfma_f32_16x16x32_bf16 v[136:139], v[224:227], a[56:59], v[136:139]\n"
"v_mul_f32 v68, v36, v68\n"
"v_mfma_f32_16x16x32_bf16 v[136:139], v[228:231], a[72:75], v[136:139]\n"
"v_mul_f32 v69, v37, v69\n"
"v_mfma_f32_16x16x32_bf16 v[140:143], v[220:223], a[44:47], v[140:143]\n"
"v_mul_f32 v70, v38, v70\n"
"v_mfma_f32_16x16x32_bf16 v[140:143], v[224:227], a[60:63], v[140:143]\n"
"s_waitcnt lgkmcnt(3)\n"
"v_mul_f32 v71, v39, v71\n"
"v_mfma_f32_16x16x32_bf16 v[140:143], v[228:231], a[76:79], v[140:143]\n"
"v_mul_f32 v36, s86, v36\n"
"v_mfma_f32_16x16x32_bf16 v[80:83], v[232:235], a[80:83], v[80:83]\n"
"ds_read_b128 v[220:223], v2 offset:10240\n"
"v_mul_f32 v37, s86, v37\n"
"v_mfma_f32_16x16x32_bf16 v[80:83], v[236:239], a[96:99], v[80:83]\n"
"v_mul_f32 v38, s86, v38\n"
"v_mul_f32 v39, s86, v39\n"
"v_mfma_f32_16x16x32_bf16 v[80:83], v[240:243], a[112:115], v[80:83]\n"
"v_exp_f32 v36, v36\n"
"v_mfma_f32_16x16x32_bf16 v[84:87], v[232:235], a[84:87], v[84:87]\n"
"ds_read_b128 v[224:227], v2 offset:11264\n"
"v_mfma_f32_16x16x32_bf16 v[84:87], v[236:239], a[100:103], v[84:87]\n"
"buffer_store_dwordx4 v[172:175], v1, s[40:43], s59 offen offset:3072 sc1\n"
"v_mfma_f32_16x16x32_bf16 v[84:87], v[240:243], a[116:119], v[84:87]\n"
"v_exp_f32 v37, v37\n"
"v_mfma_f32_16x16x32_bf16 v[88:91], v[232:235], a[88:91], v[88:91]\n"
"ds_read_b128 v[228:231], v2 offset:12288\n"
"v_mfma_f32_16x16x32_bf16 v[88:91], v[236:239], a[104:107], v[88:91]\n"
"v_exp_f32 v38, v38\n"
"v_mfma_f32_16x16x32_bf16 v[88:91], v[240:243], a[120:123], v[88:91]\n"
"v_exp_f32 v39, v39\n"
"v_mfma_f32_16x16x32_bf16 v[92:95], v[232:235], a[92:95], v[92:95]\n"
"v_add_f32 v36, 1.0, v36\n"
"v_mfma_f32_16x16x32_bf16 v[92:95], v[236:239], a[108:111], v[92:95]\n"
"s_waitcnt lgkmcnt(3)\n"
"v_add_f32 v37, 1.0, v37\n"
"v_mfma_f32_16x16x32_bf16 v[92:95], v[240:243], a[124:127], v[92:95]\n"
"s_add_u32 s82, s58, 229376\n"
"v_add_f32 v38, 1.0, v38\n"
"v_mfma_f32_16x16x32_bf16 v[96:99], v[208:211], a[80:83], v[96:99]\n"
"s_add_u32 m0, s66, 0\n"
"ds_read_b128 v[232:235], v2 offset:13312\n"
"v_add_f32 v39, 1.0, v39\n"
"v_mfma_f32_16x16x32_bf16 v[96:99], v[212:215], a[96:99], v[96:99]\n"
"buffer_load_dwordx4 v1, s[40:43], s82 offen sc1 lds\n"
"v_mfma_f32_16x16x32_bf16 v[96:99], v[216:219], a[112:115], v[96:99]\n"
"v_rcp_f32 v36, v36\n"
"v_mfma_f32_16x16x32_bf16 v[100:103], v[208:211], a[84:87], v[100:103]\n"
"ds_read_b128 v[236:239], v2 offset:14336\n"
"v_mfma_f32_16x16x32_bf16 v[100:103], v[212:215], a[100:103], v[100:103]\n"
"v_rcp_f32 v37, v37\n"
"v_mfma_f32_16x16x32_bf16 v[100:103], v[216:219], a[116:119], v[100:103]\n"
"v_rcp_f32 v38, v38\n"
"v_mfma_f32_16x16x32_bf16 v[104:107], v[208:211], a[88:91], v[104:107]\n"
"ds_read_b128 v[240:243], v2 offset:15360\n"
"v_mfma_f32_16x16x32_bf16 v[104:107], v[212:215], a[104:107], v[104:107]\n"
"buffer_load_dwordx4 v1, s[40:43], s82 offen offset:1024 sc1 lds\n"
"v_mfma_f32_16x16x32_bf16 v[104:107], v[216:219], a[120:123], v[104:107]\n"
"v_rcp_f32 v39, v39\n"
"v_mfma_f32_16x16x32_bf16 v[108:111], v[208:211], a[92:95], v[108:111]\n"
"v_mul_f32 v68, v68, v36\n"
"v_mul_f32 v69, v69, v37\n"
"v_mfma_f32_16x16x32_bf16 v[108:111], v[212:215], a[108:111], v[108:111]\n"
"s_waitcnt lgkmcnt(3)\n"
"v_mul_f32 v70, v70, v38\n"
"v_mul_f32 v71, v71, v39\n"
"v_mfma_f32_16x16x32_bf16 v[108:111], v[216:219], a[124:127], v[108:111]\n"
"buffer_load_dwordx4 v1, s[40:43], s82 offen offset:2048 sc1 lds\n"
"v_mfma_f32_16x16x32_bf16 v[112:115], v[220:223], a[80:83], v[112:115]\n"
"ds_read_b128 v[208:211], v2 offset:16384\n"
"v_cvt_pk_bf16_f32 v20, v52, v53\n"
"v_mfma_f32_16x16x32_bf16 v[112:115], v[224:227], a[96:99], v[112:115]\n"
"v_cvt_pk_bf16_f32 v21, v54, v55\n"
"v_cvt_pk_bf16_f32 v22, v68, v69\n"
"v_mfma_f32_16x16x32_bf16 v[112:115], v[228:231], a[112:115], v[112:115]\n"
"buffer_load_dwordx4 v1, s[40:43], s82 offen offset:3072 sc1 lds\n"
"v_mfma_f32_16x16x32_bf16 v[116:119], v[220:223], a[84:87], v[116:119]\n"
"ds_read_b128 v[212:215], v2 offset:17408\n"
"v_cvt_pk_bf16_f32 v23, v70, v71\n"
"v_mfma_f32_16x16x32_bf16 v[116:119], v[224:227], a[100:103], v[116:119]\n"
"buffer_store_dwordx4 v[20:23], v1, s[40:43], s85 offen offset:1024 sc1\n"
"v_mfma_f32_16x16x32_bf16 v[116:119], v[228:231], a[116:119], v[116:119]\n"
"v_mul_f32 v56, v24, v56\n"
"v_mul_f32 v57, v25, v57\n"
"v_mfma_f32_16x16x32_bf16 v[120:123], v[220:223], a[88:91], v[120:123]\n"
"ds_read_b128 v[216:219], v2 offset:18432\n"
"v_mul_f32 v58, v26, v58\n"
"v_mfma_f32_16x16x32_bf16 v[120:123], v[224:227], a[104:107], v[120:123]\n"
"v_mul_f32 v59, v27, v59\n"
"v_mfma_f32_16x16x32_bf16 v[120:123], v[228:231], a[120:123], v[120:123]\n"
"s_add_u32 s59, s59, 4096\n"
"v_mul_f32 v24, s86, v24\n"
"v_mfma_f32_16x16x32_bf16 v[124:127], v[220:223], a[92:95], v[124:127]\n"
"v_mul_f32 v25, s86, v25\n"
"v_mfma_f32_16x16x32_bf16 v[124:127], v[224:227], a[108:111], v[124:127]\n"
"s_waitcnt lgkmcnt(3)\n"
"v_mul_f32 v26, s86, v26\n"
"v_mfma_f32_16x16x32_bf16 v[124:127], v[228:231], a[124:127], v[124:127]\n"
"v_mul_f32 v27, s86, v27\n"
"v_mfma_f32_16x16x32_bf16 v[128:131], v[232:235], a[80:83], v[128:131]\n"
"ds_read_b128 v[220:223], v2 offset:19456\n"
"v_mfma_f32_16x16x32_bf16 v[128:131], v[236:239], a[96:99], v[128:131]\n"
"v_exp_f32 v24, v24\n"
"v_mfma_f32_16x16x32_bf16 v[128:131], v[240:243], a[112:115], v[128:131]\n"
"v_exp_f32 v25, v25\n"
"v_mfma_f32_16x16x32_bf16 v[132:135], v[232:235], a[84:87], v[132:135]\n"
"ds_read_b128 v[224:227], v2 offset:20480\n"
"v_mfma_f32_16x16x32_bf16 v[132:135], v[236:239], a[100:103], v[132:135]\n"
"v_exp_f32 v26, v26\n"
"v_mfma_f32_16x16x32_bf16 v[132:135], v[240:243], a[116:119], v[132:135]\n"
"v_exp_f32 v27, v27\n"
"v_mfma_f32_16x16x32_bf16 v[136:139], v[232:235], a[88:91], v[136:139]\n"
"ds_read_b128 v[228:231], v2 offset:21504\n"
"v_mfma_f32_16x16x32_bf16 v[136:139], v[236:239], a[104:107], v[136:139]\n"
"v_add_f32 v24, 1.0, v24\n"
"v_add_f32 v25, 1.0, v25\n"
"v_mfma_f32_16x16x32_bf16 v[136:139], v[240:243], a[120:123], v[136:139]\n"
"v_add_f32 v26, 1.0, v26\n"
"v_mfma_f32_16x16x32_bf16 v[140:143], v[232:235], a[92:95], v[140:143]\n"
"v_add_f32 v27, 1.0, v27\n"
"v_mfma_f32_16x16x32_bf16 v[140:143], v[236:239], a[108:111], v[140:143]\n"
"s_waitcnt lgkmcnt(3)\n"
"v_rcp_f32 v24, v24\n"
"v_mfma_f32_16x16x32_bf16 v[140:143], v[240:243], a[124:127], v[140:143]\n"
"v_mfma_f32_16x16x32_bf16 v[144:147], v[208:211], v[176:179], 0\n"
"ds_read_b128 v[232:235], v2 offset:22528\n"
"v_mfma_f32_16x16x32_bf16 v[144:147], v[212:215], v[192:195], v[144:147]\n"
"v_rcp_f32 v25, v25\n"
"v_mfma_f32_16x16x32_bf16 v[144:147], v[216:219], a[128:131], v[144:147]\n"
"v_rcp_f32 v26, v26\n"
"v_mfma_f32_16x16x32_bf16 v[148:151], v[208:211], v[180:183], 0\n"
"ds_read_b128 v[236:239], v2 offset:23552\n"
"v_mfma_f32_16x16x32_bf16 v[148:151], v[212:215], v[196:199], v[148:151]\n"
"v_rcp_f32 v27, v27\n"
"v_mfma_f32_16x16x32_bf16 v[148:151], v[216:219], a[132:135], v[148:151]\n"
"v_mul_f32 v56, v56, v24\n"
"v_mfma_f32_16x16x32_bf16 v[152:155], v[208:211], v[184:187], 0\n"
"ds_read_b128 v[240:243], v2 offset:24576\n"
"v_mul_f32 v57, v57, v25\n"
"v_mfma_f32_16x16x32_bf16 v[152:155], v[212:215], v[200:203], v[152:155]\n"
"v_mul_f32 v58, v58, v26\n"
"v_mfma_f32_16x16x32_bf16 v[152:155], v[216:219], a[136:139], v[152:155]\n"
"v_mul_f32 v59, v59, v27\n"
"v_mfma_f32_16x16x32_bf16 v[156:159], v[208:211], v[188:191], 0\n"
"v_mul_f32 v72, v40, v72\n"
"v_mul_f32 v73, v41, v73\n"
"v_mfma_f32_16x16x32_bf16 v[156:159], v[212:215], v[204:207], v[156:159]\n"
"s_waitcnt lgkmcnt(3)\n"
"v_mul_f32 v74, v42, v74\n"
"v_mfma_f32_16x16x32_bf16 v[156:159], v[216:219], a[140:143], v[156:159]\n"
"v_mul_f32 v75, v43, v75\n"
"v_mfma_f32_16x16x32_bf16 v[160:163], v[220:223], v[176:179], 0\n"
"ds_read_b128 v[208:211], v2 offset:25600\n"
"v_mul_f32 v40, s86, v40\n"
"v_mfma_f32_16x16x32_bf16 v[160:163], v[224:227], v[192:195], v[160:163]\n"
"v_mul_f32 v41, s86, v41\n"
"v_mfma_f32_16x16x32_bf16 v[160:163], v[228:231], a[128:131], v[160:163]\n"
"v_mul_f32 v42, s86, v42\n"
"v_mfma_f32_16x16x32_bf16 v[164:167], v[220:223], v[180:183], 0\n"
"ds_read_b128 v[212:215], v2 offset:26624\n"
"v_mul_f32 v43, s86, v43\n"
"v_mfma_f32_16x16x32_bf16 v[164:167], v[224:227], v[196:199], v[164:167]\n"
"v_exp_f32 v40, v40\n"
"v_mfma_f32_16x16x32_bf16 v[164:167], v[228:231], a[132:135], v[164:167]\n"
"v_exp_f32 v41, v41\n"
"v_mfma_f32_16x16x32_bf16 v[168:171], v[220:223], v[184:187], 0\n"
"ds_read_b128 v[216:219], v2 offset:27648\n"
"v_mfma_f32_16x16x32_bf16 v[168:171], v[224:227], v[200:203], v[168:171]\n"
"v_exp_f32 v42, v42\n"
"v_mfma_f32_16x16x32_bf16 v[168:171], v[228:231], a[136:139], v[168:171]\n"
"v_mfma_f32_16x16x32_bf16 v[172:175], v[220:223], v[188:191], 0\n"
"v_exp_f32 v43, v43\n"
"v_mfma_f32_16x16x32_bf16 v[172:175], v[224:227], v[204:207], v[172:175]\n"
"s_waitcnt lgkmcnt(3)\n"
"v_mfma_f32_16x16x32_bf16 v[172:175], v[228:231], a[140:143], v[172:175]\n"
"v_add_f32 v40, 1.0, v40\n"
"v_mfma_f32_16x16x32_bf16 v[144:147], v[232:235], a[144:147], v[144:147]\n"
"ds_read_b128 v[220:223], v2 offset:28672\n"
"v_add_f32 v41, 1.0, v41\n"
"v_mfma_f32_16x16x32_bf16 v[144:147], v[236:239], a[160:163], v[144:147]\n"
"v_add_f32 v42, 1.0, v42\n"
"v_add_f32 v43, 1.0, v43\n"
"v_mfma_f32_16x16x32_bf16 v[144:147], v[240:243], a[176:179], v[144:147]\n"
"v_rcp_f32 v40, v40\n"
"v_mfma_f32_16x16x32_bf16 v[148:151], v[232:235], a[148:151], v[148:151]\n"
"s_add_u32 s82, s58, 245760\n"
"ds_read_b128 v[224:227], v2 offset:29696\n"
"v_mfma_f32_16x16x32_bf16 v[148:151], v[236:239], a[164:167], v[148:151]\n"
"s_add_u32 m0, s66, 16384\n"
"v_rcp_f32 v41, v41\n"
"v_mfma_f32_16x16x32_bf16 v[148:151], v[240:243], a[180:183], v[148:151]\n"
"buffer_load_dwordx4 v1, s[40:43], s82 offen sc1 lds\n"
"v_mfma_f32_16x16x32_bf16 v[152:155], v[232:235], a[152:155], v[152:155]\n"
"ds_read_b128 v[228:231], v2 offset:30720\n"
"v_mfma_f32_16x16x32_bf16 v[152:155], v[236:239], a[168:171], v[152:155]\n"
"v_rcp_f32 v42, v42\n"
"v_mfma_f32_16x16x32_bf16 v[152:155], v[240:243], a[184:187], v[152:155]\n"
"v_rcp_f32 v43, v43\n"
"v_mfma_f32_16x16x32_bf16 v[156:159], v[232:235], a[156:159], v[156:159]\n"
"buffer_load_dwordx4 v1, s[40:43], s82 offen offset:1024 sc1 lds\n"
"v_mfma_f32_16x16x32_bf16 v[156:159], v[236:239], a[172:175], v[156:159]\n"
"s_waitcnt lgkmcnt(3)\n"
"v_mul_f32 v72, v72, v40\n"
"v_mul_f32 v73, v73, v41\n"
"v_mfma_f32_16x16x32_bf16 v[156:159], v[240:243], a[188:191], v[156:159]\n"
"v_mul_f32 v74, v74, v42\n"
"v_mfma_f32_16x16x32_bf16 v[160:163], v[208:211], a[144:147], v[160:163]\n"
"ds_read_b128 v[232:235], v2 offset:31744\n"
"v_mul_f32 v75, v75, v43\n"
"v_mfma_f32_16x16x32_bf16 v[160:163], v[212:215], a[160:163], v[160:163]\n"
"v_cvt_pk_bf16_f32 v24, v56, v57\n"
"v_mfma_f32_16x16x32_bf16 v[160:163], v[216:219], a[176:179], v[160:163]\n"
"s_waitcnt vmcnt(21)\n"
"s_barrier\n"
"v_cvt_pk_bf16_f32 v25, v58, v59\n"
"v_mfma_f32_16x16x32_bf16 v[164:167], v[208:211], a[148:151], v[164:167]\n"
"ds_read_b128 v[236:239], v2 offset:32768\n"
"v_cvt_pk_bf16_f32 v26, v72, v73\n"
"v_mfma_f32_16x16x32_bf16 v[164:167], v[212:215], a[164:167], v[164:167]\n"
"buffer_load_dwordx4 v1, s[40:43], s82 offen offset:2048 sc1 lds\n"
"v_mfma_f32_16x16x32_bf16 v[164:167], v[216:219], a[180:183], v[164:167]\n"
"v_cvt_pk_bf16_f32 v27, v74, v75\n"
"v_mfma_f32_16x16x32_bf16 v[168:171], v[208:211], a[152:155], v[168:171]\n"
"ds_read_b128 v[240:243], v2 offset:33792\n"
"v_mfma_f32_16x16x32_bf16 v[168:171], v[212:215], a[168:171], v[168:171]\n"
"buffer_store_dwordx4 v[24:27], v1, s[40:43], s85 offen offset:2048 sc1\n"
"v_mfma_f32_16x16x32_bf16 v[168:171], v[216:219], a[184:187], v[168:171]\n"
"buffer_load_dwordx4 v1, s[40:43], s82 offen offset:3072 sc1 lds\n"
"v_mfma_f32_16x16x32_bf16 v[172:175], v[208:211], a[156:159], v[172:175]\n"
"v_mul_f32 v60, v28, v60\n"
"v_mul_f32 v61, v29, v61\n"
"v_mfma_f32_16x16x32_bf16 v[172:175], v[212:215], a[172:175], v[172:175]\n"
"s_waitcnt lgkmcnt(3)\n"
"v_mul_f32 v62, v30, v62\n"
"v_mul_f32 v63, v31, v63\n"
"v_mfma_f32_16x16x32_bf16 v[172:175], v[216:219], a[188:191], v[172:175]\n"
"v_mul_f32 v28, s86, v28\n"
"v_mul_f32 v29, s86, v29\n"
"v_mfma_f32_16x16x32_bf16 v[144:147], v[220:223], a[192:195], v[144:147]\n"
"ds_read_b128 v[208:211], v2 offset:34816\n"
"v_mul_f32 v30, s86, v30\n"
"v_mfma_f32_16x16x32_bf16 v[144:147], v[224:227], a[208:211], v[144:147]\n"
"v_mul_f32 v31, s86, v31\n"
"v_mfma_f32_16x16x32_bf16 v[144:147], v[228:231], a[224:227], v[144:147]\n"
"v_exp_f32 v28, v28\n"
"v_mfma_f32_16x16x32_bf16 v[148:151], v[220:223], a[196:199], v[148:151]\n"
"ds_read_b128 v[212:215], v2 offset:35840\n"
"v_mfma_f32_16x16x32_bf16 v[148:151], v[224:227], a[212:215], v[148:151]\n"
"v_exp_f32 v29, v29\n"
"v_mfma_f32_16x16x32_bf16 v[148:151], v[228:231], a[228:231], v[148:151]\n"
"v_exp_f32 v30, v30\n"
"v_mfma_f32_16x16x32_bf16 v[152:155], v[220:223], a[200:203], v[152:155]\n"
"ds_read_b128 v[216:219], v2 offset:36864\n"
"v_mfma_f32_16x16x32_bf16 v[152:155], v[224:227], a[216:219], v[152:155]\n"
"v_exp_f32 v31, v31\n"
"v_mfma_f32_16x16x32_bf16 v[152:155], v[228:231], a[232:235], v[152:155]\n"
"v_add_f32 v28, 1.0, v28\n"
"v_mfma_f32_16x16x32_bf16 v[156:159], v[220:223], a[204:207], v[156:159]\n"
"v_add_f32 v29, 1.0, v29\n"
"v_mfma_f32_16x16x32_bf16 v[156:159], v[224:227], a[220:223], v[156:159]\n"
"s_waitcnt lgkmcnt(3)\n"
"v_add_f32 v30, 1.0, v30\n"
"v_mfma_f32_16x16x32_bf16 v[156:159], v[228:231], a[236:239], v[156:159]\n"
"v_add_f32 v31, 1.0, v31\n"
"v_mfma_f32_16x16x32_bf16 v[160:163], v[232:235], a[192:195], v[160:163]\n"
"ds_read_b128 v[220:223], v2 offset:37888\n"
"v_mfma_f32_16x16x32_bf16 v[160:163], v[236:239], a[208:211], v[160:163]\n"
"v_rcp_f32 v28, v28\n"
"v_mfma_f32_16x16x32_bf16 v[160:163], v[240:243], a[224:227], v[160:163]\n"
"v_rcp_f32 v29, v29\n"
"v_mfma_f32_16x16x32_bf16 v[164:167], v[232:235], a[196:199], v[164:167]\n"
"ds_read_b128 v[224:227], v2 offset:38912\n"
"v_mfma_f32_16x16x32_bf16 v[164:167], v[236:239], a[212:215], v[164:167]\n"
"v_rcp_f32 v30, v30\n"
"v_mfma_f32_16x16x32_bf16 v[164:167], v[240:243], a[228:231], v[164:167]\n"
"v_rcp_f32 v31, v31\n"
"v_mfma_f32_16x16x32_bf16 v[168:171], v[232:235], a[200:203], v[168:171]\n"
"ds_read_b128 v[228:231], v2 offset:39936\n"
"v_mfma_f32_16x16x32_bf16 v[168:171], v[236:239], a[216:219], v[168:171]\n"
"v_mul_f32 v60, v60, v28\n"
"v_mul_f32 v61, v61, v29\n"
"v_mfma_f32_16x16x32_bf16 v[168:171], v[240:243], a[232:235], v[168:171]\n"
"v_mul_f32 v62, v62, v30\n"
"v_mfma_f32_16x16x32_bf16 v[172:175], v[232:235], a[204:207], v[172:175]\n"
"v_mul_f32 v63, v63, v31\n"
"v_mfma_f32_16x16x32_bf16 v[172:175], v[236:239], a[220:223], v[172:175]\n"
"s_waitcnt lgkmcnt(3)\n"
"v_mul_f32 v76, v44, v76\n"
"v_mfma_f32_16x16x32_bf16 v[172:175], v[240:243], a[236:239], v[172:175]\n"
"v_mul_f32 v77, v45, v77\n"
"v_mfma_f32_16x16x32_bf16 v[144:147], v[208:211], a[240:243], v[144:147]\n"
"ds_read_b128 v[232:235], v2 offset:40960\n"
"v_mul_f32 v78, v46, v78\n"
"v_mfma_f32_16x16x32_bf16 v[144:147], v[212:215], a[0:3], v[144:147]\n"
"v_mul_f32 v79, v47, v79\n"
"v_mfma_f32_16x16x32_bf16 v[144:147], v[216:219], a[16:19], v[144:147]\n"
"v_mul_f32 v44, s86, v44\n"
"v_mfma_f32_16x16x32_bf16 v[148:151], v[208:211], a[244:247], v[148:151]\n"
"ds_read_b128 v[236:239], v2 offset:41984\n"
"v_mul_f32 v45, s86, v45\n"
"v_mfma_f32_16x16x32_bf16 v[148:151], v[212:215], a[4:7], v[148:151]\n"
"v_mul_f32 v46, s86, v46\n"
"v_mul_f32 v47, s86, v47\n"
"v_mfma_f32_16x16x32_bf16 v[148:151], v[216:219], a[20:23], v[148:151]\n"
"v_exp_f32 v44, v44\n"
"v_mfma_f32_16x16x32_bf16 v[152:155], v[208:211], a[248:251], v[152:155]\n"
"ds_read_b128 v[240:243], v2 offset:43008\n"
"v_mfma_f32_16x16x32_bf16 v[152:155], v[212:215], a[8:11], v[152:155]\n"
"v_exp_f32 v45, v45\n"
"v_mfma_f32_16x16x32_bf16 v[152:155], v[216:219], a[24:27], v[152:155]\n"
"v_mfma_f32_16x16x32_bf16 v[156:159], v[208:211], a[252:255], v[156:159]\n"
"v_exp_f32 v46, v46\n"
"v_mfma_f32_16x16x32_bf16 v[156:159], v[212:215], a[12:15], v[156:159]\n"
"s_waitcnt lgkmcnt(3)\n"
"v_exp_f32 v47, v47\n"
"v_mfma_f32_16x16x32_bf16 v[156:159], v[216:219], a[28:31], v[156:159]\n"
"v_mfma_f32_16x16x32_bf16 v[160:163], v[220:223], a[240:243], v[160:163]\n"
"s_lshl_b32 s78, s73, 5\n"
"ds_read_b128 v[208:211], v2 offset:44032\n"
"v_add_f32 v44, 1.0, v44\n"
"v_mfma_f32_16x16x32_bf16 v[160:163], v[224:227], a[0:3], v[160:163]\n"
"s_add_u32 s78, s78, s68\n"
"v_add_f32 v45, 1.0, v45\n"
"v_mfma_f32_16x16x32_bf16 v[160:163], v[228:231], a[16:19], v[160:163]\n"
"s_lshl_b32 s78, s78, 6\n"
"v_add_f32 v46, 1.0, v46\n"
"v_mfma_f32_16x16x32_bf16 v[164:167], v[220:223], a[244:247], v[164:167]\n"
"s_add_u32 s78, s78, 79233024\n"
"ds_read_b128 v[212:215], v2 offset:45056\n"
"v_mov_b32 v8, s92\n"
"v_mfma_f32_16x16x32_bf16 v[164:167], v[224:227], a[4:7], v[164:167]\n"
"v_mov_b32 v9, s93\n"
"v_add_f32 v47, 1.0, v47\n"
"v_mfma_f32_16x16x32_bf16 v[164:167], v[228:231], a[20:23], v[164:167]\n"
"s_waitcnt vmcnt(10)\n"
"buffer_store_dwordx2 v[8:9], v14, s[40:43], s78 offen sc1\n"
"v_mfma_f32_16x16x32_bf16 v[168:171], v[220:223], a[248:251], v[168:171]\n"
"ds_read_b128 v[216:219], v2 offset:46080\n"
"v_mfma_f32_16x16x32_bf16 v[168:171], v[224:227], a[8:11], v[168:171]\n"
"v_rcp_f32 v44, v44\n"
"v_mfma_f32_16x16x32_bf16 v[168:171], v[228:231], a[24:27], v[168:171]\n"
"s_add_u32 s82, s58, 262144\n"
"v_rcp_f32 v45, v45\n"
"v_mfma_f32_16x16x32_bf16 v[172:175], v[220:223], a[252:255], v[172:175]\n"
"s_add_u32 m0, s66, 32768\n"
"v_rcp_f32 v46, v46\n"
"v_mfma_f32_16x16x32_bf16 v[172:175], v[224:227], a[12:15], v[172:175]\n"
"s_waitcnt lgkmcnt(3)\n"
"v_rcp_f32 v47, v47\n"
"v_mfma_f32_16x16x32_bf16 v[172:175], v[228:231], a[28:31], v[172:175]\n"
"buffer_load_dwordx4 v1, s[40:43], s82 offen sc1 lds\n"
"v_mfma_f32_16x16x32_bf16 v[144:147], v[232:235], a[32:35], v[144:147]\n"
"ds_read_b128 v[220:223], v2 offset:47104\n"
"v_mul_f32 v76, v76, v44\n"
"v_mfma_f32_16x16x32_bf16 v[144:147], v[236:239], a[48:51], v[144:147]\n"
"v_mul_f32 v77, v77, v45\n"
"v_mul_f32 v78, v78, v46\n"
"v_mfma_f32_16x16x32_bf16 v[144:147], v[240:243], a[64:67], v[144:147]\n"
"v_mul_f32 v79, v79, v47\n"
"v_cvt_pk_bf16_f32 v28, v60, v61\n"
"v_mfma_f32_16x16x32_bf16 v[148:151], v[232:235], a[36:39], v[148:151]\n"
"ds_read_b128 v[224:227], v2 offset:48128\n"
"v_cvt_pk_bf16_f32 v29, v62, v63\n"
"v_mfma_f32_16x16x32_bf16 v[148:151], v[236:239], a[52:55], v[148:151]\n"
"buffer_load_dwordx4 v1, s[40:43], s82 offen offset:1024 sc1 lds\n"
"v_mfma_f32_16x16x32_bf16 v[148:151], v[240:243], a[68:71], v[148:151]\n"
"v_cvt_pk_bf16_f32 v30, v76, v77\n"
"v_cvt_pk_bf16_f32 v31, v78, v79\n"
"v_mfma_f32_16x16x32_bf16 v[152:155], v[232:235], a[40:43], v[152:155]\n"
"ds_read_b128 v[228:231], v2 offset:49152\n"
"v_mfma_f32_16x16x32_bf16 v[152:155], v[236:239], a[56:59], v[152:155]\n"
"buffer_store_dwordx4 v[28:31], v1, s[40:43], s85 offen offset:3072 sc1\n"
"v_mfma_f32_16x16x32_bf16 v[152:155], v[240:243], a[72:75], v[152:155]\n"
"buffer_load_dwordx4 v1, s[40:43], s82 offen offset:2048 sc1 lds\n"
"v_mfma_f32_16x16x32_bf16 v[156:159], v[232:235], a[44:47], v[156:159]\n"
"buffer_load_dwordx4 v1, s[40:43], s82 offen offset:3072 sc1 lds\n"
"v_mfma_f32_16x16x32_bf16 v[156:159], v[236:239], a[60:63], v[156:159]\n"
"s_waitcnt lgkmcnt(3)\n"
"v_mfma_f32_16x16x32_bf16 v[156:159], v[240:243], a[76:79], v[156:159]\n"
"s_add_u32 s85, s83, 32768\n"
"v_mul_f32 v112, v80, v112\n"
"v_mul_f32 v113, v81, v113\n"
"v_mfma_f32_16x16x32_bf16 v[160:163], v[208:211], a[32:35], v[160:163]\n"
"ds_read_b128 v[232:235], v2 offset:50176\n"
"v_mul_f32 v114, v82, v114\n"
"v_mfma_f32_16x16x32_bf16 v[160:163], v[212:215], a[48:51], v[160:163]\n"
"v_mul_f32 v115, v83, v115\n"
"v_mul_f32 v80, s86, v80\n"
"v_mfma_f32_16x16x32_bf16 v[160:163], v[216:219], a[64:67], v[160:163]\n"
"v_mul_f32 v81, s86, v81\n"
"v_mfma_f32_16x16x32_bf16 v[164:167], v[208:211], a[36:39], v[164:167]\n"
"ds_read_b128 v[236:239], v2 offset:51200\n"
"v_mul_f32 v82, s86, v82\n"
"v_mfma_f32_16x16x32_bf16 v[164:167], v[212:215], a[52:55], v[164:167]\n"
"v_mul_f32 v83, s86, v83\n"
"v_mfma_f32_16x16x32_bf16 v[164:167], v[216:219], a[68:71], v[164:167]\n"
"v_exp_f32 v80, v80\n"
"v_mfma_f32_16x16x32_bf16 v[168:171], v[208:211], a[40:43], v[168:171]\n"
"ds_read_b128 v[240:243], v2 offset:52224\n"
"v_mfma_f32_16x16x32_bf16 v[168:171], v[212:215], a[56:59], v[168:171]\n"
"v_exp_f32 v81, v81\n"
"v_mfma_f32_16x16x32_bf16 v[168:171], v[216:219], a[72:75], v[168:171]\n"
"v_exp_f32 v82, v82\n"
"v_mfma_f32_16x16x32_bf16 v[172:175], v[208:211], a[44:47], v[172:175]\n"
"v_mfma_f32_16x16x32_bf16 v[172:175], v[212:215], a[60:63], v[172:175]\n"
"s_waitcnt lgkmcnt(3)\n"
"v_exp_f32 v83, v83\n"
"v_mfma_f32_16x16x32_bf16 v[172:175], v[216:219], a[76:79], v[172:175]\n"
"v_mfma_f32_16x16x32_bf16 v[144:147], v[220:223], a[80:83], v[144:147]\n"
"ds_read_b128 v[208:211], v2 offset:53248\n"
"v_add_f32 v80, 1.0, v80\n"
"v_mfma_f32_16x16x32_bf16 v[144:147], v[224:227], a[96:99], v[144:147]\n"
"v_add_f32 v81, 1.0, v81\n"
"v_add_f32 v82, 1.0, v82\n"
"v_mfma_f32_16x16x32_bf16 v[144:147], v[228:231], a[112:115], v[144:147]\n"
"v_add_f32 v83, 1.0, v83\n"
"v_mfma_f32_16x16x32_bf16 v[148:151], v[220:223], a[84:87], v[148:151]\n"
"ds_read_b128 v[212:215], v2 offset:54272\n"
"v_mfma_f32_16x16x32_bf16 v[148:151], v[224:227], a[100:103], v[148:151]\n"
"v_rcp_f32 v80, v80\n"
"v_mfma_f32_16x16x32_bf16 v[148:151], v[228:231], a[116:119], v[148:151]\n"
"v_rcp_f32 v81, v81\n"
"v_mfma_f32_16x16x32_bf16 v[152:155], v[220:223], a[88:91], v[152:155]\n"
"ds_read_b128 v[216:219], v2 offset:55296\n"
"v_mfma_f32_16x16x32_bf16 v[152:155], v[224:227], a[104:107], v[152:155]\n"
"buffer_load_dword v5, v1, s[16:19], s96 offen\n"
"v_mfma_f32_16x16x32_bf16 v[152:155], v[228:231], a[120:123], v[152:155]\n"
"v_rcp_f32 v82, v82\n"
"v_mfma_f32_16x16x32_bf16 v[156:159], v[220:223], a[92:95], v[156:159]\n"
"v_rcp_f32 v83, v83\n"
"v_mfma_f32_16x16x32_bf16 v[156:159], v[224:227], a[108:111], v[156:159]\n"
"s_waitcnt lgkmcnt(3)\n"
"v_mul_f32 v112, v112, v80\n"
"v_mfma_f32_16x16x32_bf16 v[156:159], v[228:231], a[124:127], v[156:159]\n"
"v_mul_f32 v113, v113, v81\n"
"v_mfma_f32_16x16x32_bf16 v[160:163], v[232:235], a[80:83], v[160:163]\n"
"ds_read_b128 v[220:223], v2 offset:56320\n"
"v_mul_f32 v114, v114, v82\n"
"v_mfma_f32_16x16x32_bf16 v[160:163], v[236:239], a[96:99], v[160:163]\n"
"v_mul_f32 v115, v115, v83\n"
"v_mfma_f32_16x16x32_bf16 v[160:163], v[240:243], a[112:115], v[160:163]\n"
"v_mul_f32 v128, v96, v128\n"
"v_mul_f32 v129, v97, v129\n"
"v_mfma_f32_16x16x32_bf16 v[164:167], v[232:235], a[84:87], v[164:167]\n"
"ds_read_b128 v[224:227], v2 offset:57344\n"
"v_mul_f32 v130, v98, v130\n"
"v_mfma_f32_16x16x32_bf16 v[164:167], v[236:239], a[100:103], v[164:167]\n"
"v_mul_f32 v131, v99, v131\n"
"v_mfma_f32_16x16x32_bf16 v[164:167], v[240:243], a[116:119], v[164:167]\n"
"v_mul_f32 v96, s86, v96\n"
"v_mfma_f32_16x16x32_bf16 v[168:171], v[232:235], a[88:91], v[168:171]\n"
"ds_read_b128 v[228:231], v2 offset:58368\n"
"v_mul_f32 v97, s86, v97\n"
"v_mfma_f32_16x16x32_bf16 v[168:171], v[236:239], a[104:107], v[168:171]\n"
"v_mul_f32 v98, s86, v98\n"
"v_mfma_f32_16x16x32_bf16 v[168:171], v[240:243], a[120:123], v[168:171]\n"
"v_mul_f32 v99, s86, v99\n"
"v_mfma_f32_16x16x32_bf16 v[172:175], v[232:235], a[92:95], v[172:175]\n"
"v_exp_f32 v96, v96\n"
"v_mfma_f32_16x16x32_bf16 v[172:175], v[236:239], a[108:111], v[172:175]\n"
"s_waitcnt lgkmcnt(3)\n"
"v_exp_f32 v97, v97\n"
"v_mfma_f32_16x16x32_bf16 v[172:175], v[240:243], a[124:127], v[172:175]\n"
"v_mfma_f32_16x16x32_bf16 v[16:19], v[208:211], v[176:179], 0\n"
"ds_read_b128 v[232:235], v2 offset:59392\n"
"v_mfma_f32_16x16x32_bf16 v[16:19], v[212:215], v[192:195], v[16:19]\n"
"v_exp_f32 v98, v98\n"
"v_mfma_f32_16x16x32_bf16 v[16:19], v[216:219], a[128:131], v[16:19]\n"
"v_exp_f32 v99, v99\n"
"v_mfma_f32_16x16x32_bf16 v[20:23], v[208:211], v[180:183], 0\n"
"ds_read_b128 v[236:239], v2 offset:60416\n"
"v_mfma_f32_16x16x32_bf16 v[20:23], v[212:215], v[196:199], v[20:23]\n"
"v_add_f32 v96, 1.0, v96\n"
"v_mfma_f32_16x16x32_bf16 v[20:23], v[216:219], a[132:135], v[20:23]\n"
"v_add_f32 v97, 1.0, v97\n"
"v_add_f32 v98, 1.0, v98\n"
"v_mfma_f32_16x16x32_bf16 v[24:27], v[208:211], v[184:187], 0\n"
"ds_read_b128 v[240:243], v2 offset:61440\n"
"v_add_f32 v99, 1.0, v99\n"
"v_mfma_f32_16x16x32_bf16 v[24:27], v[212:215], v[200:203], v[24:27]\n"
"v_rcp_f32 v96, v96\n"
"v_mfma_f32_16x16x32_bf16 v[24:27], v[216:219], a[136:139], v[24:27]\n"
"v_mfma_f32_16x16x32_bf16 v[28:31], v[208:211], v[188:191], 0\n"
"v_rcp_f32 v97, v97\n"
"v_mfma_f32_16x16x32_bf16 v[28:31], v[212:215], v[204:207], v[28:31]\n"
"s_waitcnt lgkmcnt(3)\n"
"v_mfma_f32_16x16x32_bf16 v[28:31], v[216:219], a[140:143], v[28:31]\n"
"v_rcp_f32 v98, v98\n"
"v_mfma_f32_16x16x32_bf16 v[32:35], v[220:223], v[176:179], 0\n"
"s_add_u32 s82, s58, 278528\n"
"ds_read_b128 v[208:211], v2 offset:62464\n"
"v_mfma_f32_16x16x32_bf16 v[32:35], v[224:227], v[192:195], v[32:35]\n"
"s_add_u32 m0, s66, 49152\n"
"v_rcp_f32 v99, v99\n"
"v_mfma_f32_16x16x32_bf16 v[32:35], v[228:231], a[128:131], v[32:35]\n"
"buffer_load_dwordx4 v1, s[40:43], s82 offen sc1 lds\n"
"v_mfma_f32_16x16x32_bf16 v[36:39], v[220:223], v[180:183], 0\n"
"ds_read_b128 v[212:215], v2 offset:63488\n"
"v_mul_f32 v128, v128, v96\n"
"v_mfma_f32_16x16x32_bf16 v[36:39], v[224:227], v[196:199], v[36:39]\n"
"v_mul_f32 v129, v129, v97\n"
"v_mul_f32 v130, v130, v98\n"
"v_mfma_f32_16x16x32_bf16 v[36:39], v[228:231], a[132:135], v[36:39]\n"
"v_mul_f32 v131, v131, v99\n"
"v_mfma_f32_16x16x32_bf16 v[40:43], v[220:223], v[184:187], 0\n"
"ds_read_b128 v[216:219], v2 offset:64512\n"
"v_cvt_pk_bf16_f32 v80, v112, v113\n"
"v_mfma_f32_16x16x32_bf16 v[40:43], v[224:227], v[200:203], v[40:43]\n"
"buffer_load_dwordx4 v1, s[40:43], s82 offen offset:1024 sc1 lds\n"
"v_mfma_f32_16x16x32_bf16 v[40:43], v[228:231], a[136:139], v[40:43]\n"
"v_cvt_pk_bf16_f32 v81, v114, v115\n"
"v_cvt_pk_bf16_f32 v82, v128, v129\n"
"v_mfma_f32_16x16x32_bf16 v[44:47], v[220:223], v[188:191], 0\n"
"v_cvt_pk_bf16_f32 v83, v130, v131\n"
"v_mfma_f32_16x16x32_bf16 v[44:47], v[224:227], v[204:207], v[44:47]\n"
"s_waitcnt lgkmcnt(3)\n"
"buffer_store_dwordx4 v[80:83], v1, s[40:43], s85 offen sc1\n"
"v_mfma_f32_16x16x32_bf16 v[44:47], v[228:231], a[140:143], v[44:47]\n"
"s_barrier\n"
"v_mul_f32 v116, v84, v116\n"
"v_mfma_f32_16x16x32_bf16 v[48:51], v[232:235], v[176:179], 0\n"
"ds_read_b128 v[220:223], v3\n"
"v_mul_f32 v117, v85, v117\n"
"v_mfma_f32_16x16x32_bf16 v[48:51], v[236:239], v[192:195], v[48:51]\n"
"buffer_load_dwordx4 v1, s[40:43], s82 offen offset:2048 sc1 lds\n"
"v_mfma_f32_16x16x32_bf16 v[48:51], v[240:243], a[128:131], v[48:51]\n"
"v_mul_f32 v118, v86, v118\n"
"v_mul_f32 v119, v87, v119\n"
"v_mfma_f32_16x16x32_bf16 v[52:55], v[232:235], v[180:183], 0\n"
"ds_read_b128 v[224:227], v3 offset:1024\n"
"v_mul_f32 v84, s86, v84\n"
"v_mfma_f32_16x16x32_bf16 v[52:55], v[236:239], v[196:199], v[52:55]\n"
"buffer_load_dwordx4 v1, s[40:43], s82 offen offset:3072 sc1 lds\n"
"v_mfma_f32_16x16x32_bf16 v[52:55], v[240:243], a[132:135], v[52:55]\n"
"v_mul_f32 v85, s86, v85\n"
"v_mul_f32 v86, s86, v86\n"
"v_mfma_f32_16x16x32_bf16 v[56:59], v[232:235], v[184:187], 0\n"
"s_add_u32 s82, s58, 327680\n"
"ds_read_b128 v[228:231], v3 offset:2048\n"
"v_mul_f32 v87, s86, v87\n"
"v_mfma_f32_16x16x32_bf16 v[56:59], v[236:239], v[200:203], v[56:59]\n"
"s_add_u32 m0, s66, 98304\n"
"v_exp_f32 v84, v84\n"
"v_mfma_f32_16x16x32_bf16 v[56:59], v[240:243], a[136:139], v[56:59]\n"
"s_lshl_b32 s78, s73, 5\n"
"buffer_load_dwordx4 v1, s[40:43], s82 offen sc1 lds\n"
"v_mfma_f32_16x16x32_bf16 v[60:63], v[232:235], v[188:191], 0\n"
"s_add_u32 s78, s78, s68\n"
"v_exp_f32 v85, v85\n"
"v_mfma_f32_16x16x32_bf16 v[60:63], v[236:239], v[204:207], v[60:63]\n"
"s_waitcnt lgkmcnt(3)\n"
"s_lshl_b32 s78, s78, 6\n"
"s_add_u32 s78, s78, 78970880\n"
"v_mfma_f32_16x16x32_bf16 v[60:63], v[240:243], a[140:143], v[60:63]\n"
"v_mov_b32 v8, s92\n"
"v_mov_b32 v9, s93\n"
"v_mfma_f32_16x16x32_bf16 v[64:67], v[208:211], v[176:179], 0\n"
"s_waitcnt vmcnt(9)\n"
"buffer_store_dwordx2 v[8:9], v14, s[40:43], s78 offen sc1\n"
"v_mfma_f32_16x16x32_bf16 v[64:67], v[212:215], v[192:195], v[64:67]\n"
"ds_read_b128 v[232:235], v3 offset:3072\n"
"v_mfma_f32_16x16x32_bf16 v[64:67], v[216:219], a[128:131], v[64:67]\n"
"v_exp_f32 v86, v86\n"
"v_mfma_f32_16x16x32_bf16 v[68:71], v[208:211], v[180:183], 0\n"
"v_exp_f32 v87, v87\n"
"v_mfma_f32_16x16x32_bf16 v[68:71], v[212:215], v[196:199], v[68:71]\n"
"buffer_load_dwordx4 v1, s[40:43], s82 offen offset:1024 sc1 lds\n"
"v_mfma_f32_16x16x32_bf16 v[68:71], v[216:219], a[132:135], v[68:71]\n"
"ds_read_b128 v[236:239], v3 offset:4096\n"
"v_add_f32 v84, 1.0, v84\n"
"v_mfma_f32_16x16x32_bf16 v[72:75], v[208:211], v[184:187], 0\n"
"v_add_f32 v85, 1.0, v85\n"
"v_add_f32 v86, 1.0, v86\n"
"v_mfma_f32_16x16x32_bf16 v[72:75], v[212:215], v[200:203], v[72:75]\n"
"ds_read_b128 v[240:243], v3 offset:5120\n"
"v_add_f32 v87, 1.0, v87\n"
"v_mfma_f32_16x16x32_bf16 v[72:75], v[216:219], a[136:139], v[72:75]\n"
"buffer_load_dwordx4 v1, s[40:43], s82 offen offset:2048 sc1 lds\n"
"v_mfma_f32_16x16x32_bf16 v[76:79], v[208:211], v[188:191], 0\n"
"v_rcp_f32 v84, v84\n"
"v_mfma_f32_16x16x32_bf16 v[76:79], v[212:215], v[204:207], v[76:79]\n"
"s_waitcnt lgkmcnt(3)\n"
"v_rcp_f32 v85, v85\n"
"v_mfma_f32_16x16x32_bf16 v[76:79], v[216:219], a[140:143], v[76:79]\n"
"v_rcp_f32 v86, v86\n"
"v_mfma_f32_16x16x32_bf16 v[16:19], v[220:223], a[144:147], v[16:19]\n"
"buffer_load_dwordx4 v1, s[40:43], s82 offen offset:3072 sc1 lds\n"
"v_mfma_f32_16x16x32_bf16 v[16:19], v[224:227], a[160:163], v[16:19]\n"
"ds_read_b128 v[208:211], v3 offset:6144\n"
"v_mfma_f32_16x16x32_bf16 v[16:19], v[228:231], a[176:179], v[16:19]\n"
"v_rcp_f32 v87, v87\n"
"v_mfma_f32_16x16x32_bf16 v[20:23], v[220:223], a[148:151], v[20:23]\n"
"v_mul_f32 v116, v116, v84\n"
"v_mul_f32 v117, v117, v85\n"
"v_mfma_f32_16x16x32_bf16 v[20:23], v[224:227], a[164:167], v[20:23]\n"
"ds_read_b128 v[212:215], v3 offset:7168\n"
"v_mul_f32 v118, v118, v86\n"
"v_mfma_f32_16x16x32_bf16 v[20:23], v[228:231], a[180:183], v[20:23]\n"
"v_mul_f32 v119, v119, v87\n"
"v_mul_f32 v132, v100, v132\n"
"v_mfma_f32_16x16x32_bf16 v[24:27], v[220:223], a[152:155], v[24:27]\n"
"ds_read_b128 v[216:219], v3 offset:8192\n"
"v_mul_f32 v133, v101, v133\n"
"v_mfma_f32_16x16x32_bf16 v[24:27], v[224:227], a[168:171], v[24:27]\n"
"v_mul_f32 v134, v102, v134\n"
"v_mul_f32 v135, v103, v135\n"
"v_mfma_f32_16x16x32_bf16 v[24:27], v[228:231], a[184:187], v[24:27]\n"
"v_mul_f32 v100, s86, v100\n"
"v_mul_f32 v101, s86, v101\n"
"v_mfma_f32_16x16x32_bf16 v[28:31], v[220:223], a[156:159], v[28:31]\n"
"v_mul_f32 v102, s86, v102\n"
"v_mul_f32 v103, s86, v103\n"
"v_mfma_f32_16x16x32_bf16 v[28:31], v[224:227], a[172:175], v[28:31]\n"
"s_waitcnt lgkmcnt(3)\n"
"v_exp_f32 v100, v100\n"
"v_mfma_f32_16x16x32_bf16 v[28:31], v[228:231], a[188:191], v[28:31]\n"
"v_mfma_f32_16x16x32_bf16 v[32:35], v[232:235], a[144:147], v[32:35]\n"
"ds_read_b128 v[220:223], v3 offset:9216\n"
"v_mfma_f32_16x16x32_bf16 v[32:35], v[236:239], a[160:163], v[32:35]\n"
"v_exp_f32 v101, v101\n"
"v_mfma_f32_16x16x32_bf16 v[32:35], v[240:243], a[176:179], v[32:35]\n"
"v_exp_f32 v102, v102\n"
"v_mfma_f32_16x16x32_bf16 v[36:39], v[232:235], a[148:151], v[36:39]\n"
"ds_read_b128 v[224:227], v3 offset:10240\n"
"v_mfma_f32_16x16x32_bf16 v[36:39], v[236:239], a[164:167], v[36:39]\n"
"v_exp_f32 v103, v103\n"
"v_mfma_f32_16x16x32_bf16 v[36:39], v[240:243], a[180:183], v[36:39]\n"
"v_add_f32 v100, 1.0, v100\n"
"v_mfma_f32_16x16x32_bf16 v[40:43], v[232:235], a[152:155], v[40:43]\n"
"ds_read_b128 v[228:231], v3 offset:11264\n"
"v_add_f32 v101, 1.0, v101\n"
"v_mfma_f32_16x16x32_bf16 v[40:43], v[236:239], a[168:171], v[40:43]\n"
"v_add_f32 v102, 1.0, v102\n"
"v_mfma_f32_16x16x32_bf16 v[40:43], v[240:243], a[184:187], v[40:43]\n"
"v_add_f32 v103, 1.0, v103\n"
"v_mfma_f32_16x16x32_bf16 v[44:47], v[232:235], a[156:159], v[44:47]\n"
"v_rcp_f32 v100, v100\n"
"v_mfma_f32_16x16x32_bf16 v[44:47], v[236:239], a[172:175], v[44:47]\n"
"s_waitcnt lgkmcnt(3)\n"
"v_rcp_f32 v101, v101\n"
"v_mfma_f32_16x16x32_bf16 v[44:47], v[240:243], a[188:191], v[44:47]\n"
"v_mfma_f32_16x16x32_bf16 v[48:51], v[208:211], a[144:147], v[48:51]\n"
"ds_read_b128 v[232:235], v3 offset:12288\n"
"v_mfma_f32_16x16x32_bf16 v[48:51], v[212:215], a[160:163], v[48:51]\n"
"v_rcp_f32 v102, v102\n"
"v_mfma_f32_16x16x32_bf16 v[48:51], v[216:219], a[176:179], v[48:51]\n"
"v_rcp_f32 v103, v103\n"
"v_mfma_f32_16x16x32_bf16 v[52:55], v[208:211], a[148:151], v[52:55]\n"
"ds_read_b128 v[236:239], v3 offset:13312\n"
"v_mfma_f32_16x16x32_bf16 v[52:55], v[212:215], a[164:167], v[52:55]\n"
"v_mul_f32 v132, v132, v100\n"
"v_mfma_f32_16x16x32_bf16 v[52:55], v[216:219], a[180:183], v[52:55]\n"
"s_add_u32 s82, s58, 294912\n"
"v_mul_f32 v133, v133, v101\n"
"v_mul_f32 v134, v134, v102\n"
"v_mfma_f32_16x16x32_bf16 v[56:59], v[208:211], a[152:155], v[56:59]\n"
"s_add_u32 m0, s66, 65536\n"
"ds_read_b128 v[240:243], v3 offset:14336\n"
"v_mul_f32 v135, v135, v103\n"
"v_mfma_f32_16x16x32_bf16 v[56:59], v[212:215], a[168:171], v[56:59]\n"
"buffer_load_dwordx4 v1, s[40:43], s82 offen sc1 lds\n"
"v_mfma_f32_16x16x32_bf16 v[56:59], v[216:219], a[184:187], v[56:59]\n"
"v_cvt_pk_bf16_f32 v84, v116, v117\n"
"v_cvt_pk_bf16_f32 v85, v118, v119\n"
"v_mfma_f32_16x16x32_bf16 v[60:63], v[208:211], a[156:159], v[60:63]\n"
"v_cvt_pk_bf16_f32 v86, v132, v133\n"
"v_mfma_f32_16x16x32_bf16 v[60:63], v[212:215], a[172:175], v[60:63]\n"
"s_waitcnt lgkmcnt(3)\n"
"v_cvt_pk_bf16_f32 v87, v134, v135\n"
"v_mfma_f32_16x16x32_bf16 v[60:63], v[216:219], a[188:191], v[60:63]\n"
"buffer_load_dwordx4 v1, s[40:43], s82 offen offset:1024 sc1 lds\n"
"v_mfma_f32_16x16x32_bf16 v[64:67], v[220:223], a[144:147], v[64:67]\n"
"ds_read_b128 v[208:211], v3 offset:15360\n"
"v_mfma_f32_16x16x32_bf16 v[64:67], v[224:227], a[160:163], v[64:67]\n"
"buffer_store_dwordx4 v[84:87], v1, s[40:43], s85 offen offset:1024 sc1\n"
"v_mfma_f32_16x16x32_bf16 v[64:67], v[228:231], a[176:179], v[64:67]\n"
"v_mul_f32 v120, v88, v120\n"
"v_mul_f32 v121, v89, v121\n"
"v_mfma_f32_16x16x32_bf16 v[68:71], v[220:223], a[148:151], v[68:71]\n"
"ds_read_b128 v[212:215], v3 offset:16384\n"
"v_mul_f32 v122, v90, v122\n"
"v_mfma_f32_16x16x32_bf16 v[68:71], v[224:227], a[164:167], v[68:71]\n"
"v_mul_f32 v123, v91, v123\n"
"v_mul_f32 v88, s86, v88\n"
"v_mfma_f32_16x16x32_bf16 v[68:71], v[228:231], a[180:183], v[68:71]\n"
"buffer_load_dwordx4 v1, s[40:43], s82 offen offset:2048 sc1 lds\n"
"v_mfma_f32_16x16x32_bf16 v[72:75], v[220:223], a[152:155], v[72:75]\n"
"ds_read_b128 v[216:219], v3 offset:17408\n"
"v_mul_f32 v89, s86, v89\n"
"v_mfma_f32_16x16x32_bf16 v[72:75], v[224:227], a[168:171], v[72:75]\n"
"v_mul_f32 v90, s86, v90\n"
"v_mul_f32 v91, s86, v91\n"
"v_mfma_f32_16x16x32_bf16 v[72:75], v[228:231], a[184:187], v[72:75]\n"
"buffer_load_dwordx4 v1, s[40:43], s82 offen offset:3072 sc1 lds\n"
"v_mfma_f32_16x16x32_bf16 v[76:79], v[220:223], a[156:159], v[76:79]\n"
"v_exp_f32 v88, v88\n"
"v_mfma_f32_16x16x32_bf16 v[76:79], v[224:227], a[172:175], v[76:79]\n"
"s_waitcnt lgkmcnt(3)\n"
"v_exp_f32 v89, v89\n"
"v_mfma_f32_16x16x32_bf16 v[76:79], v[228:231], a[188:191], v[76:79]\n"
"s_lshl_b32 s23, s101, 1\n"
"v_exp_f32 v90, v90\n"
"v_mfma_f32_16x16x32_bf16 v[16:19], v[232:235], a[192:195], v[16:19]\n"
"s_add_u32 s23, s23, s67\n"
"ds_read_b128 v[220:223], v3 offset:18432\n"
"v_mfma_f32_16x16x32_bf16 v[16:19], v[236:239], a[208:211], v[16:19]\n"
"s_lshl_b32 s23, s23, 11\n"
"v_exp_f32 v91, v91\n"
"v_mfma_f32_16x16x32_bf16 v[16:19], v[240:243], a[224:227], v[16:19]\n"
"s_add_u32 s23, s23, 79626240\n"
"v_and_b32 v15, 31, v0\n"
"v_lshlrev_b32 v15, 6, v15\n"
"v_mfma_f32_16x16x32_bf16 v[20:23], v[232:235], a[196:199], v[20:23]\n"
"ds_read_b128 v[224:227], v3 offset:19456\n"
"v_add_f32 v88, 1.0, v88\n"
"v_mfma_f32_16x16x32_bf16 v[20:23], v[236:239], a[212:215], v[20:23]\n"
"buffer_load_dwordx2 v[10:11], v15, s[40:43], s23 offen sc1\n"
"v_mfma_f32_16x16x32_bf16 v[20:23], v[240:243], a[228:231], v[20:23]\n"
"v_add_f32 v89, 1.0, v89\n"
"v_add_f32 v90, 1.0, v90\n"
"v_mfma_f32_16x16x32_bf16 v[24:27], v[232:235], a[200:203], v[24:27]\n"
"ds_read_b128 v[228:231], v3 offset:20480\n"
"v_add_f32 v91, 1.0, v91\n"
"v_mfma_f32_16x16x32_bf16 v[24:27], v[236:239], a[216:219], v[24:27]\n"
"v_rcp_f32 v88, v88\n"
"v_mfma_f32_16x16x32_bf16 v[24:27], v[240:243], a[232:235], v[24:27]\n"
"v_rcp_f32 v89, v89\n"
"v_mfma_f32_16x16x32_bf16 v[28:31], v[232:235], a[204:207], v[28:31]\n"
"v_mfma_f32_16x16x32_bf16 v[28:31], v[236:239], a[220:223], v[28:31]\n"
"s_waitcnt lgkmcnt(3)\n"
"v_rcp_f32 v90, v90\n"
"v_mfma_f32_16x16x32_bf16 v[28:31], v[240:243], a[236:239], v[28:31]\n"
"v_rcp_f32 v91, v91\n"
"v_mfma_f32_16x16x32_bf16 v[32:35], v[208:211], a[192:195], v[32:35]\n"
"ds_read_b128 v[232:235], v3 offset:21504\n"
"v_mfma_f32_16x16x32_bf16 v[32:35], v[212:215], a[208:211], v[32:35]\n"
"v_mul_f32 v120, v120, v88\n"
"v_mfma_f32_16x16x32_bf16 v[32:35], v[216:219], a[224:227], v[32:35]\n"
"v_mul_f32 v121, v121, v89\n"
"v_mfma_f32_16x16x32_bf16 v[36:39], v[208:211], a[196:199], v[36:39]\n"
"ds_read_b128 v[236:239], v3 offset:22528\n"
"v_mul_f32 v122, v122, v90\n"
"v_mfma_f32_16x16x32_bf16 v[36:39], v[212:215], a[212:215], v[36:39]\n"
"v_mul_f32 v123, v123, v91\n"
"v_mfma_f32_16x16x32_bf16 v[36:39], v[216:219], a[228:231], v[36:39]\n"
"v_mul_f32 v136, v104, v136\n"
"v_mul_f32 v137, v105, v137\n"
"v_mfma_f32_16x16x32_bf16 v[40:43], v[208:211], a[200:203], v[40:43]\n"
"ds_read_b128 v[240:243], v3 offset:23552\n"
"v_mul_f32 v138, v106, v138\n"
"v_mfma_f32_16x16x32_bf16 v[40:43], v[212:215], a[216:219], v[40:43]\n"
"v_mul_f32 v139, v107, v139\n"
"v_mfma_f32_16x16x32_bf16 v[40:43], v[216:219], a[232:235], v[40:43]\n"
"v_mul_f32 v104, s86, v104\n"
"v_mfma_f32_16x16x32_bf16 v[44:47], v[208:211], a[204:207], v[44:47]\n"
"v_mul_f32 v105, s86, v105\n"
"v_mfma_f32_16x16x32_bf16 v[44:47], v[212:215], a[220:223], v[44:47]\n"
"s_waitcnt lgkmcnt(3)\n"
"v_mul_f32 v106, s86, v106\n"
"v_mfma_f32_16x16x32_bf16 v[44:47], v[216:219], a[236:239], v[44:47]\n"
"v_mul_f32 v107, s86, v107\n"
"v_mfma_f32_16x16x32_bf16 v[48:51], v[220:223], a[192:195], v[48:51]\n"
"ds_read_b128 v[208:211], v3 offset:24576\n"
"v_mfma_f32_16x16x32_bf16 v[48:51], v[224:227], a[208:211], v[48:51]\n"
"v_exp_f32 v104, v104\n"
"v_mfma_f32_16x16x32_bf16 v[48:51], v[228:231], a[224:227], v[48:51]\n"
"v_exp_f32 v105, v105\n"
"v_mfma_f32_16x16x32_bf16 v[52:55], v[220:223], a[196:199], v[52:55]\n"
"ds_read_b128 v[212:215], v3 offset:25600\n"
"v_mfma_f32_16x16x32_bf16 v[52:55], v[224:227], a[212:215], v[52:55]\n"
"v_exp_f32 v106, v106\n"
"v_mfma_f32_16x16x32_bf16 v[52:55], v[228:231], a[228:231], v[52:55]\n"
"v_exp_f32 v107, v107\n"
"v_mfma_f32_16x16x32_bf16 v[56:59], v[220:223], a[200:203], v[56:59]\n"
"ds_read_b128 v[216:219], v3 offset:26624\n"
"v_mfma_f32_16x16x32_bf16 v[56:59], v[224:227], a[216:219], v[56:59]\n"
"v_add_f32 v104, 1.0, v104\n"
"v_mfma_f32_16x16x32_bf16 v[56:59], v[228:231], a[232:235], v[56:59]\n"
"v_add_f32 v105, 1.0, v105\n"
"v_add_f32 v106, 1.0, v106\n"
"v_mfma_f32_16x16x32_bf16 v[60:63], v[220:223], a[204:207], v[60:63]\n"
"v_add_f32 v107, 1.0, v107\n"
"v_mfma_f32_16x16x32_bf16 v[60:63], v[224:227], a[220:223], v[60:63]\n"
"s_waitcnt lgkmcnt(3)\n"
"v_rcp_f32 v104, v104\n"
"v_mfma_f32_16x16x32_bf16 v[60:63], v[228:231], a[236:239], v[60:63]\n"
"v_mfma_f32_16x16x32_bf16 v[64:67], v[232:235], a[192:195], v[64:67]\n"
"ds_read_b128 v[220:223], v3 offset:27648\n"
"v_mfma_f32_16x16x32_bf16 v[64:67], v[236:239], a[208:211], v[64:67]\n"
"v_rcp_f32 v105, v105\n"
"v_mfma_f32_16x16x32_bf16 v[64:67], v[240:243], a[224:227], v[64:67]\n"
"v_rcp_f32 v106, v106\n"
"v_mfma_f32_16x16x32_bf16 v[68:71], v[232:235], a[196:199], v[68:71]\n"
"ds_read_b128 v[224:227], v3 offset:28672\n"
"v_mfma_f32_16x16x32_bf16 v[68:71], v[236:239], a[212:215], v[68:71]\n"
"v_rcp_f32 v107, v107\n"
"v_mfma_f32_16x16x32_bf16 v[68:71], v[240:243], a[228:231], v[68:71]\n"
"v_mul_f32 v136, v136, v104\n"
"v_mfma_f32_16x16x32_bf16 v[72:75], v[232:235], a[200:203], v[72:75]\n"
"ds_read_b128 v[228:231], v3 offset:29696\n"
"v_mul_f32 v137, v137, v105\n"
"v_mfma_f32_16x16x32_bf16 v[72:75], v[236:239], a[216:219], v[72:75]\n"
"v_mul_f32 v138, v138, v106\n"
"v_mfma_f32_16x16x32_bf16 v[72:75], v[240:243], a[232:235], v[72:75]\n"
"v_mul_f32 v139, v139, v107\n"
"v_mfma_f32_16x16x32_bf16 v[76:79], v[232:235], a[204:207], v[76:79]\n"
"s_add_u32 s82, s58, 311296\n"
"v_cvt_pk_bf16_f32 v88, v120, v121\n"
"v_mfma_f32_16x16x32_bf16 v[76:79], v[236:239], a[220:223], v[76:79]\n"
"s_waitcnt lgkmcnt(3)\n"
"v_cvt_pk_bf16_f32 v89, v122, v123\n"
"v_cvt_pk_bf16_f32 v90, v136, v137\n"
"v_mfma_f32_16x16x32_bf16 v[76:79], v[240:243], a[236:239], v[76:79]\n"
"s_add_u32 m0, s66, 81920\n"
"v_cvt_pk_bf16_f32 v91, v138, v139\n"
"v_mfma_f32_16x16x32_bf16 v[16:19], v[208:211], a[240:243], v[16:19]\n"
"ds_read_b128 v[232:235], v3 offset:30720\n"
"v_mfma_f32_16x16x32_bf16 v[16:19], v[212:215], a[0:3], v[16:19]\n"
"buffer_load_dwordx4 v1, s[40:43], s82 offen sc1 lds\n"
"v_mfma_f32_16x16x32_bf16 v[16:19], v[216:219], a[16:19], v[16:19]\n"
"buffer_store_dwordx4 v[88:91], v1, s[40:43], s85 offen offset:2048 sc1\n"
"v_mfma_f32_16x16x32_bf16 v[20:23], v[208:211], a[244:247], v[20:23]\n"
"ds_read_b128 v[236:239], v3 offset:31744\n"
"v_mul_f32 v124, v92, v124\n"
"v_mfma_f32_16x16x32_bf16 v[20:23], v[212:215], a[4:7], v[20:23]\n"
"buffer_load_dwordx4 v1, s[40:43], s82 offen offset:1024 sc1 lds\n"
"v_mfma_f32_16x16x32_bf16 v[20:23], v[216:219], a[20:23], v[20:23]\n"
"s_barrier\n"
"v_mul_f32 v125, v93, v125\n"
"v_mfma_f32_16x16x32_bf16 v[24:27], v[208:211], a[248:251], v[24:27]\n"
"ds_read_b128 v[240:243], v1\n"
"v_mul_f32 v126, v94, v126\n"
"v_mfma_f32_16x16x32_bf16 v[24:27], v[212:215], a[8:11], v[24:27]\n"
"v_mul_f32 v127, v95, v127\n"
"v_mul_f32 v92, s86, v92\n"
"v_mfma_f32_16x16x32_bf16 v[24:27], v[216:219], a[24:27], v[24:27]\n"
"v_mul_f32 v93, s86, v93\n"
"v_mul_f32 v94, s86, v94\n"
"v_mfma_f32_16x16x32_bf16 v[28:31], v[208:211], a[252:255], v[28:31]\n"
"buffer_load_dwordx4 v1, s[40:43], s82 offen offset:2048 sc1 lds\n"
"v_mfma_f32_16x16x32_bf16 v[28:31], v[212:215], a[12:15], v[28:31]\n"
"s_waitcnt lgkmcnt(3)\n"
"v_mul_f32 v95, s86, v95\n"
"v_mfma_f32_16x16x32_bf16 v[28:31], v[216:219], a[28:31], v[28:31]\n"
"v_exp_f32 v92, v92\n"
"v_mfma_f32_16x16x32_bf16 v[32:35], v[220:223], a[240:243], v[32:35]\n"
"ds_read_b128 v[208:211], v1 offset:1024\n"
"v_mfma_f32_16x16x32_bf16 v[32:35], v[224:227], a[0:3], v[32:35]\n"
"v_exp_f32 v93, v93\n"
"v_mfma_f32_16x16x32_bf16 v[32:35], v[228:231], a[16:19], v[32:35]\n"
"buffer_load_dwordx4 v1, s[40:43], s82 offen offset:3072 sc1 lds\n"
"v_mfma_f32_16x16x32_bf16 v[36:39], v[220:223], a[244:247], v[36:39]\n"
"s_waitcnt vmcnt(5)\n"
"ds_read_b128 v[212:215], v1 offset:2048\n"
"v_cmp_ne_u64 vcc, s[94:95], v[10:11]\n"
"v_mfma_f32_16x16x32_bf16 v[36:39], v[224:227], a[4:7], v[36:39]\n"
"s_cbranch_vccnz L_slow_13_%=\n"
"L_back_14_%=:\n"
"v_exp_f32 v94, v94\n"
"v_mfma_f32_16x16x32_bf16 v[36:39], v[228:231], a[20:23], v[36:39]\n"
"s_add_u32 s82, s64, 344064\n"
"v_exp_f32 v95, v95\n"
"v_mfma_f32_16x16x32_bf16 v[40:43], v[220:223], a[248:251], v[40:43]\n"
"s_add_u32 m0, s66, 114688\n"
"ds_read_b128 v[216:219], v1 offset:3072\n"
"v_add_f32 v92, 1.0, v92\n"
"v_mfma_f32_16x16x32_bf16 v[40:43], v[224:227], a[8:11], v[40:43]\n"
"v_add_f32 v93, 1.0, v93\n"
"v_add_f32 v94, 1.0, v94\n"
"v_mfma_f32_16x16x32_bf16 v[40:43], v[228:231], a[24:27], v[40:43]\n"
"buffer_load_dwordx4 v1, s[40:43], s82 offen sc1 lds\n"
"v_mfma_f32_16x16x32_bf16 v[44:47], v[220:223], a[252:255], v[44:47]\n"
"v_add_f32 v95, 1.0, v95\n"
"v_mfma_f32_16x16x32_bf16 v[44:47], v[224:227], a[12:15], v[44:47]\n"
"s_waitcnt lgkmcnt(3)\n"
"v_rcp_f32 v92, v92\n"
"v_mfma_f32_16x16x32_bf16 v[44:47], v[228:231], a[28:31], v[44:47]\n"
"v_rcp_f32 v93, v93\n"
"v_mfma_f32_16x16x32_bf16 v[48:51], v[232:235], a[240:243], v[48:51]\n"
"ds_read_b128 v[220:223], v1 offset:4096\n"
"v_mfma_f32_16x16x32_bf16 v[48:51], v[236:239], a[0:3], v[48:51]\n"
"buffer_load_dwordx4 v1, s[40:43], s82 offen offset:1024 sc1 lds\n"
"v_mfma_f32_16x16x32_bf16 v[48:51], v[240:243], a[16:19], v[48:51]\n"
"v_rcp_f32 v94, v94\n"
"v_mfma_f32_16x16x32_bf16 v[52:55], v[232:235], a[244:247], v[52:55]\n"
"ds_read_b128 v[224:227], v1 offset:5120\n"
"v_mfma_f32_16x16x32_bf16 v[52:55], v[236:239], a[4:7], v[52:55]\n"
"v_rcp_f32 v95, v95\n"
"v_mfma_f32_16x16x32_bf16 v[52:55], v[240:243], a[20:23], v[52:55]\n"
"v_mul_f32 v124, v124, v92\n"
"v_mul_f32 v125, v125, v93\n"
"v_mfma_f32_16x16x32_bf16 v[56:59], v[232:235], a[248:251], v[56:59]\n"
"ds_read_b128 v[228:231], v1 offset:6144\n"
"v_mul_f32 v126, v126, v94\n"
"v_mfma_f32_16x16x32_bf16 v[56:59], v[236:239], a[8:11], v[56:59]\n"
"buffer_load_dwordx4 v1, s[40:43], s82 offen offset:2048 sc1 lds\n"
"v_mfma_f32_16x16x32_bf16 v[56:59], v[240:243], a[24:27], v[56:59]\n"
"v_mul_f32 v127, v127, v95\n"
"v_mul_f32 v140, v108, v140\n"
"v_mfma_f32_16x16x32_bf16 v[60:63], v[232:235], a[252:255], v[60:63]\n"
"v_mul_f32 v141, v109, v141\n"
"v_mul_f32 v142, v110, v142\n"
"v_mfma_f32_16x16x32_bf16 v[60:63], v[236:239], a[12:15], v[60:63]\n"
"s_waitcnt lgkmcnt(3)\n"
"buffer_load_dwordx4 v1, s[40:43], s82 offen offset:3072 sc1 lds\n"
"v_mfma_f32_16x16x32_bf16 v[60:63], v[240:243], a[28:31], v[60:63]\n"
"s_add_u32 s82, s64, 360448\n"
"v_mul_f32 v143, v111, v143\n"
"v_mul_f32 v108, s86, v108\n"
"v_mfma_f32_16x16x32_bf16 v[64:67], v[208:211], a[240:243], v[64:67]\n"
"s_add_u32 m0, s66, 131072\n"
"ds_read_b128 v[232:235], v1 offset:7168\n"
"v_mul_f32 v109, s86, v109\n"
"v_mfma_f32_16x16x32_bf16 v[64:67], v[212:215], a[0:3], v[64:67]\n"
"v_mul_f32 v110, s86, v110\n"
"v_mul_f32 v111, s86, v111\n"
"v_mfma_f32_16x16x32_bf16 v[64:67], v[216:219], a[16:19], v[64:67]\n"
"buffer_load_dwordx4 v1, s[40:43], s82 offen sc1 lds\n"
"v_mfma_f32_16x16x32_bf16 v[68:71], v[208:211], a[244:247], v[68:71]\n"
"ds_read_b128 v[236:239], v1 offset:8192\n"
"v_mfma_f32_16x16x32_bf16 v[68:71], v[212:215], a[4:7], v[68:71]\n"
"v_exp_f32 v108, v108\n"
"v_mfma_f32_16x16x32_bf16 v[68:71], v[216:219], a[20:23], v[68:71]\n"
"v_exp_f32 v109, v109\n"
"v_mfma_f32_16x16x32_bf16 v[72:75], v[208:211], a[248:251], v[72:75]\n"
"ds_read_b128 v[240:243], v1 offset:9216\n"
"v_mfma_f32_16x16x32_bf16 v[72:75], v[212:215], a[8:11], v[72:75]\n"
"buffer_load_dwordx4 v1, s[40:43], s82 offen offset:1024 sc1 lds\n"
"v_mfma_f32_16x16x32_bf16 v[72:75], v[216:219], a[24:27], v[72:75]\n"
"v_exp_f32 v110, v110\n"
"v_mfma_f32_16x16x32_bf16 v[76:79], v[208:211], a[252:255], v[76:79]\n"
"v_exp_f32 v111, v111\n"
"v_mfma_f32_16x16x32_bf16 v[76:79], v[212:215], a[12:15], v[76:79]\n"
"s_waitcnt lgkmcnt(3)\n"
"v_add_f32 v108, 1.0, v108\n"
"v_add_f32 v109, 1.0, v109\n"
"v_mfma_f32_16x16x32_bf16 v[76:79], v[216:219], a[28:31], v[76:79]\n"
"buffer_load_dwordx4 v1, s[40:43], s82 offen offset:2048 sc1 lds\n"
"v_mfma_f32_16x16x32_bf16 v[16:19], v[220:223], a[32:35], v[16:19]\n"
"ds_read_b128 v[208:211], v1 offset:10240\n"
"v_add_f32 v110, 1.0, v110\n"
"v_mfma_f32_16x16x32_bf16 v[16:19], v[224:227], a[48:51], v[16:19]\n"
"v_add_f32 v111, 1.0, v111\n"
"v_mfma_f32_16x16x32_bf16 v[16:19], v[228:231], a[64:67], v[16:19]\n"
"v_rcp_f32 v108, v108\n"
"v_mfma_f32_16x16x32_bf16 v[20:23], v[220:223], a[36:39], v[20:23]\n"
"ds_read_b128 v[212:215], v1 offset:11264\n"
"v_mfma_f32_16x16x32_bf16 v[20:23], v[224:227], a[52:55], v[20:23]\n"
"v_rcp_f32 v109, v109\n"
"v_mfma_f32_16x16x32_bf16 v[20:23], v[228:231], a[68:71], v[20:23]\n"
"buffer_load_dwordx4 v1, s[40:43], s82 offen offset:3072 sc1 lds\n"
"v_mfma_f32_16x16x32_bf16 v[24:27], v[220:223], a[40:43], v[24:27]\n"
"s_add_u32 s79, s99, 1\n"
"v_rcp_f32 v110, v110\n"
"v_mfma_f32_16x16x32_bf16 v[24:27], v[224:227], a[56:59], v[24:27]\n"
"ds_read_b128 v[216:219], v1 offset:12288\n"
"v_mov_b32 v10, s79\n"
"v_mfma_f32_16x16x32_bf16 v[24:27], v[228:231], a[72:75], v[24:27]\n"
"s_xor_b32 s80, s75, 4\n"
"v_rcp_f32 v111, v111\n"
"v_mfma_f32_16x16x32_bf16 v[28:31], v[220:223], a[44:47], v[28:31]\n"
"s_lshl_b32 s80, s80, 3\n"
"s_lshl_b32 s79, s72, 1\n"
"s_or_b32 s80, s80, s79\n"
"v_mfma_f32_16x16x32_bf16 v[28:31], v[224:227], a[60:63], v[28:31]\n"
"s_waitcnt lgkmcnt(3)\n"
"s_and_b32 s79, s70, 1\n"
"s_or_b32 s80, s80, s79\n"
"v_mfma_f32_16x16x32_bf16 v[28:31], v[228:231], a[76:79], v[28:31]\n"
"s_lshl_b32 s80, s80, 22\n"
"s_lshl_b32 s79, s101, 7\n"
"s_add_u32 s79, s79, s80\n"
"s_add_u32 s79, s79, 79695872\n"
"buffer_store_dword v10, v14, s[40:43], s79 offen sc1\n"
"v_mul_f32 v140, v140, v108\n"
"v_mfma_f32_16x16x32_bf16 v[32:35], v[232:235], a[32:35], v[32:35]\n"
"v_mul_f32 v141, v141, v109\n"
"v_mul_f32 v142, v142, v110\n"
"v_mfma_f32_16x16x32_bf16 v[32:35], v[236:239], a[48:51], v[32:35]\n"
"ds_read_b128 v[220:223], v1 offset:13312\n"
"v_mul_f32 v143, v143, v111\n"
"v_mfma_f32_16x16x32_bf16 v[32:35], v[240:243], a[64:67], v[32:35]\n"
"v_cvt_pk_bf16_f32 v92, v124, v125\n"
"v_cvt_pk_bf16_f32 v93, v126, v127\n"
"v_mfma_f32_16x16x32_bf16 v[36:39], v[232:235], a[36:39], v[36:39]\n"
"ds_read_b128 v[224:227], v1 offset:14336\n"
"v_cvt_pk_bf16_f32 v94, v140, v141\n"
"v_mfma_f32_16x16x32_bf16 v[36:39], v[236:239], a[52:55], v[36:39]\n"
"v_cvt_pk_bf16_f32 v95, v142, v143\n"
"v_mfma_f32_16x16x32_bf16 v[36:39], v[240:243], a[68:71], v[36:39]\n"
"buffer_store_dwordx4 v[92:95], v1, s[40:43], s85 offen offset:3072 sc1\n"
"v_mfma_f32_16x16x32_bf16 v[40:43], v[232:235], a[40:43], v[40:43]\n"
"ds_read_b128 v[228:231], v1 offset:15360\n"
"v_mul_f32 v160, v144, v160\n"
"v_mfma_f32_16x16x32_bf16 v[40:43], v[236:239], a[56:59], v[40:43]\n"
"v_mul_f32 v161, v145, v161\n"
"v_mul_f32 v162, v146, v162\n"
"v_mfma_f32_16x16x32_bf16 v[40:43], v[240:243], a[72:75], v[40:43]\n"
"v_mul_f32 v163, v147, v163\n"
"v_mfma_f32_16x16x32_bf16 v[44:47], v[232:235], a[44:47], v[44:47]\n"
"v_mul_f32 v144, s86, v144\n"
"v_mul_f32 v145, s86, v145\n"
"v_mfma_f32_16x16x32_bf16 v[44:47], v[236:239], a[60:63], v[44:47]\n"
"s_waitcnt lgkmcnt(3)\n"
"v_mul_f32 v146, s86, v146\n"
"v_mfma_f32_16x16x32_bf16 v[44:47], v[240:243], a[76:79], v[44:47]\n"
"v_mul_f32 v147, s86, v147\n"
"v_mfma_f32_16x16x32_bf16 v[48:51], v[208:211], a[32:35], v[48:51]\n"
"ds_read_b128 v[232:235], v1 offset:16384\n"
"v_mfma_f32_16x16x32_bf16 v[48:51], v[212:215], a[48:51], v[48:51]\n"
"v_exp_f32 v144, v144\n"
"v_mfma_f32_16x16x32_bf16 v[48:51], v[216:219], a[64:67], v[48:51]\n"
"v_exp_f32 v145, v145\n"
"v_mfma_f32_16x16x32_bf16 v[52:55], v[208:211], a[36:39], v[52:55]\n"
"ds_read_b128 v[236:239], v1 offset:17408\n"
"v_mfma_f32_16x16x32_bf16 v[52:55], v[212:215], a[52:55], v[52:55]\n"
"v_exp_f32 v146, v146\n"
"v_mfma_f32_16x16x32_bf16 v[52:55], v[216:219], a[68:71], v[52:55]\n"
"v_exp_f32 v147, v147\n"
"v_mfma_f32_16x16x32_bf16 v[56:59], v[208:211], a[40:43], v[56:59]\n"
"ds_read_b128 v[240:243], v1 offset:18432\n"
"v_add_f32 v144, 1.0, v144\n"
"v_mfma_f32_16x16x32_bf16 v[56:59], v[212:215], a[56:59], v[56:59]\n"
"v_add_f32 v145, 1.0, v145\n"
"v_add_f32 v146, 1.0, v146\n"
"v_mfma_f32_16x16x32_bf16 v[56:59], v[216:219], a[72:75], v[56:59]\n"
"v_add_f32 v147, 1.0, v147\n"
"v_mfma_f32_16x16x32_bf16 v[60:63], v[208:211], a[44:47], v[60:63]\n"
"v_rcp_f32 v144, v144\n"
"v_mfma_f32_16x16x32_bf16 v[60:63], v[212:215], a[60:63], v[60:63]\n"
"s_waitcnt lgkmcnt(3)\n"
"v_rcp_f32 v145, v145\n"
"v_mfma_f32_16x16x32_bf16 v[60:63], v[216:219], a[76:79], v[60:63]\n"
"v_mfma_f32_16x16x32_bf16 v[64:67], v[220:223], a[32:35], v[64:67]\n"
"ds_read_b128 v[208:211], v1 offset:19456\n"
"v_mfma_f32_16x16x32_bf16 v[64:67], v[224:227], a[48:51], v[64:67]\n"
"v_rcp_f32 v146, v146\n"
"v_mfma_f32_16x16x32_bf16 v[64:67], v[228:231], a[64:67], v[64:67]\n"
"v_rcp_f32 v147, v147\n"
"v_mfma_f32_16x16x32_bf16 v[68:71], v[220:223], a[36:39], v[68:71]\n"
"ds_read_b128 v[212:215], v1 offset:20480\n"
"v_mul_f32 v160, v160, v144\n"
"v_mfma_f32_16x16x32_bf16 v[68:71], v[224:227], a[52:55], v[68:71]\n"
"v_mul_f32 v161, v161, v145\n"
"v_mul_f32 v162, v162, v146\n"
"v_mfma_f32_16x16x32_bf16 v[68:71], v[228:231], a[68:71], v[68:71]\n"
"v_mul_f32 v163, v163, v147\n"
"v_mfma_f32_16x16x32_bf16 v[72:75], v[220:223], a[40:43], v[72:75]\n"
"ds_read_b128 v[216:219], v1 offset:21504\n"
"v_cvt_pk_bf16_f32 v144, v160, v161\n"
"v_mfma_f32_16x16x32_bf16 v[72:75], v[224:227], a[56:59], v[72:75]\n"
"v_cvt_pk_bf16_f32 v145, v162, v163\n"
"v_mfma_f32_16x16x32_bf16 v[72:75], v[228:231], a[72:75], v[72:75]\n"
"buffer_store_dwordx2 v[144:145], v1, s[40:43], s91 offen sc1\n"
"v_mfma_f32_16x16x32_bf16 v[76:79], v[220:223], a[44:47], v[76:79]\n"
"v_mul_f32 v164, v148, v164\n"
"v_mul_f32 v165, v149, v165\n"
"v_mfma_f32_16x16x32_bf16 v[76:79], v[224:227], a[60:63], v[76:79]\n"
"s_waitcnt lgkmcnt(3)\n"
"v_mul_f32 v166, v150, v166\n"
"v_mfma_f32_16x16x32_bf16 v[76:79], v[228:231], a[76:79], v[76:79]\n"
"v_mul_f32 v167, v151, v167\n"
"v_mfma_f32_16x16x32_bf16 v[16:19], v[232:235], a[80:83], v[16:19]\n"
"ds_read_b128 v[220:223], v1 offset:22528\n"
"v_mul_f32 v148, s86, v148\n"
"v_mfma_f32_16x16x32_bf16 v[16:19], v[236:239], a[96:99], v[16:19]\n"
"v_mul_f32 v149, s86, v149\n"
"v_mul_f32 v150, s86, v150\n"
"v_mfma_f32_16x16x32_bf16 v[16:19], v[240:243], a[112:115], v[16:19]\n"
"v_mul_f32 v151, s86, v151\n"
"v_mfma_f32_16x16x32_bf16 v[20:23], v[232:235], a[84:87], v[20:23]\n"
"ds_read_b128 v[224:227], v1 offset:23552\n"
"v_mfma_f32_16x16x32_bf16 v[20:23], v[236:239], a[100:103], v[20:23]\n"
"v_exp_f32 v148, v148\n"
"v_mfma_f32_16x16x32_bf16 v[20:23], v[240:243], a[116:119], v[20:23]\n"
"v_exp_f32 v149, v149\n"
"v_mfma_f32_16x16x32_bf16 v[24:27], v[232:235], a[88:91], v[24:27]\n"
"ds_read_b128 v[228:231], v1 offset:24576\n"
"v_mfma_f32_16x16x32_bf16 v[24:27], v[236:239], a[104:107], v[24:27]\n"
"v_exp_f32 v150, v150\n"
"v_mfma_f32_16x16x32_bf16 v[24:27], v[240:243], a[120:123], v[24:27]\n"
"v_exp_f32 v151, v151\n"
"v_mfma_f32_16x16x32_bf16 v[28:31], v[232:235], a[92:95], v[28:31]\n"
"v_add_f32 v148, 1.0, v148\n"
"v_add_f32 v149, 1.0, v149\n"
"v_mfma_f32_16x16x32_bf16 v[28:31], v[236:239], a[108:111], v[28:31]\n"
"s_waitcnt lgkmcnt(3)\n"
"v_add_f32 v150, 1.0, v150\n"
"v_mfma_f32_16x16x32_bf16 v[28:31], v[240:243], a[124:127], v[28:31]\n"
"v_add_f32 v151, 1.0, v151\n"
"v_mfma_f32_16x16x32_bf16 v[32:35], v[208:211], a[80:83], v[32:35]\n"
"ds_read_b128 v[232:235], v1 offset:25600\n"
"v_mfma_f32_16x16x32_bf16 v[32:35], v[212:215], a[96:99], v[32:35]\n"
"v_rcp_f32 v148, v148\n"
"v_mfma_f32_16x16x32_bf16 v[32:35], v[216:219], a[112:115], v[32:35]\n"
"v_rcp_f32 v149, v149\n"
"v_mfma_f32_16x16x32_bf16 v[36:39], v[208:211], a[84:87], v[36:39]\n"
"ds_read_b128 v[236:239], v1 offset:26624\n"
"v_mfma_f32_16x16x32_bf16 v[36:39], v[212:215], a[100:103], v[36:39]\n"
"v_rcp_f32 v150, v150\n"
"v_mfma_f32_16x16x32_bf16 v[36:39], v[216:219], a[116:119], v[36:39]\n"
"v_rcp_f32 v151, v151\n"
"v_mfma_f32_16x16x32_bf16 v[40:43], v[208:211], a[88:91], v[40:43]\n"
"ds_read_b128 v[240:243], v1 offset:27648\n"
"v_mul_f32 v164, v164, v148\n"
"v_mfma_f32_16x16x32_bf16 v[40:43], v[212:215], a[104:107], v[40:43]\n"
"v_mul_f32 v165, v165, v149\n"
"v_mul_f32 v166, v166, v150\n"
"v_mfma_f32_16x16x32_bf16 v[40:43], v[216:219], a[120:123], v[40:43]\n"
"v_mul_f32 v167, v167, v151\n"
"v_mfma_f32_16x16x32_bf16 v[44:47], v[208:211], a[92:95], v[44:47]\n"
"v_cvt_pk_bf16_f32 v148, v164, v165\n"
"v_cvt_pk_bf16_f32 v149, v166, v167\n"
"v_mfma_f32_16x16x32_bf16 v[44:47], v[212:215], a[108:111], v[44:47]\n"
"s_waitcnt lgkmcnt(3)\n"
"buffer_store_dwordx2 v[148:149], v1, s[40:43], s91 offen offset:1024 sc1\n"
"v_mfma_f32_16x16x32_bf16 v[44:47], v[216:219], a[124:127], v[44:47]\n"
"v_mfma_f32_16x16x32_bf16 v[48:51], v[220:223], a[80:83], v[48:51]\n"
"ds_read_b128 v[208:211], v1 offset:28672\n"
"v_mul_f32 v168, v152, v168\n"
"v_mfma_f32_16x16x32_bf16 v[48:51], v[224:227], a[96:99], v[48:51]\n"
"v_mul_f32 v169, v153, v169\n"
"v_mul_f32 v170, v154, v170\n"
"v_mfma_f32_16x16x32_bf16 v[48:51], v[228:231], a[112:115], v[48:51]\n"
"v_mul_f32 v171, v155, v171\n"
"v_mfma_f32_16x16x32_bf16 v[52:55], v[220:223], a[84:87], v[52:55]\n"
"ds_read_b128 v[212:215], v1 offset:29696\n"
"v_mul_f32 v152, s86, v152\n"
"v_mfma_f32_16x16x32_bf16 v[52:55], v[224:227], a[100:103], v[52:55]\n"
"v_mul_f32 v153, s86, v153\n"
"v_mul_f32 v154, s86, v154\n"
"v_mfma_f32_16x16x32_bf16 v[52:55], v[228:231], a[116:119], v[52:55]\n"
"v_mul_f32 v155, s86, v155\n"
"v_mfma_f32_16x16x32_bf16 v[56:59], v[220:223], a[88:91], v[56:59]\n"
"ds_read_b128 v[216:219], v1 offset:30720\n"
"v_mfma_f32_16x16x32_bf16 v[56:59], v[224:227], a[104:107], v[56:59]\n"
"v_exp_f32 v152, v152\n"
"v_mfma_f32_16x16x32_bf16 v[56:59], v[228:231], a[120:123], v[56:59]\n"
"v_exp_f32 v153, v153\n"
"v_mfma_f32_16x16x32_bf16 v[60:63], v[220:223], a[92:95], v[60:63]\n"
"v_exp_f32 v154, v154\n"
"v_mfma_f32_16x16x32_bf16 v[60:63], v[224:227], a[108:111], v[60:63]\n"
"s_waitcnt lgkmcnt(3)\n"
"v_exp_f32 v155, v155\n"
"v_mfma_f32_16x16x32_bf16 v[60:63], v[228:231], a[124:127], v[60:63]\n"
"v_mfma_f32_16x16x32_bf16 v[64:67], v[232:235], a[80:83], v[64:67]\n"
"ds_read_b128 v[220:223], v1 offset:31744\n"
"v_add_f32 v152, 1.0, v152\n"
"v_mfma_f32_16x16x32_bf16 v[64:67], v[236:239], a[96:99], v[64:67]\n"
"v_add_f32 v153, 1.0, v153\n"
"v_add_f32 v154, 1.0, v154\n"
"v_mfma_f32_16x16x32_bf16 v[64:67], v[240:243], a[112:115], v[64:67]\n"
"s_barrier\n"
"v_add_f32 v155, 1.0, v155\n"
"v_mfma_f32_16x16x32_bf16 v[68:71], v[232:235], a[84:87], v[68:71]\n"
"s_lshl_b32 s78, s73, 5\n"
"ds_read_b128 v[224:227], v1 offset:32768\n"
"v_mfma_f32_16x16x32_bf16 v[68:71], v[236:239], a[100:103], v[68:71]\n"
"s_add_u32 s78, s78, s68\n"
"v_rcp_f32 v152, v152\n"
"v_mfma_f32_16x16x32_bf16 v[68:71], v[240:243], a[116:119], v[68:71]\n"
"s_lshl_b32 s78, s78, 6\n"
"v_rcp_f32 v153, v153\n"
"v_mfma_f32_16x16x32_bf16 v[72:75], v[232:235], a[88:91], v[72:75]\n"
"s_add_u32 s78, s78, 79036416\n"
"ds_read_b128 v[228:231], v1 offset:33792\n"
"v_mov_b32 v8, s92\n"
"v_mfma_f32_16x16x32_bf16 v[72:75], v[236:239], a[104:107], v[72:75]\n"
"s_add_u32 s82, s64, 376832\n"
"s_add_u32 m0, s66, 147456\n"
"v_mov_b32 v9, s93\n"
"v_mfma_f32_16x16x32_bf16 v[72:75], v[240:243], a[120:123], v[72:75]\n"
"s_waitcnt vmcnt(2)\n"
"buffer_store_dwordx2 v[8:9], v14, s[40:43], s78 offen sc1\n"
"v_mfma_f32_16x16x32_bf16 v[76:79], v[232:235], a[92:95], v[76:79]\n"
"buffer_load_dwordx4 v1, s[40:43], s82 offen sc1 lds\n"
"v_mfma_f32_16x16x32_bf16 v[76:79], v[236:239], a[108:111], v[76:79]\n"
"s_waitcnt lgkmcnt(3)\n"
"v_rcp_f32 v154, v154\n"
"v_mfma_f32_16x16x32_bf16 v[76:79], v[240:243], a[124:127], v[76:79]\n"
"v_rcp_f32 v155, v155\n"
"v_mfma_f32_16x16x32_bf16 v[80:83], v[208:211], v[176:179], 0\n"
"buffer_load_dwordx4 v1, s[40:43], s82 offen offset:1024 sc1 lds\n"
"v_mfma_f32_16x16x32_bf16 v[80:83], v[212:215], v[192:195], v[80:83]\n"
"v_mul_f32 v168, v168, v152\n"
"v_mul_f32 v169, v169, v153\n"
"v_mfma_f32_16x16x32_bf16 v[80:83], v[216:219], a[128:131], v[80:83]\n"
"ds_read_b128 v[232:235], v1 offset:34816\n"
"v_mul_f32 v170, v170, v154\n"
"v_mfma_f32_16x16x32_bf16 v[84:87], v[208:211], v[180:183], 0\n"
"v_mul_f32 v171, v171, v155\n"
"v_cvt_pk_bf16_f32 v152, v168, v169\n"
"v_mfma_f32_16x16x32_bf16 v[84:87], v[212:215], v[196:199], v[84:87]\n"
"ds_read_b128 v[236:239], v1 offset:35840\n"
"v_cvt_pk_bf16_f32 v153, v170, v171\n"
"v_mfma_f32_16x16x32_bf16 v[84:87], v[216:219], a[132:135], v[84:87]\n"
"buffer_store_dwordx2 v[152:153], v1, s[40:43], s91 offen offset:2048 sc1\n"
"v_mfma_f32_16x16x32_bf16 v[88:91], v[208:211], v[184:187], 0\n"
"buffer_load_dwordx4 v1, s[40:43], s82 offen offset:2048 sc1 lds\n"
"v_mfma_f32_16x16x32_bf16 v[88:91], v[212:215], v[200:203], v[88:91]\n"
"v_mul_f32 v172, v156, v172\n"
"v_mul_f32 v173, v157, v173\n"
"v_mfma_f32_16x16x32_bf16 v[88:91], v[216:219], a[136:139], v[88:91]\n"
"ds_read_b128 v[240:243], v1 offset:36864\n"
"v_mul_f32 v174, v158, v174\n"
"v_mfma_f32_16x16x32_bf16 v[92:95], v[208:211], v[188:191], 0\n"
"v_mul_f32 v175, v159, v175\n"
"v_mul_f32 v156, s86, v156\n"
"v_mfma_f32_16x16x32_bf16 v[92:95], v[212:215], v[204:207], v[92:95]\n"
"s_waitcnt lgkmcnt(3)\n"
"buffer_load_dwordx4 v1, s[40:43], s82 offen offset:3072 sc1 lds\n"
"v_mul_f32 v157, s86, v157\n"
"v_mfma_f32_16x16x32_bf16 v[92:95], v[216:219], a[140:143], v[92:95]\n"
"s_add_u32 s82, s64, 393216\n"
"v_mul_f32 v158, s86, v158\n"
"v_mul_f32 v159, s86, v159\n"
"v_mfma_f32_16x16x32_bf16 v[96:99], v[220:223], v[176:179], 0\n"
"s_add_u32 m0, s66, 0\n"
"v_exp_f32 v156, v156\n"
"v_mfma_f32_16x16x32_bf16 v[96:99], v[224:227], v[192:195], v[96:99]\n"
"ds_read_b128 v[208:211], v1 offset:37888\n"
"v_mfma_f32_16x16x32_bf16 v[96:99], v[228:231], a[128:131], v[96:99]\n"
"v_exp_f32 v157, v157\n"
"v_mfma_f32_16x16x32_bf16 v[100:103], v[220:223], v[180:183], 0\n"
"buffer_load_dwordx4 v1, s[40:43], s82 offen sc1 lds\n"
"v_mfma_f32_16x16x32_bf16 v[100:103], v[224:227], v[196:199], v[100:103]\n"
"v_exp_f32 v158, v158\n"
"v_mfma_f32_16x16x32_bf16 v[100:103], v[228:231], a[132:135], v[100:103]\n"
"ds_read_b128 v[212:215], v1 offset:38912\n"
"v_exp_f32 v159, v159\n"
"v_mfma_f32_16x16x32_bf16 v[104:107], v[220:223], v[184:187], 0\n"
"buffer_load_dwordx4 v1, s[40:43], s82 offen offset:1024 sc1 lds\n"
"v_mfma_f32_16x16x32_bf16 v[104:107], v[224:227], v[200:203], v[104:107]\n"
"v_add_f32 v156, 1.0, v156\n"
"v_add_f32 v157, 1.0, v157\n"
"v_mfma_f32_16x16x32_bf16 v[104:107], v[228:231], a[136:139], v[104:107]\n"
"ds_read_b128 v[216:219], v1 offset:39936\n"
"v_add_f32 v158, 1.0, v158\n"
"v_mfma_f32_16x16x32_bf16 v[108:111], v[220:223], v[188:191], 0\n"
"v_add_f32 v159, 1.0, v159\n"
"v_mfma_f32_16x16x32_bf16 v[108:111], v[224:227], v[204:207], v[108:111]\n"
"s_waitcnt lgkmcnt(3)\n"
"v_rcp_f32 v156, v156\n"
"v_mfma_f32_16x16x32_bf16 v[108:111], v[228:231], a[140:143], v[108:111]\n"
"v_rcp_f32 v157, v157\n"
"v_mfma_f32_16x16x32_bf16 v[112:115], v[232:235], v[176:179], 0\n"
"buffer_load_dwordx4 v1, s[40:43], s82 offen offset:2048 sc1 lds\n"
"v_mfma_f32_16x16x32_bf16 v[112:115], v[236:239], v[192:195], v[112:115]\n"
"v_rcp_f32 v158, v158\n"
"v_mfma_f32_16x16x32_bf16 v[112:115], v[240:243], a[128:131], v[112:115]\n"
"ds_read_b128 v[220:223], v1 offset:40960\n"
"v_rcp_f32 v159, v159\n"
"v_mfma_f32_16x16x32_bf16 v[116:119], v[232:235], v[180:183], 0\n"
"buffer_load_dwordx4 v1, s[40:43], s82 offen offset:3072 sc1 lds\n"
"v_mfma_f32_16x16x32_bf16 v[116:119], v[236:239], v[196:199], v[116:119]\n"
"v_mul_f32 v172, v172, v156\n"
"v_mul_f32 v173, v173, v157\n"
"v_mfma_f32_16x16x32_bf16 v[116:119], v[240:243], a[132:135], v[116:119]\n"
"ds_read_b128 v[224:227], v1 offset:41984\n"
"v_mul_f32 v174, v174, v158\n"
"v_mfma_f32_16x16x32_bf16 v[120:123], v[232:235], v[184:187], 0\n"
"v_mul_f32 v175, v175, v159\n"
"v_cvt_pk_bf16_f32 v156, v172, v173\n"
"v_mfma_f32_16x16x32_bf16 v[120:123], v[236:239], v[200:203], v[120:123]\n"
"ds_read_b128 v[228:231], v1 offset:43008\n"
"v_cvt_pk_bf16_f32 v157, v174, v175\n"
"v_mfma_f32_16x16x32_bf16 v[120:123], v[240:243], a[136:139], v[120:123]\n"
"buffer_store_dwordx2 v[156:157], v1, s[40:43], s91 offen offset:3072 sc1\n"
"v_mfma_f32_16x16x32_bf16 v[124:127], v[232:235], v[188:191], 0\n"
"s_add_u32 s85, s83, 81920\n"
"v_mul_f32 v48, v16, v48\n"
"v_mul_f32 v49, v17, v49\n"
"v_mfma_f32_16x16x32_bf16 v[124:127], v[236:239], v[204:207], v[124:127]\n"
"s_waitcnt lgkmcnt(3)\n"
"v_mul_f32 v50, v18, v50\n"
"v_mul_f32 v51, v19, v51\n"
"v_mfma_f32_16x16x32_bf16 v[124:127], v[240:243], a[140:143], v[124:127]\n"
"v_mul_f32 v16, s86, v16\n"
"v_mfma_f32_16x16x32_bf16 v[128:131], v[208:211], v[176:179], 0\n"
"ds_read_b128 v[232:235], v1 offset:44032\n"
"v_mul_f32 v17, s86, v17\n"
"v_mfma_f32_16x16x32_bf16 v[128:131], v[212:215], v[192:195], v[128:131]\n"
"v_mul_f32 v18, s86, v18\n"
"v_mul_f32 v19, s86, v19\n"
"v_mfma_f32_16x16x32_bf16 v[128:131], v[216:219], a[128:131], v[128:131]\n"
"v_exp_f32 v16, v16\n"
"v_mfma_f32_16x16x32_bf16 v[132:135], v[208:211], v[180:183], 0\n"
"ds_read_b128 v[236:239], v1 offset:45056\n"
"v_mfma_f32_16x16x32_bf16 v[132:135], v[212:215], v[196:199], v[132:135]\n"
"v_exp_f32 v17, v17\n"
"v_mfma_f32_16x16x32_bf16 v[132:135], v[216:219], a[132:135], v[132:135]\n"
"v_mfma_f32_16x16x32_bf16 v[136:139], v[208:211], v[184:187], 0\n"
"ds_read_b128 v[240:243], v1 offset:46080\n"
"v_mfma_f32_16x16x32_bf16 v[136:139], v[212:215], v[200:203], v[136:139]\n"
"v_exp_f32 v18, v18\n"
"v_mfma_f32_16x16x32_bf16 v[136:139], v[216:219], a[136:139], v[136:139]\n"
"v_exp_f32 v19, v19\n"
"v_mfma_f32_16x16x32_bf16 v[140:143], v[208:211], v[188:191], 0\n"
"v_add_f32 v16, 1.0, v16\n"
"v_mfma_f32_16x16x32_bf16 v[140:143], v[212:215], v[204:207], v[140:143]\n"
"s_waitcnt lgkmcnt(3)\n"
"v_add_f32 v17, 1.0, v17\n"
"v_mfma_f32_16x16x32_bf16 v[140:143], v[216:219], a[140:143], v[140:143]\n"
"v_add_f32 v18, 1.0, v18\n"
"v_mfma_f32_16x16x32_bf16 v[80:83], v[220:223], a[144:147], v[80:83]\n"
"ds_read_b128 v[208:211], v1 offset:47104\n"
"v_add_f32 v19, 1.0, v19\n"
"v_mfma_f32_16x16x32_bf16 v[80:83], v[224:227], a[160:163], v[80:83]\n"
"v_rcp_f32 v16, v16\n"
"v_mfma_f32_16x16x32_bf16 v[80:83], v[228:231], a[176:179], v[80:83]\n"
"v_rcp_f32 v17, v17\n"
"v_mfma_f32_16x16x32_bf16 v[84:87], v[220:223], a[148:151], v[84:87]\n"
"ds_read_b128 v[212:215], v1 offset:48128\n"
"v_mfma_f32_16x16x32_bf16 v[84:87], v[224:227], a[164:167], v[84:87]\n"
"v_rcp_f32 v18, v18\n"
"v_mfma_f32_16x16x32_bf16 v[84:87], v[228:231], a[180:183], v[84:87]\n"
"v_rcp_f32 v19, v19\n"
"v_mfma_f32_16x16x32_bf16 v[88:91], v[220:223], a[152:155], v[88:91]\n"
"ds_read_b128 v[216:219], v1 offset:49152\n"
"v_mfma_f32_16x16x32_bf16 v[88:91], v[224:227], a[168:171], v[88:91]\n"
"v_mul_f32 v48, v48, v16\n"
"v_mfma_f32_16x16x32_bf16 v[88:91], v[228:231], a[184:187], v[88:91]\n"
"v_mul_f32 v49, v49, v17\n"
"v_mfma_f32_16x16x32_bf16 v[92:95], v[220:223], a[156:159], v[92:95]\n"
"v_mul_f32 v50, v50, v18\n"
"v_mfma_f32_16x16x32_bf16 v[92:95], v[224:227], a[172:175], v[92:95]\n"
"s_waitcnt lgkmcnt(3)\n"
"v_mul_f32 v51, v51, v19\n"
"v_mul_f32 v64, v32, v64\n"
"v_mfma_f32_16x16x32_bf16 v[92:95], v[228:231], a[188:191], v[92:95]\n"
"v_mul_f32 v65, v33, v65\n"
"v_mfma_f32_16x16x32_bf16 v[96:99], v[232:235], a[144:147], v[96:99]\n"
"ds_read_b128 v[220:223], v1 offset:50176\n"
"v_mul_f32 v66, v34, v66\n"
"v_mfma_f32_16x16x32_bf16 v[96:99], v[236:239], a[160:163], v[96:99]\n"
"v_mul_f32 v67, v35, v67\n"
"v_mfma_f32_16x16x32_bf16 v[96:99], v[240:243], a[176:179], v[96:99]\n"
"v_mul_f32 v32, s86, v32\n"
"v_mfma_f32_16x16x32_bf16 v[100:103], v[232:235], a[148:151], v[100:103]\n"
"ds_read_b128 v[224:227], v1 offset:51200\n"
"v_mul_f32 v33, s86, v33\n"
"v_mfma_f32_16x16x32_bf16 v[100:103], v[236:239], a[164:167], v[100:103]\n"
"v_mul_f32 v34, s86, v34\n"
"v_mul_f32 v35, s86, v35\n"
"v_mfma_f32_16x16x32_bf16 v[100:103], v[240:243], a[180:183], v[100:103]\n"
"v_exp_f32 v32, v32\n"
"v_mfma_f32_16x16x32_bf16 v[104:107], v[232:235], a[152:155], v[104:107]\n"
"ds_read_b128 v[228:231], v1 offset:52224\n"
"v_mfma_f32_16x16x32_bf16 v[104:107], v[236:239], a[168:171], v[104:107]\n"
"v_exp_f32 v33, v33\n"
"v_mfma_f32_16x16x32_bf16 v[104:107], v[240:243], a[184:187], v[104:107]\n"
"v_mfma_f32_16x16x32_bf16 v[108:111], v[232:235], a[156:159], v[108:111]\n"
"v_exp_f32 v34, v34\n"
"v_mfma_f32_16x16x32_bf16 v[108:111], v[236:239], a[172:175], v[108:111]\n"
"s_waitcnt lgkmcnt(3)\n"
"v_exp_f32 v35, v35\n"
"v_mfma_f32_16x16x32_bf16 v[108:111], v[240:243], a[188:191], v[108:111]\n"
"v_mfma_f32_16x16x32_bf16 v[112:115], v[208:211], a[144:147], v[112:115]\n"
"ds_read_b128 v[232:235], v1 offset:53248\n"
"v_add_f32 v32, 1.0, v32\n"
"v_mfma_f32_16x16x32_bf16 v[112:115], v[212:215], a[160:163], v[112:115]\n"
"v_add_f32 v33, 1.0, v33\n"
"v_mfma_f32_16x16x32_bf16 v[112:115], v[216:219], a[176:179], v[112:115]\n"
"v_add_f32 v34, 1.0, v34\n"
"v_add_f32 v35, 1.0, v35\n"
"v_mfma_f32_16x16x32_bf16 v[116:119], v[208:211], a[148:151], v[116:119]\n"
"ds_read_b128 v[236:239], v1 offset:54272\n"
"v_mfma_f32_16x16x32_bf16 v[116:119], v[212:215], a[164:167], v[116:119]\n"
"v_rcp_f32 v32, v32\n"
"v_mfma_f32_16x16x32_bf16 v[116:119], v[216:219], a[180:183], v[116:119]\n"
"v_rcp_f32 v33, v33\n"
"v_mfma_f32_16x16x32_bf16 v[120:123], v[208:211], a[152:155], v[120:123]\n"
"ds_read_b128 v[240:243], v1 offset:55296\n"
"v_mfma_f32_16x16x32_bf16 v[120:123], v[212:215], a[168:171], v[120:123]\n"
"v_rcp_f32 v34, v34\n"
"v_mfma_f32_16x16x32_bf16 v[120:123], v[216:219], a[184:187], v[120:123]\n"
"v_rcp_f32 v35, v35\n"
"v_mfma_f32_16x16x32_bf16 v[124:127], v[208:211], a[156:159], v[124:127]\n"
"v_mfma_f32_16x16x32_bf16 v[124:127], v[212:215], a[172:175], v[124:127]\n"
"s_waitcnt lgkmcnt(3)\n"
"v_mul_f32 v64, v64, v32\n"
"v_mfma_f32_16x16x32_bf16 v[124:127], v[216:219], a[188:191], v[124:127]\n"
"v_mul_f32 v65, v65, v33\n"
"v_mfma_f32_16x16x32_bf16 v[128:131], v[220:223], a[144:147], v[128:131]\n"
"s_lshl_b32 s23, s73, 5\n"
"ds_read_b128 v[208:211], v1 offset:56320\n"
"v_mul_f32 v66, v66, v34\n"
"v_mfma_f32_16x16x32_bf16 v[128:131], v[224:227], a[160:163], v[128:131]\n"
"s_add_u32 s23, s23, s68\n"
"v_mul_f32 v67, v67, v35\n"
"v_cvt_pk_bf16_f32 v16, v48, v49\n"
"v_mfma_f32_16x16x32_bf16 v[128:131], v[228:231], a[176:179], v[128:131]\n"
"s_lshl_b32 s23, s23, 6\n"
"v_cvt_pk_bf16_f32 v17, v50, v51\n"
"v_mfma_f32_16x16x32_bf16 v[132:135], v[220:223], a[148:151], v[132:135]\n"
"s_and_b32 s23, s23, 0xfffffe00\n"
"ds_read_b128 v[212:215], v1 offset:57344\n"
"v_cvt_pk_bf16_f32 v18, v64, v65\n"
"v_mfma_f32_16x16x32_bf16 v[132:135], v[224:227], a[164:167], v[132:135]\n"
"s_add_u32 s23, s23, 78970880\n"
"s_mul_i32 s79, s67, 262144\n"
"s_add_u32 s23, s23, s79\n"
"v_mfma_f32_16x16x32_bf16 v[132:135], v[228:231], a[180:183], v[132:135]\n"
"v_and_b32 v15, 7, v0\n"
"v_lshlrev_b32 v15, 6, v15\n"
"v_mfma_f32_16x16x32_bf16 v[136:139], v[220:223], a[152:155], v[136:139]\n"
"buffer_load_dwordx2 v[10:11], v15, s[40:43], s23 offen sc1\n"
"v_mfma_f32_16x16x32_bf16 v[136:139], v[224:227], a[168:171], v[136:139]\n"
"ds_read_b128 v[216:219], v1 offset:58368\n"
"v_cvt_pk_bf16_f32 v19, v66, v67\n"
"v_mfma_f32_16x16x32_bf16 v[136:139], v[228:231], a[184:187], v[136:139]\n"
"buffer_store_dwordx4 v[16:19], v1, s[40:43], s85 offen sc1\n"
"v_mfma_f32_16x16x32_bf16 v[140:143], v[220:223], a[156:159], v[140:143]\n"
"v_mul_f32 v52, v20, v52\n"
"v_mul_f32 v53, v21, v53\n"
"v_mfma_f32_16x16x32_bf16 v[140:143], v[224:227], a[172:175], v[140:143]\n"
"s_waitcnt lgkmcnt(3)\n"
"v_mul_f32 v54, v22, v54\n"
"v_mul_f32 v55, v23, v55\n"
"v_mfma_f32_16x16x32_bf16 v[140:143], v[228:231], a[188:191], v[140:143]\n"
"v_mul_f32 v20, s86, v20\n"
"v_mul_f32 v21, s86, v21\n"
"v_mfma_f32_16x16x32_bf16 v[80:83], v[232:235], a[192:195], v[80:83]\n"
"s_mul_i32 s84, s73, 144\n"
"ds_read_b128 v[220:223], v1 offset:59392\n"
"v_mul_f32 v22, s86, v22\n"
"v_mfma_f32_16x16x32_bf16 v[80:83], v[236:239], a[208:211], v[80:83]\n"
"s_mul_i32 s79, s101, 36\n"
"v_mul_f32 v23, s86, v23\n"
"v_mfma_f32_16x16x32_bf16 v[80:83], v[240:243], a[224:227], v[80:83]\n"
"s_add_u32 s84, s84, s79\n"
"v_exp_f32 v20, v20\n"
"v_mfma_f32_16x16x32_bf16 v[84:87], v[232:235], a[196:199], v[84:87]\n"
"s_lshl_b32 s79, s73, 2\n"
"ds_read_b128 v[224:227], v1 offset:60416\n"
"v_mfma_f32_16x16x32_bf16 v[84:87], v[236:239], a[212:215], v[84:87]\n"
"s_add_u32 s79, s79, s101\n"
"s_lshl_b32 s79, s79, 2\n"
"s_sub_u32 s79, 4608, s79\n"
"v_mfma_f32_16x16x32_bf16 v[84:87], v[240:243], a[228:231], v[84:87]\n"
"s_mul_i32 s79, s79, s67\n"
"s_add_u32 s84, s84, s79\n"
"s_lshl_b32 s84, s84, 12\n"
"v_mfma_f32_16x16x32_bf16 v[88:91], v[232:235], a[200:203], v[88:91]\n"
"s_add_u32 s84, s84, 2359296\n"
"v_exp_f32 v21, v21\n"
"v_mfma_f32_16x16x32_bf16 v[88:91], v[236:239], a[216:219], v[88:91]\n"
"ds_read_b128 v[228:231], v1 offset:61440\n"
"v_mfma_f32_16x16x32_bf16 v[88:91], v[240:243], a[232:235], v[88:91]\n"
"v_exp_f32 v22, v22\n"
"v_mfma_f32_16x16x32_bf16 v[92:95], v[232:235], a[204:207], v[92:95]\n"
"v_exp_f32 v23, v23\n"
"v_mfma_f32_16x16x32_bf16 v[92:95], v[236:239], a[220:223], v[92:95]\n"
"s_waitcnt lgkmcnt(3)\n"
"v_add_f32 v20, 1.0, v20\n"
"v_add_f32 v21, 1.0, v21\n"
"v_mfma_f32_16x16x32_bf16 v[92:95], v[240:243], a[236:239], v[92:95]\n"
"v_add_f32 v22, 1.0, v22\n"
"v_add_f32 v23, 1.0, v23\n"
"v_mfma_f32_16x16x32_bf16 v[96:99], v[208:211], a[192:195], v[96:99]\n"
"ds_read_b128 v[232:235], v1 offset:62464\n"
"v_mfma_f32_16x16x32_bf16 v[96:99], v[212:215], a[208:211], v[96:99]\n"
"v_rcp_f32 v20, v20\n"
"v_mfma_f32_16x16x32_bf16 v[96:99], v[216:219], a[224:227], v[96:99]\n"
"v_rcp_f32 v21, v21\n"
"v_mfma_f32_16x16x32_bf16 v[100:103], v[208:211], a[196:199], v[100:103]\n"
"ds_read_b128 v[236:239], v1 offset:63488\n"
"v_mfma_f32_16x16x32_bf16 v[100:103], v[212:215], a[212:215], v[100:103]\n"
"v_rcp_f32 v22, v22\n"
"v_mfma_f32_16x16x32_bf16 v[100:103], v[216:219], a[228:231], v[100:103]\n"
"v_rcp_f32 v23, v23\n"
"v_mfma_f32_16x16x32_bf16 v[104:107], v[208:211], a[200:203], v[104:107]\n"
"ds_read_b128 v[240:243], v1 offset:64512\n"
"v_mul_f32 v52, v52, v20\n"
"v_mfma_f32_16x16x32_bf16 v[104:107], v[212:215], a[216:219], v[104:107]\n"
"v_mul_f32 v53, v53, v21\n"
"v_mfma_f32_16x16x32_bf16 v[104:107], v[216:219], a[232:235], v[104:107]\n"
"v_mul_f32 v54, v54, v22\n"
"v_mfma_f32_16x16x32_bf16 v[108:111], v[208:211], a[204:207], v[108:111]\n"
"v_mul_f32 v55, v55, v23\n"
"v_mul_f32 v68, v36, v68\n"
"v_mfma_f32_16x16x32_bf16 v[108:111], v[212:215], a[220:223], v[108:111]\n"
"s_waitcnt lgkmcnt(3)\n"
"v_mul_f32 v69, v37, v69\n"
"v_mfma_f32_16x16x32_bf16 v[108:111], v[216:219], a[236:239], v[108:111]\n"
"s_barrier\n"
"v_mul_f32 v70, v38, v70\n"
"v_mfma_f32_16x16x32_bf16 v[112:115], v[220:223], a[192:195], v[112:115]\n"
"s_add_u32 s82, s64, 409600\n"
"ds_read_b128 v[208:211], v2\n"
"v_mul_f32 v71, v39, v71\n"
"v_mfma_f32_16x16x32_bf16 v[112:115], v[224:227], a[208:211], v[112:115]\n"
"s_add_u32 m0, s66, 16384\n"
"v_mul_f32 v36, s86, v36\n"
"v_mfma_f32_16x16x32_bf16 v[112:115], v[228:231], a[224:227], v[112:115]\n"
"buffer_load_dwordx4 v1, s[40:43], s82 offen sc1 lds\n"
"v_mfma_f32_16x16x32_bf16 v[116:119], v[220:223], a[196:199], v[116:119]\n"
"ds_read_b128 v[212:215], v2 offset:1024\n"
"v_mul_f32 v37, s86, v37\n"
"v_mfma_f32_16x16x32_bf16 v[116:119], v[224:227], a[212:215], v[116:119]\n"
"v_mul_f32 v38, s86, v38\n"
"v_mul_f32 v39, s86, v39\n"
"v_mfma_f32_16x16x32_bf16 v[116:119], v[228:231], a[228:231], v[116:119]\n"
"v_exp_f32 v36, v36\n"
"v_mfma_f32_16x16x32_bf16 v[120:123], v[220:223], a[200:203], v[120:123]\n"
"ds_read_b128 v[216:219], v2 offset:2048\n"
"v_mfma_f32_16x16x32_bf16 v[120:123], v[224:227], a[216:219], v[120:123]\n"
"buffer_load_dwordx4 v1, s[40:43], s82 offen offset:1024 sc1 lds\n"
"v_mfma_f32_16x16x32_bf16 v[120:123], v[228:231], a[232:235], v[120:123]\n"
"v_exp_f32 v37, v37\n"
"v_mfma_f32_16x16x32_bf16 v[124:127], v[220:223], a[204:207], v[124:127]\n"
"v_exp_f32 v38, v38\n"
"v_mfma_f32_16x16x32_bf16 v[124:127], v[224:227], a[220:223], v[124:127]\n"
"s_waitcnt lgkmcnt(3)\n"
"v_exp_f32 v39, v39\n"
"v_mfma_f32_16x16x32_bf16 v[124:127], v[228:231], a[236:239], v[124:127]\n"
"buffer_load_dwordx4 v1, s[40:43], s82 offen offset:2048 sc1 lds\n"
"v_mfma_f32_16x16x32_bf16 v[128:131], v[232:235], a[192:195], v[128:131]\n"
"ds_read_b128 v[220:223], v2 offset:3072\n"
"v_add_f32 v36, 1.0, v36\n"
"v_mfma_f32_16x16x32_bf16 v[128:131], v[236:239], a[208:211], v[128:131]\n"
"v_add_f32 v37, 1.0, v37\n"
"v_add_f32 v38, 1.0, v38\n"
"v_mfma_f32_16x16x32_bf16 v[128:131], v[240:243], a[224:227], v[128:131]\n"
"v_add_f32 v39, 1.0, v39\n"
"v_mfma_f32_16x16x32_bf16 v[132:135], v[232:235], a[196:199], v[132:135]\n"
"ds_read_b128 v[224:227], v2 offset:4096\n"
"v_mfma_f32_16x16x32_bf16 v[132:135], v[236:239], a[212:215], v[132:135]\n"
"v_rcp_f32 v36, v36\n"
"v_mfma_f32_16x16x32_bf16 v[132:135], v[240:243], a[228:231], v[132:135]\n"
"buffer_load_dwordx4 v1, s[40:43], s82 offen offset:3072 sc1 lds\n"
"v_mfma_f32_16x16x32_bf16 v[136:139], v[232:235], a[200:203], v[136:139]\n"
"s_add_u32 s82, s64, 425984\n"
"ds_read_b128 v[228:231], v2 offset:5120\n"
"v_mfma_f32_16x16x32_bf16 v[136:139], v[236:239], a[216:219], v[136:139]\n"
"s_add_u32 m0, s66, 32768\n"
"v_rcp_f32 v37, v37\n"
"v_mfma_f32_16x16x32_bf16 v[136:139], v[240:243], a[232:235], v[136:139]\n"
"v_rcp_f32 v38, v38\n"
"v_mfma_f32_16x16x32_bf16 v[140:143], v[232:235], a[204:207], v[140:143]\n"
"v_rcp_f32 v39, v39\n"
"v_mfma_f32_16x16x32_bf16 v[140:143], v[236:239], a[220:223], v[140:143]\n"
"s_waitcnt lgkmcnt(3)\n"
"buffer_load_dwordx4 v1, s[40:43], s82 offen sc1 lds\n"
"v_mfma_f32_16x16x32_bf16 v[140:143], v[240:243], a[236:239], v[140:143]\n"
"v_mul_f32 v68, v68, v36\n"
"v_mul_f32 v69, v69, v37\n"
"v_mfma_f32_16x16x32_bf16 v[80:83], v[208:211], a[240:243], v[80:83]\n"
"ds_read_b128 v[232:235], v2 offset:6144\n"
"v_mul_f32 v70, v70, v38\n"
"v_mfma_f32_16x16x32_bf16 v[80:83], v[212:215], a[0:3], v[80:83]\n"
"buffer_load_dwordx4 v1, s[40:43], s82 offen offset:1024 sc1 lds\n"
"v_mfma_f32_16x16x32_bf16 v[80:83], v[216:219], a[16:19], v[80:83]\n"
"v_mul_f32 v71, v71, v39\n"
"v_cvt_pk_bf16_f32 v20, v52, v53\n"
"v_mfma_f32_16x16x32_bf16 v[84:87], v[208:211], a[244:247], v[84:87]\n"
"ds_read_b128 v[236:239], v2 offset:7168\n"
"v_cvt_pk_bf16_f32 v21, v54, v55\n"
"v_mfma_f32_16x16x32_bf16 v[84:87], v[212:215], a[4:7], v[84:87]\n"
"v_cvt_pk_bf16_f32 v22, v68, v69\n"
"v_cvt_pk_bf16_f32 v23, v70, v71\n"
"v_mfma_f32_16x16x32_bf16 v[84:87], v[216:219], a[20:23], v[84:87]\n"
"buffer_store_dwordx4 v[20:23], v1, s[40:43], s85 offen offset:1024 sc1\n"
"v_mfma_f32_16x16x32_bf16 v[88:91], v[208:211], a[248:251], v[88:91]\n"
"ds_read_b128 v[240:243], v2 offset:8192\n"
"v_mul_f32 v56, v24, v56\n"
"v_mfma_f32_16x16x32_bf16 v[88:91], v[212:215], a[8:11], v[88:91]\n"
"buffer_load_dwordx4 v1, s[40:43], s82 offen offset:2048 sc1 lds\n"
"v_mfma_f32_16x16x32_bf16 v[88:91], v[216:219], a[24:27], v[88:91]\n"
"v_mul_f32 v57, v25, v57\n"
"v_mul_f32 v58, v26, v58\n"
"v_mfma_f32_16x16x32_bf16 v[92:95], v[208:211], a[252:255], v[92:95]\n"
"v_mul_f32 v59, v27, v59\n"
"v_mul_f32 v24, s86, v24\n"
"v_mfma_f32_16x16x32_bf16 v[92:95], v[212:215], a[12:15], v[92:95]\n"
"s_waitcnt lgkmcnt(3)\n"
"buffer_load_dwordx4 v1, s[40:43], s82 offen offset:3072 sc1 lds\n"
"v_mfma_f32_16x16x32_bf16 v[92:95], v[216:219], a[28:31], v[92:95]\n"
"v_mul_f32 v25, s86, v25\n"
"v_mul_f32 v26, s86, v26\n"
"v_mfma_f32_16x16x32_bf16 v[96:99], v[220:223], a[240:243], v[96:99]\n"
"ds_read_b128 v[208:211], v2 offset:9216\n"
"v_mul_f32 v27, s86, v27\n"
"v_mfma_f32_16x16x32_bf16 v[96:99], v[224:227], a[0:3], v[96:99]\n"
"v_exp_f32 v24, v24\n"
"v_mfma_f32_16x16x32_bf16 v[96:99], v[228:231], a[16:19], v[96:99]\n"
"s_waitcnt vmcnt(10)\n"
"v_cmp_ne_u64 vcc, s[92:93], v[10:11]\n"
"v_mfma_f32_16x16x32_bf16 v[100:103], v[220:223], a[244:247], v[100:103]\n"
"s_cbranch_vccnz L_slow_16_%=\n"
"L_back_17_%=:\n"
"ds_read_b128 v[212:215], v2 offset:10240\n"
"v_mfma_f32_16x16x32_bf16 v[100:103], v[224:227], a[4:7], v[100:103]\n"
"v_exp_f32 v25, v25\n"
"v_mfma_f32_16x16x32_bf16 v[100:103], v[228:231], a[20:23], v[100:103]\n"
"buffer_load_dwordx4 a[128:131], v1, s[40:43], s84 offen sc1\n"
"v_mfma_f32_16x16x32_bf16 v[104:107], v[220:223], a[248:251], v[104:107]\n"
"ds_read_b128 v[216:219], v2 offset:11264\n"
"v_mfma_f32_16x16x32_bf16 v[104:107], v[224:227], a[8:11], v[104:107]\n"
"v_exp_f32 v26, v26\n"
"v_mfma_f32_16x16x32_bf16 v[104:107], v[228:231], a[24:27], v[104:107]\n"
"v_exp_f32 v27, v27\n"
"v_mfma_f32_16x16x32_bf16 v[108:111], v[220:223], a[252:255], v[108:111]\n"
"v_add_f32 v24, 1.0, v24\n"
"v_add_f32 v25, 1.0, v25\n"
"v_mfma_f32_16x16x32_bf16 v[108:111], v[224:227], a[12:15], v[108:111]\n"
"s_waitcnt lgkmcnt(3)\n"
"buffer_load_dwordx4 a[132:135], v1, s[40:43], s84 offen offset:1024 sc1\n"
"v_mfma_f32_16x16x32_bf16 v[108:111], v[228:231], a[28:31], v[108:111]\n"
"v_add_f32 v26, 1.0, v26\n"
"v_add_f32 v27, 1.0, v27\n"
"v_mfma_f32_16x16x32_bf16 v[112:115], v[232:235], a[240:243], v[112:115]\n"
"ds_read_b128 v[220:223], v2 offset:12288\n"
"v_mfma_f32_16x16x32_bf16 v[112:115], v[236:239], a[0:3], v[112:115]\n"
"v_rcp_f32 v24, v24\n"
"v_mfma_f32_16x16x32_bf16 v[112:115], v[240:243], a[16:19], v[112:115]\n"
"v_rcp_f32 v25, v25\n"
"v_mfma_f32_16x16x32_bf16 v[116:119], v[232:235], a[244:247], v[116:119]\n"
"ds_read_b128 v[224:227], v2 offset:13312\n"
"v_mfma_f32_16x16x32_bf16 v[116:119], v[236:239], a[4:7], v[116:119]\n"
"buffer_load_dwordx4 a[136:139], v1, s[40:43], s84 offen offset:2048 sc1\n"
"v_mfma_f32_16x16x32_bf16 v[116:119], v[240:243], a[20:23], v[116:119]\n"
"v_rcp_f32 v26, v26\n"
"v_mfma_f32_16x16x32_bf16 v[120:123], v[232:235], a[248:251], v[120:123]\n"
"v_rcp_f32 v27, v27\n"
"v_mfma_f32_16x16x32_bf16 v[120:123], v[236:239], a[8:11], v[120:123]\n"
"ds_read_b128 v[228:231], v2 offset:14336\n"
"v_mul_f32 v56, v56, v24\n"
"v_mfma_f32_16x16x32_bf16 v[120:123], v[240:243], a[24:27], v[120:123]\n"
"buffer_load_dwordx4 a[140:143], v1, s[40:43], s84 offen offset:3072 sc1\n"
"v_mfma_f32_16x16x32_bf16 v[124:127], v[232:235], a[252:255], v[124:127]\n"
"s_add_u32 s84, s84, 4096\n"
"v_mul_f32 v57, v57, v25\n"
"v_mul_f32 v58, v58, v26\n"
"v_mfma_f32_16x16x32_bf16 v[124:127], v[236:239], a[12:15], v[124:127]\n"
"s_waitcnt lgkmcnt(3)\n"
"v_mul_f32 v59, v59, v27\n"
"v_mul_f32 v72, v40, v72\n"
"v_mfma_f32_16x16x32_bf16 v[124:127], v[240:243], a[28:31], v[124:127]\n"
"v_mul_f32 v73, v41, v73\n"
"v_mul_f32 v74, v42, v74\n"
"v_mfma_f32_16x16x32_bf16 v[128:131], v[208:211], a[240:243], v[128:131]\n"
"ds_read_b128 v[232:235], v2 offset:15360\n"
"v_mul_f32 v75, v43, v75\n"
"v_mfma_f32_16x16x32_bf16 v[128:131], v[212:215], a[0:3], v[128:131]\n"
"buffer_load_dwordx4 a[144:147], v1, s[40:43], s84 offen sc1\n"
"v_mfma_f32_16x16x32_bf16 v[128:131], v[216:219], a[16:19], v[128:131]\n"
"v_mul_f32 v40, s86, v40\n"
"v_mul_f32 v41, s86, v41\n"
"v_mfma_f32_16x16x32_bf16 v[132:135], v[208:211], a[244:247], v[132:135]\n"
"ds_read_b128 v[236:239], v2 offset:16384\n"
"v_mul_f32 v42, s86, v42\n"
"v_mfma_f32_16x16x32_bf16 v[132:135], v[212:215], a[4:7], v[132:135]\n"
"v_mul_f32 v43, s86, v43\n"
"v_mfma_f32_16x16x32_bf16 v[132:135], v[216:219], a[20:23], v[132:135]\n"
"buffer_load_dwordx4 a[148:151], v1, s[40:43], s84 offen offset:1024 sc1\n"
"v_mfma_f32_16x16x32_bf16 v[136:139], v[208:211], a[248:251], v[136:139]\n"
"v_exp_f32 v40, v40\n"
"v_mfma_f32_16x16x32_bf16 v[136:139], v[212:215], a[8:11], v[136:139]\n"
"ds_read_b128 v[240:243], v2 offset:17408\n"
"v_mfma_f32_16x16x32_bf16 v[136:139], v[216:219], a[24:27], v[136:139]\n"
"v_exp_f32 v41, v41\n"
"v_mfma_f32_16x16x32_bf16 v[140:143], v[208:211], a[252:255], v[140:143]\n"
"v_exp_f32 v42, v42\n"
"v_mfma_f32_16x16x32_bf16 v[140:143], v[212:215], a[12:15], v[140:143]\n"
"s_waitcnt lgkmcnt(3)\n"
"buffer_load_dwordx4 a[152:155], v1, s[40:43], s84 offen offset:2048 sc1\n"
"v_mfma_f32_16x16x32_bf16 v[140:143], v[216:219], a[28:31], v[140:143]\n"
"v_exp_f32 v43, v43\n"
"v_mfma_f32_16x16x32_bf16 v[80:83], v[220:223], a[32:35], v[80:83]\n"
"ds_read_b128 v[208:211], v2 offset:18432\n"
"v_add_f32 v40, 1.0, v40\n"
"v_mfma_f32_16x16x32_bf16 v[80:83], v[224:227], a[48:51], v[80:83]\n"
"v_add_f32 v41, 1.0, v41\n"
"v_add_f32 v42, 1.0, v42\n"
"v_mfma_f32_16x16x32_bf16 v[80:83], v[228:231], a[64:67], v[80:83]\n"
"v_add_f32 v43, 1.0, v43\n"
"v_mfma_f32_16x16x32_bf16 v[84:87], v[220:223], a[36:39], v[84:87]\n"
"ds_read_b128 v[212:215], v2 offset:19456\n"
"v_mfma_f32_16x16x32_bf16 v[84:87], v[224:227], a[52:55], v[84:87]\n"
"buffer_load_dwordx4 a[156:159], v1, s[40:43], s84 offen offset:3072 sc1\n"
"v_mfma_f32_16x16x32_bf16 v[84:87], v[228:231], a[68:71], v[84:87]\n"
"s_add_u32 s84, s84, 4096\n"
"v_rcp_f32 v40, v40\n"
"v_mfma_f32_16x16x32_bf16 v[88:91], v[220:223], a[40:43], v[88:91]\n"
"v_rcp_f32 v41, v41\n"
"v_mfma_f32_16x16x32_bf16 v[88:91], v[224:227], a[56:59], v[88:91]\n"
"ds_read_b128 v[216:219], v2 offset:20480\n"
"v_mfma_f32_16x16x32_bf16 v[88:91], v[228:231], a[72:75], v[88:91]\n"
"v_rcp_f32 v42, v42\n"
"v_mfma_f32_16x16x32_bf16 v[92:95], v[220:223], a[44:47], v[92:95]\n"
"buffer_load_dwordx4 a[160:163], v1, s[40:43], s84 offen sc1\n"
"v_mfma_f32_16x16x32_bf16 v[92:95], v[224:227], a[60:63], v[92:95]\n"
"s_waitcnt lgkmcnt(3)\n"
"v_rcp_f32 v43, v43\n"
"v_mfma_f32_16x16x32_bf16 v[92:95], v[228:231], a[76:79], v[92:95]\n"
"v_mul_f32 v72, v72, v40\n"
"v_mul_f32 v73, v73, v41\n"
"v_mfma_f32_16x16x32_bf16 v[96:99], v[232:235], a[32:35], v[96:99]\n"
"ds_read_b128 v[220:223], v2 offset:21504\n"
"v_mul_f32 v74, v74, v42\n"
"v_mfma_f32_16x16x32_bf16 v[96:99], v[236:239], a[48:51], v[96:99]\n"
"buffer_load_dwordx4 a[164:167], v1, s[40:43], s84 offen offset:1024 sc1\n"
"v_mfma_f32_16x16x32_bf16 v[96:99], v[240:243], a[64:67], v[96:99]\n"
"v_mul_f32 v75, v75, v43\n"
"v_cvt_pk_bf16_f32 v24, v56, v57\n"
"v_mfma_f32_16x16x32_bf16 v[100:103], v[232:235], a[36:39], v[100:103]\n"
"ds_read_b128 v[224:227], v2 offset:22528\n"
"v_cvt_pk_bf16_f32 v25, v58, v59\n"
"v_mfma_f32_16x16x32_bf16 v[100:103], v[236:239], a[52:55], v[100:103]\n"
"v_cvt_pk_bf16_f32 v26, v72, v73\n"
"v_cvt_pk_bf16_f32 v27, v74, v75\n"
"v_mfma_f32_16x16x32_bf16 v[100:103], v[240:243], a[68:71], v[100:103]\n"
"buffer_load_dwordx4 a[168:171], v1, s[40:43], s84 offen offset:2048 sc1\n"
"v_mfma_f32_16x16x32_bf16 v[104:107], v[232:235], a[40:43], v[104:107]\n"
"buffer_store_dwordx4 v[24:27], v1, s[40:43], s85 offen offset:2048 sc1\n"
"v_mfma_f32_16x16x32_bf16 v[104:107], v[236:239], a[56:59], v[104:107]\n"
"ds_read_b128 v[228:231], v2 offset:23552\n"
"v_mul_f32 v60, v28, v60\n"
"v_mfma_f32_16x16x32_bf16 v[104:107], v[240:243], a[72:75], v[104:107]\n"
"v_mul_f32 v61, v29, v61\n"
"v_mul_f32 v62, v30, v62\n"
"v_mfma_f32_16x16x32_bf16 v[108:111], v[232:235], a[44:47], v[108:111]\n"
"v_mul_f32 v63, v31, v63\n"
"v_mul_f32 v28, s86, v28\n"
"v_mfma_f32_16x16x32_bf16 v[108:111], v[236:239], a[60:63], v[108:111]\n"
"s_waitcnt lgkmcnt(3)\n"
"buffer_load_dwordx4 a[172:175], v1, s[40:43], s84 offen offset:3072 sc1\n"
"v_mfma_f32_16x16x32_bf16 v[108:111], v[240:243], a[76:79], v[108:111]\n"
"s_add_u32 s84, s84, 4096\n"
"v_mul_f32 v29, s86, v29\n"
"v_mul_f32 v30, s86, v30\n"
"v_mfma_f32_16x16x32_bf16 v[112:115], v[208:211], a[32:35], v[112:115]\n"
"ds_read_b128 v[232:235], v2 offset:24576\n"
"v_mul_f32 v31, s86, v31\n"
"v_mfma_f32_16x16x32_bf16 v[112:115], v[212:215], a[48:51], v[112:115]\n"
"v_exp_f32 v28, v28\n"
"v_mfma_f32_16x16x32_bf16 v[112:115], v[216:219], a[64:67], v[112:115]\n"
"buffer_load_dwordx4 a[176:179], v1, s[40:43], s84 offen sc1\n"
"v_mfma_f32_16x16x32_bf16 v[116:119], v[208:211], a[36:39], v[116:119]\n"
"ds_read_b128 v[236:239], v2 offset:25600\n"
"v_mfma_f32_16x16x32_bf16 v[116:119], v[212:215], a[52:55], v[116:119]\n"
"v_exp_f32 v29, v29\n"
"v_mfma_f32_16x16x32_bf16 v[116:119], v[216:219], a[68:71], v[116:119]\n"
"v_exp_f32 v30, v30\n"
"v_mfma_f32_16x16x32_bf16 v[120:123], v[208:211], a[40:43], v[120:123]\n"
"ds_read_b128 v[240:243], v2 offset:26624\n"
"v_mfma_f32_16x16x32_bf16 v[120:123], v[212:215], a[56:59], v[120:123]\n"
"v_exp_f32 v31, v31\n"
"v_mfma_f32_16x16x32_bf16 v[120:123], v[216:219], a[72:75], v[120:123]\n"
"buffer_load_dwordx4 a[180:183], v1, s[40:43], s84 offen offset:1024 sc1\n"
"v_mfma_f32_16x16x32_bf16 v[124:127], v[208:211], a[44:47], v[124:127]\n"
"v_add_f32 v28, 1.0, v28\n"
"v_add_f32 v29, 1.0, v29\n"
"v_mfma_f32_16x16x32_bf16 v[124:127], v[212:215], a[60:63], v[124:127]\n"
"s_waitcnt lgkmcnt(3)\n"
"v_add_f32 v30, 1.0, v30\n"
"v_add_f32 v31, 1.0, v31\n"
"v_mfma_f32_16x16x32_bf16 v[124:127], v[216:219], a[76:79], v[124:127]\n"
"v_rcp_f32 v28, v28\n"
"v_mfma_f32_16x16x32_bf16 v[128:131], v[220:223], a[32:35], v[128:131]\n"
"ds_read_b128 v[208:211], v2 offset:27648\n"
"v_mfma_f32_16x16x32_bf16 v[128:131], v[224:227], a[48:51], v[128:131]\n"
"buffer_load_dwordx4 a[184:187], v1, s[40:43], s84 offen offset:2048 sc1\n"
"v_mfma_f32_16x16x32_bf16 v[128:131], v[228:231], a[64:67], v[128:131]\n"
"v_rcp_f32 v29, v29\n"
"v_mfma_f32_16x16x32_bf16 v[132:135], v[220:223], a[36:39], v[132:135]\n"
"ds_read_b128 v[212:215], v2 offset:28672\n"
"v_mfma_f32_16x16x32_bf16 v[132:135], v[224:227], a[52:55], v[132:135]\n"
"v_rcp_f32 v30, v30\n"
"v_mfma_f32_16x16x32_bf16 v[132:135], v[228:231], a[68:71], v[132:135]\n"
"v_rcp_f32 v31, v31\n"
"v_mfma_f32_16x16x32_bf16 v[136:139], v[220:223], a[40:43], v[136:139]\n"
"buffer_load_dwordx4 a[188:191], v1, s[40:43], s84 offen offset:3072 sc1\n"
"v_mfma_f32_16x16x32_bf16 v[136:139], v[224:227], a[56:59], v[136:139]\n"
"s_add_u32 s84, s84, 4096\n"
"ds_read_b128 v[216:219], v2 offset:29696\n"
"v_mul_f32 v60, v60, v28\n"
"v_mfma_f32_16x16x32_bf16 v[136:139], v[228:231], a[72:75], v[136:139]\n"
"v_mul_f32 v61, v61, v29\n"
"v_mul_f32 v62, v62, v30\n"
"v_mfma_f32_16x16x32_bf16 v[140:143], v[220:223], a[44:47], v[140:143]\n"
"v_mul_f32 v63, v63, v31\n"
"v_mul_f32 v76, v44, v76\n"
"v_mfma_f32_16x16x32_bf16 v[140:143], v[224:227], a[60:63], v[140:143]\n"
"s_waitcnt lgkmcnt(3)\n"
"v_mul_f32 v77, v45, v77\n"
"v_mul_f32 v78, v46, v78\n"
"v_mfma_f32_16x16x32_bf16 v[140:143], v[228:231], a[76:79], v[140:143]\n"
"v_mul_f32 v79, v47, v79\n"
"v_mul_f32 v44, s86, v44\n"
"v_mfma_f32_16x16x32_bf16 v[80:83], v[232:235], a[80:83], v[80:83]\n"
"ds_read_b128 v[220:223], v2 offset:30720\n"
"v_mul_f32 v45, s86, v45\n"
"v_mfma_f32_16x16x32_bf16 v[80:83], v[236:239], a[96:99], v[80:83]\n"
"v_mul_f32 v46, s86, v46\n"
"v_mul_f32 v47, s86, v47\n"
"v_mfma_f32_16x16x32_bf16 v[80:83], v[240:243], a[112:115], v[80:83]\n"
"buffer_load_dwordx4 a[192:195], v1, s[40:43], s84 offen sc1\n"
"v_mfma_f32_16x16x32_bf16 v[84:87], v[232:235], a[84:87], v[84:87]\n"
"ds_read_b128 v[224:227], v2 offset:31744\n"
"v_mfma_f32_16x16x32_bf16 v[84:87], v[236:239], a[100:103], v[84:87]\n"
"v_exp_f32 v44, v44\n"
"v_mfma_f32_16x16x32_bf16 v[84:87], v[240:243], a[116:119], v[84:87]\n"
"s_barrier\n"
"v_mfma_f32_16x16x32_bf16 v[88:91], v[232:235], a[88:91], v[88:91]\n"
"ds_read_b128 v[228:231], v2 offset:32768\n"
"v_mfma_f32_16x16x32_bf16 v[88:91], v[236:239], a[104:107], v[88:91]\n"
"v_exp_f32 v45, v45\n"
"v_mfma_f32_16x16x32_bf16 v[88:91], v[240:243], a[120:123], v[88:91]\n"
"buffer_load_dwordx4 a[196:199], v1, s[40:43], s84 offen offset:1024 sc1\n"
"v_mfma_f32_16x16x32_bf16 v[92:95], v[232:235], a[92:95], v[92:95]\n"
"v_exp_f32 v46, v46\n"
"v_mfma_f32_16x16x32_bf16 v[92:95], v[236:239], a[108:111], v[92:95]\n"
"s_waitcnt lgkmcnt(3)\n"
"v_exp_f32 v47, v47\n"
"v_mfma_f32_16x16x32_bf16 v[92:95], v[240:243], a[124:127], v[92:95]\n"
"v_add_f32 v44, 1.0, v44\n"
"v_add_f32 v45, 1.0, v45\n"
"v_mfma_f32_16x16x32_bf16 v[96:99], v[208:211], a[80:83], v[96:99]\n"
"ds_read_b128 v[232:235], v2 offset:33792\n"
"v_add_f32 v46, 1.0, v46\n"
"v_mfma_f32_16x16x32_bf16 v[96:99], v[212:215], a[96:99], v[96:99]\n"
"buffer_load_dwordx4 a[200:203], v1, s[40:43], s84 offen offset:2048 sc1\n"
"v_mfma_f32_16x16x32_bf16 v[96:99], v[216:219], a[112:115], v[96:99]\n"
"v_add_f32 v47, 1.0, v47\n"
"v_mfma_f32_16x16x32_bf16 v[100:103], v[208:211], a[84:87], v[100:103]\n"
"v_rcp_f32 v44, v44\n"
"v_mfma_f32_16x16x32_bf16 v[100:103], v[212:215], a[100:103], v[100:103]\n"
"ds_read_b128 v[236:239], v2 offset:34816\n"
"v_mfma_f32_16x16x32_bf16 v[100:103], v[216:219], a[116:119], v[100:103]\n"
"v_rcp_f32 v45, v45\n"
"v_mfma_f32_16x16x32_bf16 v[104:107], v[208:211], a[88:91], v[104:107]\n"
"ds_read_b128 v[240:243], v2 offset:35840\n"
"v_mfma_f32_16x16x32_bf16 v[104:107], v[212:215], a[104:107], v[104:107]\n"
"buffer_load_dwordx4 a[204:207], v1, s[40:43], s84 offen offset:3072 sc1\n"
"v_mfma_f32_16x16x32_bf16 v[104:107], v[216:219], a[120:123], v[104:107]\n"
"s_add_u32 s84, s84, 4096\n"
"v_rcp_f32 v46, v46\n"
"v_mfma_f32_16x16x32_bf16 v[108:111], v[208:211], a[92:95], v[108:111]\n"
"v_rcp_f32 v47, v47\n"
"v_mfma_f32_16x16x32_bf16 v[108:111], v[212:215], a[108:111], v[108:111]\n"
"s_waitcnt lgkmcnt(3)\n"
"v_mul_f32 v76, v76, v44\n"
"v_mul_f32 v77, v77, v45\n"
"v_mfma_f32_16x16x32_bf16 v[108:111], v[216:219], a[124:127], v[108:111]\n"
"buffer_load_dwordx4 a[208:211], v1, s[40:43], s84 offen sc1\n"
"v_mfma_f32_16x16x32_bf16 v[112:115], v[220:223], a[80:83], v[112:115]\n"
"ds_read_b128 a[224:227], v2 offset:49152\n"
"v_mul_f32 v78, v78, v46\n"
"v_mfma_f32_16x16x32_bf16 v[112:115], v[224:227], a[96:99], v[112:115]\n"
"v_mul_f32 v79, v79, v47\n"
"v_cvt_pk_bf16_f32 v28, v60, v61\n"
"v_mfma_f32_16x16x32_bf16 v[112:115], v[228:231], a[112:115], v[112:115]\n"
"v_cvt_pk_bf16_f32 v29, v62, v63\n"
"v_cvt_pk_bf16_f32 v30, v76, v77\n"
"v_mfma_f32_16x16x32_bf16 v[116:119], v[220:223], a[84:87], v[116:119]\n"
"ds_read_b128 a[228:231], v2 offset:50176\n"
"v_cvt_pk_bf16_f32 v31, v78, v79\n"
"v_mfma_f32_16x16x32_bf16 v[116:119], v[224:227], a[100:103], v[116:119]\n"
"buffer_load_dwordx4 a[212:215], v1, s[40:43], s84 offen offset:1024 sc1\n"
"v_mfma_f32_16x16x32_bf16 v[116:119], v[228:231], a[116:119], v[116:119]\n"
"buffer_store_dwordx4 v[28:31], v1, s[40:43], s85 offen offset:3072 sc1\n"
"v_mfma_f32_16x16x32_bf16 v[120:123], v[220:223], a[88:91], v[120:123]\n"
"buffer_load_dwordx4 a[216:219], v1, s[40:43], s84 offen offset:2048 sc1\n"
"v_mfma_f32_16x16x32_bf16 v[120:123], v[224:227], a[104:107], v[120:123]\n"
"v_mfma_f32_16x16x32_bf16 v[120:123], v[228:231], a[120:123], v[120:123]\n"
"ds_read_b128 a[232:235], v2 offset:51200\n"
"v_mfma_f32_16x16x32_bf16 v[124:127], v[220:223], a[92:95], v[124:127]\n"
"buffer_load_dwordx4 a[220:223], v1, s[40:43], s84 offen offset:3072 sc1\n"
"v_mfma_f32_16x16x32_bf16 v[124:127], v[224:227], a[108:111], v[124:127]\n"
"s_waitcnt lgkmcnt(3)\n"
"v_mfma_f32_16x16x32_bf16 v[124:127], v[228:231], a[124:127], v[124:127]\n"
"ds_read_b128 a[236:239], v2 offset:52224\n"
"v_mfma_f32_16x16x32_bf16 v[128:131], v[232:235], a[80:83], v[128:131]\n"
"v_mfma_f32_16x16x32_bf16 v[128:131], v[236:239], a[96:99], v[128:131]\n"
"v_mfma_f32_16x16x32_bf16 v[128:131], v[240:243], a[112:115], v[128:131]\n"
"s_add_u32 s84, s84, 4096\n"
"v_mfma_f32_16x16x32_bf16 v[132:135], v[232:235], a[84:87], v[132:135]\n"
"s_add_u32 s82, s64, 442368\n"
"v_mfma_f32_16x16x32_bf16 v[132:135], v[236:239], a[100:103], v[132:135]\n"
"s_add_u32 m0, s66, 49152\n"
"ds_read_b128 a[240:243], v2 offset:53248\n"
"v_mfma_f32_16x16x32_bf16 v[132:135], v[240:243], a[116:119], v[132:135]\n"
"buffer_load_dwordx4 v1, s[40:43], s82 offen sc1 lds\n"
"v_mfma_f32_16x16x32_bf16 v[136:139], v[232:235], a[88:91], v[136:139]\n"
"buffer_load_dwordx4 v1, s[40:43], s82 offen offset:1024 sc1 lds\n"
"v_mfma_f32_16x16x32_bf16 v[136:139], v[236:239], a[104:107], v[136:139]\n"
"ds_read_b128 a[244:247], v2 offset:54272\n"
"v_mfma_f32_16x16x32_bf16 v[136:139], v[240:243], a[120:123], v[136:139]\n"
"v_mfma_f32_16x16x32_bf16 v[140:143], v[232:235], a[92:95], v[140:143]\n"
"v_mfma_f32_16x16x32_bf16 v[140:143], v[236:239], a[108:111], v[140:143]\n"
"s_waitcnt vmcnt(20) lgkmcnt(4)\n"
"v_mfma_f32_16x16x32_bf16 v[140:143], v[240:243], a[124:127], v[140:143]\n"
"buffer_load_dwordx4 v1, s[40:43], s82 offen offset:2048 sc1 lds\n"
"buffer_load_dwordx4 v1, s[40:43], s82 offen offset:3072 sc1 lds\n"
"s_cmp_lg_u32 s67, 0\n"
"s_cbranch_scc1 L_roleF_19_%=\n"
"v_mfma_f32_16x16x32_bf16 v[16:19], a[224:227], a[128:131], 0\n"
"s_lshl_b32 s23, s73, 5\n"
"ds_read_b128 a[248:251], v2 offset:55296\n"
"v_mfma_f32_16x16x32_bf16 v[16:19], a[228:231], a[144:147], v[16:19]\n"
"s_add_u32 s23, s23, s68\n"
"v_mfma_f32_16x16x32_bf16 v[20:23], a[224:227], a[132:135], 0\n"
"s_add_u32 s85, s83, 114688\n"
"v_mul_f32 v112, v80, v112\n"
"v_mul_f32 v113, v81, v113\n"
"v_mfma_f32_16x16x32_bf16 v[20:23], a[228:231], a[148:151], v[20:23]\n"
"s_lshl_b32 s23, s23, 6\n"
"ds_read_b128 a[252:255], v2 offset:56320\n"
"v_mul_f32 v114, v82, v114\n"
"v_mfma_f32_16x16x32_bf16 v[24:27], a[224:227], a[136:139], 0\n"
"s_and_b32 s23, s23, 0xfffffe00\n"
"s_add_u32 s23, s23, 79036416\n"
"v_and_b32 v15, 7, v0\n"
"v_mfma_f32_16x16x32_bf16 v[24:27], a[228:231], a[152:155], v[24:27]\n"
"v_lshlrev_b32 v15, 6, v15\n"
"v_mul_f32 v115, v83, v115\n"
"v_mfma_f32_16x16x32_bf16 v[28:31], a[224:227], a[140:143], 0\n"
"s_waitcnt lgkmcnt(4)\n"
"buffer_load_dwordx2 v[10:11], v15, s[40:43], s23 offen sc1\n"
"v_mfma_f32_16x16x32_bf16 v[28:31], a[228:231], a[156:159], v[28:31]\n"
"v_mul_f32 v80, s86, v80\n"
"v_mul_f32 v81, s86, v81\n"
"v_mfma_f32_16x16x32_bf16 v[32:35], a[232:235], a[128:131], 0\n"
"v_mul_f32 v82, s86, v82\n"
"v_mul_f32 v83, s86, v83\n"
"v_mfma_f32_16x16x32_bf16 v[32:35], a[236:239], a[144:147], v[32:35]\n"
"ds_read_b128 a[224:227], v2 offset:57344\n"
"v_mfma_f32_16x16x32_bf16 v[36:39], a[232:235], a[132:135], 0\n"
"v_exp_f32 v80, v80\n"
"v_mfma_f32_16x16x32_bf16 v[36:39], a[236:239], a[148:151], v[36:39]\n"
"buffer_load_dwordx4 a[0:3], v1, s[40:43], s84 offen sc1\n"
"v_mfma_f32_16x16x32_bf16 v[40:43], a[232:235], a[136:139], 0\n"
"v_exp_f32 v81, v81\n"
"v_mfma_f32_16x16x32_bf16 v[40:43], a[236:239], a[152:155], v[40:43]\n"
"ds_read_b128 a[228:231], v2 offset:58368\n"
"v_exp_f32 v82, v82\n"
"v_mfma_f32_16x16x32_bf16 v[44:47], a[232:235], a[140:143], 0\n"
"s_waitcnt lgkmcnt(4)\n"
"v_exp_f32 v83, v83\n"
"v_mfma_f32_16x16x32_bf16 v[44:47], a[236:239], a[156:159], v[44:47]\n"
"buffer_load_dwordx4 a[4:7], v1, s[40:43], s84 offen offset:1024 sc1\n"
"v_mfma_f32_16x16x32_bf16 v[48:51], a[240:243], a[128:131], 0\n"
"v_add_f32 v80, 1.0, v80\n"
"v_add_f32 v81, 1.0, v81\n"
"v_mfma_f32_16x16x32_bf16 v[48:51], a[244:247], a[144:147], v[48:51]\n"
"ds_read_b128 a[232:235], v2 offset:59392\n"
"v_add_f32 v82, 1.0, v82\n"
"v_mfma_f32_16x16x32_bf16 v[52:55], a[240:243], a[132:135], 0\n"
"v_add_f32 v83, 1.0, v83\n"
"v_mfma_f32_16x16x32_bf16 v[52:55], a[244:247], a[148:151], v[52:55]\n"
"v_rcp_f32 v80, v80\n"
"v_mfma_f32_16x16x32_bf16 v[56:59], a[240:243], a[136:139], 0\n"
"v_rcp_f32 v81, v81\n"
"v_mfma_f32_16x16x32_bf16 v[56:59], a[244:247], a[152:155], v[56:59]\n"
"ds_read_b128 a[236:239], v2 offset:60416\n"
"buffer_load_dwordx4 a[8:11], v1, s[40:43], s84 offen offset:2048 sc1\n"
"v_rcp_f32 v82, v82\n"
"v_mfma_f32_16x16x32_bf16 v[60:63], a[240:243], a[140:143], 0\n"
"s_waitcnt lgkmcnt(4)\n"
"v_rcp_f32 v83, v83\n"
"v_mfma_f32_16x16x32_bf16 v[60:63], a[244:247], a[156:159], v[60:63]\n"
"v_mul_f32 v112, v112, v80\n"
"v_mul_f32 v113, v113, v81\n"
"v_mfma_f32_16x16x32_bf16 v[64:67], a[248:251], a[128:131], 0\n"
"buffer_load_dwordx4 a[12:15], v1, s[40:43], s84 offen offset:3072 sc1\n"
"v_mfma_f32_16x16x32_bf16 v[64:67], a[252:255], a[144:147], v[64:67]\n"
"s_add_u32 s84, s84, 4096\n"
"ds_read_b128 a[240:243], v2 offset:61440\n"
"v_mul_f32 v114, v114, v82\n"
"v_mfma_f32_16x16x32_bf16 v[68:71], a[248:251], a[132:135], 0\n"
"v_mul_f32 v115, v115, v83\n"
"v_mul_f32 v128, v96, v128\n"
"v_mfma_f32_16x16x32_bf16 v[68:71], a[252:255], a[148:151], v[68:71]\n"
"ds_read_b128 a[244:247], v2 offset:62464\n"
"v_mul_f32 v129, v97, v129\n"
"v_mfma_f32_16x16x32_bf16 v[72:75], a[248:251], a[136:139], 0\n"
"v_mul_f32 v130, v98, v130\n"
"v_mul_f32 v131, v99, v131\n"
"v_mfma_f32_16x16x32_bf16 v[72:75], a[252:255], a[152:155], v[72:75]\n"
"buffer_load_dwordx4 a[16:19], v1, s[40:43], s84 offen sc1\n"
"v_mfma_f32_16x16x32_bf16 v[76:79], a[248:251], a[140:143], 0\n"
"s_waitcnt lgkmcnt(4)\n"
"v_mul_f32 v96, s86, v96\n"
"v_mul_f32 v97, s86, v97\n"
"v_mfma_f32_16x16x32_bf16 v[76:79], a[252:255], a[156:159], v[76:79]\n"
"v_mul_f32 v98, s86, v98\n"
"v_mul_f32 v99, s86, v99\n"
"v_mfma_f32_16x16x32_bf16 v[144:147], a[224:227], a[128:131], 0\n"
"v_exp_f32 v96, v96\n"
"v_mfma_f32_16x16x32_bf16 v[144:147], a[228:231], a[144:147], v[144:147]\n"
"ds_read_b128 a[248:251], v2 offset:63488\n"
"v_mfma_f32_16x16x32_bf16 v[148:151], a[224:227], a[132:135], 0\n"
"buffer_load_dwordx4 a[20:23], v1, s[40:43], s84 offen offset:1024 sc1\n"
"v_exp_f32 v97, v97\n"
"v_mfma_f32_16x16x32_bf16 v[148:151], a[228:231], a[148:151], v[148:151]\n"
"v_exp_f32 v98, v98\n"
"v_mfma_f32_16x16x32_bf16 v[152:155], a[224:227], a[136:139], 0\n"
"ds_read_b128 a[252:255], v2 offset:64512\n"
"v_mfma_f32_16x16x32_bf16 v[152:155], a[228:231], a[152:155], v[152:155]\n"
"v_exp_f32 v99, v99\n"
"v_mfma_f32_16x16x32_bf16 v[156:159], a[224:227], a[140:143], 0\n"
"s_waitcnt lgkmcnt(4)\n"
"v_add_f32 v96, 1.0, v96\n"
"v_add_f32 v97, 1.0, v97\n"
"v_mfma_f32_16x16x32_bf16 v[156:159], a[228:231], a[156:159], v[156:159]\n"
"s_barrier\n"
"buffer_load_dwordx4 a[24:27], v1, s[40:43], s84 offen offset:2048 sc1\n"
"v_mfma_f32_16x16x32_bf16 v[160:163], a[232:235], a[128:131], 0\n"
"v_add_f32 v98, 1.0, v98\n"
"v_add_f32 v99, 1.0, v99\n"
"v_mfma_f32_16x16x32_bf16 v[160:163], a[236:239], a[144:147], v[160:163]\n"
"v_rcp_f32 v96, v96\n"
"v_mfma_f32_16x16x32_bf16 v[164:167], a[232:235], a[132:135], 0\n"
"ds_read_b128 a[224:227], v3\n"
"v_mfma_f32_16x16x32_bf16 v[164:167], a[236:239], a[148:151], v[164:167]\n"
"buffer_load_dwordx4 a[28:31], v1, s[40:43], s84 offen offset:3072 sc1\n"
"v_rcp_f32 v97, v97\n"
"v_mfma_f32_16x16x32_bf16 v[168:171], a[232:235], a[136:139], 0\n"
"s_add_u32 s84, s84, 4096\n"
"v_rcp_f32 v98, v98\n"
"v_mfma_f32_16x16x32_bf16 v[168:171], a[236:239], a[152:155], v[168:171]\n"
"ds_read_b128 a[228:231], v3 offset:1024\n"
"v_mfma_f32_16x16x32_bf16 v[172:175], a[232:235], a[140:143], 0\n"
"s_waitcnt lgkmcnt(4)\n"
"v_rcp_f32 v99, v99\n"
"v_mfma_f32_16x16x32_bf16 v[172:175], a[236:239], a[156:159], v[172:175]\n"
"v_mul_f32 v128, v128, v96\n"
"v_mul_f32 v129, v129, v97\n"
"v_mfma_f32_16x16x32_bf16 v[176:179], a[240:243], a[128:131], 0\n"
"v_mul_f32 v130, v130, v98\n"
"v_mul_f32 v131, v131, v99\n"
"v_mfma_f32_16x16x32_bf16 v[176:179], a[244:247], a[144:147], v[176:179]\n"
"ds_read_b128 a[232:235], v3 offset:2048\n"
"v_cvt_pk_bf16_f32 v80, v112, v113\n"
"v_mfma_f32_16x16x32_bf16 v[180:183], a[240:243], a[132:135], 0\n"
"v_cvt_pk_bf16_f32 v81, v114, v115\n"
"v_cvt_pk_bf16_f32 v82, v128, v129\n"
"v_mfma_f32_16x16x32_bf16 v[180:183], a[244:247], a[148:151], v[180:183]\n"
"ds_read_b128 a[236:239], v3 offset:3072\n"
"v_cvt_pk_bf16_f32 v83, v130, v131\n"
"v_mfma_f32_16x16x32_bf16 v[184:187], a[240:243], a[136:139], 0\n"
"buffer_store_dwordx4 v[80:83], v1, s[40:43], s85 offen sc1\n"
"v_mfma_f32_16x16x32_bf16 v[184:187], a[244:247], a[152:155], v[184:187]\n"
"v_mul_f32 v116, v84, v116\n"
"v_mul_f32 v117, v85, v117\n"
"v_mfma_f32_16x16x32_bf16 v[188:191], a[240:243], a[140:143], 0\n"
"s_waitcnt lgkmcnt(4)\n"
"v_mul_f32 v118, v86, v118\n"
"v_mul_f32 v119, v87, v119\n"
"v_mfma_f32_16x16x32_bf16 v[188:191], a[244:247], a[156:159], v[188:191]\n"
"v_mul_f32 v84, s86, v84\n"
"v_mul_f32 v85, s86, v85\n"
"v_mfma_f32_16x16x32_bf16 v[192:195], a[248:251], a[128:131], 0\n"
"ds_read_b128 a[240:243], v3 offset:4096\n"
"v_mul_f32 v86, s86, v86\n"
"v_mfma_f32_16x16x32_bf16 v[192:195], a[252:255], a[144:147], v[192:195]\n"
"s_waitcnt vmcnt(9)\n"
"v_mul_f32 v87, s86, v87\n"
"v_cmp_ne_u64 vcc, s[92:93], v[10:11]\n"
"v_mfma_f32_16x16x32_bf16 v[196:199], a[248:251], a[132:135], 0\n"
"s_cbranch_vccnz L_slow_20_%=\n"
"L_back_21_%=:\n"
"v_exp_f32 v84, v84\n"
"v_mfma_f32_16x16x32_bf16 v[196:199], a[252:255], a[148:151], v[196:199]\n"
"ds_read_b128 a[244:247], v3 offset:5120\n"
"v_mfma_f32_16x16x32_bf16 v[200:203], a[248:251], a[136:139], 0\n"
"s_lshl_b32 s78, s73, 5\n"
"v_exp_f32 v85, v85\n"
"v_mfma_f32_16x16x32_bf16 v[200:203], a[252:255], a[152:155], v[200:203]\n"
"s_add_u32 s78, s78, s68\n"
"buffer_load_dwordx4 a[32:35], v1, s[40:43], s84 offen sc1\n"
"v_mfma_f32_16x16x32_bf16 v[204:207], a[248:251], a[140:143], 0\n"
"s_waitcnt lgkmcnt(4)\n"
"v_exp_f32 v86, v86\n"
"v_mfma_f32_16x16x32_bf16 v[204:207], a[252:255], a[156:159], v[204:207]\n"
"s_lshl_b32 s78, s78, 6\n"
"v_exp_f32 v87, v87\n"
"v_mfma_f32_16x16x32_bf16 v[208:211], a[224:227], a[128:131], 0\n"
"s_add_u32 s78, s78, 79101952\n"
"ds_read_b128 a[248:251], v3 offset:6144\n"
"v_add_f32 v84, 1.0, v84\n"
"v_mfma_f32_16x16x32_bf16 v[208:211], a[228:231], a[144:147], v[208:211]\n"
"buffer_load_dwordx4 a[36:39], v1, s[40:43], s84 offen offset:1024 sc1\n"
"v_mfma_f32_16x16x32_bf16 v[212:215], a[224:227], a[132:135], 0\n"
"v_add_f32 v85, 1.0, v85\n"
"v_mov_b32 v8, s92\n"
"v_mov_b32 v9, s93\n"
"v_mfma_f32_16x16x32_bf16 v[212:215], a[228:231], a[148:151], v[212:215]\n"
"buffer_store_dwordx2 v[8:9], v14, s[40:43], s78 offen sc1\n"
"v_add_f32 v86, 1.0, v86\n"
"v_add_f32 v87, 1.0, v87\n"
"v_mfma_f32_16x16x32_bf16 v[216:219], a[224:227], a[136:139], 0\n"
"v_rcp_f32 v84, v84\n"
"v_mfma_f32_16x16x32_bf16 v[216:219], a[228:231], a[152:155], v[216:219]\n"
"ds_read_b128 a[252:255], v3 offset:7168\n"
"v_mfma_f32_16x16x32_bf16 v[220:223], a[224:227], a[140:143], 0\n"
"s_waitcnt lgkmcnt(4)\n"
"v_rcp_f32 v85, v85\n"
"v_mfma_f32_16x16x32_bf16 v[220:223], a[228:231], a[156:159], v[220:223]\n"
"buffer_load_dwordx4 a[40:43], v1, s[40:43], s84 offen offset:2048 sc1\n"
"v_rcp_f32 v86, v86\n"
"v_mfma_f32_16x16x32_bf16 v[16:19], a[232:235], a[160:163], v[16:19]\n"
"v_rcp_f32 v87, v87\n"
"v_mfma_f32_16x16x32_bf16 v[16:19], a[236:239], a[176:179], v[16:19]\n"
"ds_read_b128 a[224:227], v3 offset:8192\n"
"v_mul_f32 v116, v116, v84\n"
"v_mfma_f32_16x16x32_bf16 v[20:23], a[232:235], a[164:167], v[20:23]\n"
"buffer_load_dwordx4 a[44:47], v1, s[40:43], s84 offen offset:3072 sc1\n"
"v_mfma_f32_16x16x32_bf16 v[20:23], a[236:239], a[180:183], v[20:23]\n"
"s_add_u32 s84, s84, 4096\n"
"v_mul_f32 v117, v117, v85\n"
"v_mul_f32 v118, v118, v86\n"
"v_mfma_f32_16x16x32_bf16 v[24:27], a[232:235], a[168:171], v[24:27]\n"
"ds_read_b128 a[228:231], v3 offset:9216\n"
"v_mul_f32 v119, v119, v87\n"
"v_mfma_f32_16x16x32_bf16 v[24:27], a[236:239], a[184:187], v[24:27]\n"
"v_mul_f32 v132, v100, v132\n"
"v_mul_f32 v133, v101, v133\n"
"v_mfma_f32_16x16x32_bf16 v[28:31], a[232:235], a[172:175], v[28:31]\n"
"s_waitcnt lgkmcnt(4)\n"
"v_mul_f32 v134, v102, v134\n"
"v_mul_f32 v135, v103, v135\n"
"v_mfma_f32_16x16x32_bf16 v[28:31], a[236:239], a[188:191], v[28:31]\n"
"v_mul_f32 v100, s86, v100\n"
"v_mul_f32 v101, s86, v101\n"
"v_mfma_f32_16x16x32_bf16 v[32:35], a[240:243], a[160:163], v[32:35]\n"
"buffer_load_dwordx4 a[48:51], v1, s[40:43], s84 offen sc1\n"
"v_mfma_f32_16x16x32_bf16 v[32:35], a[244:247], a[176:179], v[32:35]\n"
"ds_read_b128 a[232:235], v3 offset:10240\n"
"v_mul_f32 v102, s86, v102\n"
"v_mfma_f32_16x16x32_bf16 v[36:39], a[240:243], a[164:167], v[36:39]\n"
"v_mul_f32 v103, s86, v103\n"
"v_exp_f32 v100, v100\n"
"v_mfma_f32_16x16x32_bf16 v[36:39], a[244:247], a[180:183], v[36:39]\n"
"buffer_load_dwordx4 a[52:55], v1, s[40:43], s84 offen offset:1024 sc1\n"
"v_mfma_f32_16x16x32_bf16 v[40:43], a[240:243], a[168:171], v[40:43]\n"
"v_exp_f32 v101, v101\n"
"v_mfma_f32_16x16x32_bf16 v[40:43], a[244:247], a[184:187], v[40:43]\n"
"ds_read_b128 a[236:239], v3 offset:11264\n"
"v_exp_f32 v102, v102\n"
"v_mfma_f32_16x16x32_bf16 v[44:47], a[240:243], a[172:175], v[44:47]\n"
"s_waitcnt lgkmcnt(4)\n"
"v_exp_f32 v103, v103\n"
"v_mfma_f32_16x16x32_bf16 v[44:47], a[244:247], a[188:191], v[44:47]\n"
"v_add_f32 v100, 1.0, v100\n"
"v_add_f32 v101, 1.0, v101\n"
"v_mfma_f32_16x16x32_bf16 v[48:51], a[248:251], a[160:163], v[48:51]\n"
"buffer_load_dwordx4 a[56:59], v1, s[40:43], s84 offen offset:2048 sc1\n"
"v_mfma_f32_16x16x32_bf16 v[48:51], a[252:255], a[176:179], v[48:51]\n"
"ds_read_b128 a[240:243], v3 offset:12288\n"
"v_add_f32 v102, 1.0, v102\n"
"v_mfma_f32_16x16x32_bf16 v[52:55], a[248:251], a[164:167], v[52:55]\n"
"v_add_f32 v103, 1.0, v103\n"
"v_mfma_f32_16x16x32_bf16 v[52:55], a[252:255], a[180:183], v[52:55]\n"
"v_rcp_f32 v100, v100\n"
"v_mfma_f32_16x16x32_bf16 v[56:59], a[248:251], a[168:171], v[56:59]\n"
"v_rcp_f32 v101, v101\n"
"v_mfma_f32_16x16x32_bf16 v[56:59], a[252:255], a[184:187], v[56:59]\n"
"ds_read_b128 a[244:247], v3 offset:13312\n"
"buffer_load_dwordx4 a[60:63], v1, s[40:43], s84 offen offset:3072 sc1\n"
"v_mfma_f32_16x16x32_bf16 v[60:63], a[248:251], a[172:175], v[60:63]\n"
"s_waitcnt lgkmcnt(4)\n"
"v_rcp_f32 v102, v102\n"
"v_mfma_f32_16x16x32_bf16 v[60:63], a[252:255], a[188:191], v[60:63]\n"
"s_add_u32 s84, s84, 4096\n"
"v_rcp_f32 v103, v103\n"
"v_mfma_f32_16x16x32_bf16 v[64:67], a[224:227], a[160:163], v[64:67]\n"
"v_mul_f32 v132, v132, v100\n"
"v_mul_f32 v133, v133, v101\n"
"v_mfma_f32_16x16x32_bf16 v[64:67], a[228:231], a[176:179], v[64:67]\n"
"ds_read_b128 a[248:251], v3 offset:14336\n"
"v_mul_f32 v134, v134, v102\n"
"v_mfma_f32_16x16x32_bf16 v[68:71], a[224:227], a[164:167], v[68:71]\n"
"v_mul_f32 v135, v135, v103\n"
"v_cvt_pk_bf16_f32 v84, v116, v117\n"
"v_mfma_f32_16x16x32_bf16 v[68:71], a[228:231], a[180:183], v[68:71]\n"
"ds_read_b128 a[252:255], v3 offset:15360\n"
"v_cvt_pk_bf16_f32 v85, v118, v119\n"
"v_mfma_f32_16x16x32_bf16 v[72:75], a[224:227], a[168:171], v[72:75]\n"
"v_cvt_pk_bf16_f32 v86, v132, v133\n"
"v_cvt_pk_bf16_f32 v87, v134, v135\n"
"v_mfma_f32_16x16x32_bf16 v[72:75], a[228:231], a[184:187], v[72:75]\n"
"buffer_store_dwordx4 v[84:87], v1, s[40:43], s85 offen offset:1024 sc1\n"
"v_mfma_f32_16x16x32_bf16 v[76:79], a[224:227], a[172:175], v[76:79]\n"
"s_waitcnt lgkmcnt(4)\n"
"v_mul_f32 v120, v88, v120\n"
"v_mul_f32 v121, v89, v121\n"
"v_mfma_f32_16x16x32_bf16 v[76:79], a[228:231], a[188:191], v[76:79]\n"
"v_mul_f32 v122, v90, v122\n"
"v_mul_f32 v123, v91, v123\n"
"v_mfma_f32_16x16x32_bf16 v[144:147], a[232:235], a[160:163], v[144:147]\n"
"ds_read_b128 a[224:227], v3 offset:16384\n"
"v_mul_f32 v88, s86, v88\n"
"v_mfma_f32_16x16x32_bf16 v[144:147], a[236:239], a[176:179], v[144:147]\n"
"v_mul_f32 v89, s86, v89\n"
"v_mul_f32 v90, s86, v90\n"
"v_mfma_f32_16x16x32_bf16 v[148:151], a[232:235], a[164:167], v[148:151]\n"
"v_mul_f32 v91, s86, v91\n"
"v_mfma_f32_16x16x32_bf16 v[148:151], a[236:239], a[180:183], v[148:151]\n"
"ds_read_b128 a[228:231], v3 offset:17408\n"
"v_mfma_f32_16x16x32_bf16 v[152:155], a[232:235], a[168:171], v[152:155]\n"
"v_exp_f32 v88, v88\n"
"v_mfma_f32_16x16x32_bf16 v[152:155], a[236:239], a[184:187], v[152:155]\n"
"v_exp_f32 v89, v89\n"
"v_mfma_f32_16x16x32_bf16 v[156:159], a[232:235], a[172:175], v[156:159]\n"
"s_waitcnt lgkmcnt(4)\n"
"v_exp_f32 v90, v90\n"
"v_mfma_f32_16x16x32_bf16 v[156:159], a[236:239], a[188:191], v[156:159]\n"
"v_exp_f32 v91, v91\n"
"v_mfma_f32_16x16x32_bf16 v[160:163], a[240:243], a[160:163], v[160:163]\n"
"ds_read_b128 a[232:235], v3 offset:18432\n"
"v_add_f32 v88, 1.0, v88\n"
"v_mfma_f32_16x16x32_bf16 v[160:163], a[244:247], a[176:179], v[160:163]\n"
"buffer_load_dwordx4 a[64:67], v1, s[40:43], s84 offen sc1\n"
"v_mfma_f32_16x16x32_bf16 v[164:167], a[240:243], a[164:167], v[164:167]\n"
"v_add_f32 v89, 1.0, v89\n"
"v_add_f32 v90, 1.0, v90\n"
"v_mfma_f32_16x16x32_bf16 v[164:167], a[244:247], a[180:183], v[164:167]\n"
"ds_read_b128 a[236:239], v3 offset:19456\n"
"v_add_f32 v91, 1.0, v91\n"
"v_mfma_f32_16x16x32_bf16 v[168:171], a[240:243], a[168:171], v[168:171]\n"
"v_rcp_f32 v88, v88\n"
"v_mfma_f32_16x16x32_bf16 v[168:171], a[244:247], a[184:187], v[168:171]\n"
"v_rcp_f32 v89, v89\n"
"v_mfma_f32_16x16x32_bf16 v[172:175], a[240:243], a[172:175], v[172:175]\n"
"s_waitcnt lgkmcnt(4)\n"
"buffer_load_dwordx4 a[68:71], v1, s[40:43], s84 offen offset:1024 sc1\n"
"v_mfma_f32_16x16x32_bf16 v[172:175], a[244:247], a[188:191], v[172:175]\n"
"v_rcp_f32 v90, v90\n"
"v_mfma_f32_16x16x32_bf16 v[176:179], a[248:251], a[160:163], v[176:179]\n"
"ds_read_b128 a[240:243], v3 offset:20480\n"
"v_mfma_f32_16x16x32_bf16 v[176:179], a[252:255], a[176:179], v[176:179]\n"
"v_rcp_f32 v91, v91\n"
"v_mfma_f32_16x16x32_bf16 v[180:183], a[248:251], a[164:167], v[180:183]\n"
"v_mul_f32 v120, v120, v88\n"
"v_mul_f32 v121, v121, v89\n"
"v_mfma_f32_16x16x32_bf16 v[180:183], a[252:255], a[180:183], v[180:183]\n"
"ds_read_b128 a[244:247], v3 offset:21504\n"
"v_mul_f32 v122, v122, v90\n"
"v_mfma_f32_16x16x32_bf16 v[184:187], a[248:251], a[168:171], v[184:187]\n"
"buffer_load_dwordx4 a[72:75], v1, s[40:43], s84 offen offset:2048 sc1\n"
"v_mfma_f32_16x16x32_bf16 v[184:187], a[252:255], a[184:187], v[184:187]\n"
"v_mul_f32 v123, v123, v91\n"
"v_mul_f32 v136, v104, v136\n"
"v_mfma_f32_16x16x32_bf16 v[188:191], a[248:251], a[172:175], v[188:191]\n"
"s_waitcnt lgkmcnt(4)\n"
"v_mul_f32 v137, v105, v137\n"
"v_mul_f32 v138, v106, v138\n"
"v_mfma_f32_16x16x32_bf16 v[188:191], a[252:255], a[188:191], v[188:191]\n"
"v_mul_f32 v139, v107, v139\n"
"v_mul_f32 v104, s86, v104\n"
"v_mfma_f32_16x16x32_bf16 v[192:195], a[224:227], a[160:163], v[192:195]\n"
"buffer_load_dwordx4 a[76:79], v1, s[40:43], s84 offen offset:3072 sc1\n"
"v_mfma_f32_16x16x32_bf16 v[192:195], a[228:231], a[176:179], v[192:195]\n"
"s_add_u32 s84, s84, 4096\n"
"ds_read_b128 a[248:251], v3 offset:22528\n"
"v_mul_f32 v105, s86, v105\n"
"v_mfma_f32_16x16x32_bf16 v[196:199], a[224:227], a[164:167], v[196:199]\n"
"v_mul_f32 v106, s86, v106\n"
"v_mul_f32 v107, s86, v107\n"
"v_mfma_f32_16x16x32_bf16 v[196:199], a[228:231], a[180:183], v[196:199]\n"
"v_exp_f32 v104, v104\n"
"v_mfma_f32_16x16x32_bf16 v[200:203], a[224:227], a[168:171], v[200:203]\n"
"ds_read_b128 a[252:255], v3 offset:23552\n"
"v_mfma_f32_16x16x32_bf16 v[200:203], a[228:231], a[184:187], v[200:203]\n"
"v_exp_f32 v105, v105\n"
"v_mfma_f32_16x16x32_bf16 v[204:207], a[224:227], a[172:175], v[204:207]\n"
"s_waitcnt lgkmcnt(4)\n"
"buffer_load_dwordx4 a[80:83], v1, s[40:43], s84 offen sc1\n"
"v_mfma_f32_16x16x32_bf16 v[204:207], a[228:231], a[188:191], v[204:207]\n"
"v_exp_f32 v106, v106\n"
"v_mfma_f32_16x16x32_bf16 v[208:211], a[232:235], a[160:163], v[208:211]\n"
"v_exp_f32 v107, v107\n"
"v_mfma_f32_16x16x32_bf16 v[208:211], a[236:239], a[176:179], v[208:211]\n"
"ds_read_b128 a[224:227], v3 offset:24576\n"
"v_add_f32 v104, 1.0, v104\n"
"v_mfma_f32_16x16x32_bf16 v[212:215], a[232:235], a[164:167], v[212:215]\n"
"buffer_load_dwordx4 a[84:87], v1, s[40:43], s84 offen offset:1024 sc1\n"
"v_add_f32 v105, 1.0, v105\n"
"v_mfma_f32_16x16x32_bf16 v[212:215], a[236:239], a[180:183], v[212:215]\n"
"v_add_f32 v106, 1.0, v106\n"
"v_add_f32 v107, 1.0, v107\n"
"v_mfma_f32_16x16x32_bf16 v[216:219], a[232:235], a[168:171], v[216:219]\n"
"v_rcp_f32 v104, v104\n"
"v_mfma_f32_16x16x32_bf16 v[216:219], a[236:239], a[184:187], v[216:219]\n"
"ds_read_b128 a[228:231], v3 offset:25600\n"
"v_mfma_f32_16x16x32_bf16 v[220:223], a[232:235], a[172:175], v[220:223]\n"
"s_waitcnt lgkmcnt(4)\n"
"v_rcp_f32 v105, v105\n"
"v_mfma_f32_16x16x32_bf16 v[220:223], a[236:239], a[188:191], v[220:223]\n"
"buffer_load_dwordx4 a[88:91], v1, s[40:43], s84 offen offset:2048 sc1\n"
"v_rcp_f32 v106, v106\n"
"v_mfma_f32_16x16x32_bf16 v[16:19], a[240:243], a[192:195], v[16:19]\n"
"v_rcp_f32 v107, v107\n"
"v_mfma_f32_16x16x32_bf16 v[16:19], a[244:247], a[208:211], v[16:19]\n"
"ds_read_b128 a[232:235], v3 offset:26624\n"
"v_mul_f32 v136, v136, v104\n"
"v_mfma_f32_16x16x32_bf16 v[20:23], a[240:243], a[196:199], v[20:23]\n"
"buffer_load_dwordx4 a[92:95], v1, s[40:43], s84 offen offset:3072 sc1\n"
"v_mfma_f32_16x16x32_bf16 v[20:23], a[244:247], a[212:215], v[20:23]\n"
"s_add_u32 s84, s84, 4096\n"
"v_mul_f32 v137, v137, v105\n"
"v_mul_f32 v138, v138, v106\n"
"v_mfma_f32_16x16x32_bf16 v[24:27], a[240:243], a[200:203], v[24:27]\n"
"ds_read_b128 a[236:239], v3 offset:27648\n"
"v_mul_f32 v139, v139, v107\n"
"v_mfma_f32_16x16x32_bf16 v[24:27], a[244:247], a[216:219], v[24:27]\n"
"v_cvt_pk_bf16_f32 v88, v120, v121\n"
"v_cvt_pk_bf16_f32 v89, v122, v123\n"
"v_mfma_f32_16x16x32_bf16 v[28:31], a[240:243], a[204:207], v[28:31]\n"
"s_waitcnt lgkmcnt(4)\n"
"v_cvt_pk_bf16_f32 v90, v136, v137\n"
"v_cvt_pk_bf16_f32 v91, v138, v139\n"
"v_mfma_f32_16x16x32_bf16 v[28:31], a[244:247], a[220:223], v[28:31]\n"
"buffer_store_dwordx4 v[88:91], v1, s[40:43], s85 offen offset:2048 sc1\n"
"v_mfma_f32_16x16x32_bf16 v[32:35], a[248:251], a[192:195], v[32:35]\n"
"ds_read_b128 a[240:243], v3 offset:28672\n"
"v_mul_f32 v124, v92, v124\n"
"v_mfma_f32_16x16x32_bf16 v[32:35], a[252:255], a[208:211], v[32:35]\n"
"v_mul_f32 v125, v93, v125\n"
"v_mul_f32 v126, v94, v126\n"
"v_mfma_f32_16x16x32_bf16 v[36:39], a[248:251], a[196:199], v[36:39]\n"
"v_mul_f32 v127, v95, v127\n"
"v_mul_f32 v92, s86, v92\n"
"v_mfma_f32_16x16x32_bf16 v[36:39], a[252:255], a[212:215], v[36:39]\n"
"ds_read_b128 a[244:247], v3 offset:29696\n"
"v_mul_f32 v93, s86, v93\n"
"v_mfma_f32_16x16x32_bf16 v[40:43], a[248:251], a[200:203], v[40:43]\n"
"v_mul_f32 v94, s86, v94\n"
"v_mul_f32 v95, s86, v95\n"
"v_mfma_f32_16x16x32_bf16 v[40:43], a[252:255], a[216:219], v[40:43]\n"
"v_exp_f32 v92, v92\n"
"v_mfma_f32_16x16x32_bf16 v[44:47], a[248:251], a[204:207], v[44:47]\n"
"s_waitcnt lgkmcnt(4)\n"
"v_exp_f32 v93, v93\n"
"v_mfma_f32_16x16x32_bf16 v[44:47], a[252:255], a[220:223], v[44:47]\n"
"v_exp_f32 v94, v94\n"
"v_mfma_f32_16x16x32_bf16 v[48:51], a[224:227], a[192:195], v[48:51]\n"
"ds_read_b128 a[248:251], v3 offset:30720\n"
"v_mfma_f32_16x16x32_bf16 v[48:51], a[228:231], a[208:211], v[48:51]\n"
"v_exp_f32 v95, v95\n"
"v_mfma_f32_16x16x32_bf16 v[52:55], a[224:227], a[196:199], v[52:55]\n"
"v_add_f32 v92, 1.0, v92\n"
"v_add_f32 v93, 1.0, v93\n"
"v_mfma_f32_16x16x32_bf16 v[52:55], a[228:231], a[212:215], v[52:55]\n"
"ds_read_b128 a[252:255], v3 offset:31744\n"
"v_add_f32 v94, 1.0, v94\n"
"v_mfma_f32_16x16x32_bf16 v[56:59], a[224:227], a[200:203], v[56:59]\n"
"v_add_f32 v95, 1.0, v95\n"
"v_mfma_f32_16x16x32_bf16 v[56:59], a[228:231], a[216:219], v[56:59]\n"
"v_rcp_f32 v92, v92\n"
"v_mfma_f32_16x16x32_bf16 v[60:63], a[224:227], a[204:207], v[60:63]\n"
"s_waitcnt lgkmcnt(4)\n"
"v_rcp_f32 v93, v93\n"
"v_mfma_f32_16x16x32_bf16 v[60:63], a[228:231], a[220:223], v[60:63]\n"
"s_barrier\n"
"v_mfma_f32_16x16x32_bf16 v[64:67], a[232:235], a[192:195], v[64:67]\n"
"ds_read_b128 a[224:227], v1\n"
"v_mfma_f32_16x16x32_bf16 v[64:67], a[236:239], a[208:211], v[64:67]\n"
"buffer_load_dwordx4 a[96:99], v1, s[40:43], s84 offen sc1\n"
"v_mfma_f32_16x16x32_bf16 v[68:71], a[232:235], a[196:199], v[68:71]\n"
"v_rcp_f32 v94, v94\n"
"v_mfma_f32_16x16x32_bf16 v[68:71], a[236:239], a[212:215], v[68:71]\n"
"v_rcp_f32 v95, v95\n"
"v_mfma_f32_16x16x32_bf16 v[72:75], a[232:235], a[200:203], v[72:75]\n"
"ds_read_b128 a[228:231], v1 offset:1024\n"
"v_mul_f32 v124, v124, v92\n"
"v_mfma_f32_16x16x32_bf16 v[72:75], a[236:239], a[216:219], v[72:75]\n"
"buffer_load_dwordx4 a[100:103], v1, s[40:43], s84 offen offset:1024 sc1\n"
"v_mfma_f32_16x16x32_bf16 v[76:79], a[232:235], a[204:207], v[76:79]\n"
"s_waitcnt lgkmcnt(4)\n"
"v_mul_f32 v125, v125, v93\n"
"v_mul_f32 v126, v126, v94\n"
"v_mfma_f32_16x16x32_bf16 v[76:79], a[236:239], a[220:223], v[76:79]\n"
"v_mul_f32 v127, v127, v95\n"
"v_mul_f32 v140, v108, v140\n"
"v_mfma_f32_16x16x32_bf16 v[144:147], a[240:243], a[192:195], v[144:147]\n"
"v_mul_f32 v141, v109, v141\n"
"v_mul_f32 v142, v110, v142\n"
"v_mfma_f32_16x16x32_bf16 v[144:147], a[244:247], a[208:211], v[144:147]\n"
"ds_read_b128 a[232:235], v1 offset:2048\n"
"v_mul_f32 v143, v111, v143\n"
"v_mfma_f32_16x16x32_bf16 v[148:151], a[240:243], a[196:199], v[148:151]\n"
"v_mul_f32 v108, s86, v108\n"
"v_mul_f32 v109, s86, v109\n"
"v_mfma_f32_16x16x32_bf16 v[148:151], a[244:247], a[212:215], v[148:151]\n"
"buffer_load_dwordx4 a[104:107], v1, s[40:43], s84 offen offset:2048 sc1\n"
"v_mfma_f32_16x16x32_bf16 v[152:155], a[240:243], a[200:203], v[152:155]\n"
"ds_read_b128 a[236:239], v1 offset:3072\n"
"v_mul_f32 v110, s86, v110\n"
"v_mfma_f32_16x16x32_bf16 v[152:155], a[244:247], a[216:219], v[152:155]\n"
"v_mul_f32 v111, s86, v111\n"
"v_exp_f32 v108, v108\n"
"v_mfma_f32_16x16x32_bf16 v[156:159], a[240:243], a[204:207], v[156:159]\n"
"s_waitcnt lgkmcnt(4)\n"
"buffer_load_dwordx4 a[108:111], v1, s[40:43], s84 offen offset:3072 sc1\n"
"v_mfma_f32_16x16x32_bf16 v[156:159], a[244:247], a[220:223], v[156:159]\n"
"s_add_u32 s84, s84, 4096\n"
"v_exp_f32 v109, v109\n"
"v_mfma_f32_16x16x32_bf16 v[160:163], a[248:251], a[192:195], v[160:163]\n"
"v_exp_f32 v110, v110\n"
"v_mfma_f32_16x16x32_bf16 v[160:163], a[252:255], a[208:211], v[160:163]\n"
"ds_read_b128 a[240:243], v1 offset:4096\n"
"v_mfma_f32_16x16x32_bf16 v[164:167], a[248:251], a[196:199], v[164:167]\n"
"v_exp_f32 v111, v111\n"
"v_mfma_f32_16x16x32_bf16 v[164:167], a[252:255], a[212:215], v[164:167]\n"
"v_add_f32 v108, 1.0, v108\n"
"v_add_f32 v109, 1.0, v109\n"
"v_mfma_f32_16x16x32_bf16 v[168:171], a[248:251], a[200:203], v[168:171]\n"
"buffer_load_dwordx4 a[112:115], v1, s[40:43], s84 offen sc1\n"
"v_add_f32 v110, 1.0, v110\n"
"v_mfma_f32_16x16x32_bf16 v[168:171], a[252:255], a[216:219], v[168:171]\n"
"ds_read_b128 a[244:247], v1 offset:5120\n"
"v_add_f32 v111, 1.0, v111\n"
"v_mfma_f32_16x16x32_bf16 v[172:175], a[248:251], a[204:207], v[172:175]\n"
"s_waitcnt lgkmcnt(4)\n"
"v_rcp_f32 v108, v108\n"
"v_mfma_f32_16x16x32_bf16 v[172:175], a[252:255], a[220:223], v[172:175]\n"
"v_rcp_f32 v109, v109\n"
"v_mfma_f32_16x16x32_bf16 v[176:179], a[224:227], a[192:195], v[176:179]\n"
"buffer_load_dwordx4 a[116:119], v1, s[40:43], s84 offen offset:1024 sc1\n"
"v_mfma_f32_16x16x32_bf16 v[176:179], a[228:231], a[208:211], v[176:179]\n"
"v_rcp_f32 v110, v110\n"
"v_mfma_f32_16x16x32_bf16 v[180:183], a[224:227], a[196:199], v[180:183]\n"
"ds_read_b128 a[248:251], v1 offset:6144\n"
"v_rcp_f32 v111, v111\n"
"v_mfma_f32_16x16x32_bf16 v[180:183], a[228:231], a[212:215], v[180:183]\n"
"v_mul_f32 v140, v140, v108\n"
"v_mul_f32 v141, v141, v109\n"
"v_mfma_f32_16x16x32_bf16 v[184:187], a[224:227], a[200:203], v[184:187]\n"
"ds_read_b128 a[252:255], v1 offset:7168\n"
"v_mul_f32 v142, v142, v110\n"
"v_mfma_f32_16x16x32_bf16 v[184:187], a[228:231], a[216:219], v[184:187]\n"
"buffer_load_dwordx4 a[120:123], v1, s[40:43], s84 offen offset:2048 sc1\n"
"v_mfma_f32_16x16x32_bf16 v[188:191], a[224:227], a[204:207], v[188:191]\n"
"s_waitcnt lgkmcnt(4)\n"
"v_mul_f32 v143, v143, v111\n"
"v_cvt_pk_bf16_f32 v92, v124, v125\n"
"v_mfma_f32_16x16x32_bf16 v[188:191], a[228:231], a[220:223], v[188:191]\n"
"v_cvt_pk_bf16_f32 v93, v126, v127\n"
"v_cvt_pk_bf16_f32 v94, v140, v141\n"
"v_mfma_f32_16x16x32_bf16 v[192:195], a[232:235], a[192:195], v[192:195]\n"
"ds_read_b128 a[224:227], v1 offset:8192\n"
"v_cvt_pk_bf16_f32 v95, v142, v143\n"
"v_mfma_f32_16x16x32_bf16 v[192:195], a[236:239], a[208:211], v[192:195]\n"
"buffer_load_dwordx4 a[124:127], v1, s[40:43], s84 offen offset:3072 sc1\n"
"buffer_store_dwordx4 v[92:95], v1, s[40:43], s85 offen offset:3072 sc1\n"
"v_mfma_f32_16x16x32_bf16 v[196:199], a[232:235], a[196:199], v[196:199]\n"
"s_add_u32 s84, s84, 4096\n"
"v_mfma_f32_16x16x32_bf16 v[196:199], a[236:239], a[212:215], v[196:199]\n"
"s_add_u32 s82, s64, 458752\n"
"ds_read_b128 a[228:231], v1 offset:9216\n"
"v_mfma_f32_16x16x32_bf16 v[200:203], a[232:235], a[200:203], v[200:203]\n"
"s_add_u32 m0, s66, 65536\n"
"v_mfma_f32_16x16x32_bf16 v[200:203], a[236:239], a[216:219], v[200:203]\n"
"buffer_load_dwordx4 v1, s[40:43], s82 offen sc1 lds\n"
"v_mfma_f32_16x16x32_bf16 v[204:207], a[232:235], a[204:207], v[204:207]\n"
"s_waitcnt lgkmcnt(4)\n"
"buffer_load_dwordx4 v1, s[40:43], s82 offen offset:1024 sc1 lds\n"
"v_mfma_f32_16x16x32_bf16 v[204:207], a[236:239], a[220:223], v[204:207]\n"
"v_mfma_f32_16x16x32_bf16 v[208:211], a[240:243], a[192:195], v[208:211]\n"
"ds_read_b128 a[232:235], v1 offset:10240\n"
"v_mfma_f32_16x16x32_bf16 v[208:211], a[244:247], a[208:211], v[208:211]\n"
"buffer_load_dwordx4 v1, s[40:43], s82 offen offset:2048 sc1 lds\n"
"v_mfma_f32_16x16x32_bf16 v[212:215], a[240:243], a[196:199], v[212:215]\n"
"s_lshl_b32 s96, s73, 2\n"
"v_mfma_f32_16x16x32_bf16 v[212:215], a[244:247], a[212:215], v[212:215]\n"
"s_add_u32 s96, s96, s71\n"
"ds_read_b128 a[236:239], v1 offset:11264\n"
"v_mfma_f32_16x16x32_bf16 v[216:219], a[240:243], a[200:203], v[216:219]\n"
"s_mul_i32 s96, s96, 147456\n"
"v_mfma_f32_16x16x32_bf16 v[216:219], a[244:247], a[216:219], v[216:219]\n"
"s_add_u32 s52, s96, 60030976\n"
"buffer_load_dwordx4 v1, s[40:43], s82 offen offset:3072 sc1 lds\n"
"v_mfma_f32_16x16x32_bf16 v[220:223], a[240:243], a[204:207], v[220:223]\n"
"s_waitcnt vmcnt(33) lgkmcnt(4)\n"
"v_mfma_f32_16x16x32_bf16 v[220:223], a[244:247], a[220:223], v[220:223]\n"
"s_lshl_b32 s96, s100, 11\n"
"v_mfma_f32_16x16x32_bf16 v[16:19], a[248:251], a[0:3], v[16:19]\n"
"s_add_u32 s96, s96, s52\n"
"ds_read_b128 a[240:243], v1 offset:12288\n"
"v_mfma_f32_16x16x32_bf16 v[16:19], a[252:255], a[16:19], v[16:19]\n"
"s_mul_i32 s53, s73, 73728\n"
"v_mfma_f32_16x16x32_bf16 v[20:23], a[248:251], a[4:7], v[20:23]\n"
"s_lshl_b32 s23, s73, 5\n"
"v_mfma_f32_16x16x32_bf16 v[20:23], a[252:255], a[20:23], v[20:23]\n"
"s_add_u32 s23, s23, s68\n"
"ds_read_b128 a[244:247], v1 offset:13312\n"
"v_mfma_f32_16x16x32_bf16 v[24:27], a[248:251], a[8:11], v[24:27]\n"
"s_lshl_b32 s23, s23, 6\n"
"v_mfma_f32_16x16x32_bf16 v[24:27], a[252:255], a[24:27], v[24:27]\n"
"s_and_b32 s23, s23, 0xfffffe00\n"
"v_mfma_f32_16x16x32_bf16 v[28:31], a[248:251], a[12:15], v[28:31]\n"
"s_waitcnt lgkmcnt(4)\n"
"s_add_u32 s23, s23, 79101952\n"
"v_and_b32 v15, 7, v0\n"
"v_mfma_f32_16x16x32_bf16 v[28:31], a[252:255], a[28:31], v[28:31]\n"
"s_mul_i32 s80, s71, 288\n"
"v_lshlrev_b32 v15, 6, v15\n"
"v_mfma_f32_16x16x32_bf16 v[32:35], a[224:227], a[0:3], v[32:35]\n"
"s_add_u32 s53, s53, s80\n"
"buffer_load_dwordx2 v[10:11], v15, s[40:43], s23 offen sc1\n"
"v_mfma_f32_16x16x32_bf16 v[32:35], a[228:231], a[16:19], v[32:35]\n"
"s_mov_b32 s88, 0x3f80\n"
"ds_read_b128 a[248:251], v1 offset:14336\n"
"v_mfma_f32_16x16x32_bf16 v[36:39], a[224:227], a[4:7], v[36:39]\n"
"s_mov_b32 s65, 0x3f800000\n"
"buffer_load_dwordx4 v[80:83], v1, s[40:43], s84 offen sc1\n"
"v_mfma_f32_16x16x32_bf16 v[36:39], a[228:231], a[20:23], v[36:39]\n"
"ds_read_b128 a[252:255], v1 offset:15360\n"
"v_and_b32 v6, 15, v0\n"
"v_mfma_f32_16x16x32_bf16 v[40:43], a[224:227], a[8:11], v[40:43]\n"
"buffer_load_dwordx4 v[84:87], v1, s[40:43], s84 offen offset:1024 sc1\n"
"v_mfma_f32_16x16x32_bf16 v[40:43], a[228:231], a[24:27], v[40:43]\n"
"v_mul_u32_u24 v5, 1152, v6\n"
"v_lshrrev_b32 v6, 4, v0\n"
"v_mfma_f32_16x16x32_bf16 v[44:47], a[224:227], a[12:15], v[44:47]\n"
"s_waitcnt lgkmcnt(4)\n"
"v_lshl_add_u32 v5, v6, 3, v5\n"
"v_mfma_f32_16x16x32_bf16 v[44:47], a[228:231], a[28:31], v[44:47]\n"
"s_lshl_b32 s80, s101, 6\n"
"v_mfma_f32_16x16x32_bf16 v[48:51], a[232:235], a[0:3], v[48:51]\n"
"s_add_u32 s53, s53, s80\n"
"ds_read_b128 a[224:227], v1 offset:16384\n"
"v_mfma_f32_16x16x32_bf16 v[48:51], a[236:239], a[16:19], v[48:51]\n"
"s_lshl_b32 s80, s101, 15\n"
"buffer_load_dwordx4 v[88:91], v1, s[40:43], s84 offen offset:2048 sc1\n"
"v_mfma_f32_16x16x32_bf16 v[52:55], a[232:235], a[4:7], v[52:55]\n"
"s_add_u32 s52, s52, s80\n"
"v_mfma_f32_16x16x32_bf16 v[52:55], a[236:239], a[20:23], v[52:55]\n"
"ds_read_b128 a[228:231], v1 offset:17408\n"
"v_mfma_f32_16x16x32_bf16 v[56:59], a[232:235], a[8:11], v[56:59]\n"
"v_mfma_f32_16x16x32_bf16 v[56:59], a[236:239], a[24:27], v[56:59]\n"
"buffer_load_dwordx4 v[92:95], v1, s[40:43], s84 offen offset:3072 sc1\n"
"v_mfma_f32_16x16x32_bf16 v[60:63], a[232:235], a[12:15], v[60:63]\n"
"s_waitcnt lgkmcnt(4)\n"
"v_mfma_f32_16x16x32_bf16 v[60:63], a[236:239], a[28:31], v[60:63]\n"
"v_mfma_f32_16x16x32_bf16 v[64:67], a[240:243], a[0:3], v[64:67]\n"
"ds_read_b128 a[232:235], v1 offset:18432\n"
"v_mfma_f32_16x16x32_bf16 v[64:67], a[244:247], a[16:19], v[64:67]\n"
"v_mfma_f32_16x16x32_bf16 v[68:71], a[240:243], a[4:7], v[68:71]\n"
"s_add_u32 s84, s84, 4096\n"
"buffer_load_dwordx4 v[96:99], v1, s[40:43], s84 offen sc1\n"
"v_mfma_f32_16x16x32_bf16 v[68:71], a[244:247], a[20:23], v[68:71]\n"
"ds_read_b128 a[236:239], v1 offset:19456\n"
"v_mfma_f32_16x16x32_bf16 v[72:75], a[240:243], a[8:11], v[72:75]\n"
"v_mfma_f32_16x16x32_bf16 v[72:75], a[244:247], a[24:27], v[72:75]\n"
"v_mfma_f32_16x16x32_bf16 v[76:79], a[240:243], a[12:15], v[76:79]\n"
"s_waitcnt lgkmcnt(4)\n"
"buffer_load_dwordx4 v[100:103], v1, s[40:43], s84 offen offset:1024 sc1\n"
"v_mfma_f32_16x16x32_bf16 v[76:79], a[244:247], a[28:31], v[76:79]\n"
"v_mfma_f32_16x16x32_bf16 v[144:147], a[248:251], a[0:3], v[144:147]\n"
"ds_read_b128 a[240:243], v1 offset:20480\n"
"v_mfma_f32_16x16x32_bf16 v[144:147], a[252:255], a[16:19], v[144:147]\n"
"v_mfma_f32_16x16x32_bf16 v[148:151], a[248:251], a[4:7], v[148:151]\n"
"v_mfma_f32_16x16x32_bf16 v[148:151], a[252:255], a[20:23], v[148:151]\n"
"ds_read_b128 a[244:247], v1 offset:21504\n"
"v_mfma_f32_16x16x32_bf16 v[152:155], a[248:251], a[8:11], v[152:155]\n"
"buffer_load_dwordx4 v[104:107], v1, s[40:43], s84 offen offset:2048 sc1\n"
"v_mfma_f32_16x16x32_bf16 v[152:155], a[252:255], a[24:27], v[152:155]\n"
"v_mfma_f32_16x16x32_bf16 v[156:159], a[248:251], a[12:15], v[156:159]\n"
"s_waitcnt lgkmcnt(4)\n"
"v_mfma_f32_16x16x32_bf16 v[156:159], a[252:255], a[28:31], v[156:159]\n"
"buffer_load_dwordx4 v[108:111], v1, s[40:43], s84 offen offset:3072 sc1\n"
"v_mfma_f32_16x16x32_bf16 v[160:163], a[224:227], a[0:3], v[160:163]\n"
"ds_read_b128 a[248:251], v1 offset:22528\n"
"v_mfma_f32_16x16x32_bf16 v[160:163], a[228:231], a[16:19], v[160:163]\n"
"v_mfma_f32_16x16x32_bf16 v[164:167], a[224:227], a[4:7], v[164:167]\n"
"v_mfma_f32_16x16x32_bf16 v[164:167], a[228:231], a[20:23], v[164:167]\n"
"ds_read_b128 a[252:255], v1 offset:23552\n"
"v_mfma_f32_16x16x32_bf16 v[168:171], a[224:227], a[8:11], v[168:171]\n"
"s_add_u32 s84, s84, 4096\n"
"v_mfma_f32_16x16x32_bf16 v[168:171], a[228:231], a[24:27], v[168:171]\n"
"s_add_u32 s82, s64, 475136\n"
"v_mfma_f32_16x16x32_bf16 v[172:175], a[224:227], a[12:15], v[172:175]\n"
"s_waitcnt lgkmcnt(4)\n"
"v_mfma_f32_16x16x32_bf16 v[172:175], a[228:231], a[28:31], v[172:175]\n"
"s_add_u32 m0, s66, 81920\n"
"v_mfma_f32_16x16x32_bf16 v[176:179], a[232:235], a[0:3], v[176:179]\n"
"ds_read_b128 a[224:227], v1 offset:24576\n"
"v_mfma_f32_16x16x32_bf16 v[176:179], a[236:239], a[16:19], v[176:179]\n"
"buffer_load_dwordx4 v1, s[40:43], s82 offen sc1 lds\n"
"v_mfma_f32_16x16x32_bf16 v[180:183], a[232:235], a[4:7], v[180:183]\n"
"buffer_load_dwordx4 v1, s[40:43], s82 offen offset:1024 sc1 lds\n"
"v_mfma_f32_16x16x32_bf16 v[180:183], a[236:239], a[20:23], v[180:183]\n"
"ds_read_b128 a[228:231], v1 offset:25600\n"
"v_mfma_f32_16x16x32_bf16 v[184:187], a[232:235], a[8:11], v[184:187]\n"
"v_mfma_f32_16x16x32_bf16 v[184:187], a[236:239], a[24:27], v[184:187]\n"
"buffer_load_dwordx4 v1, s[40:43], s82 offen offset:2048 sc1 lds\n"
"v_mfma_f32_16x16x32_bf16 v[188:191], a[232:235], a[12:15], v[188:191]\n"
"s_waitcnt lgkmcnt(4)\n"
"v_mfma_f32_16x16x32_bf16 v[188:191], a[236:239], a[28:31], v[188:191]\n"
"v_mfma_f32_16x16x32_bf16 v[192:195], a[240:243], a[0:3], v[192:195]\n"
"ds_read_b128 a[232:235], v1 offset:26624\n"
"v_mfma_f32_16x16x32_bf16 v[192:195], a[244:247], a[16:19], v[192:195]\n"
"buffer_load_dwordx4 v1, s[40:43], s82 offen offset:3072 sc1 lds\n"
"v_mfma_f32_16x16x32_bf16 v[196:199], a[240:243], a[4:7], v[196:199]\n"
"v_mfma_f32_16x16x32_bf16 v[196:199], a[244:247], a[20:23], v[196:199]\n"
"ds_read_b128 a[236:239], v1 offset:27648\n"
"v_mfma_f32_16x16x32_bf16 v[200:203], a[240:243], a[8:11], v[200:203]\n"
"v_mfma_f32_16x16x32_bf16 v[200:203], a[244:247], a[24:27], v[200:203]\n"
"v_mfma_f32_16x16x32_bf16 v[204:207], a[240:243], a[12:15], v[204:207]\n"
"s_waitcnt vmcnt(12) lgkmcnt(4)\n"
"v_cmp_ne_u64 vcc, s[92:93], v[10:11]\n"
"v_mfma_f32_16x16x32_bf16 v[204:207], a[244:247], a[28:31], v[204:207]\n"
"s_cbranch_vccnz L_slow_23_%=\n"
"L_back_24_%=:\n"
"v_mfma_f32_16x16x32_bf16 v[208:211], a[248:251], a[0:3], v[208:211]\n"
"s_lshl_b32 s78, s73, 5\n"
"ds_read_b128 a[240:243], v1 offset:28672\n"
"v_mfma_f32_16x16x32_bf16 v[208:211], a[252:255], a[16:19], v[208:211]\n"
"s_add_u32 s78, s78, s68\n"
"buffer_load_dwordx4 v[112:115], v1, s[40:43], s84 offen sc1\n"
"v_mfma_f32_16x16x32_bf16 v[212:215], a[248:251], a[4:7], v[212:215]\n"
"s_lshl_b32 s78, s78, 6\n"
"v_mfma_f32_16x16x32_bf16 v[212:215], a[252:255], a[20:23], v[212:215]\n"
"s_add_u32 s78, s78, 79167488\n"
"ds_read_b128 a[244:247], v1 offset:29696\n"
"v_mov_b32 v8, s92\n"
"v_mfma_f32_16x16x32_bf16 v[216:219], a[248:251], a[8:11], v[216:219]\n"
"v_mov_b32 v9, s93\n"
"v_mfma_f32_16x16x32_bf16 v[216:219], a[252:255], a[24:27], v[216:219]\n"
"buffer_store_dwordx2 v[8:9], v14, s[40:43], s78 offen sc1\n"
"v_mfma_f32_16x16x32_bf16 v[220:223], a[248:251], a[12:15], v[220:223]\n"
"s_waitcnt lgkmcnt(4)\n"
"buffer_load_dwordx4 v[116:119], v1, s[40:43], s84 offen offset:1024 sc1\n"
"v_mfma_f32_16x16x32_bf16 v[220:223], a[252:255], a[28:31], v[220:223]\n"
"v_mfma_f32_16x16x32_bf16 v[16:19], a[224:227], a[32:35], v[16:19]\n"
"ds_read_b128 a[248:251], v1 offset:30720\n"
"v_mfma_f32_16x16x32_bf16 v[16:19], a[228:231], a[48:51], v[16:19]\n"
"buffer_load_dwordx4 v[120:123], v1, s[40:43], s84 offen offset:2048 sc1\n"
"v_mfma_f32_16x16x32_bf16 v[20:23], a[224:227], a[36:39], v[20:23]\n"
"v_mfma_f32_16x16x32_bf16 v[20:23], a[228:231], a[52:55], v[20:23]\n"
"ds_read_b128 a[252:255], v1 offset:31744\n"
"v_mfma_f32_16x16x32_bf16 v[24:27], a[224:227], a[40:43], v[24:27]\n"
"v_mfma_f32_16x16x32_bf16 v[24:27], a[228:231], a[56:59], v[24:27]\n"
"buffer_load_dwordx4 v[124:127], v1, s[40:43], s84 offen offset:3072 sc1\n"
"v_mfma_f32_16x16x32_bf16 v[28:31], a[224:227], a[44:47], v[28:31]\n"
"s_waitcnt lgkmcnt(4)\n"
"v_mfma_f32_16x16x32_bf16 v[28:31], a[228:231], a[60:63], v[28:31]\n"
"s_barrier\n"
"v_mfma_f32_16x16x32_bf16 v[32:35], a[232:235], a[32:35], v[32:35]\n"
"ds_read_b128 a[224:227], v1 offset:32768\n"
"v_mfma_f32_16x16x32_bf16 v[32:35], a[236:239], a[48:51], v[32:35]\n"
"v_mfma_f32_16x16x32_bf16 v[36:39], a[232:235], a[36:39], v[36:39]\n"
"s_add_u32 s84, s84, 4096\n"
"buffer_load_dwordx4 v[128:131], v1, s[40:43], s84 offen sc1\n"
"v_mfma_f32_16x16x32_bf16 v[36:39], a[236:239], a[52:55], v[36:39]\n"
"ds_read_b128 a[228:231], v1 offset:33792\n"
"v_mfma_f32_16x16x32_bf16 v[40:43], a[232:235], a[40:43], v[40:43]\n"
"v_mfma_f32_16x16x32_bf16 v[40:43], a[236:239], a[56:59], v[40:43]\n"
"v_mfma_f32_16x16x32_bf16 v[44:47], a[232:235], a[44:47], v[44:47]\n"
"s_waitcnt lgkmcnt(4)\n"
"buffer_load_dwordx4 v[132:135], v1, s[40:43], s84 offen offset:1024 sc1\n"
"v_mfma_f32_16x16x32_bf16 v[44:47], a[236:239], a[60:63], v[44:47]\n"
"v_mfma_f32_16x16x32_bf16 v[48:51], a[240:243], a[32:35], v[48:51]\n"
"ds_read_b128 a[232:235], v1 offset:34816\n"
"v_mfma_f32_16x16x32_bf16 v[48:51], a[244:247], a[48:51], v[48:51]\n"
"v_mfma_f32_16x16x32_bf16 v[52:55], a[240:243], a[36:39], v[52:55]\n"
"v_mfma_f32_16x16x32_bf16 v[52:55], a[244:247], a[52:55], v[52:55]\n"
"ds_read_b128 a[236:239], v1 offset:35840\n"
"v_mfma_f32_16x16x32_bf16 v[56:59], a[240:243], a[40:43], v[56:59]\n"
"buffer_load_dwordx4 v[136:139], v1, s[40:43], s84 offen offset:2048 sc1\n"
"v_mfma_f32_16x16x32_bf16 v[56:59], a[244:247], a[56:59], v[56:59]\n"
"v_mfma_f32_16x16x32_bf16 v[60:63], a[240:243], a[44:47], v[60:63]\n"
"s_waitcnt lgkmcnt(4)\n"
"v_mfma_f32_16x16x32_bf16 v[60:63], a[244:247], a[60:63], v[60:63]\n"
"buffer_load_dwordx4 v[140:143], v1, s[40:43], s84 offen offset:3072 sc1\n"
"v_mfma_f32_16x16x32_bf16 v[64:67], a[248:251], a[32:35], v[64:67]\n"
"ds_read_b128 a[240:243], v1 offset:36864\n"
"v_mfma_f32_16x16x32_bf16 v[64:67], a[252:255], a[48:51], v[64:67]\n"
"v_mfma_f32_16x16x32_bf16 v[68:71], a[248:251], a[36:39], v[68:71]\n"
"v_mfma_f32_16x16x32_bf16 v[68:71], a[252:255], a[52:55], v[68:71]\n"
"ds_read_b128 a[244:247], v1 offset:37888\n"
"v_mfma_f32_16x16x32_bf16 v[72:75], a[248:251], a[40:43], v[72:75]\n"
"s_add_u32 s84, s84, 4096\n"
"v_mfma_f32_16x16x32_bf16 v[72:75], a[252:255], a[56:59], v[72:75]\n"
"v_mfma_f32_16x16x32_bf16 v[76:79], a[248:251], a[44:47], v[76:79]\n"
"s_waitcnt lgkmcnt(4)\n"
"v_mfma_f32_16x16x32_bf16 v[76:79], a[252:255], a[60:63], v[76:79]\n"
"v_mfma_f32_16x16x32_bf16 v[144:147], a[224:227], a[32:35], v[144:147]\n"
"ds_read_b128 a[248:251], v1 offset:38912\n"
"v_mfma_f32_16x16x32_bf16 v[144:147], a[228:231], a[48:51], v[144:147]\n"
"s_add_u32 s82, s64, 491520\n"
"v_mfma_f32_16x16x32_bf16 v[148:151], a[224:227], a[36:39], v[148:151]\n"
"s_add_u32 m0, s66, 98304\n"
"v_mfma_f32_16x16x32_bf16 v[148:151], a[228:231], a[52:55], v[148:151]\n"
"ds_read_b128 a[252:255], v1 offset:39936\n"
"v_mfma_f32_16x16x32_bf16 v[152:155], a[224:227], a[40:43], v[152:155]\n"
"buffer_load_dwordx4 v1, s[40:43], s82 offen sc1 lds\n"
"v_mfma_f32_16x16x32_bf16 v[152:155], a[228:231], a[56:59], v[152:155]\n"
"buffer_load_dwordx4 v1, s[40:43], s82 offen offset:1024 sc1 lds\n"
"v_mfma_f32_16x16x32_bf16 v[156:159], a[224:227], a[44:47], v[156:159]\n"
"s_waitcnt lgkmcnt(4)\n"
"v_mfma_f32_16x16x32_bf16 v[156:159], a[228:231], a[60:63], v[156:159]\n"
"v_mfma_f32_16x16x32_bf16 v[160:163], a[232:235], a[32:35], v[160:163]\n"
"ds_read_b128 a[224:227], v1 offset:40960\n"
"v_mfma_f32_16x16x32_bf16 v[160:163], a[236:239], a[48:51], v[160:163]\n"
"v_mfma_f32_16x16x32_bf16 v[164:167], a[232:235], a[36:39], v[164:167]\n"
"buffer_load_dwordx4 v1, s[40:43], s82 offen offset:2048 sc1 lds\n"
"v_mfma_f32_16x16x32_bf16 v[164:167], a[236:239], a[52:55], v[164:167]\n"
"ds_read_b128 a[228:231], v1 offset:41984\n"
"v_mfma_f32_16x16x32_bf16 v[168:171], a[232:235], a[40:43], v[168:171]\n"
"v_mfma_f32_16x16x32_bf16 v[168:171], a[236:239], a[56:59], v[168:171]\n"
"v_mfma_f32_16x16x32_bf16 v[172:175], a[232:235], a[44:47], v[172:175]\n"
"s_waitcnt lgkmcnt(4)\n"
"buffer_load_dwordx4 v1, s[40:43], s82 offen offset:3072 sc1 lds\n"
"v_mfma_f32_16x16x32_bf16 v[172:175], a[236:239], a[60:63], v[172:175]\n"
"v_mfma_f32_16x16x32_bf16 v[176:179], a[240:243], a[32:35], v[176:179]\n"
"ds_read_b128 a[232:235], v1 offset:43008\n"
"v_mfma_f32_16x16x32_bf16 v[176:179], a[244:247], a[48:51], v[176:179]\n"
"v_mfma_f32_16x16x32_bf16 v[180:183], a[240:243], a[36:39], v[180:183]\n"
"v_mfma_f32_16x16x32_bf16 v[180:183], a[244:247], a[52:55], v[180:183]\n"
"ds_read_b128 a[236:239], v1 offset:44032\n"
"v_mfma_f32_16x16x32_bf16 v[184:187], a[240:243], a[40:43], v[184:187]\n"
"buffer_load_dwordx4 a[128:131], v1, s[40:43], s84 offen sc1\n"
"v_mfma_f32_16x16x32_bf16 v[184:187], a[244:247], a[56:59], v[184:187]\n"
"v_mfma_f32_16x16x32_bf16 v[188:191], a[240:243], a[44:47], v[188:191]\n"
"s_waitcnt lgkmcnt(4)\n"
"v_mfma_f32_16x16x32_bf16 v[188:191], a[244:247], a[60:63], v[188:191]\n"
"buffer_load_dwordx4 a[132:135], v1, s[40:43], s84 offen offset:1024 sc1\n"
"v_mfma_f32_16x16x32_bf16 v[192:195], a[248:251], a[32:35], v[192:195]\n"
"ds_read_b128 a[240:243], v1 offset:45056\n"
"v_mfma_f32_16x16x32_bf16 v[192:195], a[252:255], a[48:51], v[192:195]\n"
"v_mfma_f32_16x16x32_bf16 v[196:199], a[248:251], a[36:39], v[196:199]\n"
"v_mfma_f32_16x16x32_bf16 v[196:199], a[252:255], a[52:55], v[196:199]\n"
"ds_read_b128 a[244:247], v1 offset:46080\n"
"v_mfma_f32_16x16x32_bf16 v[200:203], a[248:251], a[40:43], v[200:203]\n"
"buffer_load_dwordx4 a[136:139], v1, s[40:43], s84 offen offset:2048 sc1\n"
"v_mfma_f32_16x16x32_bf16 v[200:203], a[252:255], a[56:59], v[200:203]\n"
"v_mfma_f32_16x16x32_bf16 v[204:207], a[248:251], a[44:47], v[204:207]\n"
"s_waitcnt lgkmcnt(4)\n"
"v_mfma_f32_16x16x32_bf16 v[204:207], a[252:255], a[60:63], v[204:207]\n"
"v_mfma_f32_16x16x32_bf16 v[208:211], a[224:227], a[32:35], v[208:211]\n"
"ds_read_b128 a[248:251], v1 offset:47104\n"
"v_mfma_f32_16x16x32_bf16 v[208:211], a[228:231], a[48:51], v[208:211]\n"
"buffer_load_dwordx4 a[140:143], v1, s[40:43], s84 offen offset:3072 sc1\n"
"v_mfma_f32_16x16x32_bf16 v[212:215], a[224:227], a[36:39], v[212:215]\n"
"v_mfma_f32_16x16x32_bf16 v[212:215], a[228:231], a[52:55], v[212:215]\n"
"ds_read_b128 a[252:255], v1 offset:48128\n"
"v_mfma_f32_16x16x32_bf16 v[216:219], a[224:227], a[40:43], v[216:219]\n"
"v_mfma_f32_16x16x32_bf16 v[216:219], a[228:231], a[56:59], v[216:219]\n"
"s_add_u32 s84, s84, 4096\n"
"buffer_load_dwordx4 a[144:147], v1, s[40:43], s84 offen sc1\n"
"v_mfma_f32_16x16x32_bf16 v[220:223], a[224:227], a[44:47], v[220:223]\n"
"s_waitcnt lgkmcnt(4)\n"
"v_mfma_f32_16x16x32_bf16 v[220:223], a[228:231], a[60:63], v[220:223]\n"
"v_mfma_f32_16x16x32_bf16 v[16:19], a[232:235], a[64:67], v[16:19]\n"
"ds_read_b128 a[224:227], v1 offset:49152\n"
"v_mfma_f32_16x16x32_bf16 v[16:19], a[236:239], a[80:83], v[16:19]\n"
"buffer_load_dwordx4 a[148:151], v1, s[40:43], s84 offen offset:1024 sc1\n"
"v_mfma_f32_16x16x32_bf16 v[20:23], a[232:235], a[68:71], v[20:23]\n"
"v_mfma_f32_16x16x32_bf16 v[20:23], a[236:239], a[84:87], v[20:23]\n"
"ds_read_b128 a[228:231], v1 offset:50176\n"
"v_mfma_f32_16x16x32_bf16 v[24:27], a[232:235], a[72:75], v[24:27]\n"
"v_mfma_f32_16x16x32_bf16 v[24:27], a[236:239], a[88:91], v[24:27]\n"
"v_mfma_f32_16x16x32_bf16 v[28:31], a[232:235], a[76:79], v[28:31]\n"
"s_waitcnt lgkmcnt(4)\n"
"buffer_load_dwordx4 a[152:155], v1, s[40:43], s84 offen offset:2048 sc1\n"
"v_mfma_f32_16x16x32_bf16 v[28:31], a[236:239], a[92:95], v[28:31]\n"
"v_mfma_f32_16x16x32_bf16 v[32:35], a[240:243], a[64:67], v[32:35]\n"
"ds_read_b128 a[232:235], v1 offset:51200\n"
"v_mfma_f32_16x16x32_bf16 v[32:35], a[244:247], a[80:83], v[32:35]\n"
"v_mfma_f32_16x16x32_bf16 v[36:39], a[240:243], a[68:71], v[36:39]\n"
"buffer_load_dwordx4 a[156:159], v1, s[40:43], s84 offen offset:3072 sc1\n"
"v_mfma_f32_16x16x32_bf16 v[36:39], a[244:247], a[84:87], v[36:39]\n"
"ds_read_b128 a[236:239], v1 offset:52224\n"
"v_mfma_f32_16x16x32_bf16 v[40:43], a[240:243], a[72:75], v[40:43]\n"
"v_mfma_f32_16x16x32_bf16 v[40:43], a[244:247], a[88:91], v[40:43]\n"
"v_mfma_f32_16x16x32_bf16 v[44:47], a[240:243], a[76:79], v[44:47]\n"
"s_waitcnt lgkmcnt(4)\n"
"v_mfma_f32_16x16x32_bf16 v[44:47], a[244:247], a[92:95], v[44:47]\n"
"s_add_u32 s84, s84, 4096\n"
"v_mfma_f32_16x16x32_bf16 v[48:51], a[248:251], a[64:67], v[48:51]\n"
"ds_read_b128 a[240:243], v1 offset:53248\n"
"v_mfma_f32_16x16x32_bf16 v[48:51], a[252:255], a[80:83], v[48:51]\n"
"v_mfma_f32_16x16x32_bf16 v[52:55], a[248:251], a[68:71], v[52:55]\n"
"v_mfma_f32_16x16x32_bf16 v[52:55], a[252:255], a[84:87], v[52:55]\n"
"ds_read_b128 a[244:247], v1 offset:54272\n"
"v_mfma_f32_16x16x32_bf16 v[56:59], a[248:251], a[72:75], v[56:59]\n"
"buffer_load_dwordx4 a[160:163], v1, s[40:43], s84 offen sc1\n"
"v_mfma_f32_16x16x32_bf16 v[56:59], a[252:255], a[88:91], v[56:59]\n"
"v_mfma_f32_16x16x32_bf16 v[60:63], a[248:251], a[76:79], v[60:63]\n"
"s_waitcnt lgkmcnt(4)\n"
"v_mfma_f32_16x16x32_bf16 v[60:63], a[252:255], a[92:95], v[60:63]\n"
"v_mfma_f32_16x16x32_bf16 v[64:67], a[224:227], a[64:67], v[64:67]\n"
"ds_read_b128 a[248:251], v1 offset:55296\n"
"v_mfma_f32_16x16x32_bf16 v[64:67], a[228:231], a[80:83], v[64:67]\n"
"buffer_load_dwordx4 a[164:167], v1, s[40:43], s84 offen offset:1024 sc1\n"
"v_mfma_f32_16x16x32_bf16 v[68:71], a[224:227], a[68:71], v[68:71]\n"
"v_mfma_f32_16x16x32_bf16 v[68:71], a[228:231], a[84:87], v[68:71]\n"
"ds_read_b128 a[252:255], v1 offset:56320\n"
"v_mfma_f32_16x16x32_bf16 v[72:75], a[224:227], a[72:75], v[72:75]\n"
"v_mfma_f32_16x16x32_bf16 v[72:75], a[228:231], a[88:91], v[72:75]\n"
"buffer_load_dwordx4 a[168:171], v1, s[40:43], s84 offen offset:2048 sc1\n"
"v_mfma_f32_16x16x32_bf16 v[76:79], a[224:227], a[76:79], v[76:79]\n"
"s_waitcnt lgkmcnt(4)\n"
"v_mfma_f32_16x16x32_bf16 v[76:79], a[228:231], a[92:95], v[76:79]\n"
"v_mfma_f32_16x16x32_bf16 v[144:147], a[232:235], a[64:67], v[144:147]\n"
"ds_read_b128 a[224:227], v1 offset:57344\n"
"v_mfma_f32_16x16x32_bf16 v[144:147], a[236:239], a[80:83], v[144:147]\n"
"buffer_load_dwordx4 a[172:175], v1, s[40:43], s84 offen offset:3072 sc1\n"
"v_mfma_f32_16x16x32_bf16 v[148:151], a[232:235], a[68:71], v[148:151]\n"
"v_mfma_f32_16x16x32_bf16 v[148:151], a[236:239], a[84:87], v[148:151]\n"
"ds_read_b128 a[228:231], v1 offset:58368\n"
"v_mfma_f32_16x16x32_bf16 v[152:155], a[232:235], a[72:75], v[152:155]\n"
"v_mfma_f32_16x16x32_bf16 v[152:155], a[236:239], a[88:91], v[152:155]\n"
"v_mfma_f32_16x16x32_bf16 v[156:159], a[232:235], a[76:79], v[156:159]\n"
"s_waitcnt lgkmcnt(4)\n"
"v_mfma_f32_16x16x32_bf16 v[156:159], a[236:239], a[92:95], v[156:159]\n"
"s_add_u32 s84, s84, 4096\n"
"buffer_load_dwordx4 a[176:179], v1, s[40:43], s84 offen sc1\n"
"v_mfma_f32_16x16x32_bf16 v[160:163], a[240:243], a[64:67], v[160:163]\n"
"ds_read_b128 a[232:235], v1 offset:59392\n"
"v_mfma_f32_16x16x32_bf16 v[160:163], a[244:247], a[80:83], v[160:163]\n"
"v_mfma_f32_16x16x32_bf16 v[164:167], a[240:243], a[68:71], v[164:167]\n"
"buffer_load_dwordx4 a[180:183], v1, s[40:43], s84 offen offset:1024 sc1\n"
"v_mfma_f32_16x16x32_bf16 v[164:167], a[244:247], a[84:87], v[164:167]\n"
"ds_read_b128 a[236:239], v1 offset:60416\n"
"v_mfma_f32_16x16x32_bf16 v[168:171], a[240:243], a[72:75], v[168:171]\n"
"v_mfma_f32_16x16x32_bf16 v[168:171], a[244:247], a[88:91], v[168:171]\n"
"v_mfma_f32_16x16x32_bf16 v[172:175], a[240:243], a[76:79], v[172:175]\n"
"s_waitcnt lgkmcnt(4)\n"
"v_mfma_f32_16x16x32_bf16 v[172:175], a[244:247], a[92:95], v[172:175]\n"
"buffer_load_dwordx4 a[184:187], v1, s[40:43], s84 offen offset:2048 sc1\n"
"v_mfma_f32_16x16x32_bf16 v[176:179], a[248:251], a[64:67], v[176:179]\n"
"ds_read_b128 a[240:243], v1 offset:61440\n"
"v_mfma_f32_16x16x32_bf16 v[176:179], a[252:255], a[80:83], v[176:179]\n"
"v_mfma_f32_16x16x32_bf16 v[180:183], a[248:251], a[68:71], v[180:183]\n"
"v_mfma_f32_16x16x32_bf16 v[180:183], a[252:255], a[84:87], v[180:183]\n"
"ds_read_b128 a[244:247], v1 offset:62464\n"
"v_mfma_f32_16x16x32_bf16 v[184:187], a[248:251], a[72:75], v[184:187]\n"
"buffer_load_dwordx4 a[188:191], v1, s[40:43], s84 offen offset:3072 sc1\n"
"v_mfma_f32_16x16x32_bf16 v[184:187], a[252:255], a[88:91], v[184:187]\n"
"v_mfma_f32_16x16x32_bf16 v[188:191], a[248:251], a[76:79], v[188:191]\n"
"s_waitcnt lgkmcnt(4)\n"
"v_mfma_f32_16x16x32_bf16 v[188:191], a[252:255], a[92:95], v[188:191]\n"
"v_mfma_f32_16x16x32_bf16 v[192:195], a[224:227], a[64:67], v[192:195]\n"
"s_add_u32 s84, s84, 4096\n"
"ds_read_b128 a[248:251], v1 offset:63488\n"
"v_mfma_f32_16x16x32_bf16 v[192:195], a[228:231], a[80:83], v[192:195]\n"
"s_add_u32 s82, s64, 507904\n"
"v_mfma_f32_16x16x32_bf16 v[196:199], a[224:227], a[68:71], v[196:199]\n"
"s_add_u32 m0, s66, 114688\n"
"v_mfma_f32_16x16x32_bf16 v[196:199], a[228:231], a[84:87], v[196:199]\n"
"ds_read_b128 a[252:255], v1 offset:64512\n"
"v_mfma_f32_16x16x32_bf16 v[200:203], a[224:227], a[72:75], v[200:203]\n"
"buffer_load_dwordx4 v1, s[40:43], s82 offen sc1 lds\n"
"v_mfma_f32_16x16x32_bf16 v[200:203], a[228:231], a[88:91], v[200:203]\n"
"buffer_load_dwordx4 v1, s[40:43], s82 offen offset:1024 sc1 lds\n"
"v_mfma_f32_16x16x32_bf16 v[204:207], a[224:227], a[76:79], v[204:207]\n"
"s_waitcnt lgkmcnt(4)\n"
"v_mfma_f32_16x16x32_bf16 v[204:207], a[228:231], a[92:95], v[204:207]\n"
"s_waitcnt vmcnt(31)\n"
"s_barrier\n"
"v_mfma_f32_16x16x32_bf16 v[208:211], a[232:235], a[64:67], v[208:211]\n"
"ds_read_b128 a[224:227], v2\n"
"v_mfma_f32_16x16x32_bf16 v[208:211], a[236:239], a[80:83], v[208:211]\n"
"buffer_load_dwordx4 v1, s[40:43], s82 offen offset:2048 sc1 lds\n"
"v_mfma_f32_16x16x32_bf16 v[212:215], a[232:235], a[68:71], v[212:215]\n"
"v_mfma_f32_16x16x32_bf16 v[212:215], a[236:239], a[84:87], v[212:215]\n"
"ds_read_b128 a[228:231], v2 offset:1024\n"
"v_mfma_f32_16x16x32_bf16 v[216:219], a[232:235], a[72:75], v[216:219]\n"
"v_mfma_f32_16x16x32_bf16 v[216:219], a[236:239], a[88:91], v[216:219]\n"
"buffer_load_dwordx4 v1, s[40:43], s82 offen offset:3072 sc1 lds\n"
"v_mfma_f32_16x16x32_bf16 v[220:223], a[232:235], a[76:79], v[220:223]\n"
"s_waitcnt lgkmcnt(4)\n"
"v_mfma_f32_16x16x32_bf16 v[220:223], a[236:239], a[92:95], v[220:223]\n"
"v_mfma_f32_16x16x32_bf16 v[16:19], a[240:243], a[96:99], v[16:19]\n"
"ds_read_b128 a[232:235], v2 offset:2048\n"
"v_mfma_f32_16x16x32_bf16 v[16:19], a[244:247], a[112:115], v[16:19]\n"
"v_mfma_f32_16x16x32_bf16 v[20:23], a[240:243], a[100:103], v[20:23]\n"
"buffer_load_dwordx4 a[192:195], v1, s[40:43], s84 offen sc1\n"
"v_mfma_f32_16x16x32_bf16 v[20:23], a[244:247], a[116:119], v[20:23]\n"
"ds_read_b128 a[236:239], v2 offset:3072\n"
"v_mfma_f32_16x16x32_bf16 v[24:27], a[240:243], a[104:107], v[24:27]\n"
"v_mfma_f32_16x16x32_bf16 v[24:27], a[244:247], a[120:123], v[24:27]\n"
"v_mfma_f32_16x16x32_bf16 v[28:31], a[240:243], a[108:111], v[28:31]\n"
"s_waitcnt lgkmcnt(4)\n"
"buffer_load_dwordx4 a[196:199], v1, s[40:43], s84 offen offset:1024 sc1\n"
"v_mfma_f32_16x16x32_bf16 v[28:31], a[244:247], a[124:127], v[28:31]\n"
"v_mfma_f32_16x16x32_bf16 v[32:35], a[248:251], a[96:99], v[32:35]\n"
"ds_read_b128 a[240:243], v2 offset:4096\n"
"v_mfma_f32_16x16x32_bf16 v[32:35], a[252:255], a[112:115], v[32:35]\n"
"v_mfma_f32_16x16x32_bf16 v[36:39], a[248:251], a[100:103], v[36:39]\n"
"v_mfma_f32_16x16x32_bf16 v[36:39], a[252:255], a[116:119], v[36:39]\n"
"ds_read_b128 a[244:247], v2 offset:5120\n"
"v_mfma_f32_16x16x32_bf16 v[40:43], a[248:251], a[104:107], v[40:43]\n"
"buffer_load_dwordx4 a[200:203], v1, s[40:43], s84 offen offset:2048 sc1\n"
"v_mfma_f32_16x16x32_bf16 v[40:43], a[252:255], a[120:123], v[40:43]\n"
"v_mfma_f32_16x16x32_bf16 v[44:47], a[248:251], a[108:111], v[44:47]\n"
"s_waitcnt lgkmcnt(4)\n"
"v_mfma_f32_16x16x32_bf16 v[44:47], a[252:255], a[124:127], v[44:47]\n"
"buffer_load_dwordx4 a[204:207], v1, s[40:43], s84 offen offset:3072 sc1\n"
"v_mfma_f32_16x16x32_bf16 v[48:51], a[224:227], a[96:99], v[48:51]\n"
"ds_read_b128 a[248:251], v2 offset:6144\n"
"v_mfma_f32_16x16x32_bf16 v[48:51], a[228:231], a[112:115], v[48:51]\n"
"v_mfma_f32_16x16x32_bf16 v[52:55], a[224:227], a[100:103], v[52:55]\n"
"v_mfma_f32_16x16x32_bf16 v[52:55], a[228:231], a[116:119], v[52:55]\n"
"ds_read_b128 a[252:255], v2 offset:7168\n"
"v_mfma_f32_16x16x32_bf16 v[56:59], a[224:227], a[104:107], v[56:59]\n"
"s_add_u32 s84, s84, 4096\n"
"buffer_load_dwordx4 a[208:211], v1, s[40:43], s84 offen sc1\n"
"v_mfma_f32_16x16x32_bf16 v[56:59], a[228:231], a[120:123], v[56:59]\n"
"v_mfma_f32_16x16x32_bf16 v[60:63], a[224:227], a[108:111], v[60:63]\n"
"s_waitcnt lgkmcnt(4)\n"
"v_mfma_f32_16x16x32_bf16 v[60:63], a[228:231], a[124:127], v[60:63]\n"
"v_mfma_f32_16x16x32_bf16 v[64:67], a[232:235], a[96:99], v[64:67]\n"
"ds_read_b128 a[224:227], v2 offset:8192\n"
"v_mfma_f32_16x16x32_bf16 v[64:67], a[236:239], a[112:115], v[64:67]\n"
"buffer_load_dwordx4 a[212:215], v1, s[40:43], s84 offen offset:1024 sc1\n"
"v_mfma_f32_16x16x32_bf16 v[68:71], a[232:235], a[100:103], v[68:71]\n"
"v_mfma_f32_16x16x32_bf16 v[68:71], a[236:239], a[116:119], v[68:71]\n"
"ds_read_b128 a[228:231], v2 offset:9216\n"
"v_mfma_f32_16x16x32_bf16 v[72:75], a[232:235], a[104:107], v[72:75]\n"
"v_mfma_f32_16x16x32_bf16 v[72:75], a[236:239], a[120:123], v[72:75]\n"
"buffer_load_dwordx4 a[216:219], v1, s[40:43], s84 offen offset:2048 sc1\n"
"v_mfma_f32_16x16x32_bf16 v[76:79], a[232:235], a[108:111], v[76:79]\n"
"s_waitcnt lgkmcnt(4)\n"
"v_mfma_f32_16x16x32_bf16 v[76:79], a[236:239], a[124:127], v[76:79]\n"
"v_mfma_f32_16x16x32_bf16 v[144:147], a[240:243], a[96:99], v[144:147]\n"
"ds_read_b128 a[232:235], v2 offset:10240\n"
"v_mfma_f32_16x16x32_bf16 v[144:147], a[244:247], a[112:115], v[144:147]\n"
"buffer_load_dwordx4 a[220:223], v1, s[40:43], s84 offen offset:3072 sc1\n"
"v_mfma_f32_16x16x32_bf16 v[148:151], a[240:243], a[100:103], v[148:151]\n"
"v_mfma_f32_16x16x32_bf16 v[148:151], a[244:247], a[116:119], v[148:151]\n"
"ds_read_b128 a[236:239], v2 offset:11264\n"
"v_mfma_f32_16x16x32_bf16 v[152:155], a[240:243], a[104:107], v[152:155]\n"
"v_mfma_f32_16x16x32_bf16 v[152:155], a[244:247], a[120:123], v[152:155]\n"
"v_mfma_f32_16x16x32_bf16 v[156:159], a[240:243], a[108:111], v[156:159]\n"
"s_waitcnt lgkmcnt(4)\n"
"v_mfma_f32_16x16x32_bf16 v[156:159], a[244:247], a[124:127], v[156:159]\n"
"s_add_u32 s84, s84, 4096\n"
"v_mfma_f32_16x16x32_bf16 v[160:163], a[248:251], a[96:99], v[160:163]\n"
"s_add_u32 s82, s64, 524288\n"
"ds_read_b128 a[240:243], v2 offset:12288\n"
"v_mfma_f32_16x16x32_bf16 v[160:163], a[252:255], a[112:115], v[160:163]\n"
"s_add_u32 m0, s66, 131072\n"
"v_mfma_f32_16x16x32_bf16 v[164:167], a[248:251], a[100:103], v[164:167]\n"
"buffer_load_dwordx4 v1, s[40:43], s82 offen sc1 lds\n"
"v_mfma_f32_16x16x32_bf16 v[164:167], a[252:255], a[116:119], v[164:167]\n"
"ds_read_b128 a[244:247], v2 offset:13312\n"
"v_mfma_f32_16x16x32_bf16 v[168:171], a[248:251], a[104:107], v[168:171]\n"
"buffer_load_dwordx4 v1, s[40:43], s82 offen offset:1024 sc1 lds\n"
"v_mfma_f32_16x16x32_bf16 v[168:171], a[252:255], a[120:123], v[168:171]\n"
"v_mfma_f32_16x16x32_bf16 v[172:175], a[248:251], a[108:111], v[172:175]\n"
"s_waitcnt lgkmcnt(4)\n"
"v_mfma_f32_16x16x32_bf16 v[172:175], a[252:255], a[124:127], v[172:175]\n"
"buffer_load_dwordx4 v1, s[40:43], s82 offen offset:2048 sc1 lds\n"
"v_mfma_f32_16x16x32_bf16 v[176:179], a[224:227], a[96:99], v[176:179]\n"
"ds_read_b128 a[248:251], v2 offset:14336\n"
"v_mfma_f32_16x16x32_bf16 v[176:179], a[228:231], a[112:115], v[176:179]\n"
"v_mfma_f32_16x16x32_bf16 v[180:183], a[224:227], a[100:103], v[180:183]\n"
"v_mfma_f32_16x16x32_bf16 v[180:183], a[228:231], a[116:119], v[180:183]\n"
"ds_read_b128 a[252:255], v2 offset:15360\n"
"v_mfma_f32_16x16x32_bf16 v[184:187], a[224:227], a[104:107], v[184:187]\n"
"buffer_load_dwordx4 v1, s[40:43], s82 offen offset:3072 sc1 lds\n"
"v_mfma_f32_16x16x32_bf16 v[184:187], a[228:231], a[120:123], v[184:187]\n"
"v_mfma_f32_16x16x32_bf16 v[188:191], a[224:227], a[108:111], v[188:191]\n"
"s_waitcnt lgkmcnt(4)\n"
"v_mfma_f32_16x16x32_bf16 v[188:191], a[228:231], a[124:127], v[188:191]\n"
"v_mfma_f32_16x16x32_bf16 v[192:195], a[232:235], a[96:99], v[192:195]\n"
"ds_read_b128 a[224:227], v2 offset:16384\n"
"v_mfma_f32_16x16x32_bf16 v[192:195], a[236:239], a[112:115], v[192:195]\n"
"buffer_load_dwordx4 a[0:3], v1, s[40:43], s84 offen sc1\n"
"v_mfma_f32_16x16x32_bf16 v[196:199], a[232:235], a[100:103], v[196:199]\n"
"v_mfma_f32_16x16x32_bf16 v[196:199], a[236:239], a[116:119], v[196:199]\n"
"ds_read_b128 a[228:231], v2 offset:17408\n"
"v_mfma_f32_16x16x32_bf16 v[200:203], a[232:235], a[104:107], v[200:203]\n"
"buffer_load_dwordx4 a[4:7], v1, s[40:43], s84 offen offset:1024 sc1\n"
"v_mfma_f32_16x16x32_bf16 v[200:203], a[236:239], a[120:123], v[200:203]\n"
"v_mfma_f32_16x16x32_bf16 v[204:207], a[232:235], a[108:111], v[204:207]\n"
"s_waitcnt lgkmcnt(4)\n"
"v_mfma_f32_16x16x32_bf16 v[204:207], a[236:239], a[124:127], v[204:207]\n"
"v_mfma_f32_16x16x32_bf16 v[208:211], a[240:243], a[96:99], v[208:211]\n"
"ds_read_b128 a[232:235], v2 offset:18432\n"
"v_mfma_f32_16x16x32_bf16 v[208:211], a[244:247], a[112:115], v[208:211]\n"
"buffer_load_dwordx4 a[8:11], v1, s[40:43], s84 offen offset:2048 sc1\n"
"v_mfma_f32_16x16x32_bf16 v[212:215], a[240:243], a[100:103], v[212:215]\n"
"v_mfma_f32_16x16x32_bf16 v[212:215], a[244:247], a[116:119], v[212:215]\n"
"ds_read_b128 a[236:239], v2 offset:19456\n"
"v_mfma_f32_16x16x32_bf16 v[216:219], a[240:243], a[104:107], v[216:219]\n"
"v_mfma_f32_16x16x32_bf16 v[216:219], a[244:247], a[120:123], v[216:219]\n"
"buffer_load_dwordx4 a[12:15], v1, s[40:43], s84 offen offset:3072 sc1\n"
"v_mfma_f32_16x16x32_bf16 v[220:223], a[240:243], a[108:111], v[220:223]\n"
"s_waitcnt lgkmcnt(4)\n"
"v_mfma_f32_16x16x32_bf16 v[220:223], a[244:247], a[124:127], v[220:223]\n"
"v_mfma_f32_16x16x32_bf16 v[16:19], a[248:251], v[80:83], v[16:19]\n"
"ds_read_b128 a[240:243], v2 offset:20480\n"
"v_mfma_f32_16x16x32_bf16 v[16:19], a[252:255], v[96:99], v[16:19]\n"
"v_mfma_f32_16x16x32_bf16 v[20:23], a[248:251], v[84:87], v[20:23]\n"
"s_add_u32 s84, s84, 4096\n"
"buffer_load_dwordx4 a[16:19], v1, s[40:43], s84 offen sc1\n"
"v_mfma_f32_16x16x32_bf16 v[20:23], a[252:255], v[100:103], v[20:23]\n"
"ds_read_b128 a[244:247], v2 offset:21504\n"
"v_mfma_f32_16x16x32_bf16 v[24:27], a[248:251], v[88:91], v[24:27]\n"
"v_mfma_f32_16x16x32_bf16 v[24:27], a[252:255], v[104:107], v[24:27]\n"
"v_mfma_f32_16x16x32_bf16 v[28:31], a[248:251], v[92:95], v[28:31]\n"
"s_waitcnt lgkmcnt(4)\n"
"buffer_load_dwordx4 a[20:23], v1, s[40:43], s84 offen offset:1024 sc1\n"
"v_mfma_f32_16x16x32_bf16 v[28:31], a[252:255], v[108:111], v[28:31]\n"
"v_mfma_f32_16x16x32_bf16 v[32:35], a[224:227], v[80:83], v[32:35]\n"
"ds_read_b128 a[248:251], v2 offset:22528\n"
"v_mfma_f32_16x16x32_bf16 v[32:35], a[228:231], v[96:99], v[32:35]\n"
"v_mfma_f32_16x16x32_bf16 v[36:39], a[224:227], v[84:87], v[36:39]\n"
"v_mfma_f32_16x16x32_bf16 v[36:39], a[228:231], v[100:103], v[36:39]\n"
"ds_read_b128 a[252:255], v2 offset:23552\n"
"v_mfma_f32_16x16x32_bf16 v[40:43], a[224:227], v[88:91], v[40:43]\n"
"buffer_load_dwordx4 a[24:27], v1, s[40:43], s84 offen offset:2048 sc1\n"
"v_mfma_f32_16x16x32_bf16 v[40:43], a[228:231], v[104:107], v[40:43]\n"
"v_mfma_f32_16x16x32_bf16 v[44:47], a[224:227], v[92:95], v[44:47]\n"
"s_waitcnt lgkmcnt(4)\n"
"v_mfma_f32_16x16x32_bf16 v[44:47], a[228:231], v[108:111], v[44:47]\n"
"buffer_load_dwordx4 a[28:31], v1, s[40:43], s84 offen offset:3072 sc1\n"
"v_mfma_f32_16x16x32_bf16 v[48:51], a[232:235], v[80:83], v[48:51]\n"
"ds_read_b128 a[224:227], v2 offset:24576\n"
"v_mfma_f32_16x16x32_bf16 v[48:51], a[236:239], v[96:99], v[48:51]\n"
"v_mfma_f32_16x16x32_bf16 v[52:55], a[232:235], v[84:87], v[52:55]\n"
"v_mfma_f32_16x16x32_bf16 v[52:55], a[236:239], v[100:103], v[52:55]\n"
"ds_read_b128 a[228:231], v2 offset:25600\n"
"v_mfma_f32_16x16x32_bf16 v[56:59], a[232:235], v[88:91], v[56:59]\n"
"s_add_u32 s84, s84, 4096\n"
"v_mfma_f32_16x16x32_bf16 v[56:59], a[236:239], v[104:107], v[56:59]\n"
"s_add_u32 s82, s64, 540672\n"
"v_mfma_f32_16x16x32_bf16 v[60:63], a[232:235], v[92:95], v[60:63]\n"
"s_waitcnt lgkmcnt(4)\n"
"v_mfma_f32_16x16x32_bf16 v[60:63], a[236:239], v[108:111], v[60:63]\n"
"s_add_u32 m0, s66, 147456\n"
"v_mfma_f32_16x16x32_bf16 v[64:67], a[240:243], v[80:83], v[64:67]\n"
"ds_read_b128 a[232:235], v2 offset:26624\n"
"v_mfma_f32_16x16x32_bf16 v[64:67], a[244:247], v[96:99], v[64:67]\n"
"buffer_load_dwordx4 v1, s[40:43], s82 offen sc1 lds\n"
"v_mfma_f32_16x16x32_bf16 v[68:71], a[240:243], v[84:87], v[68:71]\n"
"buffer_load_dwordx4 v1, s[40:43], s82 offen offset:1024 sc1 lds\n"
"v_mfma_f32_16x16x32_bf16 v[68:71], a[244:247], v[100:103], v[68:71]\n"
"ds_read_b128 a[236:239], v2 offset:27648\n"
"v_mfma_f32_16x16x32_bf16 v[72:75], a[240:243], v[88:91], v[72:75]\n"
"v_mfma_f32_16x16x32_bf16 v[72:75], a[244:247], v[104:107], v[72:75]\n"
"buffer_load_dwordx4 v1, s[40:43], s82 offen offset:2048 sc1 lds\n"
"v_mfma_f32_16x16x32_bf16 v[76:79], a[240:243], v[92:95], v[76:79]\n"
"s_waitcnt lgkmcnt(4)\n"
"v_mfma_f32_16x16x32_bf16 v[76:79], a[244:247], v[108:111], v[76:79]\n"
"v_mfma_f32_16x16x32_bf16 v[144:147], a[248:251], v[80:83], v[144:147]\n"
"ds_read_b128 a[240:243], v2 offset:28672\n"
"v_mfma_f32_16x16x32_bf16 v[144:147], a[252:255], v[96:99], v[144:147]\n"
"buffer_load_dwordx4 v1, s[40:43], s82 offen offset:3072 sc1 lds\n"
"v_mfma_f32_16x16x32_bf16 v[148:151], a[248:251], v[84:87], v[148:151]\n"
"v_mfma_f32_16x16x32_bf16 v[148:151], a[252:255], v[100:103], v[148:151]\n"
"ds_read_b128 a[244:247], v2 offset:29696\n"
"v_mfma_f32_16x16x32_bf16 v[152:155], a[248:251], v[88:91], v[152:155]\n"
"v_mfma_f32_16x16x32_bf16 v[152:155], a[252:255], v[104:107], v[152:155]\n"
"v_mfma_f32_16x16x32_bf16 v[156:159], a[248:251], v[92:95], v[156:159]\n"
"s_waitcnt lgkmcnt(4)\n"
"v_mfma_f32_16x16x32_bf16 v[156:159], a[252:255], v[108:111], v[156:159]\n"
"s_lshl_b32 s23, s73, 5\n"
"v_mfma_f32_16x16x32_bf16 v[160:163], a[224:227], v[80:83], v[160:163]\n"
"s_add_u32 s23, s23, s68\n"
"ds_read_b128 a[248:251], v2 offset:30720\n"
"v_mfma_f32_16x16x32_bf16 v[160:163], a[228:231], v[96:99], v[160:163]\n"
"s_lshl_b32 s23, s23, 6\n"
"v_mfma_f32_16x16x32_bf16 v[164:167], a[224:227], v[84:87], v[164:167]\n"
"s_and_b32 s23, s23, 0xfffffe00\n"
"s_add_u32 s23, s23, 79167488\n"
"v_and_b32 v15, 7, v0\n"
"v_mfma_f32_16x16x32_bf16 v[164:167], a[228:231], v[100:103], v[164:167]\n"
"ds_read_b128 a[252:255], v2 offset:31744\n"
"v_lshlrev_b32 v15, 6, v15\n"
"v_mfma_f32_16x16x32_bf16 v[168:171], a[224:227], v[88:91], v[168:171]\n"
"buffer_load_dwordx2 v[10:11], v15, s[40:43], s23 offen sc1\n"
"v_mfma_f32_16x16x32_bf16 v[168:171], a[228:231], v[104:107], v[168:171]\n"
"buffer_load_dwordx4 a[32:35], v1, s[40:43], s84 offen sc1\n"
"v_mfma_f32_16x16x32_bf16 v[172:175], a[224:227], v[92:95], v[172:175]\n"
"s_waitcnt lgkmcnt(4)\n"
"v_mfma_f32_16x16x32_bf16 v[172:175], a[228:231], v[108:111], v[172:175]\n"
"s_waitcnt vmcnt(26)\n"
"s_barrier\n"
"v_mfma_f32_16x16x32_bf16 v[176:179], a[232:235], v[80:83], v[176:179]\n"
"ds_read_b128 a[224:227], v2 offset:32768\n"
"v_mfma_f32_16x16x32_bf16 v[176:179], a[236:239], v[96:99], v[176:179]\n"
"buffer_load_dwordx4 a[36:39], v1, s[40:43], s84 offen offset:1024 sc1\n"
"v_mfma_f32_16x16x32_bf16 v[180:183], a[232:235], v[84:87], v[180:183]\n"
"v_mfma_f32_16x16x32_bf16 v[180:183], a[236:239], v[100:103], v[180:183]\n"
"ds_read_b128 a[228:231], v2 offset:33792\n"
"v_mfma_f32_16x16x32_bf16 v[184:187], a[232:235], v[88:91], v[184:187]\n"
"v_mfma_f32_16x16x32_bf16 v[184:187], a[236:239], v[104:107], v[184:187]\n"
"buffer_load_dwordx4 a[40:43], v1, s[40:43], s84 offen offset:2048 sc1\n"
"v_mfma_f32_16x16x32_bf16 v[188:191], a[232:235], v[92:95], v[188:191]\n"
"s_waitcnt lgkmcnt(4)\n"
"v_mfma_f32_16x16x32_bf16 v[188:191], a[236:239], v[108:111], v[188:191]\n"
"v_mfma_f32_16x16x32_bf16 v[192:195], a[240:243], v[80:83], v[192:195]\n"
"ds_read_b128 a[232:235], v2 offset:34816\n"
"v_mfma_f32_16x16x32_bf16 v[192:195], a[244:247], v[96:99], v[192:195]\n"
"buffer_load_dwordx4 a[44:47], v1, s[40:43], s84 offen offset:3072 sc1\n"
"v_mfma_f32_16x16x32_bf16 v[196:199], a[240:243], v[84:87], v[196:199]\n"
"v_mfma_f32_16x16x32_bf16 v[196:199], a[244:247], v[100:103], v[196:199]\n"
"ds_read_b128 a[236:239], v2 offset:35840\n"
"v_mfma_f32_16x16x32_bf16 v[200:203], a[240:243], v[88:91], v[200:203]\n"
"v_mfma_f32_16x16x32_bf16 v[200:203], a[244:247], v[104:107], v[200:203]\n"
"v_mfma_f32_16x16x32_bf16 v[204:207], a[240:243], v[92:95], v[204:207]\n"
"s_waitcnt lgkmcnt(4)\n"
"v_mfma_f32_16x16x32_bf16 v[204:207], a[244:247], v[108:111], v[204:207]\n"
"s_add_u32 s84, s84, 4096\n"
"buffer_load_dwordx4 a[48:51], v1, s[40:43], s84 offen sc1\n"
"v_mfma_f32_16x16x32_bf16 v[208:211], a[248:251], v[80:83], v[208:211]\n"
"ds_read_b128 a[240:243], v2 offset:36864\n"
"v_mfma_f32_16x16x32_bf16 v[208:211], a[252:255], v[96:99], v[208:211]\n"
"v_mfma_f32_16x16x32_bf16 v[212:215], a[248:251], v[84:87], v[212:215]\n"
"buffer_load_dwordx4 a[52:55], v1, s[40:43], s84 offen offset:1024 sc1\n"
"v_mfma_f32_16x16x32_bf16 v[212:215], a[252:255], v[100:103], v[212:215]\n"
"ds_read_b128 a[244:247], v2 offset:37888\n"
"v_mfma_f32_16x16x32_bf16 v[216:219], a[248:251], v[88:91], v[216:219]\n"
"v_mfma_f32_16x16x32_bf16 v[216:219], a[252:255], v[104:107], v[216:219]\n"
"v_mfma_f32_16x16x32_bf16 v[220:223], a[248:251], v[92:95], v[220:223]\n"
"s_waitcnt lgkmcnt(4)\n"
"v_mfma_f32_16x16x32_bf16 v[220:223], a[252:255], v[108:111], v[220:223]\n"
"buffer_load_dwordx4 a[56:59], v1, s[40:43], s84 offen offset:2048 sc1\n"
"v_mfma_f32_16x16x32_bf16 v[16:19], a[224:227], v[112:115], v[16:19]\n"
"ds_read_b128 a[248:251], v2 offset:38912\n"
"v_mfma_f32_16x16x32_bf16 v[16:19], a[228:231], v[128:131], v[16:19]\n"
"v_mfma_f32_16x16x32_bf16 v[20:23], a[224:227], v[116:119], v[20:23]\n"
"v_mfma_f32_16x16x32_bf16 v[20:23], a[228:231], v[132:135], v[20:23]\n"
"ds_read_b128 a[252:255], v2 offset:39936\n"
"v_mfma_f32_16x16x32_bf16 v[24:27], a[224:227], v[120:123], v[24:27]\n"
"buffer_load_dwordx4 a[60:63], v1, s[40:43], s84 offen offset:3072 sc1\n"
"v_mfma_f32_16x16x32_bf16 v[24:27], a[228:231], v[136:139], v[24:27]\n"
"v_mfma_f32_16x16x32_bf16 v[28:31], a[224:227], v[124:127], v[28:31]\n"
"s_waitcnt lgkmcnt(4)\n"
"v_mfma_f32_16x16x32_bf16 v[28:31], a[228:231], v[140:143], v[28:31]\n"
"v_mfma_f32_16x16x32_bf16 v[32:35], a[232:235], v[112:115], v[32:35]\n"
"s_add_u32 s84, s84, 4096\n"
"ds_read_b128 a[224:227], v2 offset:40960\n"
"v_mfma_f32_16x16x32_bf16 v[32:35], a[236:239], v[128:131], v[32:35]\n"
"s_add_u32 s82, s64, 557056\n"
"v_mfma_f32_16x16x32_bf16 v[36:39], a[232:235], v[116:119], v[36:39]\n"
"s_add_u32 m0, s66, 0\n"
"v_mfma_f32_16x16x32_bf16 v[36:39], a[236:239], v[132:135], v[36:39]\n"
"ds_read_b128 a[228:231], v2 offset:41984\n"
"v_mfma_f32_16x16x32_bf16 v[40:43], a[232:235], v[120:123], v[40:43]\n"
"buffer_load_dwordx4 v1, s[40:43], s82 offen sc1 lds\n"
"v_mfma_f32_16x16x32_bf16 v[40:43], a[236:239], v[136:139], v[40:43]\n"
"buffer_load_dwordx4 v1, s[40:43], s82 offen offset:1024 sc1 lds\n"
"v_mfma_f32_16x16x32_bf16 v[44:47], a[232:235], v[124:127], v[44:47]\n"
"s_waitcnt lgkmcnt(4)\n"
"v_mfma_f32_16x16x32_bf16 v[44:47], a[236:239], v[140:143], v[44:47]\n"
"v_mfma_f32_16x16x32_bf16 v[48:51], a[240:243], v[112:115], v[48:51]\n"
"ds_read_b128 a[232:235], v2 offset:43008\n"
"v_mfma_f32_16x16x32_bf16 v[48:51], a[244:247], v[128:131], v[48:51]\n"
"buffer_load_dwordx4 v1, s[40:43], s82 offen offset:2048 sc1 lds\n"
"v_mfma_f32_16x16x32_bf16 v[52:55], a[240:243], v[116:119], v[52:55]\n"
"v_mfma_f32_16x16x32_bf16 v[52:55], a[244:247], v[132:135], v[52:55]\n"
"ds_read_b128 a[236:239], v2 offset:44032\n"
"v_mfma_f32_16x16x32_bf16 v[56:59], a[240:243], v[120:123], v[56:59]\n"
"v_mfma_f32_16x16x32_bf16 v[56:59], a[244:247], v[136:139], v[56:59]\n"
"buffer_load_dwordx4 v1, s[40:43], s82 offen offset:3072 sc1 lds\n"
"v_mfma_f32_16x16x32_bf16 v[60:63], a[240:243], v[124:127], v[60:63]\n"
"s_waitcnt lgkmcnt(4)\n"
"v_mfma_f32_16x16x32_bf16 v[60:63], a[244:247], v[140:143], v[60:63]\n"
"v_mfma_f32_16x16x32_bf16 v[64:67], a[248:251], v[112:115], v[64:67]\n"
"ds_read_b128 a[240:243], v2 offset:45056\n"
"v_mfma_f32_16x16x32_bf16 v[64:67], a[252:255], v[128:131], v[64:67]\n"
"v_mfma_f32_16x16x32_bf16 v[68:71], a[248:251], v[116:119], v[68:71]\n"
"s_waitcnt vmcnt(12)\n"
"v_cmp_ne_u64 vcc, s[92:93], v[10:11]\n"
"v_mfma_f32_16x16x32_bf16 v[68:71], a[252:255], v[132:135], v[68:71]\n"
"s_cbranch_vccnz L_slow_26_%=\n"
"L_back_27_%=:\n"
"ds_read_b128 a[244:247], v2 offset:46080\n"
"v_mfma_f32_16x16x32_bf16 v[72:75], a[248:251], v[120:123], v[72:75]\n"
"buffer_load_dwordx4 a[64:67], v1, s[40:43], s84 offen sc1\n"
"v_mfma_f32_16x16x32_bf16 v[72:75], a[252:255], v[136:139], v[72:75]\n"
"v_mfma_f32_16x16x32_bf16 v[76:79], a[248:251], v[124:127], v[76:79]\n"
"s_waitcnt lgkmcnt(4)\n"
"v_mfma_f32_16x16x32_bf16 v[76:79], a[252:255], v[140:143], v[76:79]\n"
"v_mfma_f32_16x16x32_bf16 v[144:147], a[224:227], v[112:115], v[144:147]\n"
"ds_read_b128 a[248:251], v2 offset:47104\n"
"v_mfma_f32_16x16x32_bf16 v[144:147], a[228:231], v[128:131], v[144:147]\n"
"buffer_load_dwordx4 a[68:71], v1, s[40:43], s84 offen offset:1024 sc1\n"
"v_mfma_f32_16x16x32_bf16 v[148:151], a[224:227], v[116:119], v[148:151]\n"
"v_mfma_f32_16x16x32_bf16 v[148:151], a[228:231], v[132:135], v[148:151]\n"
"ds_read_b128 a[252:255], v2 offset:48128\n"
"v_mfma_f32_16x16x32_bf16 v[152:155], a[224:227], v[120:123], v[152:155]\n"
"v_mfma_f32_16x16x32_bf16 v[152:155], a[228:231], v[136:139], v[152:155]\n"
"buffer_load_dwordx4 a[72:75], v1, s[40:43], s84 offen offset:2048 sc1\n"
"v_mfma_f32_16x16x32_bf16 v[156:159], a[224:227], v[124:127], v[156:159]\n"
"s_waitcnt lgkmcnt(4)\n"
"v_mfma_f32_16x16x32_bf16 v[156:159], a[228:231], v[140:143], v[156:159]\n"
"v_mfma_f32_16x16x32_bf16 v[160:163], a[232:235], v[112:115], v[160:163]\n"
"ds_read_b128 a[224:227], v2 offset:49152\n"
"v_mfma_f32_16x16x32_bf16 v[160:163], a[236:239], v[128:131], v[160:163]\n"
"buffer_load_dwordx4 a[76:79], v1, s[40:43], s84 offen offset:3072 sc1\n"
"v_mfma_f32_16x16x32_bf16 v[164:167], a[232:235], v[116:119], v[164:167]\n"
"v_mfma_f32_16x16x32_bf16 v[164:167], a[236:239], v[132:135], v[164:167]\n"
"ds_read_b128 a[228:231], v2 offset:50176\n"
"v_mfma_f32_16x16x32_bf16 v[168:171], a[232:235], v[120:123], v[168:171]\n"
"v_mfma_f32_16x16x32_bf16 v[168:171], a[236:239], v[136:139], v[168:171]\n"
"v_mfma_f32_16x16x32_bf16 v[172:175], a[232:235], v[124:127], v[172:175]\n"
"s_waitcnt lgkmcnt(4)\n"
"v_mfma_f32_16x16x32_bf16 v[172:175], a[236:239], v[140:143], v[172:175]\n"
"s_add_u32 s84, s84, 4096\n"
"buffer_load_dwordx4 a[80:83], v1, s[40:43], s84 offen sc1\n"
"v_mfma_f32_16x16x32_bf16 v[176:179], a[240:243], v[112:115], v[176:179]\n"
"ds_read_b128 a[232:235], v2 offset:51200\n"
"v_mfma_f32_16x16x32_bf16 v[176:179], a[244:247], v[128:131], v[176:179]\n"
"v_mfma_f32_16x16x32_bf16 v[180:183], a[240:243], v[116:119], v[180:183]\n"
"buffer_load_dwordx4 a[84:87], v1, s[40:43], s84 offen offset:1024 sc1\n"
"v_mfma_f32_16x16x32_bf16 v[180:183], a[244:247], v[132:135], v[180:183]\n"
"ds_read_b128 a[236:239], v2 offset:52224\n"
"v_mfma_f32_16x16x32_bf16 v[184:187], a[240:243], v[120:123], v[184:187]\n"
"v_mfma_f32_16x16x32_bf16 v[184:187], a[244:247], v[136:139], v[184:187]\n"
"v_mfma_f32_16x16x32_bf16 v[188:191], a[240:243], v[124:127], v[188:191]\n"
"s_waitcnt lgkmcnt(4)\n"
"v_mfma_f32_16x16x32_bf16 v[188:191], a[244:247], v[140:143], v[188:191]\n"
"buffer_load_dwordx4 a[88:91], v1, s[40:43], s84 offen offset:2048 sc1\n"
"v_mfma_f32_16x16x32_bf16 v[192:195], a[248:251], v[112:115], v[192:195]\n"
"ds_read_b128 a[240:243], v2 offset:53248\n"
"v_mfma_f32_16x16x32_bf16 v[192:195], a[252:255], v[128:131], v[192:195]\n"
"v_mfma_f32_16x16x32_bf16 v[196:199], a[248:251], v[116:119], v[196:199]\n"
"v_mfma_f32_16x16x32_bf16 v[196:199], a[252:255], v[132:135], v[196:199]\n"
"ds_read_b128 a[244:247], v2 offset:54272\n"
"v_mfma_f32_16x16x32_bf16 v[200:203], a[248:251], v[120:123], v[200:203]\n"
"buffer_load_dwordx4 a[92:95], v1, s[40:43], s84 offen offset:3072 sc1\n"
"v_mfma_f32_16x16x32_bf16 v[200:203], a[252:255], v[136:139], v[200:203]\n"
"v_mfma_f32_16x16x32_bf16 v[204:207], a[248:251], v[124:127], v[204:207]\n"
"s_waitcnt lgkmcnt(4)\n"
"v_mfma_f32_16x16x32_bf16 v[204:207], a[252:255], v[140:143], v[204:207]\n"
"v_mfma_f32_16x16x32_bf16 v[208:211], a[224:227], v[112:115], v[208:211]\n"
"s_add_u32 s84, s84, 4096\n"
"ds_read_b128 a[248:251], v2 offset:55296\n"
"v_mfma_f32_16x16x32_bf16 v[208:211], a[228:231], v[128:131], v[208:211]\n"
"s_add_u32 s82, s64, 573440\n"
"v_mfma_f32_16x16x32_bf16 v[212:215], a[224:227], v[116:119], v[212:215]\n"
"s_add_u32 m0, s66, 16384\n"
"v_mfma_f32_16x16x32_bf16 v[212:215], a[228:231], v[132:135], v[212:215]\n"
"ds_read_b128 a[252:255], v2 offset:56320\n"
"v_mfma_f32_16x16x32_bf16 v[216:219], a[224:227], v[120:123], v[216:219]\n"
"buffer_load_dwordx4 v1, s[40:43], s82 offen sc1 lds\n"
"v_mfma_f32_16x16x32_bf16 v[216:219], a[228:231], v[136:139], v[216:219]\n"
"buffer_load_dwordx4 v1, s[40:43], s82 offen offset:1024 sc1 lds\n"
"v_mfma_f32_16x16x32_bf16 v[220:223], a[224:227], v[124:127], v[220:223]\n"
"s_waitcnt lgkmcnt(4)\n"
"v_mfma_f32_16x16x32_bf16 v[220:223], a[228:231], v[140:143], v[220:223]\n"
"v_mfma_f32_16x16x32_bf16 v[16:19], a[232:235], a[128:131], v[16:19]\n"
"ds_read_b128 a[224:227], v2 offset:57344\n"
"v_mfma_f32_16x16x32_bf16 v[16:19], a[236:239], a[144:147], v[16:19]\n"
"v_mfma_f32_16x16x32_bf16 v[20:23], a[232:235], a[132:135], v[20:23]\n"
"buffer_load_dwordx4 v1, s[40:43], s82 offen offset:2048 sc1 lds\n"
"v_mfma_f32_16x16x32_bf16 v[20:23], a[236:239], a[148:151], v[20:23]\n"
"ds_read_b128 a[228:231], v2 offset:58368\n"
"v_mfma_f32_16x16x32_bf16 v[24:27], a[232:235], a[136:139], v[24:27]\n"
"v_mfma_f32_16x16x32_bf16 v[24:27], a[236:239], a[152:155], v[24:27]\n"
"v_mfma_f32_16x16x32_bf16 v[28:31], a[232:235], a[140:143], v[28:31]\n"
"s_waitcnt lgkmcnt(4)\n"
"buffer_load_dwordx4 v1, s[40:43], s82 offen offset:3072 sc1 lds\n"
"v_mfma_f32_16x16x32_bf16 v[28:31], a[236:239], a[156:159], v[28:31]\n"
"v_mfma_f32_16x16x32_bf16 v[32:35], a[240:243], a[128:131], v[32:35]\n"
"ds_read_b128 a[232:235], v2 offset:59392\n"
"v_mfma_f32_16x16x32_bf16 v[32:35], a[244:247], a[144:147], v[32:35]\n"
"v_mfma_f32_16x16x32_bf16 v[36:39], a[240:243], a[132:135], v[36:39]\n"
"v_mfma_f32_16x16x32_bf16 v[36:39], a[244:247], a[148:151], v[36:39]\n"
"ds_read_b128 a[236:239], v2 offset:60416\n"
"v_mfma_f32_16x16x32_bf16 v[40:43], a[240:243], a[136:139], v[40:43]\n"
"buffer_load_dwordx4 a[96:99], v1, s[40:43], s84 offen sc1\n"
"v_mfma_f32_16x16x32_bf16 v[40:43], a[244:247], a[152:155], v[40:43]\n"
"v_mfma_f32_16x16x32_bf16 v[44:47], a[240:243], a[140:143], v[44:47]\n"
"s_waitcnt lgkmcnt(4)\n"
"v_mfma_f32_16x16x32_bf16 v[44:47], a[244:247], a[156:159], v[44:47]\n"
"buffer_load_dwordx4 a[100:103], v1, s[40:43], s84 offen offset:1024 sc1\n"
"v_mfma_f32_16x16x32_bf16 v[48:51], a[248:251], a[128:131], v[48:51]\n"
"ds_read_b128 a[240:243], v2 offset:61440\n"
"v_mfma_f32_16x16x32_bf16 v[48:51], a[252:255], a[144:147], v[48:51]\n"
"v_mfma_f32_16x16x32_bf16 v[52:55], a[248:251], a[132:135], v[52:55]\n"
"v_mfma_f32_16x16x32_bf16 v[52:55], a[252:255], a[148:151], v[52:55]\n"
"ds_read_b128 a[244:247], v2 offset:62464\n"
"v_mfma_f32_16x16x32_bf16 v[56:59], a[248:251], a[136:139], v[56:59]\n"
"buffer_load_dwordx4 a[104:107], v1, s[40:43], s84 offen offset:2048 sc1\n"
"v_mfma_f32_16x16x32_bf16 v[56:59], a[252:255], a[152:155], v[56:59]\n"
"v_mfma_f32_16x16x32_bf16 v[60:63], a[248:251], a[140:143], v[60:63]\n"
"s_waitcnt lgkmcnt(4)\n"
"v_mfma_f32_16x16x32_bf16 v[60:63], a[252:255], a[156:159], v[60:63]\n"
"v_mfma_f32_16x16x32_bf16 v[64:67], a[224:227], a[128:131], v[64:67]\n"
"ds_read_b128 a[248:251], v2 offset:63488\n"
"v_mfma_f32_16x16x32_bf16 v[64:67], a[228:231], a[144:147], v[64:67]\n"
"buffer_load_dwordx4 a[108:111], v1, s[40:43], s84 offen offset:3072 sc1\n"
"v_mfma_f32_16x16x32_bf16 v[68:71], a[224:227], a[132:135], v[68:71]\n"
"v_mfma_f32_16x16x32_bf16 v[68:71], a[228:231], a[148:151], v[68:71]\n"
"ds_read_b128 a[252:255], v2 offset:64512\n"
"v_mfma_f32_16x16x32_bf16 v[72:75], a[224:227], a[136:139], v[72:75]\n"
"v_mfma_f32_16x16x32_bf16 v[72:75], a[228:231], a[152:155], v[72:75]\n"
"s_add_u32 s84, s84, 4096\n"
"buffer_load_dwordx4 a[112:115], v1, s[40:43], s84 offen sc1\n"
"v_mfma_f32_16x16x32_bf16 v[76:79], a[224:227], a[140:143], v[76:79]\n"
"s_waitcnt lgkmcnt(4)\n"
"v_mfma_f32_16x16x32_bf16 v[76:79], a[228:231], a[156:159], v[76:79]\n"
"s_barrier\n"
"v_mfma_f32_16x16x32_bf16 v[144:147], a[232:235], a[128:131], v[144:147]\n"
"ds_read_b128 a[224:227], v3\n"
"v_mfma_f32_16x16x32_bf16 v[144:147], a[236:239], a[144:147], v[144:147]\n"
"buffer_load_dwordx4 a[116:119], v1, s[40:43], s84 offen offset:1024 sc1\n"
"v_mfma_f32_16x16x32_bf16 v[148:151], a[232:235], a[132:135], v[148:151]\n"
"v_mfma_f32_16x16x32_bf16 v[148:151], a[236:239], a[148:151], v[148:151]\n"
"ds_read_b128 a[228:231], v3 offset:1024\n"
"v_mfma_f32_16x16x32_bf16 v[152:155], a[232:235], a[136:139], v[152:155]\n"
"v_mfma_f32_16x16x32_bf16 v[152:155], a[236:239], a[152:155], v[152:155]\n"
"v_mfma_f32_16x16x32_bf16 v[156:159], a[232:235], a[140:143], v[156:159]\n"
"s_waitcnt lgkmcnt(4)\n"
"buffer_load_dwordx4 a[120:123], v1, s[40:43], s84 offen offset:2048 sc1\n"
"v_mfma_f32_16x16x32_bf16 v[156:159], a[236:239], a[156:159], v[156:159]\n"
"v_mfma_f32_16x16x32_bf16 v[160:163], a[240:243], a[128:131], v[160:163]\n"
"ds_read_b128 a[232:235], v3 offset:2048\n"
"v_mfma_f32_16x16x32_bf16 v[160:163], a[244:247], a[144:147], v[160:163]\n"
"v_mfma_f32_16x16x32_bf16 v[164:167], a[240:243], a[132:135], v[164:167]\n"
"buffer_load_dwordx4 a[124:127], v1, s[40:43], s84 offen offset:3072 sc1\n"
"v_mfma_f32_16x16x32_bf16 v[164:167], a[244:247], a[148:151], v[164:167]\n"
"ds_read_b128 a[236:239], v3 offset:3072\n"
"v_mfma_f32_16x16x32_bf16 v[168:171], a[240:243], a[136:139], v[168:171]\n"
"v_mfma_f32_16x16x32_bf16 v[168:171], a[244:247], a[152:155], v[168:171]\n"
"v_mfma_f32_16x16x32_bf16 v[172:175], a[240:243], a[140:143], v[172:175]\n"
"s_waitcnt lgkmcnt(4)\n"
"v_mfma_f32_16x16x32_bf16 v[172:175], a[244:247], a[156:159], v[172:175]\n"
"s_add_u32 s84, s84, 4096\n"
"v_mfma_f32_16x16x32_bf16 v[176:179], a[248:251], a[128:131], v[176:179]\n"
"ds_read_b128 a[240:243], v3 offset:4096\n"
"v_mfma_f32_16x16x32_bf16 v[176:179], a[252:255], a[144:147], v[176:179]\n"
"v_mfma_f32_16x16x32_bf16 v[180:183], a[248:251], a[132:135], v[180:183]\n"
"v_mfma_f32_16x16x32_bf16 v[180:183], a[252:255], a[148:151], v[180:183]\n"
"ds_read_b128 a[244:247], v3 offset:5120\n"
"v_mfma_f32_16x16x32_bf16 v[184:187], a[248:251], a[136:139], v[184:187]\n"
"v_mfma_f32_16x16x32_bf16 v[184:187], a[252:255], a[152:155], v[184:187]\n"
"v_mfma_f32_16x16x32_bf16 v[188:191], a[248:251], a[140:143], v[188:191]\n"
"s_waitcnt lgkmcnt(4)\n"
"v_mfma_f32_16x16x32_bf16 v[188:191], a[252:255], a[156:159], v[188:191]\n"
"v_mfma_f32_16x16x32_bf16 v[192:195], a[224:227], a[128:131], v[192:195]\n"
"ds_read_b128 a[248:251], v3 offset:6144\n"
"v_mfma_f32_16x16x32_bf16 v[192:195], a[228:231], a[144:147], v[192:195]\n"
"buffer_load_dwordx4 v[80:83], v1, s[40:43], s84 offen sc1\n"
"v_mfma_f32_16x16x32_bf16 v[196:199], a[224:227], a[132:135], v[196:199]\n"
"v_mfma_f32_16x16x32_bf16 v[196:199], a[228:231], a[148:151], v[196:199]\n"
"ds_read_b128 a[252:255], v3 offset:7168\n"
"v_mfma_f32_16x16x32_bf16 v[200:203], a[224:227], a[136:139], v[200:203]\n"
"buffer_load_dwordx4 v[84:87], v1, s[40:43], s84 offen offset:1024 sc1\n"
"v_mfma_f32_16x16x32_bf16 v[200:203], a[228:231], a[152:155], v[200:203]\n"
"v_mfma_f32_16x16x32_bf16 v[204:207], a[224:227], a[140:143], v[204:207]\n"
"s_waitcnt lgkmcnt(4)\n"
"v_mfma_f32_16x16x32_bf16 v[204:207], a[228:231], a[156:159], v[204:207]\n"
"v_mfma_f32_16x16x32_bf16 v[208:211], a[232:235], a[128:131], v[208:211]\n"
"ds_read_b128 a[224:227], v3 offset:8192\n"
"v_mfma_f32_16x16x32_bf16 v[208:211], a[236:239], a[144:147], v[208:211]\n"
"buffer_load_dwordx4 v[88:91], v1, s[40:43], s84 offen offset:2048 sc1\n"
"v_mfma_f32_16x16x32_bf16 v[212:215], a[232:235], a[132:135], v[212:215]\n"
"v_mfma_f32_16x16x32_bf16 v[212:215], a[236:239], a[148:151], v[212:215]\n"
"ds_read_b128 a[228:231], v3 offset:9216\n"
"v_mfma_f32_16x16x32_bf16 v[216:219], a[232:235], a[136:139], v[216:219]\n"
"v_mfma_f32_16x16x32_bf16 v[216:219], a[236:239], a[152:155], v[216:219]\n"
"buffer_load_dwordx4 v[92:95], v1, s[40:43], s84 offen offset:3072 sc1\n"
"v_mfma_f32_16x16x32_bf16 v[220:223], a[232:235], a[140:143], v[220:223]\n"
"s_waitcnt lgkmcnt(4)\n"
"v_mfma_f32_16x16x32_bf16 v[220:223], a[236:239], a[156:159], v[220:223]\n"
"v_mfma_f32_16x16x32_bf16 v[16:19], a[240:243], a[160:163], v[16:19]\n"
"ds_read_b128 a[232:235], v3 offset:10240\n"
"v_mfma_f32_16x16x32_bf16 v[16:19], a[244:247], a[176:179], v[16:19]\n"
"v_mfma_f32_16x16x32_bf16 v[20:23], a[240:243], a[164:167], v[20:23]\n"
"s_add_u32 s84, s84, 4096\n"
"buffer_load_dwordx4 v[96:99], v1, s[40:43], s84 offen sc1\n"
"v_mfma_f32_16x16x32_bf16 v[20:23], a[244:247], a[180:183], v[20:23]\n"
"ds_read_b128 a[236:239], v3 offset:11264\n"
"v_mfma_f32_16x16x32_bf16 v[24:27], a[240:243], a[168:171], v[24:27]\n"
"v_mfma_f32_16x16x32_bf16 v[24:27], a[244:247], a[184:187], v[24:27]\n"
"v_mfma_f32_16x16x32_bf16 v[28:31], a[240:243], a[172:175], v[28:31]\n"
"s_waitcnt lgkmcnt(4)\n"
"buffer_load_dwordx4 v[100:103], v1, s[40:43], s84 offen offset:1024 sc1\n"
"v_mfma_f32_16x16x32_bf16 v[28:31], a[244:247], a[188:191], v[28:31]\n"
"v_mfma_f32_16x16x32_bf16 v[32:35], a[248:251], a[160:163], v[32:35]\n"
"ds_read_b128 a[240:243], v3 offset:12288\n"
"v_mfma_f32_16x16x32_bf16 v[32:35], a[252:255], a[176:179], v[32:35]\n"
"v_mfma_f32_16x16x32_bf16 v[36:39], a[248:251], a[164:167], v[36:39]\n"
"v_mfma_f32_16x16x32_bf16 v[36:39], a[252:255], a[180:183], v[36:39]\n"
"ds_read_b128 a[244:247], v3 offset:13312\n"
"v_mfma_f32_16x16x32_bf16 v[40:43], a[248:251], a[168:171], v[40:43]\n"
"buffer_load_dwordx4 v[104:107], v1, s[40:43], s84 offen offset:2048 sc1\n"
"v_mfma_f32_16x16x32_bf16 v[40:43], a[252:255], a[184:187], v[40:43]\n"
"v_mfma_f32_16x16x32_bf16 v[44:47], a[248:251], a[172:175], v[44:47]\n"
"s_waitcnt lgkmcnt(4)\n"
"v_mfma_f32_16x16x32_bf16 v[44:47], a[252:255], a[188:191], v[44:47]\n"
"buffer_load_dwordx4 v[108:111], v1, s[40:43], s84 offen offset:3072 sc1\n"
"v_mfma_f32_16x16x32_bf16 v[16:19], a[224:227], a[192:195], v[16:19]\n"
"ds_read_b128 a[248:251], v3 offset:14336\n"
"v_mfma_f32_16x16x32_bf16 v[16:19], a[228:231], a[208:211], v[16:19]\n"
"v_mfma_f32_16x16x32_bf16 v[20:23], a[224:227], a[196:199], v[20:23]\n"
"v_mfma_f32_16x16x32_bf16 v[20:23], a[228:231], a[212:215], v[20:23]\n"
"ds_read_b128 a[252:255], v3 offset:15360\n"
"v_mfma_f32_16x16x32_bf16 v[24:27], a[224:227], a[200:203], v[24:27]\n"
"s_add_u32 s84, s84, 4096\n"
"v_mfma_f32_16x16x32_bf16 v[24:27], a[228:231], a[216:219], v[24:27]\n"
"s_add_u32 s82, s64, 589824\n"
"v_mfma_f32_16x16x32_bf16 v[28:31], a[224:227], a[204:207], v[28:31]\n"
"s_waitcnt lgkmcnt(4)\n"
"v_mfma_f32_16x16x32_bf16 v[28:31], a[228:231], a[220:223], v[28:31]\n"
"s_add_u32 m0, s66, 32768\n"
"v_mfma_f32_16x16x32_bf16 v[32:35], a[232:235], a[192:195], v[32:35]\n"
"ds_read_b128 a[224:227], v3 offset:16384\n"
"v_mfma_f32_16x16x32_bf16 v[32:35], a[236:239], a[208:211], v[32:35]\n"
"buffer_load_dwordx4 v1, s[40:43], s82 offen sc1 lds\n"
"v_mfma_f32_16x16x32_bf16 v[36:39], a[232:235], a[196:199], v[36:39]\n"
"buffer_load_dwordx4 v1, s[40:43], s82 offen offset:1024 sc1 lds\n"
"v_mfma_f32_16x16x32_bf16 v[36:39], a[236:239], a[212:215], v[36:39]\n"
"ds_read_b128 a[228:231], v3 offset:17408\n"
"v_mfma_f32_16x16x32_bf16 v[40:43], a[232:235], a[200:203], v[40:43]\n"
"v_mfma_f32_16x16x32_bf16 v[40:43], a[236:239], a[216:219], v[40:43]\n"
"buffer_load_dwordx4 v1, s[40:43], s82 offen offset:2048 sc1 lds\n"
"v_mfma_f32_16x16x32_bf16 v[44:47], a[232:235], a[204:207], v[44:47]\n"
"s_waitcnt lgkmcnt(4)\n"
"v_mfma_f32_16x16x32_bf16 v[44:47], a[236:239], a[220:223], v[44:47]\n"
"v_mfma_f32_16x16x32_bf16 v[16:19], a[240:243], a[0:3], v[16:19]\n"
"ds_read_b128 a[232:235], v3 offset:18432\n"
"v_mfma_f32_16x16x32_bf16 v[16:19], a[244:247], a[16:19], v[16:19]\n"
"buffer_load_dwordx4 v1, s[40:43], s82 offen offset:3072 sc1 lds\n"
"v_mfma_f32_16x16x32_bf16 v[20:23], a[240:243], a[4:7], v[20:23]\n"
"v_mfma_f32_16x16x32_bf16 v[20:23], a[244:247], a[20:23], v[20:23]\n"
"ds_read_b128 a[236:239], v3 offset:19456\n"
"v_mfma_f32_16x16x32_bf16 v[24:27], a[240:243], a[8:11], v[24:27]\n"
"v_mfma_f32_16x16x32_bf16 v[24:27], a[244:247], a[24:27], v[24:27]\n"
"v_mfma_f32_16x16x32_bf16 v[28:31], a[240:243], a[12:15], v[28:31]\n"
"s_waitcnt lgkmcnt(4)\n"
"buffer_load_dwordx4 v[112:115], v1, s[40:43], s84 offen sc1\n"
"v_mfma_f32_16x16x32_bf16 v[28:31], a[244:247], a[28:31], v[28:31]\n"
"v_mfma_f32_16x16x32_bf16 v[32:35], a[248:251], a[0:3], v[32:35]\n"
"ds_read_b128 a[240:243], v3 offset:20480\n"
"v_mfma_f32_16x16x32_bf16 v[32:35], a[252:255], a[16:19], v[32:35]\n"
"v_mfma_f32_16x16x32_bf16 v[36:39], a[248:251], a[4:7], v[36:39]\n"
"buffer_load_dwordx4 v[116:119], v1, s[40:43], s84 offen offset:1024 sc1\n"
"v_mfma_f32_16x16x32_bf16 v[36:39], a[252:255], a[20:23], v[36:39]\n"
"ds_read_b128 a[244:247], v3 offset:21504\n"
"v_mfma_f32_16x16x32_bf16 v[40:43], a[248:251], a[8:11], v[40:43]\n"
"v_mfma_f32_16x16x32_bf16 v[40:43], a[252:255], a[24:27], v[40:43]\n"
"v_mfma_f32_16x16x32_bf16 v[44:47], a[248:251], a[12:15], v[44:47]\n"
"s_waitcnt vmcnt(38) lgkmcnt(4)\n"
"v_mfma_f32_16x16x32_bf16 v[44:47], a[252:255], a[28:31], v[44:47]\n"
"buffer_load_dwordx4 v[120:123], v1, s[40:43], s84 offen offset:2048 sc1\n"
"v_mfma_f32_16x16x32_bf16 v[16:19], a[224:227], a[32:35], v[16:19]\n"
"ds_read_b128 a[248:251], v3 offset:22528\n"
"v_mfma_f32_16x16x32_bf16 v[16:19], a[228:231], a[48:51], v[16:19]\n"
"v_mfma_f32_16x16x32_bf16 v[20:23], a[224:227], a[36:39], v[20:23]\n"
"v_mfma_f32_16x16x32_bf16 v[20:23], a[228:231], a[52:55], v[20:23]\n"
"ds_read_b128 a[252:255], v3 offset:23552\n"
"v_mfma_f32_16x16x32_bf16 v[24:27], a[224:227], a[40:43], v[24:27]\n"
"buffer_load_dwordx4 v[124:127], v1, s[40:43], s84 offen offset:3072 sc1\n"
"v_mfma_f32_16x16x32_bf16 v[24:27], a[228:231], a[56:59], v[24:27]\n"
"v_mfma_f32_16x16x32_bf16 v[28:31], a[224:227], a[44:47], v[28:31]\n"
"s_waitcnt lgkmcnt(4)\n"
"v_mfma_f32_16x16x32_bf16 v[28:31], a[228:231], a[60:63], v[28:31]\n"
"v_mfma_f32_16x16x32_bf16 v[32:35], a[232:235], a[32:35], v[32:35]\n"
"s_add_u32 s84, s84, 4096\n"
"ds_read_b128 a[224:227], v3 offset:24576\n"
"v_mfma_f32_16x16x32_bf16 v[32:35], a[236:239], a[48:51], v[32:35]\n"
"buffer_load_dwordx4 v[128:131], v1, s[40:43], s84 offen sc1\n"
"v_mfma_f32_16x16x32_bf16 v[36:39], a[232:235], a[36:39], v[36:39]\n"
"v_mfma_f32_16x16x32_bf16 v[36:39], a[236:239], a[52:55], v[36:39]\n"
"ds_read_b128 a[228:231], v3 offset:25600\n"
"v_mfma_f32_16x16x32_bf16 v[40:43], a[232:235], a[40:43], v[40:43]\n"
"buffer_load_dwordx4 v[132:135], v1, s[40:43], s84 offen offset:1024 sc1\n"
"v_mfma_f32_16x16x32_bf16 v[40:43], a[236:239], a[56:59], v[40:43]\n"
"v_mfma_f32_16x16x32_bf16 v[44:47], a[232:235], a[44:47], v[44:47]\n"
"s_waitcnt vmcnt(30) lgkmcnt(4)\n"
"v_mfma_f32_16x16x32_bf16 v[44:47], a[236:239], a[60:63], v[44:47]\n"
"v_mfma_f32_16x16x32_bf16 v[16:19], a[240:243], a[64:67], v[16:19]\n"
"ds_read_b128 a[232:235], v3 offset:26624\n"
"v_mfma_f32_16x16x32_bf16 v[16:19], a[244:247], a[80:83], v[16:19]\n"
"buffer_load_dwordx4 v[136:139], v1, s[40:43], s84 offen offset:2048 sc1\n"
"v_mfma_f32_16x16x32_bf16 v[20:23], a[240:243], a[68:71], v[20:23]\n"
"v_mfma_f32_16x16x32_bf16 v[20:23], a[244:247], a[84:87], v[20:23]\n"
"ds_read_b128 a[236:239], v3 offset:27648\n"
"v_mfma_f32_16x16x32_bf16 v[24:27], a[240:243], a[72:75], v[24:27]\n"
"v_mfma_f32_16x16x32_bf16 v[24:27], a[244:247], a[88:91], v[24:27]\n"
"buffer_load_dwordx4 v[140:143], v1, s[40:43], s84 offen offset:3072 sc1\n"
"v_mfma_f32_16x16x32_bf16 v[28:31], a[240:243], a[76:79], v[28:31]\n"
"s_waitcnt lgkmcnt(4)\n"
"v_mfma_f32_16x16x32_bf16 v[28:31], a[244:247], a[92:95], v[28:31]\n"
"v_mfma_f32_16x16x32_bf16 v[32:35], a[248:251], a[64:67], v[32:35]\n"
"ds_read_b128 a[240:243], v3 offset:28672\n"
"v_mfma_f32_16x16x32_bf16 v[32:35], a[252:255], a[80:83], v[32:35]\n"
"v_mfma_f32_16x16x32_bf16 v[36:39], a[248:251], a[68:71], v[36:39]\n"
"s_add_u32 s84, s84, 4096\n"
"v_mfma_f32_16x16x32_bf16 v[36:39], a[252:255], a[84:87], v[36:39]\n"
"s_add_u32 s82, s64, 606208\n"
"ds_read_b128 a[244:247], v3 offset:29696\n"
"v_mfma_f32_16x16x32_bf16 v[40:43], a[248:251], a[72:75], v[40:43]\n"
"s_add_u32 m0, s66, 49152\n"
"v_mfma_f32_16x16x32_bf16 v[40:43], a[252:255], a[88:91], v[40:43]\n"
"buffer_load_dwordx4 v1, s[40:43], s82 offen sc1 lds\n"
"v_mfma_f32_16x16x32_bf16 v[44:47], a[248:251], a[76:79], v[44:47]\n"
"s_waitcnt vmcnt(21) lgkmcnt(4)\n"
"buffer_load_dwordx4 v1, s[40:43], s82 offen offset:1024 sc1 lds\n"
"v_mfma_f32_16x16x32_bf16 v[44:47], a[252:255], a[92:95], v[44:47]\n"
"v_mfma_f32_16x16x32_bf16 v[16:19], a[224:227], a[96:99], v[16:19]\n"
"ds_read_b128 a[248:251], v3 offset:30720\n"
"v_mfma_f32_16x16x32_bf16 v[16:19], a[228:231], a[112:115], v[16:19]\n"
"v_mfma_f32_16x16x32_bf16 v[20:23], a[224:227], a[100:103], v[20:23]\n"
"v_mfma_f32_16x16x32_bf16 v[20:23], a[228:231], a[116:119], v[20:23]\n"
"ds_read_b128 a[252:255], v3 offset:31744\n"
"v_mfma_f32_16x16x32_bf16 v[24:27], a[224:227], a[104:107], v[24:27]\n"
"buffer_load_dwordx4 v1, s[40:43], s82 offen offset:2048 sc1 lds\n"
"v_mfma_f32_16x16x32_bf16 v[24:27], a[228:231], a[120:123], v[24:27]\n"
"v_mfma_f32_16x16x32_bf16 v[28:31], a[224:227], a[108:111], v[28:31]\n"
"s_waitcnt lgkmcnt(4)\n"
"v_mfma_f32_16x16x32_bf16 v[28:31], a[228:231], a[124:127], v[28:31]\n"
"s_barrier\n"
"v_mfma_f32_16x16x32_bf16 v[32:35], a[232:235], a[96:99], v[32:35]\n"
"ds_read_b128 a[224:227], v1\n"
"v_mfma_f32_16x16x32_bf16 v[32:35], a[236:239], a[112:115], v[32:35]\n"
"buffer_load_dwordx4 v1, s[40:43], s82 offen offset:3072 sc1 lds\n"
"v_mfma_f32_16x16x32_bf16 v[36:39], a[232:235], a[100:103], v[36:39]\n"
"v_mfma_f32_16x16x32_bf16 v[36:39], a[236:239], a[116:119], v[36:39]\n"
"ds_read_b128 a[228:231], v1 offset:1024\n"
"v_mfma_f32_16x16x32_bf16 v[40:43], a[232:235], a[104:107], v[40:43]\n"
"v_mfma_f32_16x16x32_bf16 v[40:43], a[236:239], a[120:123], v[40:43]\n"
"v_mfma_f32_16x16x32_bf16 v[44:47], a[232:235], a[108:111], v[44:47]\n"
"s_waitcnt vmcnt(16) lgkmcnt(4)\n"
"v_mfma_f32_16x16x32_bf16 v[44:47], a[236:239], a[124:127], v[44:47]\n"
"v_mfma_f32_16x16x32_bf16 v[16:19], a[240:243], v[80:83], v[16:19]\n"
"ds_read_b128 a[232:235], v1 offset:2048\n"
"v_mfma_f32_16x16x32_bf16 v[16:19], a[244:247], v[96:99], v[16:19]\n"
"v_mfma_f32_16x16x32_bf16 v[20:23], a[240:243], v[84:87], v[20:23]\n"
"v_mfma_f32_16x16x32_bf16 v[20:23], a[244:247], v[100:103], v[20:23]\n"
"ds_read_b128 a[236:239], v1 offset:3072\n"
"v_mfma_f32_16x16x32_bf16 v[24:27], a[240:243], v[88:91], v[24:27]\n"
"v_mfma_f32_16x16x32_bf16 v[24:27], a[244:247], v[104:107], v[24:27]\n"
"v_mfma_f32_16x16x32_bf16 v[28:31], a[240:243], v[92:95], v[28:31]\n"
"s_waitcnt lgkmcnt(4)\n"
"v_mfma_f32_16x16x32_bf16 v[28:31], a[244:247], v[108:111], v[28:31]\n"
"v_mfma_f32_16x16x32_bf16 v[32:35], a[248:251], v[80:83], v[32:35]\n"
"ds_read_b128 a[240:243], v1 offset:4096\n"
"v_mfma_f32_16x16x32_bf16 v[32:35], a[252:255], v[96:99], v[32:35]\n"
"v_mfma_f32_16x16x32_bf16 v[36:39], a[248:251], v[84:87], v[36:39]\n"
"v_mfma_f32_16x16x32_bf16 v[36:39], a[252:255], v[100:103], v[36:39]\n"
"ds_read_b128 a[244:247], v1 offset:5120\n"
"v_mfma_f32_16x16x32_bf16 v[40:43], a[248:251], v[88:91], v[40:43]\n"
"v_mfma_f32_16x16x32_bf16 v[40:43], a[252:255], v[104:107], v[40:43]\n"
"v_mfma_f32_16x16x32_bf16 v[44:47], a[248:251], v[92:95], v[44:47]\n"
"s_waitcnt vmcnt(4) lgkmcnt(4)\n"
"v_mfma_f32_16x16x32_bf16 v[44:47], a[252:255], v[108:111], v[44:47]\n"
"v_mfma_f32_16x16x32_bf16 v[16:19], a[224:227], v[112:115], v[16:19]\n"
"ds_read_b128 a[248:251], v1 offset:6144\n"
"v_mfma_f32_16x16x32_bf16 v[16:19], a[228:231], v[128:131], v[16:19]\n"
"s_add_u32 s82, s64, 622592\n"
"v_mfma_f32_16x16x32_bf16 v[20:23], a[224:227], v[116:119], v[20:23]\n"
"s_add_u32 m0, s66, 65536\n"
"v_mfma_f32_16x16x32_bf16 v[20:23], a[228:231], v[132:135], v[20:23]\n"
"s_lshl_b32 s80, s101, 1\n"
"s_add_u32 s80, s80, 2\n"
"ds_read_b128 a[252:255], v1 offset:7168\n"
"v_mfma_f32_16x16x32_bf16 v[24:27], a[224:227], v[120:123], v[24:27]\n"
"s_and_b32 s80, s80, 7\n"
"s_lshl_b32 s80, s80, 14\n"
"s_add_u32 s85, s96, s80\n"
"v_mfma_f32_16x16x32_bf16 v[24:27], a[228:231], v[136:139], v[24:27]\n"
"buffer_load_dwordx4 v1, s[40:43], s82 offen sc1 lds\n"
"v_mfma_f32_16x16x32_bf16 v[28:31], a[224:227], v[124:127], v[28:31]\n"
"s_waitcnt lgkmcnt(4)\n"
"buffer_load_dwordx4 v1, s[40:43], s82 offen offset:1024 sc1 lds\n"
"v_mfma_f32_16x16x32_bf16 v[28:31], a[228:231], v[140:143], v[28:31]\n"
"v_mfma_f32_16x16x32_bf16 v[32:35], a[232:235], v[112:115], v[32:35]\n"
"ds_read_b128 a[224:227], v1 offset:8192\n"
"v_mfma_f32_16x16x32_bf16 v[32:35], a[236:239], v[128:131], v[32:35]\n"
"v_mfma_f32_16x16x32_bf16 v[36:39], a[232:235], v[116:119], v[36:39]\n"
"buffer_load_dwordx4 v1, s[40:43], s82 offen offset:2048 sc1 lds\n"
"v_mfma_f32_16x16x32_bf16 v[36:39], a[236:239], v[132:135], v[36:39]\n"
"ds_read_b128 a[228:231], v1 offset:9216\n"
"v_cvt_pk_bf16_f32 v16, v16, v17\n"
"v_mfma_f32_16x16x32_bf16 v[40:43], a[232:235], v[120:123], v[40:43]\n"
"v_cvt_pk_bf16_f32 v17, v18, v19\n"
"v_cvt_pk_bf16_f32 v18, v20, v21\n"
"v_mfma_f32_16x16x32_bf16 v[40:43], a[236:239], v[136:139], v[40:43]\n"
"v_cvt_pk_bf16_f32 v19, v22, v23\n"
"v_cvt_pk_bf16_f32 v20, v24, v25\n"
"v_mfma_f32_16x16x32_bf16 v[44:47], a[232:235], v[124:127], v[44:47]\n"
"s_waitcnt lgkmcnt(4)\n"
"v_cvt_pk_bf16_f32 v21, v26, v27\n"
"v_cvt_pk_bf16_f32 v22, v28, v29\n"
"v_mfma_f32_16x16x32_bf16 v[44:47], a[236:239], v[140:143], v[44:47]\n"
"v_cvt_pk_bf16_f32 v23, v30, v31\n"
"v_mfma_f32_16x16x32_bf16 v[48:51], a[240:243], a[160:163], v[48:51]\n"
"ds_read_b128 a[232:235], v1 offset:10240\n"
"v_mfma_f32_16x16x32_bf16 v[48:51], a[244:247], a[176:179], v[48:51]\n"
"buffer_store_dwordx4 v[16:19], v1, s[40:43], s85 offen sc1\n"
"v_mfma_f32_16x16x32_bf16 v[52:55], a[240:243], a[164:167], v[52:55]\n"
"buffer_load_dwordx4 v1, s[40:43], s82 offen offset:3072 sc1 lds\n"
"v_mfma_f32_16x16x32_bf16 v[52:55], a[244:247], a[180:183], v[52:55]\n"
"ds_read_b128 a[236:239], v1 offset:11264\n"
"v_mfma_f32_16x16x32_bf16 v[56:59], a[240:243], a[168:171], v[56:59]\n"
"v_mfma_f32_16x16x32_bf16 v[56:59], a[244:247], a[184:187], v[56:59]\n"
"v_mfma_f32_16x16x32_bf16 v[60:63], a[240:243], a[172:175], v[60:63]\n"
"s_waitcnt lgkmcnt(4)\n"
"v_mfma_f32_16x16x32_bf16 v[60:63], a[244:247], a[188:191], v[60:63]\n"
"v_mfma_f32_16x16x32_bf16 v[48:51], a[248:251], a[192:195], v[48:51]\n"
"ds_read_b128 a[240:243], v1 offset:12288\n"
"v_mfma_f32_16x16x32_bf16 v[48:51], a[252:255], a[208:211], v[48:51]\n"
"v_mfma_f32_16x16x32_bf16 v[52:55], a[248:251], a[196:199], v[52:55]\n"
"v_mfma_f32_16x16x32_bf16 v[52:55], a[252:255], a[212:215], v[52:55]\n"
"ds_read_b128 a[244:247], v1 offset:13312\n"
"v_mfma_f32_16x16x32_bf16 v[56:59], a[248:251], a[200:203], v[56:59]\n"
"v_mfma_f32_16x16x32_bf16 v[56:59], a[252:255], a[216:219], v[56:59]\n"
"v_mfma_f32_16x16x32_bf16 v[60:63], a[248:251], a[204:207], v[60:63]\n"
"s_waitcnt lgkmcnt(4)\n"
"buffer_store_dwordx4 v[20:23], v1, s[40:43], s85 offen offset:1024 sc1\n"
"v_mfma_f32_16x16x32_bf16 v[60:63], a[252:255], a[220:223], v[60:63]\n"
"s_lshl_b32 s80, s101, 1\n"
"s_add_u32 s80, s80, 3\n"
"s_and_b32 s80, s80, 7\n"
"v_mfma_f32_16x16x32_bf16 v[48:51], a[224:227], a[0:3], v[48:51]\n"
"s_lshl_b32 s80, s80, 14\n"
"s_add_u32 s85, s96, s80\n"
"ds_read_b128 a[248:251], v1 offset:14336\n"
"v_mfma_f32_16x16x32_bf16 v[48:51], a[228:231], a[16:19], v[48:51]\n"
"v_mfma_f32_16x16x32_bf16 v[52:55], a[224:227], a[4:7], v[52:55]\n"
"v_mfma_f32_16x16x32_bf16 v[52:55], a[228:231], a[20:23], v[52:55]\n"
"ds_read_b128 a[252:255], v1 offset:15360\n"
"v_mfma_f32_16x16x32_bf16 v[56:59], a[224:227], a[8:11], v[56:59]\n"
"v_mfma_f32_16x16x32_bf16 v[56:59], a[228:231], a[24:27], v[56:59]\n"
"v_mfma_f32_16x16x32_bf16 v[60:63], a[224:227], a[12:15], v[60:63]\n"
"s_waitcnt lgkmcnt(4)\n"
"v_mfma_f32_16x16x32_bf16 v[60:63], a[228:231], a[28:31], v[60:63]\n"
"v_cvt_pk_bf16_f32 v32, v32, v33\n"
"v_cvt_pk_bf16_f32 v33, v34, v35\n"
"v_mfma_f32_16x16x32_bf16 v[48:51], a[232:235], a[32:35], v[48:51]\n"
"ds_read_b128 a[224:227], v1 offset:16384\n"
"v_cvt_pk_bf16_f32 v34, v36, v37\n"
"v_mfma_f32_16x16x32_bf16 v[48:51], a[236:239], a[48:51], v[48:51]\n"
"v_cvt_pk_bf16_f32 v35, v38, v39\n"
"v_cvt_pk_bf16_f32 v36, v40, v41\n"
"v_mfma_f32_16x16x32_bf16 v[52:55], a[232:235], a[36:39], v[52:55]\n"
"v_cvt_pk_bf16_f32 v37, v42, v43\n"
"v_cvt_pk_bf16_f32 v38, v44, v45\n"
"v_mfma_f32_16x16x32_bf16 v[52:55], a[236:239], a[52:55], v[52:55]\n"
"ds_read_b128 a[228:231], v1 offset:17408\n"
"v_cvt_pk_bf16_f32 v39, v46, v47\n"
"v_mfma_f32_16x16x32_bf16 v[56:59], a[232:235], a[40:43], v[56:59]\n"
"buffer_store_dwordx4 v[32:35], v1, s[40:43], s85 offen sc1\n"
"v_mfma_f32_16x16x32_bf16 v[56:59], a[236:239], a[56:59], v[56:59]\n"
"v_mfma_f32_16x16x32_bf16 v[60:63], a[232:235], a[44:47], v[60:63]\n"
"s_waitcnt lgkmcnt(4)\n"
"v_mfma_f32_16x16x32_bf16 v[60:63], a[236:239], a[60:63], v[60:63]\n"
"v_mfma_f32_16x16x32_bf16 v[48:51], a[240:243], a[64:67], v[48:51]\n"
"ds_read_b128 a[232:235], v1 offset:18432\n"
"v_mfma_f32_16x16x32_bf16 v[48:51], a[244:247], a[80:83], v[48:51]\n"
"v_mfma_f32_16x16x32_bf16 v[52:55], a[240:243], a[68:71], v[52:55]\n"
"v_mfma_f32_16x16x32_bf16 v[52:55], a[244:247], a[84:87], v[52:55]\n"
"ds_read_b128 a[236:239], v1 offset:19456\n"
"v_mfma_f32_16x16x32_bf16 v[56:59], a[240:243], a[72:75], v[56:59]\n"
"v_mfma_f32_16x16x32_bf16 v[56:59], a[244:247], a[88:91], v[56:59]\n"
"v_mfma_f32_16x16x32_bf16 v[60:63], a[240:243], a[76:79], v[60:63]\n"
"s_waitcnt lgkmcnt(4)\n"
"v_mfma_f32_16x16x32_bf16 v[60:63], a[244:247], a[92:95], v[60:63]\n"
"v_mfma_f32_16x16x32_bf16 v[48:51], a[248:251], a[96:99], v[48:51]\n"
"ds_read_b128 a[240:243], v1 offset:20480\n"
"v_mfma_f32_16x16x32_bf16 v[48:51], a[252:255], a[112:115], v[48:51]\n"
"v_mfma_f32_16x16x32_bf16 v[52:55], a[248:251], a[100:103], v[52:55]\n"
"buffer_store_dwordx4 v[36:39], v1, s[40:43], s85 offen offset:1024 sc1\n"
"v_mfma_f32_16x16x32_bf16 v[52:55], a[252:255], a[116:119], v[52:55]\n"
"ds_read_b128 a[244:247], v1 offset:21504\n"
"v_mfma_f32_16x16x32_bf16 v[56:59], a[248:251], a[104:107], v[56:59]\n"
"v_mfma_f32_16x16x32_bf16 v[56:59], a[252:255], a[120:123], v[56:59]\n"
"v_mfma_f32_16x16x32_bf16 v[60:63], a[248:251], a[108:111], v[60:63]\n"
"s_waitcnt lgkmcnt(4)\n"
"v_mfma_f32_16x16x32_bf16 v[60:63], a[252:255], a[124:127], v[60:63]\n"
"v_mfma_f32_16x16x32_bf16 v[48:51], a[224:227], v[80:83], v[48:51]\n"
"ds_read_b128 a[248:251], v1 offset:22528\n"
"v_mfma_f32_16x16x32_bf16 v[48:51], a[228:231], v[96:99], v[48:51]\n"
"s_add_u32 s82, s64, 638976\n"
"v_mfma_f32_16x16x32_bf16 v[52:55], a[224:227], v[84:87], v[52:55]\n"
"s_add_u32 m0, s66, 81920\n"
"v_mfma_f32_16x16x32_bf16 v[52:55], a[228:231], v[100:103], v[52:55]\n"
"ds_read_b128 a[252:255], v1 offset:23552\n"
"v_mfma_f32_16x16x32_bf16 v[56:59], a[224:227], v[88:91], v[56:59]\n"
"buffer_load_dwordx4 v1, s[40:43], s82 offen sc1 lds\n"
"v_mfma_f32_16x16x32_bf16 v[56:59], a[228:231], v[104:107], v[56:59]\n"
"buffer_load_dwordx4 v1, s[40:43], s82 offen offset:1024 sc1 lds\n"
"v_mfma_f32_16x16x32_bf16 v[60:63], a[224:227], v[92:95], v[60:63]\n"
"s_waitcnt lgkmcnt(4)\n"
"v_mfma_f32_16x16x32_bf16 v[60:63], a[228:231], v[108:111], v[60:63]\n"
"v_mfma_f32_16x16x32_bf16 v[48:51], a[232:235], v[112:115], v[48:51]\n"
"ds_read_b128 a[224:227], v1 offset:24576\n"
"v_mfma_f32_16x16x32_bf16 v[48:51], a[236:239], v[128:131], v[48:51]\n"
"v_mfma_f32_16x16x32_bf16 v[52:55], a[232:235], v[116:119], v[52:55]\n"
"buffer_load_dwordx4 v1, s[40:43], s82 offen offset:2048 sc1 lds\n"
"v_mfma_f32_16x16x32_bf16 v[52:55], a[236:239], v[132:135], v[52:55]\n"
"s_lshl_b32 s80, s101, 1\n"
"s_add_u32 s80, s80, 4\n"
"ds_read_b128 a[228:231], v1 offset:25600\n"
"v_mfma_f32_16x16x32_bf16 v[56:59], a[232:235], v[120:123], v[56:59]\n"
"s_and_b32 s80, s80, 7\n"
"s_lshl_b32 s80, s80, 14\n"
"s_add_u32 s85, s96, s80\n"
"v_mfma_f32_16x16x32_bf16 v[56:59], a[236:239], v[136:139], v[56:59]\n"
"v_mfma_f32_16x16x32_bf16 v[60:63], a[232:235], v[124:127], v[60:63]\n"
"s_waitcnt lgkmcnt(4)\n"
"buffer_load_dwordx4 v1, s[40:43], s82 offen offset:3072 sc1 lds\n"
"v_mfma_f32_16x16x32_bf16 v[60:63], a[236:239], v[140:143], v[60:63]\n"
"s_lshl_b32 s23, s73, 2\n"
"v_mfma_f32_16x16x32_bf16 v[64:67], a[240:243], a[160:163], v[64:67]\n"
"s_add_u32 s23, s23, s71\n"
"ds_read_b128 a[232:235], v1 offset:26624\n"
"v_mfma_f32_16x16x32_bf16 v[64:67], a[244:247], a[176:179], v[64:67]\n"
"s_lshl_b32 s23, s23, 3\n"
"v_mfma_f32_16x16x32_bf16 v[68:71], a[240:243], a[164:167], v[68:71]\n"
"s_add_u32 s23, s23, s100\n"
"v_mfma_f32_16x16x32_bf16 v[68:71], a[244:247], a[180:183], v[68:71]\n"
"s_lshl_b32 s23, s23, 6\n"
"ds_read_b128 a[236:239], v1 offset:27648\n"
"v_cvt_pk_bf16_f32 v48, v48, v49\n"
"v_mfma_f32_16x16x32_bf16 v[72:75], a[240:243], a[168:171], v[72:75]\n"
"s_and_b32 s23, s23, 0xfffffe00\n"
"v_cvt_pk_bf16_f32 v49, v50, v51\n"
"v_cvt_pk_bf16_f32 v50, v52, v53\n"
"v_mfma_f32_16x16x32_bf16 v[72:75], a[244:247], a[184:187], v[72:75]\n"
"s_add_u32 s23, s23, 79298560\n"
"v_cvt_pk_bf16_f32 v51, v54, v55\n"
"v_cvt_pk_bf16_f32 v52, v56, v57\n"
"v_mfma_f32_16x16x32_bf16 v[76:79], a[240:243], a[172:175], v[76:79]\n"
"s_waitcnt lgkmcnt(4)\n"
"v_cvt_pk_bf16_f32 v53, v58, v59\n"
"v_cvt_pk_bf16_f32 v54, v60, v61\n"
"v_mfma_f32_16x16x32_bf16 v[76:79], a[244:247], a[188:191], v[76:79]\n"
"s_mov_b32 s54, 0\n"
"v_cvt_pk_bf16_f32 v55, v62, v63\n"
"v_min_u32 v12, 6, v0\n"
"v_mfma_f32_16x16x32_bf16 v[64:67], a[248:251], a[192:195], v[64:67]\n"
"ds_read_b128 a[240:243], v1 offset:28672\n"
"v_lshlrev_b32 v13, 1, v12\n"
"v_mfma_f32_16x16x32_bf16 v[64:67], a[252:255], a[208:211], v[64:67]\n"
"buffer_store_dwordx4 v[48:51], v1, s[40:43], s85 offen sc1\n"
"v_mfma_f32_16x16x32_bf16 v[68:71], a[248:251], a[196:199], v[68:71]\n"
"v_mov_b32 v10, 39\n"
"v_lshrrev_b32 v12, v12, v10\n"
"v_mfma_f32_16x16x32_bf16 v[68:71], a[252:255], a[212:215], v[68:71]\n"
"ds_read_b128 a[244:247], v1 offset:29696\n"
"v_and_b32 v12, 1, v12\n"
"v_mfma_f32_16x16x32_bf16 v[72:75], a[248:251], a[200:203], v[72:75]\n"
"v_mov_b32 v10, 4827\n"
"v_lshrrev_b32 v15, v13, v10\n"
"v_mfma_f32_16x16x32_bf16 v[72:75], a[252:255], a[216:219], v[72:75]\n"
"v_add_u32 v15, s101, v15\n"
"v_and_b32 v15, 3, v15\n"
"v_mfma_f32_16x16x32_bf16 v[76:79], a[248:251], a[204:207], v[76:79]\n"
"s_waitcnt lgkmcnt(4)\n"
"v_lshl_add_u32 v15, v12, 2, v15\n"
"v_lshlrev_b32 v15, 6, v15\n"
"v_mfma_f32_16x16x32_bf16 v[76:79], a[252:255], a[220:223], v[76:79]\n"
"v_mov_b32 v10, 6160\n"
"v_lshrrev_b32 v13, v13, v10\n"
"v_mfma_f32_16x16x32_bf16 v[64:67], a[224:227], a[0:3], v[64:67]\n"
"ds_read_b128 a[248:251], v1 offset:30720\n"
"v_and_b32 v13, 3, v13\n"
"v_mfma_f32_16x16x32_bf16 v[64:67], a[228:231], a[16:19], v[64:67]\n"
"v_lshl_add_u32 v15, v13, 16, v15\n"
"v_mfma_f32_16x16x32_bf16 v[68:71], a[224:227], a[4:7], v[68:71]\n"
"v_mfma_f32_16x16x32_bf16 v[68:71], a[228:231], a[20:23], v[68:71]\n"
"ds_read_b128 a[252:255], v1 offset:31744\n"
"v_mfma_f32_16x16x32_bf16 v[72:75], a[224:227], a[8:11], v[72:75]\n"
"v_mfma_f32_16x16x32_bf16 v[72:75], a[228:231], a[24:27], v[72:75]\n"
"v_mfma_f32_16x16x32_bf16 v[76:79], a[224:227], a[12:15], v[76:79]\n"
"s_waitcnt lgkmcnt(4)\n"
"buffer_store_dwordx4 v[52:55], v1, s[40:43], s85 offen offset:1024 sc1\n"
"v_mfma_f32_16x16x32_bf16 v[76:79], a[228:231], a[28:31], v[76:79]\n"
"s_waitcnt vmcnt(14)\n"
"s_barrier\n"
"v_mfma_f32_16x16x32_bf16 v[64:67], a[232:235], a[32:35], v[64:67]\n"
"ds_read_b128 a[224:227], v1 offset:32768\n"
"v_mfma_f32_16x16x32_bf16 v[64:67], a[236:239], a[48:51], v[64:67]\n"
"v_lshrrev_b32 v4, 2, v0\n"
"v_mul_u32_u24 v4, 1152, v4\n"
"v_mfma_f32_16x16x32_bf16 v[68:71], a[232:235], a[36:39], v[68:71]\n"
"v_and_b32 v7, 3, v0\n"
"v_lshl_add_u32 v4, v7, 4, v4\n"
"v_mfma_f32_16x16x32_bf16 v[68:71], a[236:239], a[52:55], v[68:71]\n"
"ds_read_b128 a[228:231], v1 offset:33792\n"
"v_and_b32 v7, 15, v0\n"
"v_mfma_f32_16x16x32_bf16 v[72:75], a[232:235], a[40:43], v[72:75]\n"
"v_lshlrev_b32 v7, 6, v7\n"
"v_lshl_add_u32 v7, v6, 3, v7\n"
"v_mfma_f32_16x16x32_bf16 v[72:75], a[236:239], a[56:59], v[72:75]\n"
"s_add_u32 s55, s66, 147456\n"
"v_add_u32 v7, s55, v7\n"
"v_mfma_f32_16x16x32_bf16 v[76:79], a[232:235], a[44:47], v[76:79]\n"
"s_waitcnt lgkmcnt(4)\n"
"s_add_u32 s82, s53, 0\n"
"s_add_u32 m0, s55, 0\n"
"v_mfma_f32_16x16x32_bf16 v[76:79], a[236:239], a[60:63], v[76:79]\n"
"buffer_load_dwordx4 v4, s[44:47], s82 offen lds\n"
"buffer_load_dwordx2 v[10:11], v15, s[40:43], s23 offen sc1\n"
"v_mfma_f32_16x16x32_bf16 v[64:67], a[240:243], a[64:67], v[64:67]\n"
"ds_read_b128 a[232:235], v1 offset:34816\n"
"v_mfma_f32_16x16x32_bf16 v[64:67], a[244:247], a[80:83], v[64:67]\n"
"v_mfma_f32_16x16x32_bf16 v[68:71], a[240:243], a[68:71], v[68:71]\n"
"v_mfma_f32_16x16x32_bf16 v[68:71], a[244:247], a[84:87], v[68:71]\n"
"ds_read_b128 a[236:239], v1 offset:35840\n"
"v_mfma_f32_16x16x32_bf16 v[72:75], a[240:243], a[72:75], v[72:75]\n"
"v_mfma_f32_16x16x32_bf16 v[72:75], a[244:247], a[88:91], v[72:75]\n"
"v_mfma_f32_16x16x32_bf16 v[76:79], a[240:243], a[76:79], v[76:79]\n"
"s_waitcnt lgkmcnt(4)\n"
"v_mfma_f32_16x16x32_bf16 v[76:79], a[244:247], a[92:95], v[76:79]\n"
"v_mfma_f32_16x16x32_bf16 v[64:67], a[248:251], a[96:99], v[64:67]\n"
"s_add_u32 s82, s53, 18432\n"
"ds_read_b128 a[240:243], v1 offset:36864\n"
"v_mfma_f32_16x16x32_bf16 v[64:67], a[252:255], a[112:115], v[64:67]\n"
"s_add_u32 m0, s55, 1024\n"
"v_mfma_f32_16x16x32_bf16 v[68:71], a[248:251], a[100:103], v[68:71]\n"
"buffer_load_dwordx4 v4, s[44:47], s82 offen lds\n"
"v_mfma_f32_16x16x32_bf16 v[68:71], a[252:255], a[116:119], v[68:71]\n"
"ds_read_b128 a[244:247], v1 offset:37888\n"
"v_mfma_f32_16x16x32_bf16 v[72:75], a[248:251], a[104:107], v[72:75]\n"
"v_mfma_f32_16x16x32_bf16 v[72:75], a[252:255], a[120:123], v[72:75]\n"
"v_mfma_f32_16x16x32_bf16 v[76:79], a[248:251], a[108:111], v[76:79]\n"
"s_waitcnt lgkmcnt(4)\n"
"v_mfma_f32_16x16x32_bf16 v[76:79], a[252:255], a[124:127], v[76:79]\n"
"v_mfma_f32_16x16x32_bf16 v[64:67], a[224:227], v[80:83], v[64:67]\n"
"ds_read_b128 a[248:251], v1 offset:38912\n"
"v_mfma_f32_16x16x32_bf16 v[64:67], a[228:231], v[96:99], v[64:67]\n"
"v_mfma_f32_16x16x32_bf16 v[68:71], a[224:227], v[84:87], v[68:71]\n"
"v_mfma_f32_16x16x32_bf16 v[68:71], a[228:231], v[100:103], v[68:71]\n"
"ds_read_b128 a[252:255], v1 offset:39936\n"
"v_mfma_f32_16x16x32_bf16 v[72:75], a[224:227], v[88:91], v[72:75]\n"
"v_mfma_f32_16x16x32_bf16 v[72:75], a[228:231], v[104:107], v[72:75]\n"
"v_mfma_f32_16x16x32_bf16 v[76:79], a[224:227], v[92:95], v[76:79]\n"
"s_waitcnt lgkmcnt(4)\n"
"v_mfma_f32_16x16x32_bf16 v[76:79], a[228:231], v[108:111], v[76:79]\n"
"s_add_u32 s82, s53, 36864\n"
"v_mfma_f32_16x16x32_bf16 v[64:67], a[232:235], v[112:115], v[64:67]\n"
"s_add_u32 m0, s55, 2048\n"
"ds_read_b128 a[224:227], v1 offset:40960\n"
"v_mfma_f32_16x16x32_bf16 v[64:67], a[236:239], v[128:131], v[64:67]\n"
"buffer_load_dwordx4 v4, s[44:47], s82 offen lds\n"
"v_mfma_f32_16x16x32_bf16 v[68:71], a[232:235], v[116:119], v[68:71]\n"
"v_mfma_f32_16x16x32_bf16 v[68:71], a[236:239], v[132:135], v[68:71]\n"
"s_lshl_b32 s80, s101, 1\n"
"s_add_u32 s80, s80, 5\n"
"ds_read_b128 a[228:231], v1 offset:41984\n"
"v_mfma_f32_16x16x32_bf16 v[72:75], a[232:235], v[120:123], v[72:75]\n"
"s_and_b32 s80, s80, 7\n"
"s_lshl_b32 s80, s80, 14\n"
"s_add_u32 s85, s96, s80\n"
"v_mfma_f32_16x16x32_bf16 v[72:75], a[236:239], v[136:139], v[72:75]\n"
"v_mfma_f32_16x16x32_bf16 v[76:79], a[232:235], v[124:127], v[76:79]\n"
"s_waitcnt lgkmcnt(4)\n"
"v_mfma_f32_16x16x32_bf16 v[76:79], a[236:239], v[140:143], v[76:79]\n"
"v_mfma_f32_16x16x32_bf16 v[144:147], a[240:243], a[160:163], v[144:147]\n"
"ds_read_b128 a[232:235], v1 offset:43008\n"
"v_mfma_f32_16x16x32_bf16 v[144:147], a[244:247], a[176:179], v[144:147]\n"
"v_mfma_f32_16x16x32_bf16 v[148:151], a[240:243], a[164:167], v[148:151]\n"
"v_mfma_f32_16x16x32_bf16 v[148:151], a[244:247], a[180:183], v[148:151]\n"
"ds_read_b128 a[236:239], v1 offset:44032\n"
"v_cvt_pk_bf16_f32 v64, v64, v65\n"
"v_mfma_f32_16x16x32_bf16 v[152:155], a[240:243], a[168:171], v[152:155]\n"
"v_cvt_pk_bf16_f32 v65, v66, v67\n"
"v_cvt_pk_bf16_f32 v66, v68, v69\n"
"v_mfma_f32_16x16x32_bf16 v[152:155], a[244:247], a[184:187], v[152:155]\n"
"v_cvt_pk_bf16_f32 v67, v70, v71\n"
"v_cvt_pk_bf16_f32 v68, v72, v73\n"
"v_mfma_f32_16x16x32_bf16 v[156:159], a[240:243], a[172:175], v[156:159]\n"
"s_waitcnt lgkmcnt(4)\n"
"v_cvt_pk_bf16_f32 v69, v74, v75\n"
"v_cvt_pk_bf16_f32 v70, v76, v77\n"
"v_mfma_f32_16x16x32_bf16 v[156:159], a[244:247], a[188:191], v[156:159]\n"
"s_add_u32 s82, s53, 55296\n"
"v_cvt_pk_bf16_f32 v71, v78, v79\n"
"v_mfma_f32_16x16x32_bf16 v[144:147], a[248:251], a[192:195], v[144:147]\n"
"s_add_u32 m0, s55, 3072\n"
"ds_read_b128 a[240:243], v1 offset:45056\n"
"v_mfma_f32_16x16x32_bf16 v[144:147], a[252:255], a[208:211], v[144:147]\n"
"buffer_store_dwordx4 v[64:67], v1, s[40:43], s85 offen sc1\n"
"v_mfma_f32_16x16x32_bf16 v[148:151], a[248:251], a[196:199], v[148:151]\n"
"buffer_load_dwordx4 v4, s[44:47], s82 offen lds\n"
"v_mfma_f32_16x16x32_bf16 v[148:151], a[252:255], a[212:215], v[148:151]\n"
"ds_read_b128 a[244:247], v1 offset:46080\n"
"v_mfma_f32_16x16x32_bf16 v[152:155], a[248:251], a[200:203], v[152:155]\n"
"buffer_store_dwordx4 v[68:71], v1, s[40:43], s85 offen offset:1024 sc1\n"
"v_mfma_f32_16x16x32_bf16 v[152:155], a[252:255], a[216:219], v[152:155]\n"
"v_mfma_f32_16x16x32_bf16 v[156:159], a[248:251], a[204:207], v[156:159]\n"
"s_waitcnt lgkmcnt(4)\n"
"v_mfma_f32_16x16x32_bf16 v[156:159], a[252:255], a[220:223], v[156:159]\n"
"v_mfma_f32_16x16x32_bf16 v[144:147], a[224:227], a[0:3], v[144:147]\n"
"ds_read_b128 a[248:251], v1 offset:47104\n"
"v_mfma_f32_16x16x32_bf16 v[144:147], a[228:231], a[16:19], v[144:147]\n"
"v_mfma_f32_16x16x32_bf16 v[148:151], a[224:227], a[4:7], v[148:151]\n"
"v_mfma_f32_16x16x32_bf16 v[148:151], a[228:231], a[20:23], v[148:151]\n"
"ds_read_b128 a[252:255], v1 offset:48128\n"
"v_mfma_f32_16x16x32_bf16 v[152:155], a[224:227], a[8:11], v[152:155]\n"
"v_mfma_f32_16x16x32_bf16 v[152:155], a[228:231], a[24:27], v[152:155]\n"
"s_add_u32 s82, s64, 655360\n"
"v_mfma_f32_16x16x32_bf16 v[156:159], a[224:227], a[12:15], v[156:159]\n"
"s_waitcnt lgkmcnt(4)\n"
"v_mfma_f32_16x16x32_bf16 v[156:159], a[228:231], a[28:31], v[156:159]\n"
"s_add_u32 m0, s66, 98304\n"
"v_mfma_f32_16x16x32_bf16 v[144:147], a[232:235], a[32:35], v[144:147]\n"
"ds_read_b128 a[224:227], v1 offset:49152\n"
"v_mfma_f32_16x16x32_bf16 v[144:147], a[236:239], a[48:51], v[144:147]\n"
"buffer_load_dwordx4 v1, s[40:43], s82 offen sc1 lds\n"
"v_mfma_f32_16x16x32_bf16 v[148:151], a[232:235], a[36:39], v[148:151]\n"
"buffer_load_dwordx4 v1, s[40:43], s82 offen offset:1024 sc1 lds\n"
"v_mfma_f32_16x16x32_bf16 v[148:151], a[236:239], a[52:55], v[148:151]\n"
"ds_read_b128 a[228:231], v1 offset:50176\n"
"v_mfma_f32_16x16x32_bf16 v[152:155], a[232:235], a[40:43], v[152:155]\n"
"v_mfma_f32_16x16x32_bf16 v[152:155], a[236:239], a[56:59], v[152:155]\n"
"v_mfma_f32_16x16x32_bf16 v[156:159], a[232:235], a[44:47], v[156:159]\n"
"s_waitcnt lgkmcnt(4)\n"
"buffer_load_dwordx4 v1, s[40:43], s82 offen offset:2048 sc1 lds\n"
"v_mfma_f32_16x16x32_bf16 v[156:159], a[236:239], a[60:63], v[156:159]\n"
"v_mfma_f32_16x16x32_bf16 v[144:147], a[240:243], a[64:67], v[144:147]\n"
"ds_read_b128 a[232:235], v1 offset:51200\n"
"v_mfma_f32_16x16x32_bf16 v[144:147], a[244:247], a[80:83], v[144:147]\n"
"v_mfma_f32_16x16x32_bf16 v[148:151], a[240:243], a[68:71], v[148:151]\n"
"buffer_load_dwordx4 v1, s[40:43], s82 offen offset:3072 sc1 lds\n"
"v_mfma_f32_16x16x32_bf16 v[148:151], a[244:247], a[84:87], v[148:151]\n"
"ds_read_b128 a[236:239], v1 offset:52224\n"
"v_mfma_f32_16x16x32_bf16 v[152:155], a[240:243], a[72:75], v[152:155]\n"
"v_mfma_f32_16x16x32_bf16 v[152:155], a[244:247], a[88:91], v[152:155]\n"
"v_mfma_f32_16x16x32_bf16 v[156:159], a[240:243], a[76:79], v[156:159]\n"
"s_waitcnt lgkmcnt(4)\n"
"v_mfma_f32_16x16x32_bf16 v[156:159], a[244:247], a[92:95], v[156:159]\n"
"v_mfma_f32_16x16x32_bf16 v[144:147], a[248:251], a[96:99], v[144:147]\n"
"ds_read_b128 a[240:243], v1 offset:53248\n"
"v_mfma_f32_16x16x32_bf16 v[144:147], a[252:255], a[112:115], v[144:147]\n"
"v_mfma_f32_16x16x32_bf16 v[148:151], a[248:251], a[100:103], v[148:151]\n"
"v_mfma_f32_16x16x32_bf16 v[148:151], a[252:255], a[116:119], v[148:151]\n"
"ds_read_b128 a[244:247], v1 offset:54272\n"
"v_mfma_f32_16x16x32_bf16 v[152:155], a[248:251], a[104:107], v[152:155]\n"
"v_mfma_f32_16x16x32_bf16 v[152:155], a[252:255], a[120:123], v[152:155]\n"
"v_mfma_f32_16x16x32_bf16 v[156:159], a[248:251], a[108:111], v[156:159]\n"
"s_waitcnt vmcnt(9) lgkmcnt(4)\n"
"v_cmp_ne_u64 vcc, s[92:93], v[10:11]\n"
"v_mfma_f32_16x16x32_bf16 v[156:159], a[252:255], a[124:127], v[156:159]\n"
"s_mov_b32 s56, vcc_lo\n"
"s_and_b32 s57, s56, 3\n"
"s_or_b32 s54, s54, s57\n"
"v_mfma_f32_16x16x32_bf16 v[144:147], a[224:227], v[80:83], v[144:147]\n"
"ds_read_b128 a[248:251], v1 offset:55296\n"
"v_mfma_f32_16x16x32_bf16 v[144:147], a[228:231], v[96:99], v[144:147]\n"
"s_add_u32 s82, s64, 671744\n"
"v_mfma_f32_16x16x32_bf16 v[148:151], a[224:227], v[84:87], v[148:151]\n"
"s_add_u32 m0, s66, 114688\n"
"v_mfma_f32_16x16x32_bf16 v[148:151], a[228:231], v[100:103], v[148:151]\n"
"s_add_u32 s79, s101, 3\n"
"ds_read_b128 a[252:255], v1 offset:56320\n"
"v_mfma_f32_16x16x32_bf16 v[152:155], a[224:227], v[88:91], v[152:155]\n"
"s_and_b32 s79, s79, 3\n"
"buffer_load_dwordx4 v1, s[40:43], s82 offen sc1 lds\n"
"v_mfma_f32_16x16x32_bf16 v[152:155], a[228:231], v[104:107], v[152:155]\n"
"s_lshl_b32 s79, s79, 11\n"
"buffer_load_dwordx4 v1, s[40:43], s82 offen offset:1024 sc1 lds\n"
"v_mfma_f32_16x16x32_bf16 v[156:159], a[224:227], v[92:95], v[156:159]\n"
"s_waitcnt lgkmcnt(4)\n"
"s_add_u32 s79, s79, s52\n"
"s_add_u32 s79, s79, 8192\n"
"v_mfma_f32_16x16x32_bf16 v[156:159], a[228:231], v[108:111], v[156:159]\n"
"s_bitcmp0_b32 s56, 0\n"
"s_cselect_b32 s79, s79, 0x70000000\n"
"buffer_load_dwordx4 v[16:19], v1, s[40:43], s79 offen sc1\n"
"v_mfma_f32_16x16x32_bf16 v[144:147], a[232:235], v[112:115], v[144:147]\n"
"ds_read_b128 a[224:227], v1 offset:57344\n"
"v_mfma_f32_16x16x32_bf16 v[144:147], a[236:239], v[128:131], v[144:147]\n"
"v_mfma_f32_16x16x32_bf16 v[148:151], a[232:235], v[116:119], v[148:151]\n"
"s_lshl_b32 s78, s73, 2\n"
"buffer_load_dwordx4 v1, s[40:43], s82 offen offset:2048 sc1 lds\n"
"v_mfma_f32_16x16x32_bf16 v[148:151], a[236:239], v[132:135], v[148:151]\n"
"s_lshl_b32 s80, s101, 1\n"
"s_add_u32 s80, s80, 6\n"
"ds_read_b128 a[228:231], v1 offset:58368\n"
"v_mfma_f32_16x16x32_bf16 v[152:155], a[232:235], v[120:123], v[152:155]\n"
"s_and_b32 s80, s80, 7\n"
"s_lshl_b32 s80, s80, 14\n"
"s_add_u32 s85, s96, s80\n"
"v_mfma_f32_16x16x32_bf16 v[152:155], a[236:239], v[136:139], v[152:155]\n"
"s_add_u32 s78, s78, s71\n"
"buffer_load_dwordx4 v[20:23], v1, s[40:43], s79 offen offset:1024 sc1\n"
"v_mfma_f32_16x16x32_bf16 v[156:159], a[232:235], v[124:127], v[156:159]\n"
"s_waitcnt lgkmcnt(4)\n"
"s_lshl_b32 s78, s78, 3\n"
"s_add_u32 s78, s78, s100\n"
"v_mfma_f32_16x16x32_bf16 v[156:159], a[236:239], v[140:143], v[156:159]\n"
"s_lshl_b32 s78, s78, 6\n"
"s_add_u32 s78, s78, 79298560\n"
"v_mov_b32 v8, s92\n"
"v_mfma_f32_16x16x32_bf16 v[160:163], a[240:243], a[160:163], v[160:163]\n"
"ds_read_b128 a[232:235], v1 offset:59392\n"
"v_mov_b32 v9, s93\n"
"v_mfma_f32_16x16x32_bf16 v[160:163], a[244:247], a[176:179], v[160:163]\n"
"s_waitcnt vmcnt(9)\n"
"buffer_store_dwordx2 v[8:9], v14, s[40:43], s78 offen sc1\n"
"v_mfma_f32_16x16x32_bf16 v[164:167], a[240:243], a[164:167], v[164:167]\n"
"buffer_load_dwordx4 v1, s[40:43], s82 offen offset:3072 sc1 lds\n"
"v_mfma_f32_16x16x32_bf16 v[164:167], a[244:247], a[180:183], v[164:167]\n"
"s_add_u32 s79, s79, 16384\n"
"ds_read_b128 a[236:239], v1 offset:60416\n"
"v_cvt_pk_bf16_f32 v144, v144, v145\n"
"v_mfma_f32_16x16x32_bf16 v[168:171], a[240:243], a[168:171], v[168:171]\n"
"v_cvt_pk_bf16_f32 v145, v146, v147\n"
"v_cvt_pk_bf16_f32 v146, v148, v149\n"
"v_mfma_f32_16x16x32_bf16 v[168:171], a[244:247], a[184:187], v[168:171]\n"
"v_cvt_pk_bf16_f32 v147, v150, v151\n"
"v_cvt_pk_bf16_f32 v148, v152, v153\n"
"v_mfma_f32_16x16x32_bf16 v[172:175], a[240:243], a[172:175], v[172:175]\n"
"s_waitcnt lgkmcnt(4)\n"
"v_cvt_pk_bf16_f32 v149, v154, v155\n"
"v_cvt_pk_bf16_f32 v150, v156, v157\n"
"v_mfma_f32_16x16x32_bf16 v[172:175], a[244:247], a[188:191], v[172:175]\n"
"v_cvt_pk_bf16_f32 v151, v158, v159\n"
"v_mfma_f32_16x16x32_bf16 v[160:163], a[248:251], a[192:195], v[160:163]\n"
"buffer_load_dwordx4 v[24:27], v1, s[40:43], s79 offen sc1\n"
"v_mfma_f32_16x16x32_bf16 v[160:163], a[252:255], a[208:211], v[160:163]\n"
"ds_read_b128 a[240:243], v1 offset:61440\n"
"v_mfma_f32_16x16x32_bf16 v[164:167], a[248:251], a[196:199], v[164:167]\n"
"buffer_store_dwordx4 v[144:147], v1, s[40:43], s85 offen sc1\n"
"v_mfma_f32_16x16x32_bf16 v[164:167], a[252:255], a[212:215], v[164:167]\n"
"ds_read_b128 a[244:247], v1 offset:62464\n"
"v_mfma_f32_16x16x32_bf16 v[168:171], a[248:251], a[200:203], v[168:171]\n"
"buffer_store_dwordx4 v[148:151], v1, s[40:43], s85 offen offset:1024 sc1\n"
"v_mfma_f32_16x16x32_bf16 v[168:171], a[252:255], a[216:219], v[168:171]\n"
"buffer_load_dwordx4 v[28:31], v1, s[40:43], s79 offen offset:1024 sc1\n"
"v_mfma_f32_16x16x32_bf16 v[172:175], a[248:251], a[204:207], v[172:175]\n"
"s_waitcnt lgkmcnt(4)\n"
"v_mfma_f32_16x16x32_bf16 v[172:175], a[252:255], a[220:223], v[172:175]\n"
"v_mfma_f32_16x16x32_bf16 v[160:163], a[224:227], a[0:3], v[160:163]\n"
"ds_read_b128 a[248:251], v1 offset:63488\n"
"v_mfma_f32_16x16x32_bf16 v[160:163], a[228:231], a[16:19], v[160:163]\n"
"v_mfma_f32_16x16x32_bf16 v[164:167], a[224:227], a[4:7], v[164:167]\n"
"v_mfma_f32_16x16x32_bf16 v[164:167], a[228:231], a[20:23], v[164:167]\n"
"ds_read_b128 a[252:255], v1 offset:64512\n"
"v_mfma_f32_16x16x32_bf16 v[168:171], a[224:227], a[8:11], v[168:171]\n"
"s_add_u32 s79, s101, 2\n"
"v_mfma_f32_16x16x32_bf16 v[168:171], a[228:231], a[24:27], v[168:171]\n"
"s_and_b32 s79, s79, 3\n"
"v_mfma_f32_16x16x32_bf16 v[172:175], a[224:227], a[12:15], v[172:175]\n"
"s_waitcnt lgkmcnt(4)\n"
"v_mfma_f32_16x16x32_bf16 v[172:175], a[228:231], a[28:31], v[172:175]\n"
"s_barrier\n"
"v_mfma_f32_16x16x32_bf16 v[160:163], a[232:235], a[32:35], v[160:163]\n"
"s_lshl_b32 s79, s79, 11\n"
"s_add_u32 s79, s79, s52\n"
"ds_read_b128 a[224:227], v2\n"
"v_mfma_f32_16x16x32_bf16 v[160:163], a[236:239], a[48:51], v[160:163]\n"
"s_add_u32 s79, s79, 8192\n"
"s_bitcmp0_b32 s56, 1\n"
"s_cselect_b32 s79, s79, 0x70000000\n"
"v_mfma_f32_16x16x32_bf16 v[164:167], a[232:235], a[36:39], v[164:167]\n"
"buffer_load_dwordx4 v[32:35], v1, s[40:43], s79 offen sc1\n"
"v_mfma_f32_16x16x32_bf16 v[164:167], a[236:239], a[52:55], v[164:167]\n"
"ds_read_b128 a[228:231], v2 offset:1024\n"
"v_mfma_f32_16x16x32_bf16 v[168:171], a[232:235], a[40:43], v[168:171]\n"
"buffer_load_dwordx4 v[36:39], v1, s[40:43], s79 offen offset:1024 sc1\n"
"v_mfma_f32_16x16x32_bf16 v[168:171], a[236:239], a[56:59], v[168:171]\n"
"v_mfma_f32_16x16x32_bf16 v[172:175], a[232:235], a[44:47], v[172:175]\n"
"s_waitcnt lgkmcnt(4)\n"
"v_mfma_f32_16x16x32_bf16 v[172:175], a[236:239], a[60:63], v[172:175]\n"
"v_mfma_f32_16x16x32_bf16 v[160:163], a[240:243], a[64:67], v[160:163]\n"
"ds_read_b128 a[232:235], v2 offset:2048\n"
"v_mfma_f32_16x16x32_bf16 v[160:163], a[244:247], a[80:83], v[160:163]\n"
"v_mfma_f32_16x16x32_bf16 v[164:167], a[240:243], a[68:71], v[164:167]\n"
"v_mfma_f32_16x16x32_bf16 v[164:167], a[244:247], a[84:87], v[164:167]\n"
"ds_read_b128 a[236:239], v2 offset:3072\n"
"v_mfma_f32_16x16x32_bf16 v[168:171], a[240:243], a[72:75], v[168:171]\n"
"v_mfma_f32_16x16x32_bf16 v[168:171], a[244:247], a[88:91], v[168:171]\n"
"s_add_u32 s79, s79, 16384\n"
"buffer_load_dwordx4 v[40:43], v1, s[40:43], s79 offen sc1\n"
"v_mfma_f32_16x16x32_bf16 v[172:175], a[240:243], a[76:79], v[172:175]\n"
"s_waitcnt lgkmcnt(4)\n"
"v_mfma_f32_16x16x32_bf16 v[172:175], a[244:247], a[92:95], v[172:175]\n"
"v_mfma_f32_16x16x32_bf16 v[160:163], a[248:251], a[96:99], v[160:163]\n"
"ds_read_b128 a[240:243], v2 offset:4096\n"
"v_mfma_f32_16x16x32_bf16 v[160:163], a[252:255], a[112:115], v[160:163]\n"
"v_mfma_f32_16x16x32_bf16 v[164:167], a[248:251], a[100:103], v[164:167]\n"
"v_mfma_f32_16x16x32_bf16 v[164:167], a[252:255], a[116:119], v[164:167]\n"
"ds_read_b128 a[244:247], v2 offset:5120\n"
"v_mfma_f32_16x16x32_bf16 v[168:171], a[248:251], a[104:107], v[168:171]\n"
"v_mfma_f32_16x16x32_bf16 v[168:171], a[252:255], a[120:123], v[168:171]\n"
"buffer_load_dwordx4 v[44:47], v1, s[40:43], s79 offen offset:1024 sc1\n"
"v_mfma_f32_16x16x32_bf16 v[172:175], a[248:251], a[108:111], v[172:175]\n"
"s_waitcnt lgkmcnt(4)\n"
"v_mfma_f32_16x16x32_bf16 v[172:175], a[252:255], a[124:127], v[172:175]\n"
"v_mfma_f32_16x16x32_bf16 v[160:163], a[224:227], v[80:83], v[160:163]\n"
"ds_read_b128 a[248:251], v2 offset:6144\n"
"v_mfma_f32_16x16x32_bf16 v[160:163], a[228:231], v[96:99], v[160:163]\n"
"v_mfma_f32_16x16x32_bf16 v[164:167], a[224:227], v[84:87], v[164:167]\n"
"v_mfma_f32_16x16x32_bf16 v[164:167], a[228:231], v[100:103], v[164:167]\n"
"ds_read_b128 a[252:255], v2 offset:7168\n"
"v_mfma_f32_16x16x32_bf16 v[168:171], a[224:227], v[88:91], v[168:171]\n"
"v_mfma_f32_16x16x32_bf16 v[168:171], a[228:231], v[104:107], v[168:171]\n"
"v_mfma_f32_16x16x32_bf16 v[172:175], a[224:227], v[92:95], v[172:175]\n"
"s_waitcnt lgkmcnt(4)\n"
"v_mfma_f32_16x16x32_bf16 v[172:175], a[228:231], v[108:111], v[172:175]\n"
"v_mfma_f32_16x16x32_bf16 v[160:163], a[232:235], v[112:115], v[160:163]\n"
"ds_read_b128 a[224:227], v2 offset:8192\n"
"v_mfma_f32_16x16x32_bf16 v[160:163], a[236:239], v[128:131], v[160:163]\n"
"buffer_load_dwordx2 v[10:11], v15, s[40:43], s23 offen sc1\n"
"v_mfma_f32_16x16x32_bf16 v[164:167], a[232:235], v[116:119], v[164:167]\n"
"v_mfma_f32_16x16x32_bf16 v[164:167], a[236:239], v[132:135], v[164:167]\n"
"s_lshl_b32 s80, s101, 1\n"
"s_add_u32 s80, s80, 7\n"
"ds_read_b128 a[228:231], v2 offset:9216\n"
"v_mfma_f32_16x16x32_bf16 v[168:171], a[232:235], v[120:123], v[168:171]\n"
"s_and_b32 s80, s80, 7\n"
"s_lshl_b32 s80, s80, 14\n"
"s_add_u32 s85, s96, s80\n"
"v_mfma_f32_16x16x32_bf16 v[168:171], a[236:239], v[136:139], v[168:171]\n"
"v_mfma_f32_16x16x32_bf16 v[172:175], a[232:235], v[124:127], v[172:175]\n"
"s_waitcnt lgkmcnt(4)\n"
"v_mfma_f32_16x16x32_bf16 v[172:175], a[236:239], v[140:143], v[172:175]\n"
"v_mfma_f32_16x16x32_bf16 v[176:179], a[240:243], a[160:163], v[176:179]\n"
"ds_read_b128 a[232:235], v2 offset:10240\n"
"v_mfma_f32_16x16x32_bf16 v[176:179], a[244:247], a[176:179], v[176:179]\n"
"v_mfma_f32_16x16x32_bf16 v[180:183], a[240:243], a[164:167], v[180:183]\n"
"v_mfma_f32_16x16x32_bf16 v[180:183], a[244:247], a[180:183], v[180:183]\n"
"ds_read_b128 a[236:239], v2 offset:11264\n"
"v_cvt_pk_bf16_f32 v160, v160, v161\n"
"v_mfma_f32_16x16x32_bf16 v[184:187], a[240:243], a[168:171], v[184:187]\n"
"v_cvt_pk_bf16_f32 v161, v162, v163\n"
"v_cvt_pk_bf16_f32 v162, v164, v165\n"
"v_mfma_f32_16x16x32_bf16 v[184:187], a[244:247], a[184:187], v[184:187]\n"
"v_cvt_pk_bf16_f32 v163, v166, v167\n"
"v_cvt_pk_bf16_f32 v164, v168, v169\n"
"v_mfma_f32_16x16x32_bf16 v[188:191], a[240:243], a[172:175], v[188:191]\n"
"s_waitcnt lgkmcnt(4)\n"
"v_cvt_pk_bf16_f32 v165, v170, v171\n"
"v_cvt_pk_bf16_f32 v166, v172, v173\n"
"v_mfma_f32_16x16x32_bf16 v[188:191], a[244:247], a[188:191], v[188:191]\n"
"v_cvt_pk_bf16_f32 v167, v174, v175\n"
"v_mfma_f32_16x16x32_bf16 v[176:179], a[248:251], a[192:195], v[176:179]\n"
"ds_read_b128 a[240:243], v2 offset:12288\n"
"v_mfma_f32_16x16x32_bf16 v[176:179], a[252:255], a[208:211], v[176:179]\n"
"buffer_store_dwordx4 v[160:163], v1, s[40:43], s85 offen sc1\n"
"v_mfma_f32_16x16x32_bf16 v[180:183], a[248:251], a[196:199], v[180:183]\n"
"v_mfma_f32_16x16x32_bf16 v[180:183], a[252:255], a[212:215], v[180:183]\n"
"ds_read_b128 a[244:247], v2 offset:13312\n"
"v_mfma_f32_16x16x32_bf16 v[184:187], a[248:251], a[200:203], v[184:187]\n"
"buffer_store_dwordx4 v[164:167], v1, s[40:43], s85 offen offset:1024 sc1\n"
"v_mfma_f32_16x16x32_bf16 v[184:187], a[252:255], a[216:219], v[184:187]\n"
"v_mfma_f32_16x16x32_bf16 v[188:191], a[248:251], a[204:207], v[188:191]\n"
"s_waitcnt lgkmcnt(4)\n"
"v_mfma_f32_16x16x32_bf16 v[188:191], a[252:255], a[220:223], v[188:191]\n"
"v_mfma_f32_16x16x32_bf16 v[176:179], a[224:227], a[0:3], v[176:179]\n"
"ds_read_b128 a[248:251], v2 offset:14336\n"
"v_mfma_f32_16x16x32_bf16 v[176:179], a[228:231], a[16:19], v[176:179]\n"
"v_mfma_f32_16x16x32_bf16 v[180:183], a[224:227], a[4:7], v[180:183]\n"
"v_mfma_f32_16x16x32_bf16 v[180:183], a[228:231], a[20:23], v[180:183]\n"
"ds_read_b128 a[252:255], v2 offset:15360\n"
"v_mfma_f32_16x16x32_bf16 v[184:187], a[224:227], a[8:11], v[184:187]\n"
"v_mfma_f32_16x16x32_bf16 v[184:187], a[228:231], a[24:27], v[184:187]\n"
"v_mfma_f32_16x16x32_bf16 v[188:191], a[224:227], a[12:15], v[188:191]\n"
"s_waitcnt vmcnt(15) lgkmcnt(4)\n"
"v_lshlrev_b32 v224, 16, v16\n"
"v_and_b32 v225, 0xffff0000, v16\n"
"v_mfma_f32_16x16x32_bf16 v[188:191], a[228:231], a[28:31], v[188:191]\n"
"v_lshlrev_b32 v226, 16, v17\n"
"v_mfma_f32_16x16x32_bf16 v[176:179], a[232:235], a[32:35], v[176:179]\n"
"ds_read_b128 a[224:227], v2 offset:16384\n"
"v_and_b32 v227, 0xffff0000, v17\n"
"v_mfma_f32_16x16x32_bf16 v[176:179], a[236:239], a[48:51], v[176:179]\n"
"v_lshlrev_b32 v228, 16, v18\n"
"v_and_b32 v229, 0xffff0000, v18\n"
"v_mfma_f32_16x16x32_bf16 v[180:183], a[232:235], a[36:39], v[180:183]\n"
"v_lshlrev_b32 v230, 16, v19\n"
"v_mfma_f32_16x16x32_bf16 v[180:183], a[236:239], a[52:55], v[180:183]\n"
"ds_read_b128 a[228:231], v2 offset:17408\n"
"v_and_b32 v231, 0xffff0000, v19\n"
"v_mfma_f32_16x16x32_bf16 v[184:187], a[232:235], a[40:43], v[184:187]\n"
"s_waitcnt vmcnt(13)\n"
"v_lshlrev_b32 v232, 16, v20\n"
"v_mfma_f32_16x16x32_bf16 v[184:187], a[236:239], a[56:59], v[184:187]\n"
"v_and_b32 v233, 0xffff0000, v20\n"
"v_lshlrev_b32 v234, 16, v21\n"
"v_mfma_f32_16x16x32_bf16 v[188:191], a[232:235], a[44:47], v[188:191]\n"
"s_waitcnt lgkmcnt(4)\n"
"v_and_b32 v235, 0xffff0000, v21\n"
"v_mfma_f32_16x16x32_bf16 v[188:191], a[236:239], a[60:63], v[188:191]\n"
"v_lshlrev_b32 v236, 16, v22\n"
"v_mfma_f32_16x16x32_bf16 v[176:179], a[240:243], a[64:67], v[176:179]\n"
"ds_read_b128 a[232:235], v2 offset:18432\n"
"v_and_b32 v237, 0xffff0000, v22\n"
"v_mfma_f32_16x16x32_bf16 v[176:179], a[244:247], a[80:83], v[176:179]\n"
"v_lshlrev_b32 v238, 16, v23\n"
"v_and_b32 v239, 0xffff0000, v23\n"
"v_mfma_f32_16x16x32_bf16 v[180:183], a[240:243], a[68:71], v[180:183]\n"
"s_waitcnt vmcnt(10)\n"
"v_lshlrev_b32 v240, 16, v24\n"
"v_mfma_f32_16x16x32_bf16 v[180:183], a[244:247], a[84:87], v[180:183]\n"
"ds_read_b128 a[236:239], v2 offset:19456\n"
"v_and_b32 v241, 0xffff0000, v24\n"
"v_mfma_f32_16x16x32_bf16 v[184:187], a[240:243], a[72:75], v[184:187]\n"
"v_lshlrev_b32 v242, 16, v25\n"
"v_and_b32 v243, 0xffff0000, v25\n"
"v_mfma_f32_16x16x32_bf16 v[184:187], a[244:247], a[88:91], v[184:187]\n"
"v_lshlrev_b32 v244, 16, v26\n"
"v_mfma_f32_16x16x32_bf16 v[188:191], a[240:243], a[76:79], v[188:191]\n"
"s_waitcnt lgkmcnt(4)\n"
"v_and_b32 v245, 0xffff0000, v26\n"
"v_mfma_f32_16x16x32_bf16 v[188:191], a[244:247], a[92:95], v[188:191]\n"
"v_lshlrev_b32 v246, 16, v27\n"
"v_and_b32 v247, 0xffff0000, v27\n"
"v_mfma_f32_16x16x32_bf16 v[176:179], a[248:251], a[96:99], v[176:179]\n"
"s_waitcnt vmcnt(7)\n"
"ds_read_b128 a[240:243], v2 offset:20480\n"
"v_lshlrev_b32 v248, 16, v28\n"
"v_mfma_f32_16x16x32_bf16 v[176:179], a[252:255], a[112:115], v[176:179]\n"
"v_and_b32 v249, 0xffff0000, v28\n"
"v_mfma_f32_16x16x32_bf16 v[180:183], a[248:251], a[100:103], v[180:183]\n"
"v_lshlrev_b32 v250, 16, v29\n"
"v_mfma_f32_16x16x32_bf16 v[180:183], a[252:255], a[116:119], v[180:183]\n"
"ds_read_b128 a[244:247], v2 offset:21504\n"
"v_and_b32 v251, 0xffff0000, v29\n"
"v_mfma_f32_16x16x32_bf16 v[184:187], a[248:251], a[104:107], v[184:187]\n"
"v_lshlrev_b32 v252, 16, v30\n"
"v_and_b32 v253, 0xffff0000, v30\n"
"v_mfma_f32_16x16x32_bf16 v[184:187], a[252:255], a[120:123], v[184:187]\n"
"v_lshlrev_b32 v254, 16, v31\n"
"v_mfma_f32_16x16x32_bf16 v[188:191], a[248:251], a[108:111], v[188:191]\n"
"s_waitcnt lgkmcnt(4)\n"
"v_and_b32 v255, 0xffff0000, v31\n"
"v_mfma_f32_16x16x32_bf16 v[188:191], a[252:255], a[124:127], v[188:191]\n"
"v_mfma_f32_16x16x32_bf16 v[176:179], a[224:227], v[80:83], v[176:179]\n"
"ds_read_b128 a[248:251], v2 offset:22528\n"
"v_mfma_f32_16x16x32_bf16 v[176:179], a[228:231], v[96:99], v[176:179]\n"
"ds_read_b64 v[16:17], v7\n"
"v_mfma_f32_16x16x32_bf16 v[180:183], a[224:227], v[84:87], v[180:183]\n"
"v_mfma_f32_16x16x32_bf16 v[180:183], a[228:231], v[100:103], v[180:183]\n"
"ds_read_b128 a[252:255], v2 offset:23552\n"
"ds_read_b64 v[24:25], v7 offset:32\n"
"v_mfma_f32_16x16x32_bf16 v[184:187], a[224:227], v[88:91], v[184:187]\n"
"v_mfma_f32_16x16x32_bf16 v[184:187], a[228:231], v[104:107], v[184:187]\n"
"ds_read_b64 v[18:19], v7 offset:1024\n"
"v_mfma_f32_16x16x32_bf16 v[188:191], a[224:227], v[92:95], v[188:191]\n"
"s_waitcnt lgkmcnt(7)\n"
"v_mfma_f32_16x16x32_bf16 v[188:191], a[228:231], v[108:111], v[188:191]\n"
"ds_read_b64 v[26:27], v7 offset:1056\n"
"v_mfma_f32_16x16x32_bf16 v[176:179], a[232:235], v[112:115], v[176:179]\n"
"ds_read_b128 a[224:227], v2 offset:24576\n"
"v_mfma_f32_16x16x32_bf16 v[176:179], a[236:239], v[128:131], v[176:179]\n"
"ds_read_b64 v[20:21], v7 offset:2048\n"
"v_mfma_f32_16x16x32_bf16 v[180:183], a[232:235], v[116:119], v[180:183]\n"
"s_lshl_b32 s78, s73, 2\n"
"v_mfma_f32_16x16x32_bf16 v[180:183], a[236:239], v[132:135], v[180:183]\n"
"s_add_u32 s85, s96, 131072\n"
"ds_read_b128 a[228:231], v2 offset:25600\n"
"ds_read_b64 v[28:29], v7 offset:2080\n"
"v_mfma_f32_16x16x32_bf16 v[184:187], a[232:235], v[120:123], v[184:187]\n"
"s_add_u32 s78, s78, s71\n"
"v_mfma_f32_16x16x32_bf16 v[184:187], a[236:239], v[136:139], v[184:187]\n"
"s_lshl_b32 s78, s78, 3\n"
"ds_read_b64 v[22:23], v7 offset:3072\n"
"v_mfma_f32_16x16x32_bf16 v[188:191], a[232:235], v[124:127], v[188:191]\n"
"s_waitcnt lgkmcnt(11)\n"
"s_add_u32 s78, s78, s100\n"
"s_lshl_b32 s78, s78, 6\n"
"v_mfma_f32_16x16x32_bf16 v[188:191], a[236:239], v[140:143], v[188:191]\n"
"s_add_u32 s78, s78, 79364096\n"
"v_mov_b32 v8, s92\n"
"v_mov_b32 v9, s93\n"
"v_mfma_f32_16x16x32_bf16 v[192:195], a[240:243], a[160:163], v[192:195]\n"
"s_waitcnt vmcnt(0)\n"
"buffer_store_dwordx2 v[8:9], v14, s[40:43], s78 offen sc1\n"
"v_mfma_f32_16x16x32_bf16 v[192:195], a[244:247], a[176:179], v[192:195]\n"
"ds_read_b128 a[232:235], v2 offset:26624\n"
"ds_read_b64 v[30:31], v7 offset:3104\n"
"v_mfma_f32_16x16x32_bf16 v[196:199], a[240:243], a[164:167], v[196:199]\n"
"v_mfma_f32_16x16x32_bf16 v[196:199], a[244:247], a[180:183], v[196:199]\n"
"ds_read_b128 a[236:239], v2 offset:27648\n"
"v_cvt_pk_bf16_f32 v176, v176, v177\n"
"v_mfma_f32_16x16x32_bf16 v[200:203], a[240:243], a[168:171], v[200:203]\n"
"v_cvt_pk_bf16_f32 v177, v178, v179\n"
"v_cvt_pk_bf16_f32 v178, v180, v181\n"
"v_mfma_f32_16x16x32_bf16 v[200:203], a[244:247], a[184:187], v[200:203]\n"
"v_cvt_pk_bf16_f32 v179, v182, v183\n"
"v_cvt_pk_bf16_f32 v180, v184, v185\n"
"v_mfma_f32_16x16x32_bf16 v[204:207], a[240:243], a[172:175], v[204:207]\n"
"s_waitcnt lgkmcnt(11)\n"
"v_cvt_pk_bf16_f32 v181, v186, v187\n"
"v_cvt_pk_bf16_f32 v182, v188, v189\n"
"v_mfma_f32_16x16x32_bf16 v[204:207], a[244:247], a[188:191], v[204:207]\n"
"v_cvt_pk_bf16_f32 v183, v190, v191\n"
"v_mfma_f32_16x16x32_bf16 v[192:195], a[248:251], a[192:195], v[192:195]\n"
"ds_read_b128 a[240:243], v2 offset:28672\n"
"v_mfma_f32_16x16x32_bf16 v[192:195], a[252:255], a[208:211], v[192:195]\n"
"buffer_store_dwordx4 v[176:179], v1, s[40:43], s85 offen sc1\n"
"v_mfma_f32_16x16x32_bf16 v[196:199], a[248:251], a[196:199], v[196:199]\n"
"v_cmp_ne_u64 vcc, s[92:93], v[10:11]\n"
"v_mfma_f32_16x16x32_bf16 v[196:199], a[252:255], a[212:215], v[196:199]\n"
"s_mov_b32 s56, vcc_lo\n"
"s_and_b32 s57, s56, 28\n"
"s_or_b32 s54, s54, s57\n"
"v_mfma_f32_16x16x32_bf16 v[200:203], a[248:251], a[200:203], v[200:203]\n"
"ds_read_b128 a[244:247], v2 offset:29696\n"
"v_mfma_f32_16x16x32_bf16 v[200:203], a[252:255], a[216:219], v[200:203]\n"
"s_add_u32 s79, s101, 1\n"
"buffer_store_dwordx4 v[180:183], v1, s[40:43], s85 offen offset:1024 sc1\n"
"v_mfma_f32_16x16x32_bf16 v[204:207], a[248:251], a[204:207], v[204:207]\n"
"s_waitcnt lgkmcnt(7)\n"
"v_lshlrev_b32 v12, 16, v32\n"
"v_and_b32 v13, 0xffff0000, v32\n"
"v_mfma_f32_16x16x32_bf16 v[204:207], a[252:255], a[220:223], v[204:207]\n"
"s_and_b32 s79, s79, 3\n"
"v_add_f32 v224, v224, v12\n"
"v_add_f32 v225, v225, v13\n"
"v_mfma_f32_16x16x32_bf16 v[192:195], a[224:227], a[0:3], v[192:195]\n"
"s_lshl_b32 s79, s79, 11\n"
"ds_read_b128 a[248:251], v2 offset:30720\n"
"v_lshlrev_b32 v12, 16, v33\n"
"v_mfma_f32_16x16x32_bf16 v[192:195], a[228:231], a[16:19], v[192:195]\n"
"s_add_u32 s79, s79, s52\n"
"s_add_u32 s79, s79, 8192\n"
"s_bitcmp0_b32 s56, 2\n"
"s_cselect_b32 s79, s79, 0x70000000\n"
"v_mfma_f32_16x16x32_bf16 v[196:199], a[224:227], a[4:7], v[196:199]\n"
"buffer_load_dwordx4 v[48:51], v1, s[40:43], s79 offen sc1\n"
"v_mfma_f32_16x16x32_bf16 v[196:199], a[228:231], a[20:23], v[196:199]\n"
"ds_read_b128 a[252:255], v2 offset:31744\n"
"v_and_b32 v13, 0xffff0000, v33\n"
"v_mfma_f32_16x16x32_bf16 v[200:203], a[224:227], a[8:11], v[200:203]\n"
"v_add_f32 v226, v226, v12\n"
"v_add_f32 v227, v227, v13\n"
"v_mfma_f32_16x16x32_bf16 v[200:203], a[228:231], a[24:27], v[200:203]\n"
"v_lshlrev_b32 v12, 16, v34\n"
"v_and_b32 v13, 0xffff0000, v34\n"
"v_mfma_f32_16x16x32_bf16 v[204:207], a[224:227], a[12:15], v[204:207]\n"
"s_waitcnt lgkmcnt(4)\n"
"v_add_f32 v228, v228, v12\n"
"v_add_f32 v229, v229, v13\n"
"v_mfma_f32_16x16x32_bf16 v[204:207], a[228:231], a[28:31], v[204:207]\n"
"s_barrier\n"
"v_lshlrev_b32 v12, 16, v35\n"
"v_mfma_f32_16x16x32_bf16 v[192:195], a[232:235], a[32:35], v[192:195]\n"
"ds_read_b128 a[224:227], v2 offset:32768\n"
"v_and_b32 v13, 0xffff0000, v35\n"
"v_mfma_f32_16x16x32_bf16 v[192:195], a[236:239], a[48:51], v[192:195]\n"
"buffer_load_dwordx4 v[52:55], v1, s[40:43], s79 offen offset:1024 sc1\n"
"v_mfma_f32_16x16x32_bf16 v[196:199], a[232:235], a[36:39], v[196:199]\n"
"v_add_f32 v230, v230, v12\n"
"v_add_f32 v231, v231, v13\n"
"v_mfma_f32_16x16x32_bf16 v[196:199], a[236:239], a[52:55], v[196:199]\n"
"ds_read_b128 a[228:231], v2 offset:33792\n"
"v_lshlrev_b32 v12, 16, v36\n"
"v_mfma_f32_16x16x32_bf16 v[200:203], a[232:235], a[40:43], v[200:203]\n"
"v_and_b32 v13, 0xffff0000, v36\n"
"v_add_f32 v232, v232, v12\n"
"v_mfma_f32_16x16x32_bf16 v[200:203], a[236:239], a[56:59], v[200:203]\n"
"v_add_f32 v233, v233, v13\n"
"v_mfma_f32_16x16x32_bf16 v[204:207], a[232:235], a[44:47], v[204:207]\n"
"s_waitcnt lgkmcnt(4)\n"
"v_lshlrev_b32 v12, 16, v37\n"
"v_mfma_f32_16x16x32_bf16 v[204:207], a[236:239], a[60:63], v[204:207]\n"
"s_add_u32 s79, s79, 16384\n"
"v_and_b32 v13, 0xffff0000, v37\n"
"v_add_f32 v234, v234, v12\n"
"v_mfma_f32_16x16x32_bf16 v[192:195], a[240:243], a[64:67], v[192:195]\n"
"ds_read_b128 a[232:235], v2 offset:34816\n"
"v_add_f32 v235, v235, v13\n"
"v_mfma_f32_16x16x32_bf16 v[192:195], a[244:247], a[80:83], v[192:195]\n"
"buffer_load_dwordx4 v[56:59], v1, s[40:43], s79 offen sc1\n"
"v_mfma_f32_16x16x32_bf16 v[196:199], a[240:243], a[68:71], v[196:199]\n"
"v_lshlrev_b32 v12, 16, v38\n"
"v_and_b32 v13, 0xffff0000, v38\n"
"v_mfma_f32_16x16x32_bf16 v[196:199], a[244:247], a[84:87], v[196:199]\n"
"ds_read_b128 a[236:239], v2 offset:35840\n"
"v_add_f32 v236, v236, v12\n"
"v_mfma_f32_16x16x32_bf16 v[200:203], a[240:243], a[72:75], v[200:203]\n"
"v_add_f32 v237, v237, v13\n"
"v_mfma_f32_16x16x32_bf16 v[200:203], a[244:247], a[88:91], v[200:203]\n"
"v_lshlrev_b32 v12, 16, v39\n"
"v_and_b32 v13, 0xffff0000, v39\n"
"v_mfma_f32_16x16x32_bf16 v[204:207], a[240:243], a[76:79], v[204:207]\n"
"s_waitcnt lgkmcnt(4)\n"
"v_add_f32 v238, v238, v12\n"
"v_mfma_f32_16x16x32_bf16 v[204:207], a[244:247], a[92:95], v[204:207]\n"
"v_add_f32 v239, v239, v13\n"
"v_mfma_f32_16x16x32_bf16 v[192:195], a[248:251], a[96:99], v[192:195]\n"
"ds_read_b128 a[240:243], v2 offset:36864\n"
"v_lshlrev_b32 v12, 16, v40\n"
"v_mfma_f32_16x16x32_bf16 v[192:195], a[252:255], a[112:115], v[192:195]\n"
"v_and_b32 v13, 0xffff0000, v40\n"
"v_mfma_f32_16x16x32_bf16 v[196:199], a[248:251], a[100:103], v[196:199]\n"
"buffer_load_dwordx4 v[60:63], v1, s[40:43], s79 offen offset:1024 sc1\n"
"v_mfma_f32_16x16x32_bf16 v[196:199], a[252:255], a[116:119], v[196:199]\n"
"ds_read_b128 a[244:247], v2 offset:37888\n"
"v_add_f32 v240, v240, v12\n"
"v_mfma_f32_16x16x32_bf16 v[200:203], a[248:251], a[104:107], v[200:203]\n"
"v_add_f32 v241, v241, v13\n"
"v_lshlrev_b32 v12, 16, v41\n"
"v_mfma_f32_16x16x32_bf16 v[200:203], a[252:255], a[120:123], v[200:203]\n"
"v_and_b32 v13, 0xffff0000, v41\n"
"v_add_f32 v242, v242, v12\n"
"v_mfma_f32_16x16x32_bf16 v[204:207], a[248:251], a[108:111], v[204:207]\n"
"s_waitcnt lgkmcnt(4)\n"
"v_add_f32 v243, v243, v13\n"
"v_mfma_f32_16x16x32_bf16 v[204:207], a[252:255], a[124:127], v[204:207]\n"
"v_lshlrev_b32 v12, 16, v42\n"
"v_mfma_f32_16x16x32_bf16 v[192:195], a[224:227], v[80:83], v[192:195]\n"
"s_add_u32 s79, s101, 3\n"
"ds_read_b128 a[248:251], v2 offset:38912\n"
"v_and_b32 v13, 0xffff0000, v42\n"
"v_mfma_f32_16x16x32_bf16 v[192:195], a[228:231], v[96:99], v[192:195]\n"
"s_and_b32 s79, s79, 3\n"
"v_add_f32 v244, v244, v12\n"
"v_add_f32 v245, v245, v13\n"
"v_mfma_f32_16x16x32_bf16 v[196:199], a[224:227], v[84:87], v[196:199]\n"
"s_lshl_b32 s79, s79, 11\n"
"v_lshlrev_b32 v12, 16, v43\n"
"v_mfma_f32_16x16x32_bf16 v[196:199], a[228:231], v[100:103], v[196:199]\n"
"s_add_u32 s79, s79, s52\n"
"ds_read_b128 a[252:255], v2 offset:39936\n"
"v_and_b32 v13, 0xffff0000, v43\n"
"v_mfma_f32_16x16x32_bf16 v[200:203], a[224:227], v[88:91], v[200:203]\n"
"s_add_u32 s79, s79, 0\n"
"s_bitcmp0_b32 s56, 3\n"
"s_cselect_b32 s79, s79, 0x70000000\n"
"v_add_f32 v246, v246, v12\n"
"v_mfma_f32_16x16x32_bf16 v[200:203], a[228:231], v[104:107], v[200:203]\n"
"buffer_load_dwordx4 v[64:67], v1, s[40:43], s79 offen sc1\n"
"v_mfma_f32_16x16x32_bf16 v[204:207], a[224:227], v[92:95], v[204:207]\n"
"s_waitcnt lgkmcnt(4)\n"
"v_add_f32 v247, v247, v13\n"
"v_lshlrev_b32 v12, 16, v44\n"
"v_mfma_f32_16x16x32_bf16 v[204:207], a[228:231], v[108:111], v[204:207]\n"
"v_and_b32 v13, 0xffff0000, v44\n"
"v_add_f32 v248, v248, v12\n"
"v_mfma_f32_16x16x32_bf16 v[192:195], a[232:235], v[112:115], v[192:195]\n"
"ds_read_b128 a[224:227], v2 offset:40960\n"
"v_add_f32 v249, v249, v13\n"
"v_mfma_f32_16x16x32_bf16 v[192:195], a[236:239], v[128:131], v[192:195]\n"
"v_lshlrev_b32 v12, 16, v45\n"
"v_mfma_f32_16x16x32_bf16 v[196:199], a[232:235], v[116:119], v[196:199]\n"
"s_lshl_b32 s78, s73, 2\n"
"v_and_b32 v13, 0xffff0000, v45\n"
"v_mfma_f32_16x16x32_bf16 v[196:199], a[236:239], v[132:135], v[196:199]\n"
"s_add_u32 s78, s78, s71\n"
"ds_read_b128 a[228:231], v2 offset:41984\n"
"v_add_f32 v250, v250, v12\n"
"v_mfma_f32_16x16x32_bf16 v[200:203], a[232:235], v[120:123], v[200:203]\n"
"s_lshl_b32 s78, s78, 3\n"
"buffer_load_dwordx4 v[68:71], v1, s[40:43], s79 offen offset:1024 sc1\n"
"v_mfma_f32_16x16x32_bf16 v[200:203], a[236:239], v[136:139], v[200:203]\n"
"s_add_u32 s78, s78, s100\n"
"v_add_f32 v251, v251, v13\n"
"v_lshlrev_b32 v12, 16, v46\n"
"v_mfma_f32_16x16x32_bf16 v[204:207], a[232:235], v[124:127], v[204:207]\n"
"s_waitcnt lgkmcnt(4)\n"
"s_lshl_b32 s78, s78, 6\n"
"s_add_u32 s78, s78, 79495168\n"
"v_mfma_f32_16x16x32_bf16 v[204:207], a[236:239], v[140:143], v[204:207]\n"
"v_mov_b32 v8, s92\n"
"v_mov_b32 v9, s93\n"
"v_mfma_f32_16x16x32_bf16 v[208:211], a[240:243], a[160:163], v[208:211]\n"
"s_waitcnt vmcnt(6)\n"
"buffer_store_dwordx2 v[8:9], v14, s[40:43], s78 offen sc1\n"
"v_mfma_f32_16x16x32_bf16 v[208:211], a[244:247], a[176:179], v[208:211]\n"
"ds_read_b128 a[232:235], v2 offset:43008\n"
"v_and_b32 v13, 0xffff0000, v46\n"
"v_mfma_f32_16x16x32_bf16 v[212:215], a[240:243], a[164:167], v[212:215]\n"
"s_add_u32 s79, s79, 16384\n"
"v_add_f32 v252, v252, v12\n"
"v_add_f32 v253, v253, v13\n"
"v_mfma_f32_16x16x32_bf16 v[212:215], a[244:247], a[180:183], v[212:215]\n"
"ds_read_b128 a[236:239], v2 offset:44032\n"
"v_lshlrev_b32 v12, 16, v47\n"
"v_mfma_f32_16x16x32_bf16 v[216:219], a[240:243], a[168:171], v[216:219]\n"
"v_and_b32 v13, 0xffff0000, v47\n"
"v_add_f32 v254, v254, v12\n"
"v_mfma_f32_16x16x32_bf16 v[216:219], a[244:247], a[184:187], v[216:219]\n"
"v_add_f32 v255, v255, v13\n"
"v_mfma_f32_16x16x32_bf16 v[220:223], a[240:243], a[172:175], v[220:223]\n"
"s_waitcnt lgkmcnt(4)\n"
"buffer_load_dwordx4 v[72:75], v1, s[40:43], s79 offen sc1\n"
"v_mfma_f32_16x16x32_bf16 v[220:223], a[244:247], a[188:191], v[220:223]\n"
"v_mfma_f32_16x16x32_bf16 v[208:211], a[248:251], a[192:195], v[208:211]\n"
"ds_read_b128 a[240:243], v2 offset:45056\n"
"v_mfma_f32_16x16x32_bf16 v[208:211], a[252:255], a[208:211], v[208:211]\n"
"v_mfma_f32_16x16x32_bf16 v[212:215], a[248:251], a[196:199], v[212:215]\n"
"v_mfma_f32_16x16x32_bf16 v[212:215], a[252:255], a[212:215], v[212:215]\n"
"ds_read_b128 a[244:247], v2 offset:46080\n"
"v_mfma_f32_16x16x32_bf16 v[216:219], a[248:251], a[200:203], v[216:219]\n"
"buffer_load_dwordx4 v[76:79], v1, s[40:43], s79 offen offset:1024 sc1\n"
"v_mfma_f32_16x16x32_bf16 v[216:219], a[252:255], a[216:219], v[216:219]\n"
"v_mfma_f32_16x16x32_bf16 v[220:223], a[248:251], a[204:207], v[220:223]\n"
"s_waitcnt lgkmcnt(4)\n"
"v_mfma_f32_16x16x32_bf16 v[220:223], a[252:255], a[220:223], v[220:223]\n"
"v_mfma_f32_16x16x32_bf16 v[208:211], a[224:227], a[0:3], v[208:211]\n"
"ds_read_b128 a[248:251], v2 offset:47104\n"
"v_mfma_f32_16x16x32_bf16 v[208:211], a[228:231], a[16:19], v[208:211]\n"
"v_mfma_f32_16x16x32_bf16 v[212:215], a[224:227], a[4:7], v[212:215]\n"
"v_mfma_f32_16x16x32_bf16 v[212:215], a[228:231], a[20:23], v[212:215]\n"
"ds_read_b128 a[252:255], v2 offset:48128\n"
"v_mfma_f32_16x16x32_bf16 v[216:219], a[224:227], a[8:11], v[216:219]\n"
"buffer_load_dwordx2 v[10:11], v15, s[40:43], s23 offen sc1\n"
"v_mfma_f32_16x16x32_bf16 v[216:219], a[228:231], a[24:27], v[216:219]\n"
"s_add_u32 s79, s101, 2\n"
"v_mfma_f32_16x16x32_bf16 v[220:223], a[224:227], a[12:15], v[220:223]\n"
"s_waitcnt lgkmcnt(4)\n"
"v_mfma_f32_16x16x32_bf16 v[220:223], a[228:231], a[28:31], v[220:223]\n"
"s_and_b32 s79, s79, 3\n"
"v_mfma_f32_16x16x32_bf16 v[208:211], a[232:235], a[32:35], v[208:211]\n"
"s_lshl_b32 s79, s79, 11\n"
"s_add_u32 s79, s79, s52\n"
"ds_read_b128 a[224:227], v2 offset:49152\n"
"v_mfma_f32_16x16x32_bf16 v[208:211], a[236:239], a[48:51], v[208:211]\n"
"s_add_u32 s79, s79, 0\n"
"s_bitcmp0_b32 s56, 4\n"
"s_cselect_b32 s79, s79, 0x70000000\n"
"v_mfma_f32_16x16x32_bf16 v[212:215], a[232:235], a[36:39], v[212:215]\n"
"buffer_load_dwordx4 v[144:147], v1, s[40:43], s79 offen sc1\n"
"v_mfma_f32_16x16x32_bf16 v[212:215], a[236:239], a[52:55], v[212:215]\n"
"ds_read_b128 a[228:231], v2 offset:50176\n"
"v_mfma_f32_16x16x32_bf16 v[216:219], a[232:235], a[40:43], v[216:219]\n"
"v_mfma_f32_16x16x32_bf16 v[216:219], a[236:239], a[56:59], v[216:219]\n"
"buffer_load_dwordx4 v[148:151], v1, s[40:43], s79 offen offset:1024 sc1\n"
"v_mfma_f32_16x16x32_bf16 v[220:223], a[232:235], a[44:47], v[220:223]\n"
"s_waitcnt lgkmcnt(4)\n"
"v_mfma_f32_16x16x32_bf16 v[220:223], a[236:239], a[60:63], v[220:223]\n"
"v_mfma_f32_16x16x32_bf16 v[208:211], a[240:243], a[64:67], v[208:211]\n"
"ds_read_b128 a[232:235], v2 offset:51200\n"
"v_mfma_f32_16x16x32_bf16 v[208:211], a[244:247], a[80:83], v[208:211]\n"
"v_mfma_f32_16x16x32_bf16 v[212:215], a[240:243], a[68:71], v[212:215]\n"
"v_mfma_f32_16x16x32_bf16 v[212:215], a[244:247], a[84:87], v[212:215]\n"
"ds_read_b128 a[236:239], v2 offset:52224\n"
"v_mfma_f32_16x16x32_bf16 v[216:219], a[240:243], a[72:75], v[216:219]\n"
"v_mfma_f32_16x16x32_bf16 v[216:219], a[244:247], a[88:91], v[216:219]\n"
"v_mfma_f32_16x16x32_bf16 v[220:223], a[240:243], a[76:79], v[220:223]\n"
"s_waitcnt lgkmcnt(4)\n"
"v_mfma_f32_16x16x32_bf16 v[220:223], a[244:247], a[92:95], v[220:223]\n"
"s_add_u32 s79, s79, 16384\n"
"buffer_load_dwordx4 v[152:155], v1, s[40:43], s79 offen sc1\n"
"v_mfma_f32_16x16x32_bf16 v[208:211], a[248:251], a[96:99], v[208:211]\n"
"v_mfma_f32_16x16x32_bf16 v[208:211], a[252:255], a[112:115], v[208:211]\n"
"v_mfma_f32_16x16x32_bf16 v[212:215], a[248:251], a[100:103], v[212:215]\n"
"v_mfma_f32_16x16x32_bf16 v[212:215], a[252:255], a[116:119], v[212:215]\n"
"v_mfma_f32_16x16x32_bf16 v[216:219], a[248:251], a[104:107], v[216:219]\n"
"v_mfma_f32_16x16x32_bf16 v[216:219], a[252:255], a[120:123], v[216:219]\n"
"v_mfma_f32_16x16x32_bf16 v[220:223], a[248:251], a[108:111], v[220:223]\n"
"s_waitcnt lgkmcnt(2)\n"
"v_mfma_f32_16x16x32_bf16 v[220:223], a[252:255], a[124:127], v[220:223]\n"
"buffer_load_dwordx4 v[156:159], v1, s[40:43], s79 offen offset:1024 sc1\n"
"v_mfma_f32_16x16x32_bf16 v[208:211], a[224:227], v[80:83], v[208:211]\n"
"v_mfma_f32_16x16x32_bf16 v[208:211], a[228:231], v[96:99], v[208:211]\n"
"v_mfma_f32_16x16x32_bf16 v[212:215], a[224:227], v[84:87], v[212:215]\n"
"v_mfma_f32_16x16x32_bf16 v[212:215], a[228:231], v[100:103], v[212:215]\n"
"v_mfma_f32_16x16x32_bf16 v[216:219], a[224:227], v[88:91], v[216:219]\n"
"v_mfma_f32_16x16x32_bf16 v[216:219], a[228:231], v[104:107], v[216:219]\n"
"v_mfma_f32_16x16x32_bf16 v[220:223], a[224:227], v[92:95], v[220:223]\n"
"s_waitcnt lgkmcnt(0)\n"
"v_mfma_f32_16x16x32_bf16 v[220:223], a[228:231], v[108:111], v[220:223]\n"
"v_mfma_f32_16x16x32_bf16 v[208:211], a[232:235], v[112:115], v[208:211]\n"
"v_mfma_f32_16x16x32_bf16 v[208:211], a[236:239], v[128:131], v[208:211]\n"
"v_mfma_f32_16x16x32_bf16 v[212:215], a[232:235], v[116:119], v[212:215]\n"
"v_mfma_f32_16x16x32_bf16 v[212:215], a[236:239], v[132:135], v[212:215]\n"
"v_mfma_f32_16x16x32_bf16 v[216:219], a[232:235], v[120:123], v[216:219]\n"
"v_mfma_f32_16x16x32_bf16 v[216:219], a[236:239], v[136:139], v[216:219]\n"
"v_mfma_f32_16x16x32_bf16 v[220:223], a[232:235], v[124:127], v[220:223]\n"
"v_mfma_f32_16x16x32_bf16 v[220:223], a[236:239], v[140:143], v[220:223]\n"
"s_waitcnt vmcnt(4)\n"
"v_cmp_ne_u64 vcc, s[92:93], v[10:11]\n"
"s_nop 0\n"
"s_mov_b32 s56, vcc_lo\n"
"s_and_b32 s57, s56, 96\n"
"s_or_b32 s54, s54, s57\n"
"s_add_u32 s79, s101, 0\n"
"s_and_b32 s79, s79, 3\n"
"s_lshl_b32 s79, s79, 11\n"
"s_add_u32 s79, s79, s52\n"
"s_add_u32 s79, s79, 8192\n"
"s_bitcmp0_b32 s56, 5\n"
"s_cselect_b32 s79, s79, 0x70000000\n"
"buffer_load_dwordx4 v[160:163], v1, s[40:43], s79 offen sc1\n"
"buffer_load_dwordx4 v[164:167], v1, s[40:43], s79 offen offset:1024 sc1\n"
"s_add_u32 s79, s79, 16384\n"
"buffer_load_dwordx4 v[168:171], v1, s[40:43], s79 offen sc1\n"
"buffer_load_dwordx4 v[172:175], v1, s[40:43], s79 offen offset:1024 sc1\n"
"s_add_u32 s79, s101, 1\n"
"s_and_b32 s79, s79, 3\n"
"s_lshl_b32 s79, s79, 11\n"
"s_add_u32 s79, s79, s52\n"
"s_add_u32 s79, s79, 0\n"
"s_bitcmp0_b32 s56, 6\n"
"s_cselect_b32 s79, s79, 0x70000000\n"
"buffer_load_dwordx4 v[176:179], v1, s[40:43], s79 offen sc1\n"
"buffer_load_dwordx4 v[180:183], v1, s[40:43], s79 offen offset:1024 sc1\n"
"s_add_u32 s79, s79, 16384\n"
"buffer_load_dwordx4 v[184:187], v1, s[40:43], s79 offen sc1\n"
"buffer_load_dwordx4 v[188:191], v1, s[40:43], s79 offen offset:1024 sc1\n"
"v_lshlrev_b32 v80, 16, v16\n"
"v_and_b32 v81, 0xffff0000, v16\n"
"v_lshlrev_b32 v82, 16, v17\n"
"v_and_b32 v83, 0xffff0000, v17\n"
"v_lshlrev_b32 v84, 16, v18\n"
"v_and_b32 v85, 0xffff0000, v18\n"
"v_lshlrev_b32 v86, 16, v19\n"
"v_and_b32 v87, 0xffff0000, v19\n"
"v_lshlrev_b32 v88, 16, v20\n"
"v_and_b32 v89, 0xffff0000, v20\n"
"v_lshlrev_b32 v90, 16, v21\n"
"v_and_b32 v91, 0xffff0000, v21\n"
"v_lshlrev_b32 v92, 16, v22\n"
"v_and_b32 v93, 0xffff0000, v22\n"
"v_lshlrev_b32 v94, 16, v23\n"
"v_and_b32 v95, 0xffff0000, v23\n"
"v_lshlrev_b32 v96, 16, v24\n"
"v_and_b32 v97, 0xffff0000, v24\n"
"v_lshlrev_b32 v98, 16, v25\n"
"v_and_b32 v99, 0xffff0000, v25\n"
"v_lshlrev_b32 v100, 16, v26\n"
"v_and_b32 v101, 0xffff0000, v26\n"
"v_lshlrev_b32 v102, 16, v27\n"
"v_and_b32 v103, 0xffff0000, v27\n"
"v_lshlrev_b32 v104, 16, v28\n"
"v_and_b32 v105, 0xffff0000, v28\n"
"v_lshlrev_b32 v106, 16, v29\n"
"v_and_b32 v107, 0xffff0000, v29\n"
"v_lshlrev_b32 v108, 16, v30\n"
"v_and_b32 v109, 0xffff0000, v30\n"
"v_lshlrev_b32 v110, 16, v31\n"
"v_and_b32 v111, 0xffff0000, v31\n"
"s_or_b32 s59, s54, 124\n"
"s_mov_b32 s81, 400000\n"
"L_accum_30_%=:\n"
"s_xor_b32 s78, s59, 4\n"
"s_or_b32 s78, s78, s54\n"
"s_and_b32 s78, s78, 7\n"
"s_cbranch_scc1 L_skip_34_%=\n"
"v_dot2c_f32_bf16 v224, s88, v48\n"
"v_dot2c_f32_bf16 v225, s65, v48\n"
"v_dot2c_f32_bf16 v226, s88, v49\n"
"v_dot2c_f32_bf16 v227, s65, v49\n"
"v_dot2c_f32_bf16 v228, s88, v50\n"
"v_dot2c_f32_bf16 v229, s65, v50\n"
"v_dot2c_f32_bf16 v230, s88, v51\n"
"v_dot2c_f32_bf16 v231, s65, v51\n"
"v_dot2c_f32_bf16 v232, s88, v52\n"
"v_dot2c_f32_bf16 v233, s65, v52\n"
"v_dot2c_f32_bf16 v234, s88, v53\n"
"v_dot2c_f32_bf16 v235, s65, v53\n"
"v_dot2c_f32_bf16 v236, s88, v54\n"
"v_dot2c_f32_bf16 v237, s65, v54\n"
"v_dot2c_f32_bf16 v238, s88, v55\n"
"v_dot2c_f32_bf16 v239, s65, v55\n"
"v_dot2c_f32_bf16 v240, s88, v56\n"
"v_dot2c_f32_bf16 v241, s65, v56\n"
"v_dot2c_f32_bf16 v242, s88, v57\n"
"v_dot2c_f32_bf16 v243, s65, v57\n"
"v_dot2c_f32_bf16 v244, s88, v58\n"
"v_dot2c_f32_bf16 v245, s65, v58\n"
"v_dot2c_f32_bf16 v246, s88, v59\n"
"v_dot2c_f32_bf16 v247, s65, v59\n"
"v_dot2c_f32_bf16 v248, s88, v60\n"
"v_dot2c_f32_bf16 v249, s65, v60\n"
"v_dot2c_f32_bf16 v250, s88, v61\n"
"v_dot2c_f32_bf16 v251, s65, v61\n"
"v_dot2c_f32_bf16 v252, s88, v62\n"
"v_dot2c_f32_bf16 v253, s65, v62\n"
"v_dot2c_f32_bf16 v254, s88, v63\n"
"v_dot2c_f32_bf16 v255, s65, v63\n"
"s_xor_b32 s59, s59, 4\n"
"L_skip_34_%=:\n"
"s_xor_b32 s78, s59, 8\n"
"s_or_b32 s78, s78, s54\n"
"s_and_b32 s78, s78, 8\n"
"s_cbranch_scc1 L_skip_35_%=\n"
"v_dot2c_f32_bf16 v192, s88, v64\n"
"v_dot2c_f32_bf16 v193, s65, v64\n"
"v_dot2c_f32_bf16 v194, s88, v65\n"
"v_dot2c_f32_bf16 v195, s65, v65\n"
"v_dot2c_f32_bf16 v196, s88, v66\n"
"v_dot2c_f32_bf16 v197, s65, v66\n"
"v_dot2c_f32_bf16 v198, s88, v67\n"
"v_dot2c_f32_bf16 v199, s65, v67\n"
"v_dot2c_f32_bf16 v200, s88, v68\n"
"v_dot2c_f32_bf16 v201, s65, v68\n"
"v_dot2c_f32_bf16 v202, s88, v69\n"
"v_dot2c_f32_bf16 v203, s65, v69\n"
"v_dot2c_f32_bf16 v204, s88, v70\n"
"v_dot2c_f32_bf16 v205, s65, v70\n"
"v_dot2c_f32_bf16 v206, s88, v71\n"
"v_dot2c_f32_bf16 v207, s65, v71\n"
"v_dot2c_f32_bf16 v208, s88, v72\n"
"v_dot2c_f32_bf16 v209, s65, v72\n"
"v_dot2c_f32_bf16 v210, s88, v73\n"
"v_dot2c_f32_bf16 v211, s65, v73\n"
"v_dot2c_f32_bf16 v212, s88, v74\n"
"v_dot2c_f32_bf16 v213, s65, v74\n"
"v_dot2c_f32_bf16 v214, s88, v75\n"
"v_dot2c_f32_bf16 v215, s65, v75\n"
"v_dot2c_f32_bf16 v216, s88, v76\n"
"v_dot2c_f32_bf16 v217, s65, v76\n"
"v_dot2c_f32_bf16 v218, s88, v77\n"
"v_dot2c_f32_bf16 v219, s65, v77\n"
"v_dot2c_f32_bf16 v220, s88, v78\n"
"v_dot2c_f32_bf16 v221, s65, v78\n"
"v_dot2c_f32_bf16 v222, s88, v79\n"
"v_dot2c_f32_bf16 v223, s65, v79\n"
"s_xor_b32 s59, s59, 8\n"
"L_skip_35_%=:\n"
"s_xor_b32 s78, s59, 16\n"
"s_or_b32 s78, s78, s54\n"
"s_and_b32 s78, s78, 24\n"
"s_cbranch_scc1 L_skip_36_%=\n"
"s_waitcnt vmcnt(11)\n"
"v_dot2c_f32_bf16 v192, s88, v144\n"
"v_dot2c_f32_bf16 v193, s65, v144\n"
"v_dot2c_f32_bf16 v194, s88, v145\n"
"v_dot2c_f32_bf16 v195, s65, v145\n"
"v_dot2c_f32_bf16 v196, s88, v146\n"
"v_dot2c_f32_bf16 v197, s65, v146\n"
"v_dot2c_f32_bf16 v198, s88, v147\n"
"v_dot2c_f32_bf16 v199, s65, v147\n"
"s_waitcnt vmcnt(10)\n"
"v_dot2c_f32_bf16 v200, s88, v148\n"
"v_dot2c_f32_bf16 v201, s65, v148\n"
"v_dot2c_f32_bf16 v202, s88, v149\n"
"v_dot2c_f32_bf16 v203, s65, v149\n"
"v_dot2c_f32_bf16 v204, s88, v150\n"
"v_dot2c_f32_bf16 v205, s65, v150\n"
"v_dot2c_f32_bf16 v206, s88, v151\n"
"v_dot2c_f32_bf16 v207, s65, v151\n"
"s_waitcnt vmcnt(9)\n"
"v_dot2c_f32_bf16 v208, s88, v152\n"
"v_dot2c_f32_bf16 v209, s65, v152\n"
"v_dot2c_f32_bf16 v210, s88, v153\n"
"v_dot2c_f32_bf16 v211, s65, v153\n"
"v_dot2c_f32_bf16 v212, s88, v154\n"
"v_dot2c_f32_bf16 v213, s65, v154\n"
"v_dot2c_f32_bf16 v214, s88, v155\n"
"v_dot2c_f32_bf16 v215, s65, v155\n"
"s_waitcnt vmcnt(8)\n"
"v_dot2c_f32_bf16 v216, s88, v156\n"
"v_dot2c_f32_bf16 v217, s65, v156\n"
"v_dot2c_f32_bf16 v218, s88, v157\n"
"v_dot2c_f32_bf16 v219, s65, v157\n"
"v_dot2c_f32_bf16 v220, s88, v158\n"
"v_dot2c_f32_bf16 v221, s65, v158\n"
"v_dot2c_f32_bf16 v222, s88, v159\n"
"v_dot2c_f32_bf16 v223, s65, v159\n"
"s_xor_b32 s59, s59, 16\n"
"L_skip_36_%=:\n"
"s_xor_b32 s78, s59, 32\n"
"s_or_b32 s78, s78, s54\n"
"s_and_b32 s78, s78, 39\n"
"s_cbranch_scc1 L_skip_37_%=\n"
"s_waitcnt vmcnt(7)\n"
"v_dot2c_f32_bf16 v224, s88, v160\n"
"v_dot2c_f32_bf16 v225, s65, v160\n"
"v_dot2c_f32_bf16 v226, s88, v161\n"
"v_dot2c_f32_bf16 v227, s65, v161\n"
"v_dot2c_f32_bf16 v228, s88, v162\n"
"v_dot2c_f32_bf16 v229, s65, v162\n"
"v_dot2c_f32_bf16 v230, s88, v163\n"
"v_dot2c_f32_bf16 v231, s65, v163\n"
"s_waitcnt vmcnt(6)\n"
"v_dot2c_f32_bf16 v232, s88, v164\n"
"v_dot2c_f32_bf16 v233, s65, v164\n"
"v_dot2c_f32_bf16 v234, s88, v165\n"
"v_dot2c_f32_bf16 v235, s65, v165\n"
"v_dot2c_f32_bf16 v236, s88, v166\n"
"v_dot2c_f32_bf16 v237, s65, v166\n"
"v_dot2c_f32_bf16 v238, s88, v167\n"
"v_dot2c_f32_bf16 v239, s65, v167\n"
"s_waitcnt vmcnt(5)\n"
"v_dot2c_f32_bf16 v240, s88, v168\n"
"v_dot2c_f32_bf16 v241, s65, v168\n"
"v_dot2c_f32_bf16 v242, s88, v169\n"
"v_dot2c_f32_bf16 v243, s65, v169\n"
"v_dot2c_f32_bf16 v244, s88, v170\n"
"v_dot2c_f32_bf16 v245, s65, v170\n"
"v_dot2c_f32_bf16 v246, s88, v171\n"
"v_dot2c_f32_bf16 v247, s65, v171\n"
"s_waitcnt vmcnt(4)\n"
"v_dot2c_f32_bf16 v248, s88, v172\n"
"v_dot2c_f32_bf16 v249, s65, v172\n"
"v_dot2c_f32_bf16 v250, s88, v173\n"
"v_dot2c_f32_bf16 v251, s65, v173\n"
"v_dot2c_f32_bf16 v252, s88, v174\n"
"v_dot2c_f32_bf16 v253, s65, v174\n"
"v_dot2c_f32_bf16 v254, s88, v175\n"
"v_dot2c_f32_bf16 v255, s65, v175\n"
"s_xor_b32 s59, s59, 32\n"
"L_skip_37_%=:\n"
"s_xor_b32 s78, s59, 64\n"
"s_or_b32 s78, s78, s54\n"
"s_and_b32 s78, s78, 88\n"
"s_cbranch_scc1 L_skip_38_%=\n"
"s_waitcnt vmcnt(3)\n"
"v_dot2c_f32_bf16 v192, s88, v176\n"
"v_dot2c_f32_bf16 v193, s65, v176\n"
"v_dot2c_f32_bf16 v194, s88, v177\n"
"v_dot2c_f32_bf16 v195, s65, v177\n"
"v_dot2c_f32_bf16 v196, s88, v178\n"
"v_dot2c_f32_bf16 v197, s65, v178\n"
"v_dot2c_f32_bf16 v198, s88, v179\n"
"v_dot2c_f32_bf16 v199, s65, v179\n"
"s_waitcnt vmcnt(2)\n"
"v_dot2c_f32_bf16 v200, s88, v180\n"
"v_dot2c_f32_bf16 v201, s65, v180\n"
"v_dot2c_f32_bf16 v202, s88, v181\n"
"v_dot2c_f32_bf16 v203, s65, v181\n"
"v_dot2c_f32_bf16 v204, s88, v182\n"
"v_dot2c_f32_bf16 v205, s65, v182\n"
"v_dot2c_f32_bf16 v206, s88, v183\n"
"v_dot2c_f32_bf16 v207, s65, v183\n"
"s_waitcnt vmcnt(1)\n"
"v_dot2c_f32_bf16 v208, s88, v184\n"
"v_dot2c_f32_bf16 v209, s65, v184\n"
"v_dot2c_f32_bf16 v210, s88, v185\n"
"v_dot2c_f32_bf16 v211, s65, v185\n"
"v_dot2c_f32_bf16 v212, s88, v186\n"
"v_dot2c_f32_bf16 v213, s65, v186\n"
"v_dot2c_f32_bf16 v214, s88, v187\n"
"v_dot2c_f32_bf16 v215, s65, v187\n"
"s_waitcnt vmcnt(0)\n"
"v_dot2c_f32_bf16 v216, s88, v188\n"
"v_dot2c_f32_bf16 v217, s65, v188\n"
"v_dot2c_f32_bf16 v218, s88, v189\n"
"v_dot2c_f32_bf16 v219, s65, v189\n"
"v_dot2c_f32_bf16 v220, s88, v190\n"
"v_dot2c_f32_bf16 v221, s65, v190\n"
"v_dot2c_f32_bf16 v222, s88, v191\n"
"v_dot2c_f32_bf16 v223, s65, v191\n"
"s_xor_b32 s59, s59, 64\n"
"L_skip_38_%=:\n"
"s_cmp_lg_u32 s59, 0\n"
"s_cbranch_scc1 L_late_29_%=\n"
"s_nop 2\n"
"v_pk_mul_f32 v[112:113], s[86:87], v[192:193] op_sel_hi:[0,1]\n"
"v_pk_mul_f32 v[114:115], s[86:87], v[194:195] op_sel_hi:[0,1]\n"
"v_pk_mul_f32 v[116:117], s[86:87], v[196:197] op_sel_hi:[0,1]\n"
"v_pk_mul_f32 v[118:119], s[86:87], v[198:199] op_sel_hi:[0,1]\n"
"v_pk_mul_f32 v[120:121], s[86:87], v[200:201] op_sel_hi:[0,1]\n"
"v_pk_mul_f32 v[122:123], s[86:87], v[202:203] op_sel_hi:[0,1]\n"
"v_pk_mul_f32 v[124:125], s[86:87], v[204:205] op_sel_hi:[0,1]\n"
"v_pk_mul_f32 v[126:127], s[86:87], v[206:207] op_sel_hi:[0,1]\n"
"v_pk_mul_f32 v[128:129], s[86:87], v[208:209] op_sel_hi:[0,1]\n"
"v_pk_mul_f32 v[130:131], s[86:87], v[210:211] op_sel_hi:[0,1]\n"
"v_pk_mul_f32 v[132:133], s[86:87], v[212:213] op_sel_hi:[0,1]\n"
"v_pk_mul_f32 v[134:135], s[86:87], v[214:215] op_sel_hi:[0,1]\n"
"v_pk_mul_f32 v[136:137], s[86:87], v[216:217] op_sel_hi:[0,1]\n"
"v_pk_mul_f32 v[138:139], s[86:87], v[218:219] op_sel_hi:[0,1]\n"
"v_pk_mul_f32 v[140:141], s[86:87], v[220:221] op_sel_hi:[0,1]\n"
"v_pk_mul_f32 v[142:143], s[86:87], v[222:223] op_sel_hi:[0,1]\n"
"v_exp_f32 v112, v112\n"
"v_exp_f32 v113, v113\n"
"v_exp_f32 v114, v114\n"
"v_exp_f32 v115, v115\n"
"v_exp_f32 v116, v116\n"
"v_exp_f32 v117, v117\n"
"v_exp_f32 v118, v118\n"
"v_exp_f32 v119, v119\n"
"v_exp_f32 v120, v120\n"
"v_exp_f32 v121, v121\n"
"v_exp_f32 v122, v122\n"
"v_exp_f32 v123, v123\n"
"v_exp_f32 v124, v124\n"
"v_exp_f32 v125, v125\n"
"v_exp_f32 v126, v126\n"
"v_exp_f32 v127, v127\n"
"v_exp_f32 v128, v128\n"
"v_exp_f32 v129, v129\n"
"v_exp_f32 v130, v130\n"
"v_exp_f32 v131, v131\n"
"v_exp_f32 v132, v132\n"
"v_exp_f32 v133, v133\n"
"v_exp_f32 v134, v134\n"
"v_exp_f32 v135, v135\n"
"v_exp_f32 v136, v136\n"
"v_exp_f32 v137, v137\n"
"v_exp_f32 v138, v138\n"
"v_exp_f32 v139, v139\n"
"v_exp_f32 v140, v140\n"
"v_exp_f32 v141, v141\n"
"v_exp_f32 v142, v142\n"
"v_exp_f32 v143, v143\n"
"v_pk_mul_f32 v[224:225], v[224:225], v[192:193]\n"
"v_pk_mul_f32 v[226:227], v[226:227], v[194:195]\n"
"v_pk_mul_f32 v[228:229], v[228:229], v[196:197]\n"
"v_pk_mul_f32 v[230:231], v[230:231], v[198:199]\n"
"v_pk_mul_f32 v[232:233], v[232:233], v[200:201]\n"
"v_pk_mul_f32 v[234:235], v[234:235], v[202:203]\n"
"v_pk_mul_f32 v[236:237], v[236:237], v[204:205]\n"
"v_pk_mul_f32 v[238:239], v[238:239], v[206:207]\n"
"v_pk_mul_f32 v[240:241], v[240:241], v[208:209]\n"
"v_pk_mul_f32 v[242:243], v[242:243], v[210:211]\n"
"v_pk_mul_f32 v[244:245], v[244:245], v[212:213]\n"
"v_pk_mul_f32 v[246:247], v[246:247], v[214:215]\n"
"v_pk_mul_f32 v[248:249], v[248:249], v[216:217]\n"
"v_pk_mul_f32 v[250:251], v[250:251], v[218:219]\n"
"v_pk_mul_f32 v[252:253], v[252:253], v[220:221]\n"
"v_pk_mul_f32 v[254:255], v[254:255], v[222:223]\n"
"v_pk_add_f32 v[112:113], 1.0, v[112:113] op_sel_hi:[0,1]\n"
"v_pk_add_f32 v[114:115], 1.0, v[114:115] op_sel_hi:[0,1]\n"
"v_pk_add_f32 v[116:117], 1.0, v[116:117] op_sel_hi:[0,1]\n"
"v_pk_add_f32 v[118:119], 1.0, v[118:119] op_sel_hi:[0,1]\n"
"v_pk_add_f32 v[120:121], 1.0, v[120:121] op_sel_hi:[0,1]\n"
"v_pk_add_f32 v[122:123], 1.0, v[122:123] op_sel_hi:[0,1]\n"
"v_pk_add_f32 v[124:125], 1.0, v[124:125] op_sel_hi:[0,1]\n"
"v_pk_add_f32 v[126:127], 1.0, v[126:127] op_sel_hi:[0,1]\n"
"v_pk_add_f32 v[128:129], 1.0, v[128:129] op_sel_hi:[0,1]\n"
"v_pk_add_f32 v[130:131], 1.0, v[130:131] op_sel_hi:[0,1]\n"
"v_pk_add_f32 v[132:133], 1.0, v[132:133] op_sel_hi:[0,1]\n"
"v_pk_add_f32 v[134:135], 1.0, v[134:135] op_sel_hi:[0,1]\n"
"v_pk_add_f32 v[136:137], 1.0, v[136:137] op_sel_hi:[0,1]\n"
"v_pk_add_f32 v[138:139], 1.0, v[138:139] op_sel_hi:[0,1]\n"
"v_pk_add_f32 v[140:141], 1.0, v[140:141] op_sel_hi:[0,1]\n"
"v_pk_add_f32 v[142:143], 1.0, v[142:143] op_sel_hi:[0,1]\n"
"v_rcp_f32 v112, v112\n"
"v_rcp_f32 v113, v113\n"
"v_rcp_f32 v114, v114\n"
"v_rcp_f32 v115, v115\n"
"v_rcp_f32 v116, v116\n"
"v_rcp_f32 v117, v117\n"
"v_rcp_f32 v118, v118\n"
"v_rcp_f32 v119, v119\n"
"v_rcp_f32 v120, v120\n"
"v_rcp_f32 v121, v121\n"
"v_rcp_f32 v122, v122\n"
"v_rcp_f32 v123, v123\n"
"v_rcp_f32 v124, v124\n"
"v_rcp_f32 v125, v125\n"
"v_rcp_f32 v126, v126\n"
"v_rcp_f32 v127, v127\n"
"v_rcp_f32 v128, v128\n"
"v_rcp_f32 v129, v129\n"
"v_rcp_f32 v130, v130\n"
"v_rcp_f32 v131, v131\n"
"v_rcp_f32 v132, v132\n"
"v_rcp_f32 v133, v133\n"
"v_rcp_f32 v134, v134\n"
"v_rcp_f32 v135, v135\n"
"v_rcp_f32 v136, v136\n"
"v_rcp_f32 v137, v137\n"
"v_rcp_f32 v138, v138\n"
"v_rcp_f32 v139, v139\n"
"v_rcp_f32 v140, v140\n"
"v_rcp_f32 v141, v141\n"
"v_rcp_f32 v142, v142\n"
"v_rcp_f32 v143, v143\n"
"v_pk_fma_f32 v[224:225], v[224:225], v[112:113], v[80:81]\n"
"v_pk_fma_f32 v[226:227], v[226:227], v[114:115], v[82:83]\n"
"v_pk_fma_f32 v[228:229], v[228:229], v[116:117], v[84:85]\n"
"v_pk_fma_f32 v[230:231], v[230:231], v[118:119], v[86:87]\n"
"v_pk_fma_f32 v[232:233], v[232:233], v[120:121], v[88:89]\n"
"v_pk_fma_f32 v[234:235], v[234:235], v[122:123], v[90:91]\n"
"v_pk_fma_f32 v[236:237], v[236:237], v[124:125], v[92:93]\n"
"v_pk_fma_f32 v[238:239], v[238:239], v[126:127], v[94:95]\n"
"v_pk_fma_f32 v[240:241], v[240:241], v[128:129], v[96:97]\n"
"v_pk_fma_f32 v[242:243], v[242:243], v[130:131], v[98:99]\n"
"v_pk_fma_f32 v[244:245], v[244:245], v[132:133], v[100:101]\n"
"v_pk_fma_f32 v[246:247], v[246:247], v[134:135], v[102:103]\n"
"v_pk_fma_f32 v[248:249], v[248:249], v[136:137], v[104:105]\n"
"v_pk_fma_f32 v[250:251], v[250:251], v[138:139], v[106:107]\n"
"v_pk_fma_f32 v[252:253], v[252:253], v[140:141], v[108:109]\n"
"v_pk_fma_f32 v[254:255], v[254:255], v[142:143], v[110:111]\n"
"v_cvt_pk_bf16_f32 v224, v224, v225\n"
"v_cvt_pk_bf16_f32 v225, v226, v227\n"
"v_cvt_pk_bf16_f32 v226, v228, v229\n"
"v_cvt_pk_bf16_f32 v227, v230, v231\n"
"v_cvt_pk_bf16_f32 v228, v232, v233\n"
"v_cvt_pk_bf16_f32 v229, v234, v235\n"
"v_cvt_pk_bf16_f32 v230, v236, v237\n"
"v_cvt_pk_bf16_f32 v231, v238, v239\n"
"v_cvt_pk_bf16_f32 v232, v240, v241\n"
"v_cvt_pk_bf16_f32 v233, v242, v243\n"
"v_cvt_pk_bf16_f32 v234, v244, v245\n"
"v_cvt_pk_bf16_f32 v235, v246, v247\n"
"v_cvt_pk_bf16_f32 v236, v248, v249\n"
"v_cvt_pk_bf16_f32 v237, v250, v251\n"
"v_cvt_pk_bf16_f32 v238, v252, v253\n"
"v_cvt_pk_bf16_f32 v239, v254, v255\n"
"ds_write_b64 v7, v[224:225]\n"
"ds_write_b64 v7, v[232:233] offset:32\n"
"ds_write_b64 v7, v[226:227] offset:1024\n"
"ds_write_b64 v7, v[234:235] offset:1056\n"
"ds_write_b64 v7, v[228:229] offset:2048\n"
"ds_write_b64 v7, v[236:237] offset:2080\n"
"ds_write_b64 v7, v[230:231] offset:3072\n"
"ds_write_b64 v7, v[238:239] offset:3104\n"
"v_add_u32 v7, s55, v1\n"
"s_waitcnt lgkmcnt(0)\n"
"ds_read_b128 v[112:115], v7\n"
"ds_read_b128 v[116:119], v7 offset:1024\n"
"ds_read_b128 v[120:123], v7 offset:2048\n"
"ds_read_b128 v[124:127], v7 offset:3072\n"
"s_add_u32 s78, s53, 0\n"
"s_waitcnt lgkmcnt(3)\n"
"buffer_store_dwordx4 v[112:115], v4, s[48:51], s78 offen sc1\n"
"s_add_u32 s78, s53, 18432\n"
"s_waitcnt lgkmcnt(2)\n"
"buffer_store_dwordx4 v[116:119], v4, s[48:51], s78 offen sc1\n"
"s_add_u32 s78, s53, 36864\n"
"s_waitcnt lgkmcnt(1)\n"
"buffer_store_dwordx4 v[120:123], v4, s[48:51], s78 offen sc1\n"
"s_add_u32 s78, s53, 55296\n"
"s_waitcnt lgkmcnt(0)\n"
"buffer_store_dwordx4 v[124:127], v4, s[48:51], s78 offen sc1\n"
"s_endpgm\n"
"L_roleF_19_%=:\n"
"v_mfma_f32_16x16x32_bf16 v[16:19], a[224:227], a[128:131], 0\n"
"ds_read_b128 a[248:251], v2 offset:55296\n"
"v_mfma_f32_16x16x32_bf16 v[16:19], a[228:231], a[144:147], v[16:19]\n"
"v_mfma_f32_16x16x32_bf16 v[20:23], a[224:227], a[132:135], 0\n"
"s_add_u32 s85, s83, 114688\n"
"v_mul_f32 v112, v80, v112\n"
"v_mul_f32 v113, v81, v113\n"
"v_mfma_f32_16x16x32_bf16 v[20:23], a[228:231], a[148:151], v[20:23]\n"
"ds_read_b128 a[252:255], v2 offset:56320\n"
"v_mul_f32 v114, v82, v114\n"
"v_mfma_f32_16x16x32_bf16 v[24:27], a[224:227], a[136:139], 0\n"
"v_mul_f32 v115, v83, v115\n"
"v_mul_f32 v80, s86, v80\n"
"v_mfma_f32_16x16x32_bf16 v[24:27], a[228:231], a[152:155], v[24:27]\n"
"buffer_load_dwordx4 a[0:3], v1, s[40:43], s84 offen sc1\n"
"v_mfma_f32_16x16x32_bf16 v[28:31], a[224:227], a[140:143], 0\n"
"s_waitcnt lgkmcnt(4)\n"
"v_mul_f32 v81, s86, v81\n"
"v_mul_f32 v82, s86, v82\n"
"v_mfma_f32_16x16x32_bf16 v[28:31], a[228:231], a[156:159], v[28:31]\n"
"v_mul_f32 v83, s86, v83\n"
"v_mfma_f32_16x16x32_bf16 v[32:35], a[232:235], a[128:131], 0\n"
"ds_read_b128 a[224:227], v2 offset:57344\n"
"v_mfma_f32_16x16x32_bf16 v[32:35], a[236:239], a[144:147], v[32:35]\n"
"v_exp_f32 v80, v80\n"
"v_mfma_f32_16x16x32_bf16 v[36:39], a[232:235], a[132:135], 0\n"
"buffer_load_dwordx4 a[4:7], v1, s[40:43], s84 offen offset:1024 sc1\n"
"v_mfma_f32_16x16x32_bf16 v[36:39], a[236:239], a[148:151], v[36:39]\n"
"v_exp_f32 v81, v81\n"
"v_mfma_f32_16x16x32_bf16 v[40:43], a[232:235], a[136:139], 0\n"
"ds_read_b128 a[228:231], v2 offset:58368\n"
"v_mfma_f32_16x16x32_bf16 v[40:43], a[236:239], a[152:155], v[40:43]\n"
"v_exp_f32 v82, v82\n"
"v_mfma_f32_16x16x32_bf16 v[44:47], a[232:235], a[140:143], 0\n"
"s_waitcnt lgkmcnt(4)\n"
"v_exp_f32 v83, v83\n"
"v_mfma_f32_16x16x32_bf16 v[44:47], a[236:239], a[156:159], v[44:47]\n"
"v_add_f32 v80, 1.0, v80\n"
"v_add_f32 v81, 1.0, v81\n"
"v_mfma_f32_16x16x32_bf16 v[48:51], a[240:243], a[128:131], 0\n"
"buffer_load_dwordx4 a[8:11], v1, s[40:43], s84 offen offset:2048 sc1\n"
"v_mfma_f32_16x16x32_bf16 v[48:51], a[244:247], a[144:147], v[48:51]\n"
"ds_read_b128 a[232:235], v2 offset:59392\n"
"v_add_f32 v82, 1.0, v82\n"
"v_mfma_f32_16x16x32_bf16 v[52:55], a[240:243], a[132:135], 0\n"
"v_add_f32 v83, 1.0, v83\n"
"v_mfma_f32_16x16x32_bf16 v[52:55], a[244:247], a[148:151], v[52:55]\n"
"v_rcp_f32 v80, v80\n"
"v_mfma_f32_16x16x32_bf16 v[56:59], a[240:243], a[136:139], 0\n"
"v_rcp_f32 v81, v81\n"
"v_mfma_f32_16x16x32_bf16 v[56:59], a[244:247], a[152:155], v[56:59]\n"
"ds_read_b128 a[236:239], v2 offset:60416\n"
"buffer_load_dwordx4 a[12:15], v1, s[40:43], s84 offen offset:3072 sc1\n"
"v_rcp_f32 v82, v82\n"
"v_mfma_f32_16x16x32_bf16 v[60:63], a[240:243], a[140:143], 0\n"
"s_waitcnt lgkmcnt(4)\n"
"v_rcp_f32 v83, v83\n"
"v_mfma_f32_16x16x32_bf16 v[60:63], a[244:247], a[156:159], v[60:63]\n"
"s_add_u32 s84, s84, 4096\n"
"v_mul_f32 v112, v112, v80\n"
"v_mul_f32 v113, v113, v81\n"
"v_mfma_f32_16x16x32_bf16 v[64:67], a[248:251], a[128:131], 0\n"
"ds_read_b128 a[240:243], v2 offset:61440\n"
"v_mul_f32 v114, v114, v82\n"
"v_mfma_f32_16x16x32_bf16 v[64:67], a[252:255], a[144:147], v[64:67]\n"
"buffer_load_dwordx4 a[16:19], v1, s[40:43], s84 offen sc1\n"
"v_mfma_f32_16x16x32_bf16 v[68:71], a[248:251], a[132:135], 0\n"
"v_mul_f32 v115, v115, v83\n"
"v_mul_f32 v128, v96, v128\n"
"v_mfma_f32_16x16x32_bf16 v[68:71], a[252:255], a[148:151], v[68:71]\n"
"ds_read_b128 a[244:247], v2 offset:62464\n"
"v_mul_f32 v129, v97, v129\n"
"v_mfma_f32_16x16x32_bf16 v[72:75], a[248:251], a[136:139], 0\n"
"v_mul_f32 v130, v98, v130\n"
"v_mul_f32 v131, v99, v131\n"
"v_mfma_f32_16x16x32_bf16 v[72:75], a[252:255], a[152:155], v[72:75]\n"
"buffer_load_dwordx4 a[20:23], v1, s[40:43], s84 offen offset:1024 sc1\n"
"v_mfma_f32_16x16x32_bf16 v[76:79], a[248:251], a[140:143], 0\n"
"s_waitcnt lgkmcnt(4)\n"
"v_mul_f32 v96, s86, v96\n"
"v_mul_f32 v97, s86, v97\n"
"v_mfma_f32_16x16x32_bf16 v[76:79], a[252:255], a[156:159], v[76:79]\n"
"v_mul_f32 v98, s86, v98\n"
"v_mul_f32 v99, s86, v99\n"
"v_mfma_f32_16x16x32_bf16 v[144:147], a[224:227], a[128:131], 0\n"
"v_exp_f32 v96, v96\n"
"v_mfma_f32_16x16x32_bf16 v[144:147], a[228:231], a[144:147], v[144:147]\n"
"ds_read_b128 a[248:251], v2 offset:63488\n"
"v_mfma_f32_16x16x32_bf16 v[148:151], a[224:227], a[132:135], 0\n"
"v_exp_f32 v97, v97\n"
"v_mfma_f32_16x16x32_bf16 v[148:151], a[228:231], a[148:151], v[148:151]\n"
"buffer_load_dwordx4 a[24:27], v1, s[40:43], s84 offen offset:2048 sc1\n"
"v_mfma_f32_16x16x32_bf16 v[152:155], a[224:227], a[136:139], 0\n"
"v_exp_f32 v98, v98\n"
"v_mfma_f32_16x16x32_bf16 v[152:155], a[228:231], a[152:155], v[152:155]\n"
"ds_read_b128 a[252:255], v2 offset:64512\n"
"v_exp_f32 v99, v99\n"
"v_mfma_f32_16x16x32_bf16 v[156:159], a[224:227], a[140:143], 0\n"
"s_waitcnt lgkmcnt(4)\n"
"v_add_f32 v96, 1.0, v96\n"
"v_add_f32 v97, 1.0, v97\n"
"v_mfma_f32_16x16x32_bf16 v[156:159], a[228:231], a[156:159], v[156:159]\n"
"s_barrier\n"
"buffer_load_dwordx4 a[28:31], v1, s[40:43], s84 offen offset:3072 sc1\n"
"v_mfma_f32_16x16x32_bf16 v[160:163], a[232:235], a[128:131], 0\n"
"s_add_u32 s84, s84, 4096\n"
"v_add_f32 v98, 1.0, v98\n"
"v_add_f32 v99, 1.0, v99\n"
"v_mfma_f32_16x16x32_bf16 v[160:163], a[236:239], a[144:147], v[160:163]\n"
"v_rcp_f32 v96, v96\n"
"v_mfma_f32_16x16x32_bf16 v[164:167], a[232:235], a[132:135], 0\n"
"ds_read_b128 a[224:227], v3\n"
"v_mfma_f32_16x16x32_bf16 v[164:167], a[236:239], a[148:151], v[164:167]\n"
"v_rcp_f32 v97, v97\n"
"v_mfma_f32_16x16x32_bf16 v[168:171], a[232:235], a[136:139], 0\n"
"v_rcp_f32 v98, v98\n"
"v_mfma_f32_16x16x32_bf16 v[168:171], a[236:239], a[152:155], v[168:171]\n"
"ds_read_b128 a[228:231], v3 offset:1024\n"
"v_mfma_f32_16x16x32_bf16 v[172:175], a[232:235], a[140:143], 0\n"
"s_waitcnt lgkmcnt(4)\n"
"v_rcp_f32 v99, v99\n"
"v_mfma_f32_16x16x32_bf16 v[172:175], a[236:239], a[156:159], v[172:175]\n"
"v_mul_f32 v128, v128, v96\n"
"v_mul_f32 v129, v129, v97\n"
"v_mfma_f32_16x16x32_bf16 v[176:179], a[240:243], a[128:131], 0\n"
"v_mul_f32 v130, v130, v98\n"
"v_mul_f32 v131, v131, v99\n"
"v_mfma_f32_16x16x32_bf16 v[176:179], a[244:247], a[144:147], v[176:179]\n"
"ds_read_b128 a[232:235], v3 offset:2048\n"
"v_cvt_pk_bf16_f32 v80, v112, v113\n"
"v_mfma_f32_16x16x32_bf16 v[180:183], a[240:243], a[132:135], 0\n"
"v_cvt_pk_bf16_f32 v81, v114, v115\n"
"v_cvt_pk_bf16_f32 v82, v128, v129\n"
"v_mfma_f32_16x16x32_bf16 v[180:183], a[244:247], a[148:151], v[180:183]\n"
"ds_read_b128 a[236:239], v3 offset:3072\n"
"v_cvt_pk_bf16_f32 v83, v130, v131\n"
"v_mfma_f32_16x16x32_bf16 v[184:187], a[240:243], a[136:139], 0\n"
"buffer_store_dwordx4 v[80:83], v1, s[40:43], s85 offen sc1\n"
"v_mfma_f32_16x16x32_bf16 v[184:187], a[244:247], a[152:155], v[184:187]\n"
"v_mul_f32 v116, v84, v116\n"
"v_mul_f32 v117, v85, v117\n"
"v_mfma_f32_16x16x32_bf16 v[188:191], a[240:243], a[140:143], 0\n"
"s_waitcnt lgkmcnt(4)\n"
"v_mul_f32 v118, v86, v118\n"
"v_mul_f32 v119, v87, v119\n"
"v_mfma_f32_16x16x32_bf16 v[188:191], a[244:247], a[156:159], v[188:191]\n"
"v_mul_f32 v84, s86, v84\n"
"v_mul_f32 v85, s86, v85\n"
"v_mfma_f32_16x16x32_bf16 v[192:195], a[248:251], a[128:131], 0\n"
"ds_read_b128 a[240:243], v3 offset:4096\n"
"v_mul_f32 v86, s86, v86\n"
"v_mfma_f32_16x16x32_bf16 v[192:195], a[252:255], a[144:147], v[192:195]\n"
"v_mul_f32 v87, s86, v87\n"
"v_mfma_f32_16x16x32_bf16 v[196:199], a[248:251], a[132:135], 0\n"
"v_exp_f32 v84, v84\n"
"v_mfma_f32_16x16x32_bf16 v[196:199], a[252:255], a[148:151], v[196:199]\n"
"ds_read_b128 a[244:247], v3 offset:5120\n"
"v_mfma_f32_16x16x32_bf16 v[200:203], a[248:251], a[136:139], 0\n"
"s_lshl_b32 s78, s73, 5\n"
"v_exp_f32 v85, v85\n"
"v_mfma_f32_16x16x32_bf16 v[200:203], a[252:255], a[152:155], v[200:203]\n"
"s_add_u32 s78, s78, s68\n"
"buffer_load_dwordx4 a[32:35], v1, s[40:43], s84 offen sc1\n"
"v_mfma_f32_16x16x32_bf16 v[204:207], a[248:251], a[140:143], 0\n"
"s_waitcnt lgkmcnt(4)\n"
"v_exp_f32 v86, v86\n"
"v_mfma_f32_16x16x32_bf16 v[204:207], a[252:255], a[156:159], v[204:207]\n"
"s_lshl_b32 s78, s78, 6\n"
"v_exp_f32 v87, v87\n"
"v_mfma_f32_16x16x32_bf16 v[208:211], a[224:227], a[128:131], 0\n"
"s_add_u32 s78, s78, 79101952\n"
"ds_read_b128 a[248:251], v3 offset:6144\n"
"v_add_f32 v84, 1.0, v84\n"
"v_mfma_f32_16x16x32_bf16 v[208:211], a[228:231], a[144:147], v[208:211]\n"
"buffer_load_dwordx4 a[36:39], v1, s[40:43], s84 offen offset:1024 sc1\n"
"v_mfma_f32_16x16x32_bf16 v[212:215], a[224:227], a[132:135], 0\n"
"v_add_f32 v85, 1.0, v85\n"
"v_mov_b32 v8, s92\n"
"v_mov_b32 v9, s93\n"
"v_mfma_f32_16x16x32_bf16 v[212:215], a[228:231], a[148:151], v[212:215]\n"
"s_waitcnt vmcnt(17)\n"
"buffer_store_dwordx2 v[8:9], v14, s[40:43], s78 offen sc1\n"
"v_add_f32 v86, 1.0, v86\n"
"v_add_f32 v87, 1.0, v87\n"
"v_mfma_f32_16x16x32_bf16 v[216:219], a[224:227], a[136:139], 0\n"
"v_rcp_f32 v84, v84\n"
"v_mfma_f32_16x16x32_bf16 v[216:219], a[228:231], a[152:155], v[216:219]\n"
"ds_read_b128 a[252:255], v3 offset:7168\n"
"v_mfma_f32_16x16x32_bf16 v[220:223], a[224:227], a[140:143], 0\n"
"s_waitcnt lgkmcnt(4)\n"
"v_rcp_f32 v85, v85\n"
"v_mfma_f32_16x16x32_bf16 v[220:223], a[228:231], a[156:159], v[220:223]\n"
"buffer_load_dwordx4 a[40:43], v1, s[40:43], s84 offen offset:2048 sc1\n"
"v_rcp_f32 v86, v86\n"
"v_mfma_f32_16x16x32_bf16 v[16:19], a[232:235], a[160:163], v[16:19]\n"
"v_rcp_f32 v87, v87\n"
"v_mfma_f32_16x16x32_bf16 v[16:19], a[236:239], a[176:179], v[16:19]\n"
"ds_read_b128 a[224:227], v3 offset:8192\n"
"v_mul_f32 v116, v116, v84\n"
"v_mfma_f32_16x16x32_bf16 v[20:23], a[232:235], a[164:167], v[20:23]\n"
"buffer_load_dwordx4 a[44:47], v1, s[40:43], s84 offen offset:3072 sc1\n"
"v_mfma_f32_16x16x32_bf16 v[20:23], a[236:239], a[180:183], v[20:23]\n"
"s_add_u32 s84, s84, 4096\n"
"v_mul_f32 v117, v117, v85\n"
"v_mul_f32 v118, v118, v86\n"
"v_mfma_f32_16x16x32_bf16 v[24:27], a[232:235], a[168:171], v[24:27]\n"
"ds_read_b128 a[228:231], v3 offset:9216\n"
"v_mul_f32 v119, v119, v87\n"
"v_mfma_f32_16x16x32_bf16 v[24:27], a[236:239], a[184:187], v[24:27]\n"
"v_mul_f32 v132, v100, v132\n"
"v_mul_f32 v133, v101, v133\n"
"v_mfma_f32_16x16x32_bf16 v[28:31], a[232:235], a[172:175], v[28:31]\n"
"s_waitcnt lgkmcnt(4)\n"
"v_mul_f32 v134, v102, v134\n"
"v_mul_f32 v135, v103, v135\n"
"v_mfma_f32_16x16x32_bf16 v[28:31], a[236:239], a[188:191], v[28:31]\n"
"v_mul_f32 v100, s86, v100\n"
"v_mul_f32 v101, s86, v101\n"
"v_mfma_f32_16x16x32_bf16 v[32:35], a[240:243], a[160:163], v[32:35]\n"
"buffer_load_dwordx4 a[48:51], v1, s[40:43], s84 offen sc1\n"
"v_mfma_f32_16x16x32_bf16 v[32:35], a[244:247], a[176:179], v[32:35]\n"
"ds_read_b128 a[232:235], v3 offset:10240\n"
"v_mul_f32 v102, s86, v102\n"
"v_mfma_f32_16x16x32_bf16 v[36:39], a[240:243], a[164:167], v[36:39]\n"
"v_mul_f32 v103, s86, v103\n"
"v_exp_f32 v100, v100\n"
"v_mfma_f32_16x16x32_bf16 v[36:39], a[244:247], a[180:183], v[36:39]\n"
"buffer_load_dwordx4 a[52:55], v1, s[40:43], s84 offen offset:1024 sc1\n"
"v_mfma_f32_16x16x32_bf16 v[40:43], a[240:243], a[168:171], v[40:43]\n"
"v_exp_f32 v101, v101\n"
"v_mfma_f32_16x16x32_bf16 v[40:43], a[244:247], a[184:187], v[40:43]\n"
"ds_read_b128 a[236:239], v3 offset:11264\n"
"v_exp_f32 v102, v102\n"
"v_mfma_f32_16x16x32_bf16 v[44:47], a[240:243], a[172:175], v[44:47]\n"
"s_waitcnt lgkmcnt(4)\n"
"v_exp_f32 v103, v103\n"
"v_mfma_f32_16x16x32_bf16 v[44:47], a[244:247], a[188:191], v[44:47]\n"
"v_add_f32 v100, 1.0, v100\n"
"v_add_f32 v101, 1.0, v101\n"
"v_mfma_f32_16x16x32_bf16 v[48:51], a[248:251], a[160:163], v[48:51]\n"
"buffer_load_dwordx4 a[56:59], v1, s[40:43], s84 offen offset:2048 sc1\n"
"v_mfma_f32_16x16x32_bf16 v[48:51], a[252:255], a[176:179], v[48:51]\n"
"ds_read_b128 a[240:243], v3 offset:12288\n"
"v_add_f32 v102, 1.0, v102\n"
"v_mfma_f32_16x16x32_bf16 v[52:55], a[248:251], a[164:167], v[52:55]\n"
"v_add_f32 v103, 1.0, v103\n"
"v_mfma_f32_16x16x32_bf16 v[52:55], a[252:255], a[180:183], v[52:55]\n"
"v_rcp_f32 v100, v100\n"
"v_mfma_f32_16x16x32_bf16 v[56:59], a[248:251], a[168:171], v[56:59]\n"
"v_rcp_f32 v101, v101\n"
"v_mfma_f32_16x16x32_bf16 v[56:59], a[252:255], a[184:187], v[56:59]\n"
"ds_read_b128 a[244:247], v3 offset:13312\n"
"buffer_load_dwordx4 a[60:63], v1, s[40:43], s84 offen offset:3072 sc1\n"
"v_mfma_f32_16x16x32_bf16 v[60:63], a[248:251], a[172:175], v[60:63]\n"
"s_waitcnt lgkmcnt(4)\n"
"v_rcp_f32 v102, v102\n"
"v_mfma_f32_16x16x32_bf16 v[60:63], a[252:255], a[188:191], v[60:63]\n"
"s_add_u32 s84, s84, 4096\n"
"v_rcp_f32 v103, v103\n"
"v_mfma_f32_16x16x32_bf16 v[64:67], a[224:227], a[160:163], v[64:67]\n"
"v_mul_f32 v132, v132, v100\n"
"v_mul_f32 v133, v133, v101\n"
"v_mfma_f32_16x16x32_bf16 v[64:67], a[228:231], a[176:179], v[64:67]\n"
"ds_read_b128 a[248:251], v3 offset:14336\n"
"v_mul_f32 v134, v134, v102\n"
"v_mfma_f32_16x16x32_bf16 v[68:71], a[224:227], a[164:167], v[68:71]\n"
"v_mul_f32 v135, v135, v103\n"
"v_cvt_pk_bf16_f32 v84, v116, v117\n"
"v_mfma_f32_16x16x32_bf16 v[68:71], a[228:231], a[180:183], v[68:71]\n"
"ds_read_b128 a[252:255], v3 offset:15360\n"
"v_cvt_pk_bf16_f32 v85, v118, v119\n"
"v_mfma_f32_16x16x32_bf16 v[72:75], a[224:227], a[168:171], v[72:75]\n"
"v_cvt_pk_bf16_f32 v86, v132, v133\n"
"v_cvt_pk_bf16_f32 v87, v134, v135\n"
"v_mfma_f32_16x16x32_bf16 v[72:75], a[228:231], a[184:187], v[72:75]\n"
"buffer_store_dwordx4 v[84:87], v1, s[40:43], s85 offen offset:1024 sc1\n"
"v_mfma_f32_16x16x32_bf16 v[76:79], a[224:227], a[172:175], v[76:79]\n"
"s_waitcnt lgkmcnt(4)\n"
"v_mul_f32 v120, v88, v120\n"
"v_mul_f32 v121, v89, v121\n"
"v_mfma_f32_16x16x32_bf16 v[76:79], a[228:231], a[188:191], v[76:79]\n"
"v_mul_f32 v122, v90, v122\n"
"v_mul_f32 v123, v91, v123\n"
"v_mfma_f32_16x16x32_bf16 v[144:147], a[232:235], a[160:163], v[144:147]\n"
"ds_read_b128 a[224:227], v3 offset:16384\n"
"v_mul_f32 v88, s86, v88\n"
"v_mfma_f32_16x16x32_bf16 v[144:147], a[236:239], a[176:179], v[144:147]\n"
"v_mul_f32 v89, s86, v89\n"
"v_mul_f32 v90, s86, v90\n"
"v_mfma_f32_16x16x32_bf16 v[148:151], a[232:235], a[164:167], v[148:151]\n"
"v_mul_f32 v91, s86, v91\n"
"v_mfma_f32_16x16x32_bf16 v[148:151], a[236:239], a[180:183], v[148:151]\n"
"ds_read_b128 a[228:231], v3 offset:17408\n"
"v_mfma_f32_16x16x32_bf16 v[152:155], a[232:235], a[168:171], v[152:155]\n"
"v_exp_f32 v88, v88\n"
"v_mfma_f32_16x16x32_bf16 v[152:155], a[236:239], a[184:187], v[152:155]\n"
"v_exp_f32 v89, v89\n"
"v_mfma_f32_16x16x32_bf16 v[156:159], a[232:235], a[172:175], v[156:159]\n"
"s_waitcnt lgkmcnt(4)\n"
"v_exp_f32 v90, v90\n"
"v_mfma_f32_16x16x32_bf16 v[156:159], a[236:239], a[188:191], v[156:159]\n"
"v_exp_f32 v91, v91\n"
"v_mfma_f32_16x16x32_bf16 v[160:163], a[240:243], a[160:163], v[160:163]\n"
"ds_read_b128 a[232:235], v3 offset:18432\n"
"v_add_f32 v88, 1.0, v88\n"
"v_mfma_f32_16x16x32_bf16 v[160:163], a[244:247], a[176:179], v[160:163]\n"
"buffer_load_dwordx4 a[64:67], v1, s[40:43], s84 offen sc1\n"
"v_mfma_f32_16x16x32_bf16 v[164:167], a[240:243], a[164:167], v[164:167]\n"
"v_add_f32 v89, 1.0, v89\n"
"v_add_f32 v90, 1.0, v90\n"
"v_mfma_f32_16x16x32_bf16 v[164:167], a[244:247], a[180:183], v[164:167]\n"
"ds_read_b128 a[236:239], v3 offset:19456\n"
"v_add_f32 v91, 1.0, v91\n"
"v_mfma_f32_16x16x32_bf16 v[168:171], a[240:243], a[168:171], v[168:171]\n"
"v_rcp_f32 v88, v88\n"
"v_mfma_f32_16x16x32_bf16 v[168:171], a[244:247], a[184:187], v[168:171]\n"
"v_rcp_f32 v89, v89\n"
"v_mfma_f32_16x16x32_bf16 v[172:175], a[240:243], a[172:175], v[172:175]\n"
"s_waitcnt lgkmcnt(4)\n"
"buffer_load_dwordx4 a[68:71], v1, s[40:43], s84 offen offset:1024 sc1\n"
"v_mfma_f32_16x16x32_bf16 v[172:175], a[244:247], a[188:191], v[172:175]\n"
"v_rcp_f32 v90, v90\n"
"v_mfma_f32_16x16x32_bf16 v[176:179], a[248:251], a[160:163], v[176:179]\n"
"ds_read_b128 a[240:243], v3 offset:20480\n"
"v_mfma_f32_16x16x32_bf16 v[176:179], a[252:255], a[176:179], v[176:179]\n"
"v_rcp_f32 v91, v91\n"
"v_mfma_f32_16x16x32_bf16 v[180:183], a[248:251], a[164:167], v[180:183]\n"
"v_mul_f32 v120, v120, v88\n"
"v_mul_f32 v121, v121, v89\n"
"v_mfma_f32_16x16x32_bf16 v[180:183], a[252:255], a[180:183], v[180:183]\n"
"ds_read_b128 a[244:247], v3 offset:21504\n"
"v_mul_f32 v122, v122, v90\n"
"v_mfma_f32_16x16x32_bf16 v[184:187], a[248:251], a[168:171], v[184:187]\n"
"buffer_load_dwordx4 a[72:75], v1, s[40:43], s84 offen offset:2048 sc1\n"
"v_mfma_f32_16x16x32_bf16 v[184:187], a[252:255], a[184:187], v[184:187]\n"
"v_mul_f32 v123, v123, v91\n"
"v_mul_f32 v136, v104, v136\n"
"v_mfma_f32_16x16x32_bf16 v[188:191], a[248:251], a[172:175], v[188:191]\n"
"s_waitcnt lgkmcnt(4)\n"
"v_mul_f32 v137, v105, v137\n"
"v_mul_f32 v138, v106, v138\n"
"v_mfma_f32_16x16x32_bf16 v[188:191], a[252:255], a[188:191], v[188:191]\n"
"v_mul_f32 v139, v107, v139\n"
"v_mul_f32 v104, s86, v104\n"
"v_mfma_f32_16x16x32_bf16 v[192:195], a[224:227], a[160:163], v[192:195]\n"
"buffer_load_dwordx4 a[76:79], v1, s[40:43], s84 offen offset:3072 sc1\n"
"v_mfma_f32_16x16x32_bf16 v[192:195], a[228:231], a[176:179], v[192:195]\n"
"s_add_u32 s84, s84, 4096\n"
"ds_read_b128 a[248:251], v3 offset:22528\n"
"v_mul_f32 v105, s86, v105\n"
"v_mfma_f32_16x16x32_bf16 v[196:199], a[224:227], a[164:167], v[196:199]\n"
"v_mul_f32 v106, s86, v106\n"
"v_mul_f32 v107, s86, v107\n"
"v_mfma_f32_16x16x32_bf16 v[196:199], a[228:231], a[180:183], v[196:199]\n"
"v_exp_f32 v104, v104\n"
"v_mfma_f32_16x16x32_bf16 v[200:203], a[224:227], a[168:171], v[200:203]\n"
"ds_read_b128 a[252:255], v3 offset:23552\n"
"v_mfma_f32_16x16x32_bf16 v[200:203], a[228:231], a[184:187], v[200:203]\n"
"v_exp_f32 v105, v105\n"
"v_mfma_f32_16x16x32_bf16 v[204:207], a[224:227], a[172:175], v[204:207]\n"
"s_waitcnt lgkmcnt(4)\n"
"buffer_load_dwordx4 a[80:83], v1, s[40:43], s84 offen sc1\n"
"v_mfma_f32_16x16x32_bf16 v[204:207], a[228:231], a[188:191], v[204:207]\n"
"v_exp_f32 v106, v106\n"
"v_mfma_f32_16x16x32_bf16 v[208:211], a[232:235], a[160:163], v[208:211]\n"
"v_exp_f32 v107, v107\n"
"v_mfma_f32_16x16x32_bf16 v[208:211], a[236:239], a[176:179], v[208:211]\n"
"ds_read_b128 a[224:227], v3 offset:24576\n"
"v_add_f32 v104, 1.0, v104\n"
"v_mfma_f32_16x16x32_bf16 v[212:215], a[232:235], a[164:167], v[212:215]\n"
"buffer_load_dwordx4 a[84:87], v1, s[40:43], s84 offen offset:1024 sc1\n"
"v_add_f32 v105, 1.0, v105\n"
"v_mfma_f32_16x16x32_bf16 v[212:215], a[236:239], a[180:183], v[212:215]\n"
"v_add_f32 v106, 1.0, v106\n"
"v_add_f32 v107, 1.0, v107\n"
"v_mfma_f32_16x16x32_bf16 v[216:219], a[232:235], a[168:171], v[216:219]\n"
"v_rcp_f32 v104, v104\n"
"v_mfma_f32_16x16x32_bf16 v[216:219], a[236:239], a[184:187], v[216:219]\n"
"ds_read_b128 a[228:231], v3 offset:25600\n"
"v_mfma_f32_16x16x32_bf16 v[220:223], a[232:235], a[172:175], v[220:223]\n"
"s_waitcnt vmcnt(29) lgkmcnt(4)\n"
"v_rcp_f32 v105, v105\n"
"v_mfma_f32_16x16x32_bf16 v[220:223], a[236:239], a[188:191], v[220:223]\n"
"buffer_load_dwordx4 a[88:91], v1, s[40:43], s84 offen offset:2048 sc1\n"
"v_rcp_f32 v106, v106\n"
"v_mfma_f32_16x16x32_bf16 v[16:19], a[240:243], a[192:195], v[16:19]\n"
"v_rcp_f32 v107, v107\n"
"v_mfma_f32_16x16x32_bf16 v[16:19], a[244:247], a[208:211], v[16:19]\n"
"ds_read_b128 a[232:235], v3 offset:26624\n"
"v_mul_f32 v136, v136, v104\n"
"v_mfma_f32_16x16x32_bf16 v[20:23], a[240:243], a[196:199], v[20:23]\n"
"buffer_load_dwordx4 a[92:95], v1, s[40:43], s84 offen offset:3072 sc1\n"
"v_mfma_f32_16x16x32_bf16 v[20:23], a[244:247], a[212:215], v[20:23]\n"
"s_add_u32 s84, s84, 4096\n"
"v_mul_f32 v137, v137, v105\n"
"v_mul_f32 v138, v138, v106\n"
"v_mfma_f32_16x16x32_bf16 v[24:27], a[240:243], a[200:203], v[24:27]\n"
"ds_read_b128 a[236:239], v3 offset:27648\n"
"v_mul_f32 v139, v139, v107\n"
"v_mfma_f32_16x16x32_bf16 v[24:27], a[244:247], a[216:219], v[24:27]\n"
"v_cvt_pk_bf16_f32 v88, v120, v121\n"
"v_cvt_pk_bf16_f32 v89, v122, v123\n"
"v_mfma_f32_16x16x32_bf16 v[28:31], a[240:243], a[204:207], v[28:31]\n"
"s_waitcnt lgkmcnt(4)\n"
"v_cvt_pk_bf16_f32 v90, v136, v137\n"
"v_cvt_pk_bf16_f32 v91, v138, v139\n"
"v_mfma_f32_16x16x32_bf16 v[28:31], a[244:247], a[220:223], v[28:31]\n"
"buffer_store_dwordx4 v[88:91], v1, s[40:43], s85 offen offset:2048 sc1\n"
"v_mfma_f32_16x16x32_bf16 v[32:35], a[248:251], a[192:195], v[32:35]\n"
"ds_read_b128 a[240:243], v3 offset:28672\n"
"v_mul_f32 v124, v92, v124\n"
"v_mfma_f32_16x16x32_bf16 v[32:35], a[252:255], a[208:211], v[32:35]\n"
"v_mul_f32 v125, v93, v125\n"
"v_mul_f32 v126, v94, v126\n"
"v_mfma_f32_16x16x32_bf16 v[36:39], a[248:251], a[196:199], v[36:39]\n"
"v_mul_f32 v127, v95, v127\n"
"v_mul_f32 v92, s86, v92\n"
"v_mfma_f32_16x16x32_bf16 v[36:39], a[252:255], a[212:215], v[36:39]\n"
"ds_read_b128 a[244:247], v3 offset:29696\n"
"v_mul_f32 v93, s86, v93\n"
"v_mfma_f32_16x16x32_bf16 v[40:43], a[248:251], a[200:203], v[40:43]\n"
"v_mul_f32 v94, s86, v94\n"
"v_mul_f32 v95, s86, v95\n"
"v_mfma_f32_16x16x32_bf16 v[40:43], a[252:255], a[216:219], v[40:43]\n"
"v_exp_f32 v92, v92\n"
"v_mfma_f32_16x16x32_bf16 v[44:47], a[248:251], a[204:207], v[44:47]\n"
"s_waitcnt lgkmcnt(4)\n"
"v_exp_f32 v93, v93\n"
"v_mfma_f32_16x16x32_bf16 v[44:47], a[252:255], a[220:223], v[44:47]\n"
"v_exp_f32 v94, v94\n"
"v_mfma_f32_16x16x32_bf16 v[48:51], a[224:227], a[192:195], v[48:51]\n"
"ds_read_b128 a[248:251], v3 offset:30720\n"
"v_mfma_f32_16x16x32_bf16 v[48:51], a[228:231], a[208:211], v[48:51]\n"
"v_exp_f32 v95, v95\n"
"v_mfma_f32_16x16x32_bf16 v[52:55], a[224:227], a[196:199], v[52:55]\n"
"v_add_f32 v92, 1.0, v92\n"
"v_add_f32 v93, 1.0, v93\n"
"v_mfma_f32_16x16x32_bf16 v[52:55], a[228:231], a[212:215], v[52:55]\n"
"ds_read_b128 a[252:255], v3 offset:31744\n"
"v_add_f32 v94, 1.0, v94\n"
"v_mfma_f32_16x16x32_bf16 v[56:59], a[224:227], a[200:203], v[56:59]\n"
"v_add_f32 v95, 1.0, v95\n"
"v_mfma_f32_16x16x32_bf16 v[56:59], a[228:231], a[216:219], v[56:59]\n"
"v_rcp_f32 v92, v92\n"
"v_mfma_f32_16x16x32_bf16 v[60:63], a[224:227], a[204:207], v[60:63]\n"
"s_waitcnt lgkmcnt(4)\n"
"v_rcp_f32 v93, v93\n"
"v_mfma_f32_16x16x32_bf16 v[60:63], a[228:231], a[220:223], v[60:63]\n"
"s_barrier\n"
"v_mfma_f32_16x16x32_bf16 v[64:67], a[232:235], a[192:195], v[64:67]\n"
"ds_read_b128 a[224:227], v1\n"
"v_mfma_f32_16x16x32_bf16 v[64:67], a[236:239], a[208:211], v[64:67]\n"
"buffer_load_dwordx4 a[96:99], v1, s[40:43], s84 offen sc1\n"
"v_mfma_f32_16x16x32_bf16 v[68:71], a[232:235], a[196:199], v[68:71]\n"
"v_rcp_f32 v94, v94\n"
"v_mfma_f32_16x16x32_bf16 v[68:71], a[236:239], a[212:215], v[68:71]\n"
"v_rcp_f32 v95, v95\n"
"v_mfma_f32_16x16x32_bf16 v[72:75], a[232:235], a[200:203], v[72:75]\n"
"ds_read_b128 a[228:231], v1 offset:1024\n"
"v_mul_f32 v124, v124, v92\n"
"v_mfma_f32_16x16x32_bf16 v[72:75], a[236:239], a[216:219], v[72:75]\n"
"buffer_load_dwordx4 a[100:103], v1, s[40:43], s84 offen offset:1024 sc1\n"
"v_mfma_f32_16x16x32_bf16 v[76:79], a[232:235], a[204:207], v[76:79]\n"
"s_waitcnt lgkmcnt(4)\n"
"v_mul_f32 v125, v125, v93\n"
"v_mul_f32 v126, v126, v94\n"
"v_mfma_f32_16x16x32_bf16 v[76:79], a[236:239], a[220:223], v[76:79]\n"
"v_mul_f32 v127, v127, v95\n"
"v_mul_f32 v140, v108, v140\n"
"v_mfma_f32_16x16x32_bf16 v[144:147], a[240:243], a[192:195], v[144:147]\n"
"v_mul_f32 v141, v109, v141\n"
"v_mul_f32 v142, v110, v142\n"
"v_mfma_f32_16x16x32_bf16 v[144:147], a[244:247], a[208:211], v[144:147]\n"
"ds_read_b128 a[232:235], v1 offset:2048\n"
"v_mul_f32 v143, v111, v143\n"
"v_mfma_f32_16x16x32_bf16 v[148:151], a[240:243], a[196:199], v[148:151]\n"
"v_mul_f32 v108, s86, v108\n"
"v_mul_f32 v109, s86, v109\n"
"v_mfma_f32_16x16x32_bf16 v[148:151], a[244:247], a[212:215], v[148:151]\n"
"buffer_load_dwordx4 a[104:107], v1, s[40:43], s84 offen offset:2048 sc1\n"
"v_mfma_f32_16x16x32_bf16 v[152:155], a[240:243], a[200:203], v[152:155]\n"
"ds_read_b128 a[236:239], v1 offset:3072\n"
"v_mul_f32 v110, s86, v110\n"
"v_mfma_f32_16x16x32_bf16 v[152:155], a[244:247], a[216:219], v[152:155]\n"
"v_mul_f32 v111, s86, v111\n"
"v_exp_f32 v108, v108\n"
"v_mfma_f32_16x16x32_bf16 v[156:159], a[240:243], a[204:207], v[156:159]\n"
"s_waitcnt lgkmcnt(4)\n"
"buffer_load_dwordx4 a[108:111], v1, s[40:43], s84 offen offset:3072 sc1\n"
"v_mfma_f32_16x16x32_bf16 v[156:159], a[244:247], a[220:223], v[156:159]\n"
"s_add_u32 s84, s84, 4096\n"
"v_exp_f32 v109, v109\n"
"v_mfma_f32_16x16x32_bf16 v[160:163], a[248:251], a[192:195], v[160:163]\n"
"v_exp_f32 v110, v110\n"
"v_mfma_f32_16x16x32_bf16 v[160:163], a[252:255], a[208:211], v[160:163]\n"
"ds_read_b128 a[240:243], v1 offset:4096\n"
"v_mfma_f32_16x16x32_bf16 v[164:167], a[248:251], a[196:199], v[164:167]\n"
"v_exp_f32 v111, v111\n"
"v_mfma_f32_16x16x32_bf16 v[164:167], a[252:255], a[212:215], v[164:167]\n"
"v_add_f32 v108, 1.0, v108\n"
"v_add_f32 v109, 1.0, v109\n"
"v_mfma_f32_16x16x32_bf16 v[168:171], a[248:251], a[200:203], v[168:171]\n"
"buffer_load_dwordx4 a[112:115], v1, s[40:43], s84 offen sc1\n"
"v_add_f32 v110, 1.0, v110\n"
"v_mfma_f32_16x16x32_bf16 v[168:171], a[252:255], a[216:219], v[168:171]\n"
"ds_read_b128 a[244:247], v1 offset:5120\n"
"v_add_f32 v111, 1.0, v111\n"
"v_mfma_f32_16x16x32_bf16 v[172:175], a[248:251], a[204:207], v[172:175]\n"
"s_waitcnt lgkmcnt(4)\n"
"v_rcp_f32 v108, v108\n"
"v_mfma_f32_16x16x32_bf16 v[172:175], a[252:255], a[220:223], v[172:175]\n"
"v_rcp_f32 v109, v109\n"
"v_mfma_f32_16x16x32_bf16 v[176:179], a[224:227], a[192:195], v[176:179]\n"
"buffer_load_dwordx4 a[116:119], v1, s[40:43], s84 offen offset:1024 sc1\n"
"v_mfma_f32_16x16x32_bf16 v[176:179], a[228:231], a[208:211], v[176:179]\n"
"v_rcp_f32 v110, v110\n"
"v_mfma_f32_16x16x32_bf16 v[180:183], a[224:227], a[196:199], v[180:183]\n"
"ds_read_b128 a[248:251], v1 offset:6144\n"
"v_rcp_f32 v111, v111\n"
"v_mfma_f32_16x16x32_bf16 v[180:183], a[228:231], a[212:215], v[180:183]\n"
"v_mul_f32 v140, v140, v108\n"
"v_mul_f32 v141, v141, v109\n"
"v_mfma_f32_16x16x32_bf16 v[184:187], a[224:227], a[200:203], v[184:187]\n"
"ds_read_b128 a[252:255], v1 offset:7168\n"
"v_mul_f32 v142, v142, v110\n"
"v_mfma_f32_16x16x32_bf16 v[184:187], a[228:231], a[216:219], v[184:187]\n"
"buffer_load_dwordx4 a[120:123], v1, s[40:43], s84 offen offset:2048 sc1\n"
"v_mfma_f32_16x16x32_bf16 v[188:191], a[224:227], a[204:207], v[188:191]\n"
"s_waitcnt lgkmcnt(4)\n"
"v_mul_f32 v143, v143, v111\n"
"v_cvt_pk_bf16_f32 v92, v124, v125\n"
"v_mfma_f32_16x16x32_bf16 v[188:191], a[228:231], a[220:223], v[188:191]\n"
"v_cvt_pk_bf16_f32 v93, v126, v127\n"
"v_cvt_pk_bf16_f32 v94, v140, v141\n"
"v_mfma_f32_16x16x32_bf16 v[192:195], a[232:235], a[192:195], v[192:195]\n"
"ds_read_b128 a[224:227], v1 offset:8192\n"
"v_cvt_pk_bf16_f32 v95, v142, v143\n"
"v_mfma_f32_16x16x32_bf16 v[192:195], a[236:239], a[208:211], v[192:195]\n"
"buffer_load_dwordx4 a[124:127], v1, s[40:43], s84 offen offset:3072 sc1\n"
"buffer_store_dwordx4 v[92:95], v1, s[40:43], s85 offen offset:3072 sc1\n"
"v_mfma_f32_16x16x32_bf16 v[196:199], a[232:235], a[196:199], v[196:199]\n"
"s_add_u32 s84, s84, 4096\n"
"v_mfma_f32_16x16x32_bf16 v[196:199], a[236:239], a[212:215], v[196:199]\n"
"s_add_u32 s82, s64, 458752\n"
"ds_read_b128 a[228:231], v1 offset:9216\n"
"v_mfma_f32_16x16x32_bf16 v[200:203], a[232:235], a[200:203], v[200:203]\n"
"s_add_u32 m0, s66, 65536\n"
"v_mfma_f32_16x16x32_bf16 v[200:203], a[236:239], a[216:219], v[200:203]\n"
"buffer_load_dwordx4 v1, s[40:43], s82 offen sc1 lds\n"
"v_mfma_f32_16x16x32_bf16 v[204:207], a[232:235], a[204:207], v[204:207]\n"
"s_waitcnt lgkmcnt(4)\n"
"buffer_load_dwordx4 v1, s[40:43], s82 offen offset:1024 sc1 lds\n"
"v_mfma_f32_16x16x32_bf16 v[204:207], a[236:239], a[220:223], v[204:207]\n"
"v_mfma_f32_16x16x32_bf16 v[208:211], a[240:243], a[192:195], v[208:211]\n"
"ds_read_b128 a[232:235], v1 offset:10240\n"
"v_mfma_f32_16x16x32_bf16 v[208:211], a[244:247], a[208:211], v[208:211]\n"
"buffer_load_dwordx4 v1, s[40:43], s82 offen offset:2048 sc1 lds\n"
"v_mfma_f32_16x16x32_bf16 v[212:215], a[240:243], a[196:199], v[212:215]\n"
"s_lshl_b32 s96, s73, 2\n"
"v_mfma_f32_16x16x32_bf16 v[212:215], a[244:247], a[212:215], v[212:215]\n"
"s_add_u32 s96, s96, s71\n"
"ds_read_b128 a[236:239], v1 offset:11264\n"
"v_mfma_f32_16x16x32_bf16 v[216:219], a[240:243], a[200:203], v[216:219]\n"
"s_mul_i32 s96, s96, 147456\n"
"v_mfma_f32_16x16x32_bf16 v[216:219], a[244:247], a[216:219], v[216:219]\n"
"s_add_u32 s52, s96, 60030976\n"
"buffer_load_dwordx4 v1, s[40:43], s82 offen offset:3072 sc1 lds\n"
"v_mfma_f32_16x16x32_bf16 v[220:223], a[240:243], a[204:207], v[220:223]\n"
"s_waitcnt vmcnt(33) lgkmcnt(4)\n"
"v_mfma_f32_16x16x32_bf16 v[220:223], a[244:247], a[220:223], v[220:223]\n"
"s_lshl_b32 s96, s100, 11\n"
"v_mfma_f32_16x16x32_bf16 v[16:19], a[248:251], a[0:3], v[16:19]\n"
"s_add_u32 s96, s96, s52\n"
"ds_read_b128 a[240:243], v1 offset:12288\n"
"v_mfma_f32_16x16x32_bf16 v[16:19], a[252:255], a[16:19], v[16:19]\n"
"s_mul_i32 s53, s73, 73728\n"
"v_mfma_f32_16x16x32_bf16 v[20:23], a[248:251], a[4:7], v[20:23]\n"
"s_mul_i32 s80, s71, 288\n"
"buffer_load_dwordx4 v[80:83], v1, s[40:43], s84 offen sc1\n"
"v_mfma_f32_16x16x32_bf16 v[20:23], a[252:255], a[20:23], v[20:23]\n"
"s_add_u32 s53, s53, s80\n"
"ds_read_b128 a[244:247], v1 offset:13312\n"
"v_mfma_f32_16x16x32_bf16 v[24:27], a[248:251], a[8:11], v[24:27]\n"
"s_mov_b32 s88, 0x3f80\n"
"v_mfma_f32_16x16x32_bf16 v[24:27], a[252:255], a[24:27], v[24:27]\n"
"s_mov_b32 s65, 0x3f800000\n"
"v_and_b32 v6, 15, v0\n"
"v_mul_u32_u24 v5, 1152, v6\n"
"v_mfma_f32_16x16x32_bf16 v[28:31], a[248:251], a[12:15], v[28:31]\n"
"s_waitcnt lgkmcnt(4)\n"
"buffer_load_dwordx4 v[84:87], v1, s[40:43], s84 offen offset:1024 sc1\n"
"v_mfma_f32_16x16x32_bf16 v[28:31], a[252:255], a[28:31], v[28:31]\n"
"v_lshrrev_b32 v6, 4, v0\n"
"v_lshl_add_u32 v5, v6, 3, v5\n"
"v_mfma_f32_16x16x32_bf16 v[32:35], a[224:227], a[0:3], v[32:35]\n"
"s_mul_i32 s80, s101, 18432\n"
"ds_read_b128 a[248:251], v1 offset:14336\n"
"v_mfma_f32_16x16x32_bf16 v[32:35], a[228:231], a[16:19], v[32:35]\n"
"s_add_u32 s53, s53, s80\n"
"v_mfma_f32_16x16x32_bf16 v[36:39], a[224:227], a[4:7], v[36:39]\n"
"s_add_u32 s53, s53, 256\n"
"v_mfma_f32_16x16x32_bf16 v[36:39], a[228:231], a[20:23], v[36:39]\n"
"s_lshr_b32 s80, s101, 1\n"
"ds_read_b128 a[252:255], v1 offset:15360\n"
"v_mfma_f32_16x16x32_bf16 v[40:43], a[224:227], a[8:11], v[40:43]\n"
"s_lshl_b32 s80, s80, 10\n"
"buffer_load_dwordx4 v[88:91], v1, s[40:43], s84 offen offset:2048 sc1\n"
"v_mfma_f32_16x16x32_bf16 v[40:43], a[228:231], a[24:27], v[40:43]\n"
"s_add_u32 s52, s52, s80\n"
"v_mfma_f32_16x16x32_bf16 v[44:47], a[224:227], a[12:15], v[44:47]\n"
"s_waitcnt lgkmcnt(4)\n"
"v_mfma_f32_16x16x32_bf16 v[44:47], a[228:231], a[28:31], v[44:47]\n"
"s_and_b32 s80, s101, 1\n"
"buffer_load_dwordx4 v[92:95], v1, s[40:43], s84 offen offset:3072 sc1\n"
"v_mfma_f32_16x16x32_bf16 v[48:51], a[232:235], a[0:3], v[48:51]\n"
"s_lshl_b32 s80, s80, 3\n"
"ds_read_b128 a[224:227], v1 offset:16384\n"
"v_mfma_f32_16x16x32_bf16 v[48:51], a[236:239], a[16:19], v[48:51]\n"
"s_add_u32 s52, s52, s80\n"
"v_mfma_f32_16x16x32_bf16 v[52:55], a[232:235], a[4:7], v[52:55]\n"
"s_add_u32 s52, s52, 131072\n"
"v_mfma_f32_16x16x32_bf16 v[52:55], a[236:239], a[20:23], v[52:55]\n"
"ds_read_b128 a[228:231], v1 offset:17408\n"
"v_mfma_f32_16x16x32_bf16 v[56:59], a[232:235], a[8:11], v[56:59]\n"
"s_add_u32 s84, s84, 4096\n"
"buffer_load_dwordx4 v[96:99], v1, s[40:43], s84 offen sc1\n"
"v_mfma_f32_16x16x32_bf16 v[56:59], a[236:239], a[24:27], v[56:59]\n"
"v_mfma_f32_16x16x32_bf16 v[60:63], a[232:235], a[12:15], v[60:63]\n"
"s_waitcnt lgkmcnt(4)\n"
"v_mfma_f32_16x16x32_bf16 v[60:63], a[236:239], a[28:31], v[60:63]\n"
"v_mfma_f32_16x16x32_bf16 v[64:67], a[240:243], a[0:3], v[64:67]\n"
"ds_read_b128 a[232:235], v1 offset:18432\n"
"v_mfma_f32_16x16x32_bf16 v[64:67], a[244:247], a[16:19], v[64:67]\n"
"buffer_load_dwordx4 v[100:103], v1, s[40:43], s84 offen offset:1024 sc1\n"
"v_mfma_f32_16x16x32_bf16 v[68:71], a[240:243], a[4:7], v[68:71]\n"
"v_mfma_f32_16x16x32_bf16 v[68:71], a[244:247], a[20:23], v[68:71]\n"
"ds_read_b128 a[236:239], v1 offset:19456\n"
"v_mfma_f32_16x16x32_bf16 v[72:75], a[240:243], a[8:11], v[72:75]\n"
"v_mfma_f32_16x16x32_bf16 v[72:75], a[244:247], a[24:27], v[72:75]\n"
"buffer_load_dwordx4 v[104:107], v1, s[40:43], s84 offen offset:2048 sc1\n"
"v_mfma_f32_16x16x32_bf16 v[76:79], a[240:243], a[12:15], v[76:79]\n"
"s_waitcnt lgkmcnt(4)\n"
"v_mfma_f32_16x16x32_bf16 v[76:79], a[244:247], a[28:31], v[76:79]\n"
"v_mfma_f32_16x16x32_bf16 v[144:147], a[248:251], a[0:3], v[144:147]\n"
"ds_read_b128 a[240:243], v1 offset:20480\n"
"v_mfma_f32_16x16x32_bf16 v[144:147], a[252:255], a[16:19], v[144:147]\n"
"buffer_load_dwordx4 v[108:111], v1, s[40:43], s84 offen offset:3072 sc1\n"
"v_mfma_f32_16x16x32_bf16 v[148:151], a[248:251], a[4:7], v[148:151]\n"
"v_mfma_f32_16x16x32_bf16 v[148:151], a[252:255], a[20:23], v[148:151]\n"
"ds_read_b128 a[244:247], v1 offset:21504\n"
"v_mfma_f32_16x16x32_bf16 v[152:155], a[248:251], a[8:11], v[152:155]\n"
"v_mfma_f32_16x16x32_bf16 v[152:155], a[252:255], a[24:27], v[152:155]\n"
"v_mfma_f32_16x16x32_bf16 v[156:159], a[248:251], a[12:15], v[156:159]\n"
"s_waitcnt lgkmcnt(4)\n"
"v_mfma_f32_16x16x32_bf16 v[156:159], a[252:255], a[28:31], v[156:159]\n"
"s_add_u32 s84, s84, 4096\n"
"v_mfma_f32_16x16x32_bf16 v[160:163], a[224:227], a[0:3], v[160:163]\n"
"ds_read_b128 a[248:251], v1 offset:22528\n"
"v_mfma_f32_16x16x32_bf16 v[160:163], a[228:231], a[16:19], v[160:163]\n"
"s_add_u32 s82, s64, 475136\n"
"v_mfma_f32_16x16x32_bf16 v[164:167], a[224:227], a[4:7], v[164:167]\n"
"s_add_u32 m0, s66, 81920\n"
"v_mfma_f32_16x16x32_bf16 v[164:167], a[228:231], a[20:23], v[164:167]\n"
"ds_read_b128 a[252:255], v1 offset:23552\n"
"v_mfma_f32_16x16x32_bf16 v[168:171], a[224:227], a[8:11], v[168:171]\n"
"buffer_load_dwordx4 v1, s[40:43], s82 offen sc1 lds\n"
"v_mfma_f32_16x16x32_bf16 v[168:171], a[228:231], a[24:27], v[168:171]\n"
"buffer_load_dwordx4 v1, s[40:43], s82 offen offset:1024 sc1 lds\n"
"v_mfma_f32_16x16x32_bf16 v[172:175], a[224:227], a[12:15], v[172:175]\n"
"s_waitcnt lgkmcnt(4)\n"
"v_mfma_f32_16x16x32_bf16 v[172:175], a[228:231], a[28:31], v[172:175]\n"
"v_mfma_f32_16x16x32_bf16 v[176:179], a[232:235], a[0:3], v[176:179]\n"
"ds_read_b128 a[224:227], v1 offset:24576\n"
"v_mfma_f32_16x16x32_bf16 v[176:179], a[236:239], a[16:19], v[176:179]\n"
"v_mfma_f32_16x16x32_bf16 v[180:183], a[232:235], a[4:7], v[180:183]\n"
"buffer_load_dwordx4 v1, s[40:43], s82 offen offset:2048 sc1 lds\n"
"v_mfma_f32_16x16x32_bf16 v[180:183], a[236:239], a[20:23], v[180:183]\n"
"ds_read_b128 a[228:231], v1 offset:25600\n"
"v_mfma_f32_16x16x32_bf16 v[184:187], a[232:235], a[8:11], v[184:187]\n"
"v_mfma_f32_16x16x32_bf16 v[184:187], a[236:239], a[24:27], v[184:187]\n"
"v_mfma_f32_16x16x32_bf16 v[188:191], a[232:235], a[12:15], v[188:191]\n"
"s_waitcnt lgkmcnt(4)\n"
"buffer_load_dwordx4 v1, s[40:43], s82 offen offset:3072 sc1 lds\n"
"v_mfma_f32_16x16x32_bf16 v[188:191], a[236:239], a[28:31], v[188:191]\n"
"v_mfma_f32_16x16x32_bf16 v[192:195], a[240:243], a[0:3], v[192:195]\n"
"ds_read_b128 a[232:235], v1 offset:26624\n"
"v_mfma_f32_16x16x32_bf16 v[192:195], a[244:247], a[16:19], v[192:195]\n"
"v_mfma_f32_16x16x32_bf16 v[196:199], a[240:243], a[4:7], v[196:199]\n"
"v_mfma_f32_16x16x32_bf16 v[196:199], a[244:247], a[20:23], v[196:199]\n"
"ds_read_b128 a[236:239], v1 offset:27648\n"
"v_mfma_f32_16x16x32_bf16 v[200:203], a[240:243], a[8:11], v[200:203]\n"
"buffer_load_dwordx4 v[112:115], v1, s[40:43], s84 offen sc1\n"
"v_mfma_f32_16x16x32_bf16 v[200:203], a[244:247], a[24:27], v[200:203]\n"
"v_mfma_f32_16x16x32_bf16 v[204:207], a[240:243], a[12:15], v[204:207]\n"
"s_waitcnt lgkmcnt(4)\n"
"v_mfma_f32_16x16x32_bf16 v[204:207], a[244:247], a[28:31], v[204:207]\n"
"buffer_load_dwordx4 v[116:119], v1, s[40:43], s84 offen offset:1024 sc1\n"
"v_mfma_f32_16x16x32_bf16 v[208:211], a[248:251], a[0:3], v[208:211]\n"
"s_lshl_b32 s78, s73, 5\n"
"ds_read_b128 a[240:243], v1 offset:28672\n"
"v_mfma_f32_16x16x32_bf16 v[208:211], a[252:255], a[16:19], v[208:211]\n"
"s_add_u32 s78, s78, s68\n"
"v_mfma_f32_16x16x32_bf16 v[212:215], a[248:251], a[4:7], v[212:215]\n"
"s_lshl_b32 s78, s78, 6\n"
"v_mfma_f32_16x16x32_bf16 v[212:215], a[252:255], a[20:23], v[212:215]\n"
"s_add_u32 s78, s78, 79167488\n"
"ds_read_b128 a[244:247], v1 offset:29696\n"
"v_mov_b32 v8, s92\n"
"v_mfma_f32_16x16x32_bf16 v[216:219], a[248:251], a[8:11], v[216:219]\n"
"v_mov_b32 v9, s93\n"
"v_mfma_f32_16x16x32_bf16 v[216:219], a[252:255], a[24:27], v[216:219]\n"
"s_waitcnt vmcnt(18)\n"
"buffer_store_dwordx2 v[8:9], v14, s[40:43], s78 offen sc1\n"
"v_mfma_f32_16x16x32_bf16 v[220:223], a[248:251], a[12:15], v[220:223]\n"
"s_waitcnt lgkmcnt(4)\n"
"buffer_load_dwordx4 v[120:123], v1, s[40:43], s84 offen offset:2048 sc1\n"
"v_mfma_f32_16x16x32_bf16 v[220:223], a[252:255], a[28:31], v[220:223]\n"
"v_mfma_f32_16x16x32_bf16 v[16:19], a[224:227], a[32:35], v[16:19]\n"
"ds_read_b128 a[248:251], v1 offset:30720\n"
"v_mfma_f32_16x16x32_bf16 v[16:19], a[228:231], a[48:51], v[16:19]\n"
"buffer_load_dwordx4 v[124:127], v1, s[40:43], s84 offen offset:3072 sc1\n"
"v_mfma_f32_16x16x32_bf16 v[20:23], a[224:227], a[36:39], v[20:23]\n"
"v_mfma_f32_16x16x32_bf16 v[20:23], a[228:231], a[52:55], v[20:23]\n"
"ds_read_b128 a[252:255], v1 offset:31744\n"
"v_mfma_f32_16x16x32_bf16 v[24:27], a[224:227], a[40:43], v[24:27]\n"
"v_mfma_f32_16x16x32_bf16 v[24:27], a[228:231], a[56:59], v[24:27]\n"
"s_add_u32 s84, s84, 4096\n"
"buffer_load_dwordx4 v[128:131], v1, s[40:43], s84 offen sc1\n"
"v_mfma_f32_16x16x32_bf16 v[28:31], a[224:227], a[44:47], v[28:31]\n"
"s_waitcnt lgkmcnt(4)\n"
"v_mfma_f32_16x16x32_bf16 v[28:31], a[228:231], a[60:63], v[28:31]\n"
"s_barrier\n"
"v_mfma_f32_16x16x32_bf16 v[32:35], a[232:235], a[32:35], v[32:35]\n"
"ds_read_b128 a[224:227], v1 offset:32768\n"
"v_mfma_f32_16x16x32_bf16 v[32:35], a[236:239], a[48:51], v[32:35]\n"
"buffer_load_dwordx4 v[132:135], v1, s[40:43], s84 offen offset:1024 sc1\n"
"v_mfma_f32_16x16x32_bf16 v[36:39], a[232:235], a[36:39], v[36:39]\n"
"v_mfma_f32_16x16x32_bf16 v[36:39], a[236:239], a[52:55], v[36:39]\n"
"ds_read_b128 a[228:231], v1 offset:33792\n"
"v_mfma_f32_16x16x32_bf16 v[40:43], a[232:235], a[40:43], v[40:43]\n"
"v_mfma_f32_16x16x32_bf16 v[40:43], a[236:239], a[56:59], v[40:43]\n"
"v_mfma_f32_16x16x32_bf16 v[44:47], a[232:235], a[44:47], v[44:47]\n"
"s_waitcnt lgkmcnt(4)\n"
"buffer_load_dwordx4 v[136:139], v1, s[40:43], s84 offen offset:2048 sc1\n"
"v_mfma_f32_16x16x32_bf16 v[44:47], a[236:239], a[60:63], v[44:47]\n"
"v_mfma_f32_16x16x32_bf16 v[48:51], a[240:243], a[32:35], v[48:51]\n"
"ds_read_b128 a[232:235], v1 offset:34816\n"
"v_mfma_f32_16x16x32_bf16 v[48:51], a[244:247], a[48:51], v[48:51]\n"
"v_mfma_f32_16x16x32_bf16 v[52:55], a[240:243], a[36:39], v[52:55]\n"
"buffer_load_dwordx4 v[140:143], v1, s[40:43], s84 offen offset:3072 sc1\n"
"v_mfma_f32_16x16x32_bf16 v[52:55], a[244:247], a[52:55], v[52:55]\n"
"ds_read_b128 a[236:239], v1 offset:35840\n"
"v_mfma_f32_16x16x32_bf16 v[56:59], a[240:243], a[40:43], v[56:59]\n"
"v_mfma_f32_16x16x32_bf16 v[56:59], a[244:247], a[56:59], v[56:59]\n"
"v_mfma_f32_16x16x32_bf16 v[60:63], a[240:243], a[44:47], v[60:63]\n"
"s_waitcnt lgkmcnt(4)\n"
"v_mfma_f32_16x16x32_bf16 v[60:63], a[244:247], a[60:63], v[60:63]\n"
"s_add_u32 s84, s84, 4096\n"
"v_mfma_f32_16x16x32_bf16 v[64:67], a[248:251], a[32:35], v[64:67]\n"
"ds_read_b128 a[240:243], v1 offset:36864\n"
"v_mfma_f32_16x16x32_bf16 v[64:67], a[252:255], a[48:51], v[64:67]\n"
"v_mfma_f32_16x16x32_bf16 v[68:71], a[248:251], a[36:39], v[68:71]\n"
"v_mfma_f32_16x16x32_bf16 v[68:71], a[252:255], a[52:55], v[68:71]\n"
"ds_read_b128 a[244:247], v1 offset:37888\n"
"v_mfma_f32_16x16x32_bf16 v[72:75], a[248:251], a[40:43], v[72:75]\n"
"v_mfma_f32_16x16x32_bf16 v[72:75], a[252:255], a[56:59], v[72:75]\n"
"v_mfma_f32_16x16x32_bf16 v[76:79], a[248:251], a[44:47], v[76:79]\n"
"s_waitcnt lgkmcnt(4)\n"
"v_mfma_f32_16x16x32_bf16 v[76:79], a[252:255], a[60:63], v[76:79]\n"
"v_mfma_f32_16x16x32_bf16 v[144:147], a[224:227], a[32:35], v[144:147]\n"
"ds_read_b128 a[248:251], v1 offset:38912\n"
"v_mfma_f32_16x16x32_bf16 v[144:147], a[228:231], a[48:51], v[144:147]\n"
"s_add_u32 s82, s64, 491520\n"
"v_mfma_f32_16x16x32_bf16 v[148:151], a[224:227], a[36:39], v[148:151]\n"
"s_add_u32 m0, s66, 98304\n"
"v_mfma_f32_16x16x32_bf16 v[148:151], a[228:231], a[52:55], v[148:151]\n"
"ds_read_b128 a[252:255], v1 offset:39936\n"
"v_mfma_f32_16x16x32_bf16 v[152:155], a[224:227], a[40:43], v[152:155]\n"
"buffer_load_dwordx4 v1, s[40:43], s82 offen sc1 lds\n"
"v_mfma_f32_16x16x32_bf16 v[152:155], a[228:231], a[56:59], v[152:155]\n"
"buffer_load_dwordx4 v1, s[40:43], s82 offen offset:1024 sc1 lds\n"
"v_mfma_f32_16x16x32_bf16 v[156:159], a[224:227], a[44:47], v[156:159]\n"
"s_waitcnt lgkmcnt(4)\n"
"v_mfma_f32_16x16x32_bf16 v[156:159], a[228:231], a[60:63], v[156:159]\n"
"v_mfma_f32_16x16x32_bf16 v[160:163], a[232:235], a[32:35], v[160:163]\n"
"ds_read_b128 a[224:227], v1 offset:40960\n"
"v_mfma_f32_16x16x32_bf16 v[160:163], a[236:239], a[48:51], v[160:163]\n"
"v_mfma_f32_16x16x32_bf16 v[164:167], a[232:235], a[36:39], v[164:167]\n"
"buffer_load_dwordx4 v1, s[40:43], s82 offen offset:2048 sc1 lds\n"
"v_mfma_f32_16x16x32_bf16 v[164:167], a[236:239], a[52:55], v[164:167]\n"
"ds_read_b128 a[228:231], v1 offset:41984\n"
"v_mfma_f32_16x16x32_bf16 v[168:171], a[232:235], a[40:43], v[168:171]\n"
"v_mfma_f32_16x16x32_bf16 v[168:171], a[236:239], a[56:59], v[168:171]\n"
"v_mfma_f32_16x16x32_bf16 v[172:175], a[232:235], a[44:47], v[172:175]\n"
"s_waitcnt lgkmcnt(4)\n"
"buffer_load_dwordx4 v1, s[40:43], s82 offen offset:3072 sc1 lds\n"
"v_mfma_f32_16x16x32_bf16 v[172:175], a[236:239], a[60:63], v[172:175]\n"
"v_mfma_f32_16x16x32_bf16 v[176:179], a[240:243], a[32:35], v[176:179]\n"
"ds_read_b128 a[232:235], v1 offset:43008\n"
"v_mfma_f32_16x16x32_bf16 v[176:179], a[244:247], a[48:51], v[176:179]\n"
"v_mfma_f32_16x16x32_bf16 v[180:183], a[240:243], a[36:39], v[180:183]\n"
"v_mfma_f32_16x16x32_bf16 v[180:183], a[244:247], a[52:55], v[180:183]\n"
"ds_read_b128 a[236:239], v1 offset:44032\n"
"v_mfma_f32_16x16x32_bf16 v[184:187], a[240:243], a[40:43], v[184:187]\n"
"buffer_load_dwordx4 a[128:131], v1, s[40:43], s84 offen sc1\n"
"v_mfma_f32_16x16x32_bf16 v[184:187], a[244:247], a[56:59], v[184:187]\n"
"v_mfma_f32_16x16x32_bf16 v[188:191], a[240:243], a[44:47], v[188:191]\n"
"s_waitcnt lgkmcnt(4)\n"
"v_mfma_f32_16x16x32_bf16 v[188:191], a[244:247], a[60:63], v[188:191]\n"
"buffer_load_dwordx4 a[132:135], v1, s[40:43], s84 offen offset:1024 sc1\n"
"v_mfma_f32_16x16x32_bf16 v[192:195], a[248:251], a[32:35], v[192:195]\n"
"ds_read_b128 a[240:243], v1 offset:45056\n"
"v_mfma_f32_16x16x32_bf16 v[192:195], a[252:255], a[48:51], v[192:195]\n"
"v_mfma_f32_16x16x32_bf16 v[196:199], a[248:251], a[36:39], v[196:199]\n"
"v_mfma_f32_16x16x32_bf16 v[196:199], a[252:255], a[52:55], v[196:199]\n"
"ds_read_b128 a[244:247], v1 offset:46080\n"
"v_mfma_f32_16x16x32_bf16 v[200:203], a[248:251], a[40:43], v[200:203]\n"
"buffer_load_dwordx4 a[136:139], v1, s[40:43], s84 offen offset:2048 sc1\n"
"v_mfma_f32_16x16x32_bf16 v[200:203], a[252:255], a[56:59], v[200:203]\n"
"v_mfma_f32_16x16x32_bf16 v[204:207], a[248:251], a[44:47], v[204:207]\n"
"s_waitcnt lgkmcnt(4)\n"
"v_mfma_f32_16x16x32_bf16 v[204:207], a[252:255], a[60:63], v[204:207]\n"
"v_mfma_f32_16x16x32_bf16 v[208:211], a[224:227], a[32:35], v[208:211]\n"
"ds_read_b128 a[248:251], v1 offset:47104\n"
"v_mfma_f32_16x16x32_bf16 v[208:211], a[228:231], a[48:51], v[208:211]\n"
"buffer_load_dwordx4 a[140:143], v1, s[40:43], s84 offen offset:3072 sc1\n"
"v_mfma_f32_16x16x32_bf16 v[212:215], a[224:227], a[36:39], v[212:215]\n"
"v_mfma_f32_16x16x32_bf16 v[212:215], a[228:231], a[52:55], v[212:215]\n"
"ds_read_b128 a[252:255], v1 offset:48128\n"
"v_mfma_f32_16x16x32_bf16 v[216:219], a[224:227], a[40:43], v[216:219]\n"
"v_mfma_f32_16x16x32_bf16 v[216:219], a[228:231], a[56:59], v[216:219]\n"
"s_add_u32 s84, s84, 4096\n"
"buffer_load_dwordx4 a[144:147], v1, s[40:43], s84 offen sc1\n"
"v_mfma_f32_16x16x32_bf16 v[220:223], a[224:227], a[44:47], v[220:223]\n"
"s_waitcnt lgkmcnt(4)\n"
"v_mfma_f32_16x16x32_bf16 v[220:223], a[228:231], a[60:63], v[220:223]\n"
"v_mfma_f32_16x16x32_bf16 v[16:19], a[232:235], a[64:67], v[16:19]\n"
"ds_read_b128 a[224:227], v1 offset:49152\n"
"v_mfma_f32_16x16x32_bf16 v[16:19], a[236:239], a[80:83], v[16:19]\n"
"buffer_load_dwordx4 a[148:151], v1, s[40:43], s84 offen offset:1024 sc1\n"
"v_mfma_f32_16x16x32_bf16 v[20:23], a[232:235], a[68:71], v[20:23]\n"
"v_mfma_f32_16x16x32_bf16 v[20:23], a[236:239], a[84:87], v[20:23]\n"
"ds_read_b128 a[228:231], v1 offset:50176\n"
"v_mfma_f32_16x16x32_bf16 v[24:27], a[232:235], a[72:75], v[24:27]\n"
"v_mfma_f32_16x16x32_bf16 v[24:27], a[236:239], a[88:91], v[24:27]\n"
"v_mfma_f32_16x16x32_bf16 v[28:31], a[232:235], a[76:79], v[28:31]\n"
"s_waitcnt lgkmcnt(4)\n"
"buffer_load_dwordx4 a[152:155], v1, s[40:43], s84 offen offset:2048 sc1\n"
"v_mfma_f32_16x16x32_bf16 v[28:31], a[236:239], a[92:95], v[28:31]\n"
"v_mfma_f32_16x16x32_bf16 v[32:35], a[240:243], a[64:67], v[32:35]\n"
"ds_read_b128 a[232:235], v1 offset:51200\n"
"v_mfma_f32_16x16x32_bf16 v[32:35], a[244:247], a[80:83], v[32:35]\n"
"v_mfma_f32_16x16x32_bf16 v[36:39], a[240:243], a[68:71], v[36:39]\n"
"buffer_load_dwordx4 a[156:159], v1, s[40:43], s84 offen offset:3072 sc1\n"
"v_mfma_f32_16x16x32_bf16 v[36:39], a[244:247], a[84:87], v[36:39]\n"
"ds_read_b128 a[236:239], v1 offset:52224\n"
"v_mfma_f32_16x16x32_bf16 v[40:43], a[240:243], a[72:75], v[40:43]\n"
"v_mfma_f32_16x16x32_bf16 v[40:43], a[244:247], a[88:91], v[40:43]\n"
"v_mfma_f32_16x16x32_bf16 v[44:47], a[240:243], a[76:79], v[44:47]\n"
"s_waitcnt lgkmcnt(4)\n"
"v_mfma_f32_16x16x32_bf16 v[44:47], a[244:247], a[92:95], v[44:47]\n"
"s_add_u32 s84, s84, 4096\n"
"v_mfma_f32_16x16x32_bf16 v[48:51], a[248:251], a[64:67], v[48:51]\n"
"ds_read_b128 a[240:243], v1 offset:53248\n"
"v_mfma_f32_16x16x32_bf16 v[48:51], a[252:255], a[80:83], v[48:51]\n"
"v_mfma_f32_16x16x32_bf16 v[52:55], a[248:251], a[68:71], v[52:55]\n"
"v_mfma_f32_16x16x32_bf16 v[52:55], a[252:255], a[84:87], v[52:55]\n"
"ds_read_b128 a[244:247], v1 offset:54272\n"
"v_mfma_f32_16x16x32_bf16 v[56:59], a[248:251], a[72:75], v[56:59]\n"
"buffer_load_dwordx4 a[160:163], v1, s[40:43], s84 offen sc1\n"
"v_mfma_f32_16x16x32_bf16 v[56:59], a[252:255], a[88:91], v[56:59]\n"
"v_mfma_f32_16x16x32_bf16 v[60:63], a[248:251], a[76:79], v[60:63]\n"
"s_waitcnt lgkmcnt(4)\n"
"v_mfma_f32_16x16x32_bf16 v[60:63], a[252:255], a[92:95], v[60:63]\n"
"v_mfma_f32_16x16x32_bf16 v[64:67], a[224:227], a[64:67], v[64:67]\n"
"ds_read_b128 a[248:251], v1 offset:55296\n"
"v_mfma_f32_16x16x32_bf16 v[64:67], a[228:231], a[80:83], v[64:67]\n"
"buffer_load_dwordx4 a[164:167], v1, s[40:43], s84 offen offset:1024 sc1\n"
"v_mfma_f32_16x16x32_bf16 v[68:71], a[224:227], a[68:71], v[68:71]\n"
"v_mfma_f32_16x16x32_bf16 v[68:71], a[228:231], a[84:87], v[68:71]\n"
"ds_read_b128 a[252:255], v1 offset:56320\n"
"v_mfma_f32_16x16x32_bf16 v[72:75], a[224:227], a[72:75], v[72:75]\n"
"v_mfma_f32_16x16x32_bf16 v[72:75], a[228:231], a[88:91], v[72:75]\n"
"buffer_load_dwordx4 a[168:171], v1, s[40:43], s84 offen offset:2048 sc1\n"
"v_mfma_f32_16x16x32_bf16 v[76:79], a[224:227], a[76:79], v[76:79]\n"
"s_waitcnt lgkmcnt(4)\n"
"v_mfma_f32_16x16x32_bf16 v[76:79], a[228:231], a[92:95], v[76:79]\n"
"v_mfma_f32_16x16x32_bf16 v[144:147], a[232:235], a[64:67], v[144:147]\n"
"ds_read_b128 a[224:227], v1 offset:57344\n"
"v_mfma_f32_16x16x32_bf16 v[144:147], a[236:239], a[80:83], v[144:147]\n"
"buffer_load_dwordx4 a[172:175], v1, s[40:43], s84 offen offset:3072 sc1\n"
"v_mfma_f32_16x16x32_bf16 v[148:151], a[232:235], a[68:71], v[148:151]\n"
"v_mfma_f32_16x16x32_bf16 v[148:151], a[236:239], a[84:87], v[148:151]\n"
"ds_read_b128 a[228:231], v1 offset:58368\n"
"v_mfma_f32_16x16x32_bf16 v[152:155], a[232:235], a[72:75], v[152:155]\n"
"v_mfma_f32_16x16x32_bf16 v[152:155], a[236:239], a[88:91], v[152:155]\n"
"v_mfma_f32_16x16x32_bf16 v[156:159], a[232:235], a[76:79], v[156:159]\n"
"s_waitcnt lgkmcnt(4)\n"
"v_mfma_f32_16x16x32_bf16 v[156:159], a[236:239], a[92:95], v[156:159]\n"
"s_add_u32 s84, s84, 4096\n"
"buffer_load_dwordx4 a[176:179], v1, s[40:43], s84 offen sc1\n"
"v_mfma_f32_16x16x32_bf16 v[160:163], a[240:243], a[64:67], v[160:163]\n"
"ds_read_b128 a[232:235], v1 offset:59392\n"
"v_mfma_f32_16x16x32_bf16 v[160:163], a[244:247], a[80:83], v[160:163]\n"
"v_mfma_f32_16x16x32_bf16 v[164:167], a[240:243], a[68:71], v[164:167]\n"
"buffer_load_dwordx4 a[180:183], v1, s[40:43], s84 offen offset:1024 sc1\n"
"v_mfma_f32_16x16x32_bf16 v[164:167], a[244:247], a[84:87], v[164:167]\n"
"ds_read_b128 a[236:239], v1 offset:60416\n"
"v_mfma_f32_16x16x32_bf16 v[168:171], a[240:243], a[72:75], v[168:171]\n"
"v_mfma_f32_16x16x32_bf16 v[168:171], a[244:247], a[88:91], v[168:171]\n"
"v_mfma_f32_16x16x32_bf16 v[172:175], a[240:243], a[76:79], v[172:175]\n"
"s_waitcnt lgkmcnt(4)\n"
"v_mfma_f32_16x16x32_bf16 v[172:175], a[244:247], a[92:95], v[172:175]\n"
"buffer_load_dwordx4 a[184:187], v1, s[40:43], s84 offen offset:2048 sc1\n"
"v_mfma_f32_16x16x32_bf16 v[176:179], a[248:251], a[64:67], v[176:179]\n"
"ds_read_b128 a[240:243], v1 offset:61440\n"
"v_mfma_f32_16x16x32_bf16 v[176:179], a[252:255], a[80:83], v[176:179]\n"
"v_mfma_f32_16x16x32_bf16 v[180:183], a[248:251], a[68:71], v[180:183]\n"
"v_mfma_f32_16x16x32_bf16 v[180:183], a[252:255], a[84:87], v[180:183]\n"
"ds_read_b128 a[244:247], v1 offset:62464\n"
"v_mfma_f32_16x16x32_bf16 v[184:187], a[248:251], a[72:75], v[184:187]\n"
"buffer_load_dwordx4 a[188:191], v1, s[40:43], s84 offen offset:3072 sc1\n"
"v_mfma_f32_16x16x32_bf16 v[184:187], a[252:255], a[88:91], v[184:187]\n"
"v_mfma_f32_16x16x32_bf16 v[188:191], a[248:251], a[76:79], v[188:191]\n"
"s_waitcnt lgkmcnt(4)\n"
"v_mfma_f32_16x16x32_bf16 v[188:191], a[252:255], a[92:95], v[188:191]\n"
"v_mfma_f32_16x16x32_bf16 v[192:195], a[224:227], a[64:67], v[192:195]\n"
"s_add_u32 s84, s84, 4096\n"
"ds_read_b128 a[248:251], v1 offset:63488\n"
"v_mfma_f32_16x16x32_bf16 v[192:195], a[228:231], a[80:83], v[192:195]\n"
"s_add_u32 s82, s64, 507904\n"
"v_mfma_f32_16x16x32_bf16 v[196:199], a[224:227], a[68:71], v[196:199]\n"
"s_add_u32 m0, s66, 114688\n"
"v_mfma_f32_16x16x32_bf16 v[196:199], a[228:231], a[84:87], v[196:199]\n"
"ds_read_b128 a[252:255], v1 offset:64512\n"
"v_mfma_f32_16x16x32_bf16 v[200:203], a[224:227], a[72:75], v[200:203]\n"
"buffer_load_dwordx4 v1, s[40:43], s82 offen sc1 lds\n"
"v_mfma_f32_16x16x32_bf16 v[200:203], a[228:231], a[88:91], v[200:203]\n"
"buffer_load_dwordx4 v1, s[40:43], s82 offen offset:1024 sc1 lds\n"
"v_mfma_f32_16x16x32_bf16 v[204:207], a[224:227], a[76:79], v[204:207]\n"
"s_waitcnt lgkmcnt(4)\n"
"v_mfma_f32_16x16x32_bf16 v[204:207], a[228:231], a[92:95], v[204:207]\n"
"s_waitcnt vmcnt(31)\n"
"s_barrier\n"
"v_mfma_f32_16x16x32_bf16 v[208:211], a[232:235], a[64:67], v[208:211]\n"
"ds_read_b128 a[224:227], v2\n"
"v_mfma_f32_16x16x32_bf16 v[208:211], a[236:239], a[80:83], v[208:211]\n"
"buffer_load_dwordx4 v1, s[40:43], s82 offen offset:2048 sc1 lds\n"
"v_mfma_f32_16x16x32_bf16 v[212:215], a[232:235], a[68:71], v[212:215]\n"
"v_mfma_f32_16x16x32_bf16 v[212:215], a[236:239], a[84:87], v[212:215]\n"
"ds_read_b128 a[228:231], v2 offset:1024\n"
"v_mfma_f32_16x16x32_bf16 v[216:219], a[232:235], a[72:75], v[216:219]\n"
"v_mfma_f32_16x16x32_bf16 v[216:219], a[236:239], a[88:91], v[216:219]\n"
"buffer_load_dwordx4 v1, s[40:43], s82 offen offset:3072 sc1 lds\n"
"v_mfma_f32_16x16x32_bf16 v[220:223], a[232:235], a[76:79], v[220:223]\n"
"s_waitcnt lgkmcnt(4)\n"
"v_mfma_f32_16x16x32_bf16 v[220:223], a[236:239], a[92:95], v[220:223]\n"
"v_mfma_f32_16x16x32_bf16 v[16:19], a[240:243], a[96:99], v[16:19]\n"
"ds_read_b128 a[232:235], v2 offset:2048\n"
"v_mfma_f32_16x16x32_bf16 v[16:19], a[244:247], a[112:115], v[16:19]\n"
"v_mfma_f32_16x16x32_bf16 v[20:23], a[240:243], a[100:103], v[20:23]\n"
"buffer_load_dwordx4 a[192:195], v1, s[40:43], s84 offen sc1\n"
"v_mfma_f32_16x16x32_bf16 v[20:23], a[244:247], a[116:119], v[20:23]\n"
"ds_read_b128 a[236:239], v2 offset:3072\n"
"v_mfma_f32_16x16x32_bf16 v[24:27], a[240:243], a[104:107], v[24:27]\n"
"v_mfma_f32_16x16x32_bf16 v[24:27], a[244:247], a[120:123], v[24:27]\n"
"v_mfma_f32_16x16x32_bf16 v[28:31], a[240:243], a[108:111], v[28:31]\n"
"s_waitcnt lgkmcnt(4)\n"
"buffer_load_dwordx4 a[196:199], v1, s[40:43], s84 offen offset:1024 sc1\n"
"v_mfma_f32_16x16x32_bf16 v[28:31], a[244:247], a[124:127], v[28:31]\n"
"v_mfma_f32_16x16x32_bf16 v[32:35], a[248:251], a[96:99], v[32:35]\n"
"ds_read_b128 a[240:243], v2 offset:4096\n"
"v_mfma_f32_16x16x32_bf16 v[32:35], a[252:255], a[112:115], v[32:35]\n"
"v_mfma_f32_16x16x32_bf16 v[36:39], a[248:251], a[100:103], v[36:39]\n"
"v_mfma_f32_16x16x32_bf16 v[36:39], a[252:255], a[116:119], v[36:39]\n"
"ds_read_b128 a[244:247], v2 offset:5120\n"
"v_mfma_f32_16x16x32_bf16 v[40:43], a[248:251], a[104:107], v[40:43]\n"
"buffer_load_dwordx4 a[200:203], v1, s[40:43], s84 offen offset:2048 sc1\n"
"v_mfma_f32_16x16x32_bf16 v[40:43], a[252:255], a[120:123], v[40:43]\n"
"v_mfma_f32_16x16x32_bf16 v[44:47], a[248:251], a[108:111], v[44:47]\n"
"s_waitcnt lgkmcnt(4)\n"
"v_mfma_f32_16x16x32_bf16 v[44:47], a[252:255], a[124:127], v[44:47]\n"
"buffer_load_dwordx4 a[204:207], v1, s[40:43], s84 offen offset:3072 sc1\n"
"v_mfma_f32_16x16x32_bf16 v[48:51], a[224:227], a[96:99], v[48:51]\n"
"ds_read_b128 a[248:251], v2 offset:6144\n"
"v_mfma_f32_16x16x32_bf16 v[48:51], a[228:231], a[112:115], v[48:51]\n"
"v_mfma_f32_16x16x32_bf16 v[52:55], a[224:227], a[100:103], v[52:55]\n"
"v_mfma_f32_16x16x32_bf16 v[52:55], a[228:231], a[116:119], v[52:55]\n"
"ds_read_b128 a[252:255], v2 offset:7168\n"
"v_mfma_f32_16x16x32_bf16 v[56:59], a[224:227], a[104:107], v[56:59]\n"
"s_add_u32 s84, s84, 4096\n"
"buffer_load_dwordx4 a[208:211], v1, s[40:43], s84 offen sc1\n"
"v_mfma_f32_16x16x32_bf16 v[56:59], a[228:231], a[120:123], v[56:59]\n"
"v_mfma_f32_16x16x32_bf16 v[60:63], a[224:227], a[108:111], v[60:63]\n"
"s_waitcnt lgkmcnt(4)\n"
"v_mfma_f32_16x16x32_bf16 v[60:63], a[228:231], a[124:127], v[60:63]\n"
"v_mfma_f32_16x16x32_bf16 v[64:67], a[232:235], a[96:99], v[64:67]\n"
"ds_read_b128 a[224:227], v2 offset:8192\n"
"v_mfma_f32_16x16x32_bf16 v[64:67], a[236:239], a[112:115], v[64:67]\n"
"buffer_load_dwordx4 a[212:215], v1, s[40:43], s84 offen offset:1024 sc1\n"
"v_mfma_f32_16x16x32_bf16 v[68:71], a[232:235], a[100:103], v[68:71]\n"
"v_mfma_f32_16x16x32_bf16 v[68:71], a[236:239], a[116:119], v[68:71]\n"
"ds_read_b128 a[228:231], v2 offset:9216\n"
"v_mfma_f32_16x16x32_bf16 v[72:75], a[232:235], a[104:107], v[72:75]\n"
"v_mfma_f32_16x16x32_bf16 v[72:75], a[236:239], a[120:123], v[72:75]\n"
"buffer_load_dwordx4 a[216:219], v1, s[40:43], s84 offen offset:2048 sc1\n"
"v_mfma_f32_16x16x32_bf16 v[76:79], a[232:235], a[108:111], v[76:79]\n"
"s_waitcnt lgkmcnt(4)\n"
"v_mfma_f32_16x16x32_bf16 v[76:79], a[236:239], a[124:127], v[76:79]\n"
"v_mfma_f32_16x16x32_bf16 v[144:147], a[240:243], a[96:99], v[144:147]\n"
"ds_read_b128 a[232:235], v2 offset:10240\n"
"v_mfma_f32_16x16x32_bf16 v[144:147], a[244:247], a[112:115], v[144:147]\n"
"buffer_load_dwordx4 a[220:223], v1, s[40:43], s84 offen offset:3072 sc1\n"
"v_mfma_f32_16x16x32_bf16 v[148:151], a[240:243], a[100:103], v[148:151]\n"
"v_mfma_f32_16x16x32_bf16 v[148:151], a[244:247], a[116:119], v[148:151]\n"
"ds_read_b128 a[236:239], v2 offset:11264\n"
"v_mfma_f32_16x16x32_bf16 v[152:155], a[240:243], a[104:107], v[152:155]\n"
"v_mfma_f32_16x16x32_bf16 v[152:155], a[244:247], a[120:123], v[152:155]\n"
"v_mfma_f32_16x16x32_bf16 v[156:159], a[240:243], a[108:111], v[156:159]\n"
"s_waitcnt lgkmcnt(4)\n"
"v_mfma_f32_16x16x32_bf16 v[156:159], a[244:247], a[124:127], v[156:159]\n"
"s_add_u32 s84, s84, 4096\n"
"v_mfma_f32_16x16x32_bf16 v[160:163], a[248:251], a[96:99], v[160:163]\n"
"s_add_u32 s82, s64, 524288\n"
"ds_read_b128 a[240:243], v2 offset:12288\n"
"v_mfma_f32_16x16x32_bf16 v[160:163], a[252:255], a[112:115], v[160:163]\n"
"s_add_u32 m0, s66, 131072\n"
"v_mfma_f32_16x16x32_bf16 v[164:167], a[248:251], a[100:103], v[164:167]\n"
"buffer_load_dwordx4 v1, s[40:43], s82 offen sc1 lds\n"
"v_mfma_f32_16x16x32_bf16 v[164:167], a[252:255], a[116:119], v[164:167]\n"
"ds_read_b128 a[244:247], v2 offset:13312\n"
"v_mfma_f32_16x16x32_bf16 v[168:171], a[248:251], a[104:107], v[168:171]\n"
"buffer_load_dwordx4 v1, s[40:43], s82 offen offset:1024 sc1 lds\n"
"v_mfma_f32_16x16x32_bf16 v[168:171], a[252:255], a[120:123], v[168:171]\n"
"v_mfma_f32_16x16x32_bf16 v[172:175], a[248:251], a[108:111], v[172:175]\n"
"s_waitcnt lgkmcnt(4)\n"
"v_mfma_f32_16x16x32_bf16 v[172:175], a[252:255], a[124:127], v[172:175]\n"
"buffer_load_dwordx4 v1, s[40:43], s82 offen offset:2048 sc1 lds\n"
"v_mfma_f32_16x16x32_bf16 v[176:179], a[224:227], a[96:99], v[176:179]\n"
"ds_read_b128 a[248:251], v2 offset:14336\n"
"v_mfma_f32_16x16x32_bf16 v[176:179], a[228:231], a[112:115], v[176:179]\n"
"v_mfma_f32_16x16x32_bf16 v[180:183], a[224:227], a[100:103], v[180:183]\n"
"v_mfma_f32_16x16x32_bf16 v[180:183], a[228:231], a[116:119], v[180:183]\n"
"ds_read_b128 a[252:255], v2 offset:15360\n"
"v_mfma_f32_16x16x32_bf16 v[184:187], a[224:227], a[104:107], v[184:187]\n"
"buffer_load_dwordx4 v1, s[40:43], s82 offen offset:3072 sc1 lds\n"
"v_mfma_f32_16x16x32_bf16 v[184:187], a[228:231], a[120:123], v[184:187]\n"
"v_mfma_f32_16x16x32_bf16 v[188:191], a[224:227], a[108:111], v[188:191]\n"
"s_waitcnt lgkmcnt(4)\n"
"v_mfma_f32_16x16x32_bf16 v[188:191], a[228:231], a[124:127], v[188:191]\n"
"v_mfma_f32_16x16x32_bf16 v[192:195], a[232:235], a[96:99], v[192:195]\n"
"ds_read_b128 a[224:227], v2 offset:16384\n"
"v_mfma_f32_16x16x32_bf16 v[192:195], a[236:239], a[112:115], v[192:195]\n"
"buffer_load_dwordx4 a[0:3], v1, s[40:43], s84 offen sc1\n"
"v_mfma_f32_16x16x32_bf16 v[196:199], a[232:235], a[100:103], v[196:199]\n"
"v_mfma_f32_16x16x32_bf16 v[196:199], a[236:239], a[116:119], v[196:199]\n"
"ds_read_b128 a[228:231], v2 offset:17408\n"
"v_mfma_f32_16x16x32_bf16 v[200:203], a[232:235], a[104:107], v[200:203]\n"
"buffer_load_dwordx4 a[4:7], v1, s[40:43], s84 offen offset:1024 sc1\n"
"v_mfma_f32_16x16x32_bf16 v[200:203], a[236:239], a[120:123], v[200:203]\n"
"v_mfma_f32_16x16x32_bf16 v[204:207], a[232:235], a[108:111], v[204:207]\n"
"s_waitcnt lgkmcnt(4)\n"
"v_mfma_f32_16x16x32_bf16 v[204:207], a[236:239], a[124:127], v[204:207]\n"
"v_mfma_f32_16x16x32_bf16 v[208:211], a[240:243], a[96:99], v[208:211]\n"
"ds_read_b128 a[232:235], v2 offset:18432\n"
"v_mfma_f32_16x16x32_bf16 v[208:211], a[244:247], a[112:115], v[208:211]\n"
"buffer_load_dwordx4 a[8:11], v1, s[40:43], s84 offen offset:2048 sc1\n"
"v_mfma_f32_16x16x32_bf16 v[212:215], a[240:243], a[100:103], v[212:215]\n"
"v_mfma_f32_16x16x32_bf16 v[212:215], a[244:247], a[116:119], v[212:215]\n"
"ds_read_b128 a[236:239], v2 offset:19456\n"
"v_mfma_f32_16x16x32_bf16 v[216:219], a[240:243], a[104:107], v[216:219]\n"
"v_mfma_f32_16x16x32_bf16 v[216:219], a[244:247], a[120:123], v[216:219]\n"
"buffer_load_dwordx4 a[12:15], v1, s[40:43], s84 offen offset:3072 sc1\n"
"v_mfma_f32_16x16x32_bf16 v[220:223], a[240:243], a[108:111], v[220:223]\n"
"s_waitcnt lgkmcnt(4)\n"
"v_mfma_f32_16x16x32_bf16 v[220:223], a[244:247], a[124:127], v[220:223]\n"
"v_mfma_f32_16x16x32_bf16 v[16:19], a[248:251], v[80:83], v[16:19]\n"
"ds_read_b128 a[240:243], v2 offset:20480\n"
"v_mfma_f32_16x16x32_bf16 v[16:19], a[252:255], v[96:99], v[16:19]\n"
"v_mfma_f32_16x16x32_bf16 v[20:23], a[248:251], v[84:87], v[20:23]\n"
"s_add_u32 s84, s84, 4096\n"
"buffer_load_dwordx4 a[16:19], v1, s[40:43], s84 offen sc1\n"
"v_mfma_f32_16x16x32_bf16 v[20:23], a[252:255], v[100:103], v[20:23]\n"
"ds_read_b128 a[244:247], v2 offset:21504\n"
"v_mfma_f32_16x16x32_bf16 v[24:27], a[248:251], v[88:91], v[24:27]\n"
"v_mfma_f32_16x16x32_bf16 v[24:27], a[252:255], v[104:107], v[24:27]\n"
"v_mfma_f32_16x16x32_bf16 v[28:31], a[248:251], v[92:95], v[28:31]\n"
"s_waitcnt lgkmcnt(4)\n"
"buffer_load_dwordx4 a[20:23], v1, s[40:43], s84 offen offset:1024 sc1\n"
"v_mfma_f32_16x16x32_bf16 v[28:31], a[252:255], v[108:111], v[28:31]\n"
"v_mfma_f32_16x16x32_bf16 v[32:35], a[224:227], v[80:83], v[32:35]\n"
"ds_read_b128 a[248:251], v2 offset:22528\n"
"v_mfma_f32_16x16x32_bf16 v[32:35], a[228:231], v[96:99], v[32:35]\n"
"v_mfma_f32_16x16x32_bf16 v[36:39], a[224:227], v[84:87], v[36:39]\n"
"v_mfma_f32_16x16x32_bf16 v[36:39], a[228:231], v[100:103], v[36:39]\n"
"ds_read_b128 a[252:255], v2 offset:23552\n"
"v_mfma_f32_16x16x32_bf16 v[40:43], a[224:227], v[88:91], v[40:43]\n"
"buffer_load_dwordx4 a[24:27], v1, s[40:43], s84 offen offset:2048 sc1\n"
"v_mfma_f32_16x16x32_bf16 v[40:43], a[228:231], v[104:107], v[40:43]\n"
"v_mfma_f32_16x16x32_bf16 v[44:47], a[224:227], v[92:95], v[44:47]\n"
"s_waitcnt lgkmcnt(4)\n"
"v_mfma_f32_16x16x32_bf16 v[44:47], a[228:231], v[108:111], v[44:47]\n"
"buffer_load_dwordx4 a[28:31], v1, s[40:43], s84 offen offset:3072 sc1\n"
"v_mfma_f32_16x16x32_bf16 v[48:51], a[232:235], v[80:83], v[48:51]\n"
"ds_read_b128 a[224:227], v2 offset:24576\n"
"v_mfma_f32_16x16x32_bf16 v[48:51], a[236:239], v[96:99], v[48:51]\n"
"v_mfma_f32_16x16x32_bf16 v[52:55], a[232:235], v[84:87], v[52:55]\n"
"v_mfma_f32_16x16x32_bf16 v[52:55], a[236:239], v[100:103], v[52:55]\n"
"ds_read_b128 a[228:231], v2 offset:25600\n"
"v_mfma_f32_16x16x32_bf16 v[56:59], a[232:235], v[88:91], v[56:59]\n"
"s_add_u32 s84, s84, 4096\n"
"v_mfma_f32_16x16x32_bf16 v[56:59], a[236:239], v[104:107], v[56:59]\n"
"s_add_u32 s82, s64, 540672\n"
"v_mfma_f32_16x16x32_bf16 v[60:63], a[232:235], v[92:95], v[60:63]\n"
"s_waitcnt lgkmcnt(4)\n"
"v_mfma_f32_16x16x32_bf16 v[60:63], a[236:239], v[108:111], v[60:63]\n"
"s_add_u32 m0, s66, 147456\n"
"v_mfma_f32_16x16x32_bf16 v[64:67], a[240:243], v[80:83], v[64:67]\n"
"ds_read_b128 a[232:235], v2 offset:26624\n"
"v_mfma_f32_16x16x32_bf16 v[64:67], a[244:247], v[96:99], v[64:67]\n"
"buffer_load_dwordx4 v1, s[40:43], s82 offen sc1 lds\n"
"v_mfma_f32_16x16x32_bf16 v[68:71], a[240:243], v[84:87], v[68:71]\n"
"buffer_load_dwordx4 v1, s[40:43], s82 offen offset:1024 sc1 lds\n"
"v_mfma_f32_16x16x32_bf16 v[68:71], a[244:247], v[100:103], v[68:71]\n"
"ds_read_b128 a[236:239], v2 offset:27648\n"
"v_mfma_f32_16x16x32_bf16 v[72:75], a[240:243], v[88:91], v[72:75]\n"
"v_mfma_f32_16x16x32_bf16 v[72:75], a[244:247], v[104:107], v[72:75]\n"
"buffer_load_dwordx4 v1, s[40:43], s82 offen offset:2048 sc1 lds\n"
"v_mfma_f32_16x16x32_bf16 v[76:79], a[240:243], v[92:95], v[76:79]\n"
"s_waitcnt lgkmcnt(4)\n"
"v_mfma_f32_16x16x32_bf16 v[76:79], a[244:247], v[108:111], v[76:79]\n"
"v_mfma_f32_16x16x32_bf16 v[144:147], a[248:251], v[80:83], v[144:147]\n"
"ds_read_b128 a[240:243], v2 offset:28672\n"
"v_mfma_f32_16x16x32_bf16 v[144:147], a[252:255], v[96:99], v[144:147]\n"
"buffer_load_dwordx4 v1, s[40:43], s82 offen offset:3072 sc1 lds\n"
"v_mfma_f32_16x16x32_bf16 v[148:151], a[248:251], v[84:87], v[148:151]\n"
"v_mfma_f32_16x16x32_bf16 v[148:151], a[252:255], v[100:103], v[148:151]\n"
"ds_read_b128 a[244:247], v2 offset:29696\n"
"v_mfma_f32_16x16x32_bf16 v[152:155], a[248:251], v[88:91], v[152:155]\n"
"v_mfma_f32_16x16x32_bf16 v[152:155], a[252:255], v[104:107], v[152:155]\n"
"v_mfma_f32_16x16x32_bf16 v[156:159], a[248:251], v[92:95], v[156:159]\n"
"s_waitcnt lgkmcnt(4)\n"
"buffer_load_dwordx4 a[32:35], v1, s[40:43], s84 offen sc1\n"
"v_mfma_f32_16x16x32_bf16 v[156:159], a[252:255], v[108:111], v[156:159]\n"
"v_mfma_f32_16x16x32_bf16 v[160:163], a[224:227], v[80:83], v[160:163]\n"
"ds_read_b128 a[248:251], v2 offset:30720\n"
"v_mfma_f32_16x16x32_bf16 v[160:163], a[228:231], v[96:99], v[160:163]\n"
"v_mfma_f32_16x16x32_bf16 v[164:167], a[224:227], v[84:87], v[164:167]\n"
"buffer_load_dwordx4 a[36:39], v1, s[40:43], s84 offen offset:1024 sc1\n"
"v_mfma_f32_16x16x32_bf16 v[164:167], a[228:231], v[100:103], v[164:167]\n"
"ds_read_b128 a[252:255], v2 offset:31744\n"
"v_mfma_f32_16x16x32_bf16 v[168:171], a[224:227], v[88:91], v[168:171]\n"
"v_mfma_f32_16x16x32_bf16 v[168:171], a[228:231], v[104:107], v[168:171]\n"
"v_mfma_f32_16x16x32_bf16 v[172:175], a[224:227], v[92:95], v[172:175]\n"
"s_waitcnt lgkmcnt(4)\n"
"v_mfma_f32_16x16x32_bf16 v[172:175], a[228:231], v[108:111], v[172:175]\n"
"s_waitcnt vmcnt(26)\n"
"s_barrier\n"
"v_mfma_f32_16x16x32_bf16 v[176:179], a[232:235], v[80:83], v[176:179]\n"
"ds_read_b128 a[224:227], v2 offset:32768\n"
"v_mfma_f32_16x16x32_bf16 v[176:179], a[236:239], v[96:99], v[176:179]\n"
"buffer_load_dwordx4 a[40:43], v1, s[40:43], s84 offen offset:2048 sc1\n"
"v_mfma_f32_16x16x32_bf16 v[180:183], a[232:235], v[84:87], v[180:183]\n"
"v_mfma_f32_16x16x32_bf16 v[180:183], a[236:239], v[100:103], v[180:183]\n"
"ds_read_b128 a[228:231], v2 offset:33792\n"
"v_mfma_f32_16x16x32_bf16 v[184:187], a[232:235], v[88:91], v[184:187]\n"
"buffer_load_dwordx4 a[44:47], v1, s[40:43], s84 offen offset:3072 sc1\n"
"v_mfma_f32_16x16x32_bf16 v[184:187], a[236:239], v[104:107], v[184:187]\n"
"v_mfma_f32_16x16x32_bf16 v[188:191], a[232:235], v[92:95], v[188:191]\n"
"s_waitcnt lgkmcnt(4)\n"
"v_mfma_f32_16x16x32_bf16 v[188:191], a[236:239], v[108:111], v[188:191]\n"
"v_mfma_f32_16x16x32_bf16 v[192:195], a[240:243], v[80:83], v[192:195]\n"
"s_add_u32 s84, s84, 4096\n"
"ds_read_b128 a[232:235], v2 offset:34816\n"
"v_mfma_f32_16x16x32_bf16 v[192:195], a[244:247], v[96:99], v[192:195]\n"
"buffer_load_dwordx4 a[48:51], v1, s[40:43], s84 offen sc1\n"
"v_mfma_f32_16x16x32_bf16 v[196:199], a[240:243], v[84:87], v[196:199]\n"
"v_mfma_f32_16x16x32_bf16 v[196:199], a[244:247], v[100:103], v[196:199]\n"
"ds_read_b128 a[236:239], v2 offset:35840\n"
"v_mfma_f32_16x16x32_bf16 v[200:203], a[240:243], v[88:91], v[200:203]\n"
"buffer_load_dwordx4 a[52:55], v1, s[40:43], s84 offen offset:1024 sc1\n"
"v_mfma_f32_16x16x32_bf16 v[200:203], a[244:247], v[104:107], v[200:203]\n"
"v_mfma_f32_16x16x32_bf16 v[204:207], a[240:243], v[92:95], v[204:207]\n"
"s_waitcnt lgkmcnt(4)\n"
"v_mfma_f32_16x16x32_bf16 v[204:207], a[244:247], v[108:111], v[204:207]\n"
"v_mfma_f32_16x16x32_bf16 v[208:211], a[248:251], v[80:83], v[208:211]\n"
"ds_read_b128 a[240:243], v2 offset:36864\n"
"v_mfma_f32_16x16x32_bf16 v[208:211], a[252:255], v[96:99], v[208:211]\n"
"buffer_load_dwordx4 a[56:59], v1, s[40:43], s84 offen offset:2048 sc1\n"
"v_mfma_f32_16x16x32_bf16 v[212:215], a[248:251], v[84:87], v[212:215]\n"
"v_mfma_f32_16x16x32_bf16 v[212:215], a[252:255], v[100:103], v[212:215]\n"
"ds_read_b128 a[244:247], v2 offset:37888\n"
"v_mfma_f32_16x16x32_bf16 v[216:219], a[248:251], v[88:91], v[216:219]\n"
"v_mfma_f32_16x16x32_bf16 v[216:219], a[252:255], v[104:107], v[216:219]\n"
"buffer_load_dwordx4 a[60:63], v1, s[40:43], s84 offen offset:3072 sc1\n"
"v_mfma_f32_16x16x32_bf16 v[220:223], a[248:251], v[92:95], v[220:223]\n"
"s_waitcnt lgkmcnt(4)\n"
"v_mfma_f32_16x16x32_bf16 v[220:223], a[252:255], v[108:111], v[220:223]\n"
"v_mfma_f32_16x16x32_bf16 v[16:19], a[224:227], v[112:115], v[16:19]\n"
"ds_read_b128 a[248:251], v2 offset:38912\n"
"v_mfma_f32_16x16x32_bf16 v[16:19], a[228:231], v[128:131], v[16:19]\n"
"v_mfma_f32_16x16x32_bf16 v[20:23], a[224:227], v[116:119], v[20:23]\n"
"s_add_u32 s84, s84, 4096\n"
"v_mfma_f32_16x16x32_bf16 v[20:23], a[228:231], v[132:135], v[20:23]\n"
"s_add_u32 s82, s64, 557056\n"
"ds_read_b128 a[252:255], v2 offset:39936\n"
"v_mfma_f32_16x16x32_bf16 v[24:27], a[224:227], v[120:123], v[24:27]\n"
"s_add_u32 m0, s66, 0\n"
"v_mfma_f32_16x16x32_bf16 v[24:27], a[228:231], v[136:139], v[24:27]\n"
"buffer_load_dwordx4 v1, s[40:43], s82 offen sc1 lds\n"
"v_mfma_f32_16x16x32_bf16 v[28:31], a[224:227], v[124:127], v[28:31]\n"
"s_waitcnt lgkmcnt(4)\n"
"buffer_load_dwordx4 v1, s[40:43], s82 offen offset:1024 sc1 lds\n"
"v_mfma_f32_16x16x32_bf16 v[28:31], a[228:231], v[140:143], v[28:31]\n"
"v_mfma_f32_16x16x32_bf16 v[32:35], a[232:235], v[112:115], v[32:35]\n"
"ds_read_b128 a[224:227], v2 offset:40960\n"
"v_mfma_f32_16x16x32_bf16 v[32:35], a[236:239], v[128:131], v[32:35]\n"
"v_mfma_f32_16x16x32_bf16 v[36:39], a[232:235], v[116:119], v[36:39]\n"
"v_mfma_f32_16x16x32_bf16 v[36:39], a[236:239], v[132:135], v[36:39]\n"
"ds_read_b128 a[228:231], v2 offset:41984\n"
"v_mfma_f32_16x16x32_bf16 v[40:43], a[232:235], v[120:123], v[40:43]\n"
"buffer_load_dwordx4 v1, s[40:43], s82 offen offset:2048 sc1 lds\n"
"v_mfma_f32_16x16x32_bf16 v[40:43], a[236:239], v[136:139], v[40:43]\n"
"v_mfma_f32_16x16x32_bf16 v[44:47], a[232:235], v[124:127], v[44:47]\n"
"s_waitcnt lgkmcnt(4)\n"
"v_mfma_f32_16x16x32_bf16 v[44:47], a[236:239], v[140:143], v[44:47]\n"
"buffer_load_dwordx4 v1, s[40:43], s82 offen offset:3072 sc1 lds\n"
"v_mfma_f32_16x16x32_bf16 v[48:51], a[240:243], v[112:115], v[48:51]\n"
"ds_read_b128 a[232:235], v2 offset:43008\n"
"v_mfma_f32_16x16x32_bf16 v[48:51], a[244:247], v[128:131], v[48:51]\n"
"v_mfma_f32_16x16x32_bf16 v[52:55], a[240:243], v[116:119], v[52:55]\n"
"v_mfma_f32_16x16x32_bf16 v[52:55], a[244:247], v[132:135], v[52:55]\n"
"ds_read_b128 a[236:239], v2 offset:44032\n"
"v_mfma_f32_16x16x32_bf16 v[56:59], a[240:243], v[120:123], v[56:59]\n"
"buffer_load_dwordx4 a[64:67], v1, s[40:43], s84 offen sc1\n"
"v_mfma_f32_16x16x32_bf16 v[56:59], a[244:247], v[136:139], v[56:59]\n"
"v_mfma_f32_16x16x32_bf16 v[60:63], a[240:243], v[124:127], v[60:63]\n"
"s_waitcnt lgkmcnt(4)\n"
"v_mfma_f32_16x16x32_bf16 v[60:63], a[244:247], v[140:143], v[60:63]\n"
"v_mfma_f32_16x16x32_bf16 v[16:19], a[248:251], a[128:131], v[16:19]\n"
"ds_read_b128 a[240:243], v2 offset:45056\n"
"v_mfma_f32_16x16x32_bf16 v[16:19], a[252:255], a[144:147], v[16:19]\n"
"buffer_load_dwordx4 a[68:71], v1, s[40:43], s84 offen offset:1024 sc1\n"
"v_mfma_f32_16x16x32_bf16 v[20:23], a[248:251], a[132:135], v[20:23]\n"
"v_mfma_f32_16x16x32_bf16 v[20:23], a[252:255], a[148:151], v[20:23]\n"
"ds_read_b128 a[244:247], v2 offset:46080\n"
"v_mfma_f32_16x16x32_bf16 v[24:27], a[248:251], a[136:139], v[24:27]\n"
"v_mfma_f32_16x16x32_bf16 v[24:27], a[252:255], a[152:155], v[24:27]\n"
"buffer_load_dwordx4 a[72:75], v1, s[40:43], s84 offen offset:2048 sc1\n"
"v_mfma_f32_16x16x32_bf16 v[28:31], a[248:251], a[140:143], v[28:31]\n"
"s_waitcnt lgkmcnt(4)\n"
"v_mfma_f32_16x16x32_bf16 v[28:31], a[252:255], a[156:159], v[28:31]\n"
"v_mfma_f32_16x16x32_bf16 v[32:35], a[224:227], a[128:131], v[32:35]\n"
"ds_read_b128 a[248:251], v2 offset:47104\n"
"v_mfma_f32_16x16x32_bf16 v[32:35], a[228:231], a[144:147], v[32:35]\n"
"buffer_load_dwordx4 a[76:79], v1, s[40:43], s84 offen offset:3072 sc1\n"
"v_mfma_f32_16x16x32_bf16 v[36:39], a[224:227], a[132:135], v[36:39]\n"
"v_mfma_f32_16x16x32_bf16 v[36:39], a[228:231], a[148:151], v[36:39]\n"
"ds_read_b128 a[252:255], v2 offset:48128\n"
"v_mfma_f32_16x16x32_bf16 v[40:43], a[224:227], a[136:139], v[40:43]\n"
"v_mfma_f32_16x16x32_bf16 v[40:43], a[228:231], a[152:155], v[40:43]\n"
"v_mfma_f32_16x16x32_bf16 v[44:47], a[224:227], a[140:143], v[44:47]\n"
"s_waitcnt lgkmcnt(4)\n"
"v_mfma_f32_16x16x32_bf16 v[44:47], a[228:231], a[156:159], v[44:47]\n"
"s_add_u32 s84, s84, 4096\n"
"buffer_load_dwordx4 a[80:83], v1, s[40:43], s84 offen sc1\n"
"v_mfma_f32_16x16x32_bf16 v[48:51], a[232:235], a[128:131], v[48:51]\n"
"ds_read_b128 a[224:227], v2 offset:49152\n"
"v_mfma_f32_16x16x32_bf16 v[48:51], a[236:239], a[144:147], v[48:51]\n"
"v_mfma_f32_16x16x32_bf16 v[52:55], a[232:235], a[132:135], v[52:55]\n"
"buffer_load_dwordx4 a[84:87], v1, s[40:43], s84 offen offset:1024 sc1\n"
"v_mfma_f32_16x16x32_bf16 v[52:55], a[236:239], a[148:151], v[52:55]\n"
"ds_read_b128 a[228:231], v2 offset:50176\n"
"v_mfma_f32_16x16x32_bf16 v[56:59], a[232:235], a[136:139], v[56:59]\n"
"v_mfma_f32_16x16x32_bf16 v[56:59], a[236:239], a[152:155], v[56:59]\n"
"v_mfma_f32_16x16x32_bf16 v[60:63], a[232:235], a[140:143], v[60:63]\n"
"s_waitcnt lgkmcnt(4)\n"
"v_mfma_f32_16x16x32_bf16 v[60:63], a[236:239], a[156:159], v[60:63]\n"
"buffer_load_dwordx4 a[88:91], v1, s[40:43], s84 offen offset:2048 sc1\n"
"v_mfma_f32_16x16x32_bf16 v[16:19], a[240:243], a[160:163], v[16:19]\n"
"ds_read_b128 a[232:235], v2 offset:51200\n"
"v_mfma_f32_16x16x32_bf16 v[16:19], a[244:247], a[176:179], v[16:19]\n"
"v_mfma_f32_16x16x32_bf16 v[20:23], a[240:243], a[164:167], v[20:23]\n"
"v_mfma_f32_16x16x32_bf16 v[20:23], a[244:247], a[180:183], v[20:23]\n"
"ds_read_b128 a[236:239], v2 offset:52224\n"
"v_mfma_f32_16x16x32_bf16 v[24:27], a[240:243], a[168:171], v[24:27]\n"
"buffer_load_dwordx4 a[92:95], v1, s[40:43], s84 offen offset:3072 sc1\n"
"v_mfma_f32_16x16x32_bf16 v[24:27], a[244:247], a[184:187], v[24:27]\n"
"v_mfma_f32_16x16x32_bf16 v[28:31], a[240:243], a[172:175], v[28:31]\n"
"s_waitcnt lgkmcnt(4)\n"
"v_mfma_f32_16x16x32_bf16 v[28:31], a[244:247], a[188:191], v[28:31]\n"
"v_mfma_f32_16x16x32_bf16 v[32:35], a[248:251], a[160:163], v[32:35]\n"
"s_add_u32 s84, s84, 4096\n"
"ds_read_b128 a[240:243], v2 offset:53248\n"
"v_mfma_f32_16x16x32_bf16 v[32:35], a[252:255], a[176:179], v[32:35]\n"
"v_mfma_f32_16x16x32_bf16 v[36:39], a[248:251], a[164:167], v[36:39]\n"
"v_mfma_f32_16x16x32_bf16 v[36:39], a[252:255], a[180:183], v[36:39]\n"
"ds_read_b128 a[244:247], v2 offset:54272\n"
"v_mfma_f32_16x16x32_bf16 v[40:43], a[248:251], a[168:171], v[40:43]\n"
"v_mfma_f32_16x16x32_bf16 v[40:43], a[252:255], a[184:187], v[40:43]\n"
"v_mfma_f32_16x16x32_bf16 v[44:47], a[248:251], a[172:175], v[44:47]\n"
"s_waitcnt lgkmcnt(4)\n"
"v_mfma_f32_16x16x32_bf16 v[44:47], a[252:255], a[188:191], v[44:47]\n"
"v_mfma_f32_16x16x32_bf16 v[48:51], a[224:227], a[160:163], v[48:51]\n"
"ds_read_b128 a[248:251], v2 offset:55296\n"
"v_mfma_f32_16x16x32_bf16 v[48:51], a[228:231], a[176:179], v[48:51]\n"
"s_add_u32 s82, s64, 573440\n"
"v_mfma_f32_16x16x32_bf16 v[52:55], a[224:227], a[164:167], v[52:55]\n"
"s_add_u32 m0, s66, 16384\n"
"v_mfma_f32_16x16x32_bf16 v[52:55], a[228:231], a[180:183], v[52:55]\n"
"ds_read_b128 a[252:255], v2 offset:56320\n"
"v_mfma_f32_16x16x32_bf16 v[56:59], a[224:227], a[168:171], v[56:59]\n"
"buffer_load_dwordx4 v1, s[40:43], s82 offen sc1 lds\n"
"v_mfma_f32_16x16x32_bf16 v[56:59], a[228:231], a[184:187], v[56:59]\n"
"buffer_load_dwordx4 v1, s[40:43], s82 offen offset:1024 sc1 lds\n"
"v_mfma_f32_16x16x32_bf16 v[60:63], a[224:227], a[172:175], v[60:63]\n"
"s_waitcnt vmcnt(38) lgkmcnt(4)\n"
"v_mfma_f32_16x16x32_bf16 v[60:63], a[228:231], a[188:191], v[60:63]\n"
"v_mfma_f32_16x16x32_bf16 v[16:19], a[232:235], a[192:195], v[16:19]\n"
"ds_read_b128 a[224:227], v2 offset:57344\n"
"v_mfma_f32_16x16x32_bf16 v[16:19], a[236:239], a[208:211], v[16:19]\n"
"v_mfma_f32_16x16x32_bf16 v[20:23], a[232:235], a[196:199], v[20:23]\n"
"buffer_load_dwordx4 v1, s[40:43], s82 offen offset:2048 sc1 lds\n"
"v_mfma_f32_16x16x32_bf16 v[20:23], a[236:239], a[212:215], v[20:23]\n"
"ds_read_b128 a[228:231], v2 offset:58368\n"
"v_mfma_f32_16x16x32_bf16 v[24:27], a[232:235], a[200:203], v[24:27]\n"
"v_mfma_f32_16x16x32_bf16 v[24:27], a[236:239], a[216:219], v[24:27]\n"
"v_mfma_f32_16x16x32_bf16 v[28:31], a[232:235], a[204:207], v[28:31]\n"
"s_waitcnt lgkmcnt(4)\n"
"buffer_load_dwordx4 v1, s[40:43], s82 offen offset:3072 sc1 lds\n"
"v_mfma_f32_16x16x32_bf16 v[28:31], a[236:239], a[220:223], v[28:31]\n"
"v_mfma_f32_16x16x32_bf16 v[32:35], a[240:243], a[192:195], v[32:35]\n"
"ds_read_b128 a[232:235], v2 offset:59392\n"
"v_mfma_f32_16x16x32_bf16 v[32:35], a[244:247], a[208:211], v[32:35]\n"
"v_mfma_f32_16x16x32_bf16 v[36:39], a[240:243], a[196:199], v[36:39]\n"
"v_mfma_f32_16x16x32_bf16 v[36:39], a[244:247], a[212:215], v[36:39]\n"
"ds_read_b128 a[236:239], v2 offset:60416\n"
"v_mfma_f32_16x16x32_bf16 v[40:43], a[240:243], a[200:203], v[40:43]\n"
"buffer_load_dwordx4 a[96:99], v1, s[40:43], s84 offen sc1\n"
"v_mfma_f32_16x16x32_bf16 v[40:43], a[244:247], a[216:219], v[40:43]\n"
"v_mfma_f32_16x16x32_bf16 v[44:47], a[240:243], a[204:207], v[44:47]\n"
"s_waitcnt lgkmcnt(4)\n"
"v_mfma_f32_16x16x32_bf16 v[44:47], a[244:247], a[220:223], v[44:47]\n"
"buffer_load_dwordx4 a[100:103], v1, s[40:43], s84 offen offset:1024 sc1\n"
"v_mfma_f32_16x16x32_bf16 v[48:51], a[248:251], a[192:195], v[48:51]\n"
"ds_read_b128 a[240:243], v2 offset:61440\n"
"v_mfma_f32_16x16x32_bf16 v[48:51], a[252:255], a[208:211], v[48:51]\n"
"v_mfma_f32_16x16x32_bf16 v[52:55], a[248:251], a[196:199], v[52:55]\n"
"v_mfma_f32_16x16x32_bf16 v[52:55], a[252:255], a[212:215], v[52:55]\n"
"ds_read_b128 a[244:247], v2 offset:62464\n"
"v_mfma_f32_16x16x32_bf16 v[56:59], a[248:251], a[200:203], v[56:59]\n"
"buffer_load_dwordx4 a[104:107], v1, s[40:43], s84 offen offset:2048 sc1\n"
"v_mfma_f32_16x16x32_bf16 v[56:59], a[252:255], a[216:219], v[56:59]\n"
"v_mfma_f32_16x16x32_bf16 v[60:63], a[248:251], a[204:207], v[60:63]\n"
"s_waitcnt vmcnt(31) lgkmcnt(4)\n"
"v_mfma_f32_16x16x32_bf16 v[60:63], a[252:255], a[220:223], v[60:63]\n"
"v_mfma_f32_16x16x32_bf16 v[16:19], a[224:227], a[0:3], v[16:19]\n"
"ds_read_b128 a[248:251], v2 offset:63488\n"
"v_mfma_f32_16x16x32_bf16 v[16:19], a[228:231], a[16:19], v[16:19]\n"
"buffer_load_dwordx4 a[108:111], v1, s[40:43], s84 offen offset:3072 sc1\n"
"v_mfma_f32_16x16x32_bf16 v[20:23], a[224:227], a[4:7], v[20:23]\n"
"v_mfma_f32_16x16x32_bf16 v[20:23], a[228:231], a[20:23], v[20:23]\n"
"ds_read_b128 a[252:255], v2 offset:64512\n"
"v_mfma_f32_16x16x32_bf16 v[24:27], a[224:227], a[8:11], v[24:27]\n"
"v_mfma_f32_16x16x32_bf16 v[24:27], a[228:231], a[24:27], v[24:27]\n"
"s_add_u32 s84, s84, 4096\n"
"buffer_load_dwordx4 a[112:115], v1, s[40:43], s84 offen sc1\n"
"v_mfma_f32_16x16x32_bf16 v[28:31], a[224:227], a[12:15], v[28:31]\n"
"s_waitcnt lgkmcnt(4)\n"
"v_mfma_f32_16x16x32_bf16 v[28:31], a[228:231], a[28:31], v[28:31]\n"
"s_waitcnt vmcnt(29)\n"
"s_barrier\n"
"v_mfma_f32_16x16x32_bf16 v[32:35], a[232:235], a[0:3], v[32:35]\n"
"ds_read_b128 a[224:227], v3\n"
"v_mfma_f32_16x16x32_bf16 v[32:35], a[236:239], a[16:19], v[32:35]\n"
"buffer_load_dwordx4 a[116:119], v1, s[40:43], s84 offen offset:1024 sc1\n"
"v_mfma_f32_16x16x32_bf16 v[36:39], a[232:235], a[4:7], v[36:39]\n"
"v_mfma_f32_16x16x32_bf16 v[36:39], a[236:239], a[20:23], v[36:39]\n"
"ds_read_b128 a[228:231], v3 offset:1024\n"
"v_mfma_f32_16x16x32_bf16 v[40:43], a[232:235], a[8:11], v[40:43]\n"
"v_mfma_f32_16x16x32_bf16 v[40:43], a[236:239], a[24:27], v[40:43]\n"
"v_mfma_f32_16x16x32_bf16 v[44:47], a[232:235], a[12:15], v[44:47]\n"
"s_waitcnt lgkmcnt(4)\n"
"buffer_load_dwordx4 a[120:123], v1, s[40:43], s84 offen offset:2048 sc1\n"
"v_mfma_f32_16x16x32_bf16 v[44:47], a[236:239], a[28:31], v[44:47]\n"
"v_mfma_f32_16x16x32_bf16 v[48:51], a[240:243], a[0:3], v[48:51]\n"
"ds_read_b128 a[232:235], v3 offset:2048\n"
"v_mfma_f32_16x16x32_bf16 v[48:51], a[244:247], a[16:19], v[48:51]\n"
"v_mfma_f32_16x16x32_bf16 v[52:55], a[240:243], a[4:7], v[52:55]\n"
"buffer_load_dwordx4 a[124:127], v1, s[40:43], s84 offen offset:3072 sc1\n"
"v_mfma_f32_16x16x32_bf16 v[52:55], a[244:247], a[20:23], v[52:55]\n"
"ds_read_b128 a[236:239], v3 offset:3072\n"
"v_mfma_f32_16x16x32_bf16 v[56:59], a[240:243], a[8:11], v[56:59]\n"
"v_mfma_f32_16x16x32_bf16 v[56:59], a[244:247], a[24:27], v[56:59]\n"
"v_mfma_f32_16x16x32_bf16 v[60:63], a[240:243], a[12:15], v[60:63]\n"
"s_waitcnt vmcnt(24) lgkmcnt(4)\n"
"v_mfma_f32_16x16x32_bf16 v[60:63], a[244:247], a[28:31], v[60:63]\n"
"s_add_u32 s84, s84, 4096\n"
"v_mfma_f32_16x16x32_bf16 v[16:19], a[248:251], a[32:35], v[16:19]\n"
"ds_read_b128 a[240:243], v3 offset:4096\n"
"v_mfma_f32_16x16x32_bf16 v[16:19], a[252:255], a[48:51], v[16:19]\n"
"v_mfma_f32_16x16x32_bf16 v[20:23], a[248:251], a[36:39], v[20:23]\n"
"v_mfma_f32_16x16x32_bf16 v[20:23], a[252:255], a[52:55], v[20:23]\n"
"ds_read_b128 a[244:247], v3 offset:5120\n"
"v_mfma_f32_16x16x32_bf16 v[24:27], a[248:251], a[40:43], v[24:27]\n"
"v_mfma_f32_16x16x32_bf16 v[24:27], a[252:255], a[56:59], v[24:27]\n"
"v_mfma_f32_16x16x32_bf16 v[28:31], a[248:251], a[44:47], v[28:31]\n"
"s_waitcnt lgkmcnt(4)\n"
"v_mfma_f32_16x16x32_bf16 v[28:31], a[252:255], a[60:63], v[28:31]\n"
"v_mfma_f32_16x16x32_bf16 v[32:35], a[224:227], a[32:35], v[32:35]\n"
"ds_read_b128 a[248:251], v3 offset:6144\n"
"v_mfma_f32_16x16x32_bf16 v[32:35], a[228:231], a[48:51], v[32:35]\n"
"s_add_u32 s82, s64, 589824\n"
"v_mfma_f32_16x16x32_bf16 v[36:39], a[224:227], a[36:39], v[36:39]\n"
"s_add_u32 m0, s66, 32768\n"
"v_mfma_f32_16x16x32_bf16 v[36:39], a[228:231], a[52:55], v[36:39]\n"
"ds_read_b128 a[252:255], v3 offset:7168\n"
"v_mfma_f32_16x16x32_bf16 v[40:43], a[224:227], a[40:43], v[40:43]\n"
"buffer_load_dwordx4 v1, s[40:43], s82 offen sc1 lds\n"
"v_mfma_f32_16x16x32_bf16 v[40:43], a[228:231], a[56:59], v[40:43]\n"
"buffer_load_dwordx4 v1, s[40:43], s82 offen offset:1024 sc1 lds\n"
"v_mfma_f32_16x16x32_bf16 v[44:47], a[224:227], a[44:47], v[44:47]\n"
"s_waitcnt lgkmcnt(4)\n"
"v_mfma_f32_16x16x32_bf16 v[44:47], a[228:231], a[60:63], v[44:47]\n"
"v_mfma_f32_16x16x32_bf16 v[48:51], a[232:235], a[32:35], v[48:51]\n"
"ds_read_b128 a[224:227], v3 offset:8192\n"
"v_mfma_f32_16x16x32_bf16 v[48:51], a[236:239], a[48:51], v[48:51]\n"
"v_mfma_f32_16x16x32_bf16 v[52:55], a[232:235], a[36:39], v[52:55]\n"
"buffer_load_dwordx4 v1, s[40:43], s82 offen offset:2048 sc1 lds\n"
"v_mfma_f32_16x16x32_bf16 v[52:55], a[236:239], a[52:55], v[52:55]\n"
"ds_read_b128 a[228:231], v3 offset:9216\n"
"v_mfma_f32_16x16x32_bf16 v[56:59], a[232:235], a[40:43], v[56:59]\n"
"v_mfma_f32_16x16x32_bf16 v[56:59], a[236:239], a[56:59], v[56:59]\n"
"v_mfma_f32_16x16x32_bf16 v[60:63], a[232:235], a[44:47], v[60:63]\n"
"s_waitcnt vmcnt(15) lgkmcnt(4)\n"
"buffer_load_dwordx4 v1, s[40:43], s82 offen offset:3072 sc1 lds\n"
"v_mfma_f32_16x16x32_bf16 v[60:63], a[236:239], a[60:63], v[60:63]\n"
"v_mfma_f32_16x16x32_bf16 v[16:19], a[240:243], a[64:67], v[16:19]\n"
"ds_read_b128 a[232:235], v3 offset:10240\n"
"v_mfma_f32_16x16x32_bf16 v[16:19], a[244:247], a[80:83], v[16:19]\n"
"v_mfma_f32_16x16x32_bf16 v[20:23], a[240:243], a[68:71], v[20:23]\n"
"v_mfma_f32_16x16x32_bf16 v[20:23], a[244:247], a[84:87], v[20:23]\n"
"ds_read_b128 a[236:239], v3 offset:11264\n"
"v_mfma_f32_16x16x32_bf16 v[24:27], a[240:243], a[72:75], v[24:27]\n"
"v_mfma_f32_16x16x32_bf16 v[24:27], a[244:247], a[88:91], v[24:27]\n"
"v_mfma_f32_16x16x32_bf16 v[28:31], a[240:243], a[76:79], v[28:31]\n"
"s_waitcnt lgkmcnt(4)\n"
"v_mfma_f32_16x16x32_bf16 v[28:31], a[244:247], a[92:95], v[28:31]\n"
"v_mfma_f32_16x16x32_bf16 v[32:35], a[248:251], a[64:67], v[32:35]\n"
"ds_read_b128 a[240:243], v3 offset:12288\n"
"v_mfma_f32_16x16x32_bf16 v[32:35], a[252:255], a[80:83], v[32:35]\n"
"v_mfma_f32_16x16x32_bf16 v[36:39], a[248:251], a[68:71], v[36:39]\n"
"v_mfma_f32_16x16x32_bf16 v[36:39], a[252:255], a[84:87], v[36:39]\n"
"ds_read_b128 a[244:247], v3 offset:13312\n"
"v_mfma_f32_16x16x32_bf16 v[40:43], a[248:251], a[72:75], v[40:43]\n"
"v_mfma_f32_16x16x32_bf16 v[40:43], a[252:255], a[88:91], v[40:43]\n"
"v_mfma_f32_16x16x32_bf16 v[44:47], a[248:251], a[76:79], v[44:47]\n"
"s_waitcnt lgkmcnt(4)\n"
"v_mfma_f32_16x16x32_bf16 v[44:47], a[252:255], a[92:95], v[44:47]\n"
"v_mfma_f32_16x16x32_bf16 v[48:51], a[224:227], a[64:67], v[48:51]\n"
"ds_read_b128 a[248:251], v3 offset:14336\n"
"v_mfma_f32_16x16x32_bf16 v[48:51], a[228:231], a[80:83], v[48:51]\n"
"v_mfma_f32_16x16x32_bf16 v[52:55], a[224:227], a[68:71], v[52:55]\n"
"v_mfma_f32_16x16x32_bf16 v[52:55], a[228:231], a[84:87], v[52:55]\n"
"ds_read_b128 a[252:255], v3 offset:15360\n"
"v_mfma_f32_16x16x32_bf16 v[56:59], a[224:227], a[72:75], v[56:59]\n"
"v_mfma_f32_16x16x32_bf16 v[56:59], a[228:231], a[88:91], v[56:59]\n"
"v_mfma_f32_16x16x32_bf16 v[60:63], a[224:227], a[76:79], v[60:63]\n"
"s_waitcnt vmcnt(4) lgkmcnt(4)\n"
"v_mfma_f32_16x16x32_bf16 v[60:63], a[228:231], a[92:95], v[60:63]\n"
"v_mfma_f32_16x16x32_bf16 v[16:19], a[232:235], a[96:99], v[16:19]\n"
"ds_read_b128 a[224:227], v3 offset:16384\n"
"v_mfma_f32_16x16x32_bf16 v[16:19], a[236:239], a[112:115], v[16:19]\n"
"v_mfma_f32_16x16x32_bf16 v[20:23], a[232:235], a[100:103], v[20:23]\n"
"v_mfma_f32_16x16x32_bf16 v[20:23], a[236:239], a[116:119], v[20:23]\n"
"s_lshl_b32 s80, s101, 1\n"
"s_add_u32 s80, s80, 2\n"
"ds_read_b128 a[228:231], v3 offset:17408\n"
"v_mfma_f32_16x16x32_bf16 v[24:27], a[232:235], a[104:107], v[24:27]\n"
"s_and_b32 s80, s80, 7\n"
"s_lshl_b32 s80, s80, 14\n"
"s_add_u32 s85, s96, s80\n"
"v_mfma_f32_16x16x32_bf16 v[24:27], a[236:239], a[120:123], v[24:27]\n"
"v_mfma_f32_16x16x32_bf16 v[28:31], a[232:235], a[108:111], v[28:31]\n"
"s_waitcnt lgkmcnt(4)\n"
"v_mfma_f32_16x16x32_bf16 v[28:31], a[236:239], a[124:127], v[28:31]\n"
"v_mfma_f32_16x16x32_bf16 v[32:35], a[240:243], a[96:99], v[32:35]\n"
"ds_read_b128 a[232:235], v3 offset:18432\n"
"v_mfma_f32_16x16x32_bf16 v[32:35], a[244:247], a[112:115], v[32:35]\n"
"v_mfma_f32_16x16x32_bf16 v[36:39], a[240:243], a[100:103], v[36:39]\n"
"v_mfma_f32_16x16x32_bf16 v[36:39], a[244:247], a[116:119], v[36:39]\n"
"ds_read_b128 a[236:239], v3 offset:19456\n"
"v_cvt_pk_bf16_f32 v16, v16, v17\n"
"v_mfma_f32_16x16x32_bf16 v[40:43], a[240:243], a[104:107], v[40:43]\n"
"v_cvt_pk_bf16_f32 v17, v18, v19\n"
"v_cvt_pk_bf16_f32 v18, v20, v21\n"
"v_mfma_f32_16x16x32_bf16 v[40:43], a[244:247], a[120:123], v[40:43]\n"
"v_cvt_pk_bf16_f32 v19, v22, v23\n"
"v_cvt_pk_bf16_f32 v20, v24, v25\n"
"v_mfma_f32_16x16x32_bf16 v[44:47], a[240:243], a[108:111], v[44:47]\n"
"s_waitcnt lgkmcnt(4)\n"
"v_cvt_pk_bf16_f32 v21, v26, v27\n"
"v_cvt_pk_bf16_f32 v22, v28, v29\n"
"v_mfma_f32_16x16x32_bf16 v[44:47], a[244:247], a[124:127], v[44:47]\n"
"v_cvt_pk_bf16_f32 v23, v30, v31\n"
"v_mfma_f32_16x16x32_bf16 v[48:51], a[248:251], a[96:99], v[48:51]\n"
"ds_read_b128 a[240:243], v3 offset:20480\n"
"v_mfma_f32_16x16x32_bf16 v[48:51], a[252:255], a[112:115], v[48:51]\n"
"buffer_store_dwordx4 v[16:19], v1, s[40:43], s85 offen sc1\n"
"v_mfma_f32_16x16x32_bf16 v[52:55], a[248:251], a[100:103], v[52:55]\n"
"v_mfma_f32_16x16x32_bf16 v[52:55], a[252:255], a[116:119], v[52:55]\n"
"ds_read_b128 a[244:247], v3 offset:21504\n"
"v_mfma_f32_16x16x32_bf16 v[56:59], a[248:251], a[104:107], v[56:59]\n"
"v_mfma_f32_16x16x32_bf16 v[56:59], a[252:255], a[120:123], v[56:59]\n"
"v_mfma_f32_16x16x32_bf16 v[60:63], a[248:251], a[108:111], v[60:63]\n"
"s_waitcnt lgkmcnt(4)\n"
"v_mfma_f32_16x16x32_bf16 v[60:63], a[252:255], a[124:127], v[60:63]\n"
"v_mfma_f32_16x16x32_bf16 v[64:67], a[224:227], v[112:115], v[64:67]\n"
"ds_read_b128 a[248:251], v3 offset:22528\n"
"v_mfma_f32_16x16x32_bf16 v[64:67], a[228:231], v[128:131], v[64:67]\n"
"s_add_u32 s82, s64, 606208\n"
"v_mfma_f32_16x16x32_bf16 v[68:71], a[224:227], v[116:119], v[68:71]\n"
"s_add_u32 m0, s66, 49152\n"
"v_mfma_f32_16x16x32_bf16 v[68:71], a[228:231], v[132:135], v[68:71]\n"
"ds_read_b128 a[252:255], v3 offset:23552\n"
"v_mfma_f32_16x16x32_bf16 v[72:75], a[224:227], v[120:123], v[72:75]\n"
"buffer_load_dwordx4 v1, s[40:43], s82 offen sc1 lds\n"
"v_mfma_f32_16x16x32_bf16 v[72:75], a[228:231], v[136:139], v[72:75]\n"
"buffer_load_dwordx4 v1, s[40:43], s82 offen offset:1024 sc1 lds\n"
"v_mfma_f32_16x16x32_bf16 v[76:79], a[224:227], v[124:127], v[76:79]\n"
"s_waitcnt lgkmcnt(4)\n"
"buffer_store_dwordx4 v[20:23], v1, s[40:43], s85 offen offset:1024 sc1\n"
"v_mfma_f32_16x16x32_bf16 v[76:79], a[228:231], v[140:143], v[76:79]\n"
"s_lshl_b32 s80, s101, 1\n"
"s_add_u32 s80, s80, 3\n"
"s_and_b32 s80, s80, 7\n"
"v_mfma_f32_16x16x32_bf16 v[64:67], a[232:235], a[128:131], v[64:67]\n"
"s_lshl_b32 s80, s80, 14\n"
"s_add_u32 s85, s96, s80\n"
"ds_read_b128 a[224:227], v3 offset:24576\n"
"v_mfma_f32_16x16x32_bf16 v[64:67], a[236:239], a[144:147], v[64:67]\n"
"v_mfma_f32_16x16x32_bf16 v[68:71], a[232:235], a[132:135], v[68:71]\n"
"buffer_load_dwordx4 v1, s[40:43], s82 offen offset:2048 sc1 lds\n"
"v_mfma_f32_16x16x32_bf16 v[68:71], a[236:239], a[148:151], v[68:71]\n"
"ds_read_b128 a[228:231], v3 offset:25600\n"
"v_mfma_f32_16x16x32_bf16 v[72:75], a[232:235], a[136:139], v[72:75]\n"
"v_mfma_f32_16x16x32_bf16 v[72:75], a[236:239], a[152:155], v[72:75]\n"
"v_mfma_f32_16x16x32_bf16 v[76:79], a[232:235], a[140:143], v[76:79]\n"
"s_waitcnt lgkmcnt(4)\n"
"buffer_load_dwordx4 v1, s[40:43], s82 offen offset:3072 sc1 lds\n"
"v_mfma_f32_16x16x32_bf16 v[76:79], a[236:239], a[156:159], v[76:79]\n"
"v_cvt_pk_bf16_f32 v32, v32, v33\n"
"v_cvt_pk_bf16_f32 v33, v34, v35\n"
"v_mfma_f32_16x16x32_bf16 v[64:67], a[240:243], a[160:163], v[64:67]\n"
"ds_read_b128 a[232:235], v3 offset:26624\n"
"v_cvt_pk_bf16_f32 v34, v36, v37\n"
"v_mfma_f32_16x16x32_bf16 v[64:67], a[244:247], a[176:179], v[64:67]\n"
"v_cvt_pk_bf16_f32 v35, v38, v39\n"
"v_cvt_pk_bf16_f32 v36, v40, v41\n"
"v_mfma_f32_16x16x32_bf16 v[68:71], a[240:243], a[164:167], v[68:71]\n"
"v_cvt_pk_bf16_f32 v37, v42, v43\n"
"v_cvt_pk_bf16_f32 v38, v44, v45\n"
"v_mfma_f32_16x16x32_bf16 v[68:71], a[244:247], a[180:183], v[68:71]\n"
"ds_read_b128 a[236:239], v3 offset:27648\n"
"v_cvt_pk_bf16_f32 v39, v46, v47\n"
"v_mfma_f32_16x16x32_bf16 v[72:75], a[240:243], a[168:171], v[72:75]\n"
"buffer_store_dwordx4 v[32:35], v1, s[40:43], s85 offen sc1\n"
"v_mfma_f32_16x16x32_bf16 v[72:75], a[244:247], a[184:187], v[72:75]\n"
"v_mfma_f32_16x16x32_bf16 v[76:79], a[240:243], a[172:175], v[76:79]\n"
"s_waitcnt lgkmcnt(4)\n"
"v_mfma_f32_16x16x32_bf16 v[76:79], a[244:247], a[188:191], v[76:79]\n"
"v_mfma_f32_16x16x32_bf16 v[64:67], a[248:251], a[192:195], v[64:67]\n"
"ds_read_b128 a[240:243], v3 offset:28672\n"
"v_mfma_f32_16x16x32_bf16 v[64:67], a[252:255], a[208:211], v[64:67]\n"
"v_mfma_f32_16x16x32_bf16 v[68:71], a[248:251], a[196:199], v[68:71]\n"
"v_mfma_f32_16x16x32_bf16 v[68:71], a[252:255], a[212:215], v[68:71]\n"
"ds_read_b128 a[244:247], v3 offset:29696\n"
"v_mfma_f32_16x16x32_bf16 v[72:75], a[248:251], a[200:203], v[72:75]\n"
"v_mfma_f32_16x16x32_bf16 v[72:75], a[252:255], a[216:219], v[72:75]\n"
"v_mfma_f32_16x16x32_bf16 v[76:79], a[248:251], a[204:207], v[76:79]\n"
"s_waitcnt lgkmcnt(4)\n"
"buffer_load_dwordx2 v[224:225], v5, s[44:47], s53 offen\n"
"v_mfma_f32_16x16x32_bf16 v[76:79], a[252:255], a[220:223], v[76:79]\n"
"v_mfma_f32_16x16x32_bf16 v[64:67], a[224:227], a[0:3], v[64:67]\n"
"ds_read_b128 a[248:251], v3 offset:30720\n"
"v_mfma_f32_16x16x32_bf16 v[64:67], a[228:231], a[16:19], v[64:67]\n"
"v_mfma_f32_16x16x32_bf16 v[68:71], a[224:227], a[4:7], v[68:71]\n"
"buffer_store_dwordx4 v[36:39], v1, s[40:43], s85 offen offset:1024 sc1\n"
"v_mfma_f32_16x16x32_bf16 v[68:71], a[228:231], a[20:23], v[68:71]\n"
"s_lshl_b32 s80, s101, 1\n"
"s_add_u32 s80, s80, 4\n"
"ds_read_b128 a[252:255], v3 offset:31744\n"
"v_mfma_f32_16x16x32_bf16 v[72:75], a[224:227], a[8:11], v[72:75]\n"
"s_and_b32 s80, s80, 7\n"
"s_lshl_b32 s80, s80, 14\n"
"s_add_u32 s85, s96, s80\n"
"v_mfma_f32_16x16x32_bf16 v[72:75], a[228:231], a[24:27], v[72:75]\n"
"v_mfma_f32_16x16x32_bf16 v[76:79], a[224:227], a[12:15], v[76:79]\n"
"s_waitcnt lgkmcnt(4)\n"
"v_mfma_f32_16x16x32_bf16 v[76:79], a[228:231], a[28:31], v[76:79]\n"
"s_barrier\n"
"v_mfma_f32_16x16x32_bf16 v[64:67], a[232:235], a[32:35], v[64:67]\n"
"ds_read_b128 a[224:227], v1\n"
"v_mfma_f32_16x16x32_bf16 v[64:67], a[236:239], a[48:51], v[64:67]\n"
"v_mfma_f32_16x16x32_bf16 v[68:71], a[232:235], a[36:39], v[68:71]\n"
"v_mfma_f32_16x16x32_bf16 v[68:71], a[236:239], a[52:55], v[68:71]\n"
"ds_read_b128 a[228:231], v1 offset:1024\n"
"v_cvt_pk_bf16_f32 v48, v48, v49\n"
"v_mfma_f32_16x16x32_bf16 v[72:75], a[232:235], a[40:43], v[72:75]\n"
"v_cvt_pk_bf16_f32 v49, v50, v51\n"
"v_cvt_pk_bf16_f32 v50, v52, v53\n"
"v_mfma_f32_16x16x32_bf16 v[72:75], a[236:239], a[56:59], v[72:75]\n"
"v_cvt_pk_bf16_f32 v51, v54, v55\n"
"v_cvt_pk_bf16_f32 v52, v56, v57\n"
"v_mfma_f32_16x16x32_bf16 v[76:79], a[232:235], a[44:47], v[76:79]\n"
"s_waitcnt lgkmcnt(4)\n"
"v_cvt_pk_bf16_f32 v53, v58, v59\n"
"v_cvt_pk_bf16_f32 v54, v60, v61\n"
"v_mfma_f32_16x16x32_bf16 v[76:79], a[236:239], a[60:63], v[76:79]\n"
"v_cvt_pk_bf16_f32 v55, v62, v63\n"
"v_mfma_f32_16x16x32_bf16 v[64:67], a[240:243], a[64:67], v[64:67]\n"
"ds_read_b128 a[232:235], v1 offset:2048\n"
"v_mfma_f32_16x16x32_bf16 v[64:67], a[244:247], a[80:83], v[64:67]\n"
"buffer_store_dwordx4 v[48:51], v1, s[40:43], s85 offen sc1\n"
"v_mfma_f32_16x16x32_bf16 v[68:71], a[240:243], a[68:71], v[68:71]\n"
"v_mfma_f32_16x16x32_bf16 v[68:71], a[244:247], a[84:87], v[68:71]\n"
"ds_read_b128 a[236:239], v1 offset:3072\n"
"v_mfma_f32_16x16x32_bf16 v[72:75], a[240:243], a[72:75], v[72:75]\n"
"v_mfma_f32_16x16x32_bf16 v[72:75], a[244:247], a[88:91], v[72:75]\n"
"v_mfma_f32_16x16x32_bf16 v[76:79], a[240:243], a[76:79], v[76:79]\n"
"s_waitcnt lgkmcnt(4)\n"
"v_mfma_f32_16x16x32_bf16 v[76:79], a[244:247], a[92:95], v[76:79]\n"
"v_mfma_f32_16x16x32_bf16 v[64:67], a[248:251], a[96:99], v[64:67]\n"
"ds_read_b128 a[240:243], v1 offset:4096\n"
"v_mfma_f32_16x16x32_bf16 v[64:67], a[252:255], a[112:115], v[64:67]\n"
"v_mfma_f32_16x16x32_bf16 v[68:71], a[248:251], a[100:103], v[68:71]\n"
"v_mfma_f32_16x16x32_bf16 v[68:71], a[252:255], a[116:119], v[68:71]\n"
"ds_read_b128 a[244:247], v1 offset:5120\n"
"v_mfma_f32_16x16x32_bf16 v[72:75], a[248:251], a[104:107], v[72:75]\n"
"v_mfma_f32_16x16x32_bf16 v[72:75], a[252:255], a[120:123], v[72:75]\n"
"v_mfma_f32_16x16x32_bf16 v[76:79], a[248:251], a[108:111], v[76:79]\n"
"s_waitcnt lgkmcnt(4)\n"
"buffer_store_dwordx4 v[52:55], v1, s[40:43], s85 offen offset:1024 sc1\n"
"v_mfma_f32_16x16x32_bf16 v[76:79], a[252:255], a[124:127], v[76:79]\n"
"s_lshl_b32 s80, s101, 1\n"
"s_add_u32 s80, s80, 5\n"
"s_and_b32 s80, s80, 7\n"
"v_mfma_f32_16x16x32_bf16 v[144:147], a[224:227], v[112:115], v[144:147]\n"
"s_lshl_b32 s80, s80, 14\n"
"s_add_u32 s85, s96, s80\n"
"ds_read_b128 a[248:251], v1 offset:6144\n"
"v_mfma_f32_16x16x32_bf16 v[144:147], a[228:231], v[128:131], v[144:147]\n"
"s_add_u32 s82, s64, 622592\n"
"v_mfma_f32_16x16x32_bf16 v[148:151], a[224:227], v[116:119], v[148:151]\n"
"s_add_u32 m0, s66, 65536\n"
"v_mfma_f32_16x16x32_bf16 v[148:151], a[228:231], v[132:135], v[148:151]\n"
"ds_read_b128 a[252:255], v1 offset:7168\n"
"v_mfma_f32_16x16x32_bf16 v[152:155], a[224:227], v[120:123], v[152:155]\n"
"buffer_load_dwordx4 v1, s[40:43], s82 offen sc1 lds\n"
"v_mfma_f32_16x16x32_bf16 v[152:155], a[228:231], v[136:139], v[152:155]\n"
"buffer_load_dwordx4 v1, s[40:43], s82 offen offset:1024 sc1 lds\n"
"v_mfma_f32_16x16x32_bf16 v[156:159], a[224:227], v[124:127], v[156:159]\n"
"s_waitcnt lgkmcnt(4)\n"
"v_mfma_f32_16x16x32_bf16 v[156:159], a[228:231], v[140:143], v[156:159]\n"
"v_cvt_pk_bf16_f32 v64, v64, v65\n"
"v_cvt_pk_bf16_f32 v65, v66, v67\n"
"v_mfma_f32_16x16x32_bf16 v[144:147], a[232:235], a[128:131], v[144:147]\n"
"ds_read_b128 a[224:227], v1 offset:8192\n"
"v_cvt_pk_bf16_f32 v66, v68, v69\n"
"v_mfma_f32_16x16x32_bf16 v[144:147], a[236:239], a[144:147], v[144:147]\n"
"v_cvt_pk_bf16_f32 v67, v70, v71\n"
"v_cvt_pk_bf16_f32 v68, v72, v73\n"
"v_mfma_f32_16x16x32_bf16 v[148:151], a[232:235], a[132:135], v[148:151]\n"
"v_cvt_pk_bf16_f32 v69, v74, v75\n"
"v_cvt_pk_bf16_f32 v70, v76, v77\n"
"v_mfma_f32_16x16x32_bf16 v[148:151], a[236:239], a[148:151], v[148:151]\n"
"ds_read_b128 a[228:231], v1 offset:9216\n"
"v_cvt_pk_bf16_f32 v71, v78, v79\n"
"v_mfma_f32_16x16x32_bf16 v[152:155], a[232:235], a[136:139], v[152:155]\n"
"buffer_store_dwordx4 v[64:67], v1, s[40:43], s85 offen sc1\n"
"v_mfma_f32_16x16x32_bf16 v[152:155], a[236:239], a[152:155], v[152:155]\n"
"buffer_load_dwordx4 v1, s[40:43], s82 offen offset:2048 sc1 lds\n"
"v_mfma_f32_16x16x32_bf16 v[156:159], a[232:235], a[140:143], v[156:159]\n"
"s_waitcnt lgkmcnt(4)\n"
"buffer_load_dwordx4 v1, s[40:43], s82 offen offset:3072 sc1 lds\n"
"v_mfma_f32_16x16x32_bf16 v[156:159], a[236:239], a[156:159], v[156:159]\n"
"v_mfma_f32_16x16x32_bf16 v[144:147], a[240:243], a[160:163], v[144:147]\n"
"ds_read_b128 a[232:235], v1 offset:10240\n"
"v_mfma_f32_16x16x32_bf16 v[144:147], a[244:247], a[176:179], v[144:147]\n"
"v_mfma_f32_16x16x32_bf16 v[148:151], a[240:243], a[164:167], v[148:151]\n"
"v_mfma_f32_16x16x32_bf16 v[148:151], a[244:247], a[180:183], v[148:151]\n"
"ds_read_b128 a[236:239], v1 offset:11264\n"
"v_mfma_f32_16x16x32_bf16 v[152:155], a[240:243], a[168:171], v[152:155]\n"
"v_mfma_f32_16x16x32_bf16 v[152:155], a[244:247], a[184:187], v[152:155]\n"
"v_mfma_f32_16x16x32_bf16 v[156:159], a[240:243], a[172:175], v[156:159]\n"
"s_waitcnt lgkmcnt(4)\n"
"v_mfma_f32_16x16x32_bf16 v[156:159], a[244:247], a[188:191], v[156:159]\n"
"v_mfma_f32_16x16x32_bf16 v[144:147], a[248:251], a[192:195], v[144:147]\n"
"ds_read_b128 a[240:243], v1 offset:12288\n"
"v_mfma_f32_16x16x32_bf16 v[144:147], a[252:255], a[208:211], v[144:147]\n"
"v_mfma_f32_16x16x32_bf16 v[148:151], a[248:251], a[196:199], v[148:151]\n"
"buffer_store_dwordx4 v[68:71], v1, s[40:43], s85 offen offset:1024 sc1\n"
"v_mfma_f32_16x16x32_bf16 v[148:151], a[252:255], a[212:215], v[148:151]\n"
"ds_read_b128 a[244:247], v1 offset:13312\n"
"v_mfma_f32_16x16x32_bf16 v[152:155], a[248:251], a[200:203], v[152:155]\n"
"v_mfma_f32_16x16x32_bf16 v[152:155], a[252:255], a[216:219], v[152:155]\n"
"v_mfma_f32_16x16x32_bf16 v[156:159], a[248:251], a[204:207], v[156:159]\n"
"s_waitcnt lgkmcnt(4)\n"
"v_mfma_f32_16x16x32_bf16 v[156:159], a[252:255], a[220:223], v[156:159]\n"
"v_mfma_f32_16x16x32_bf16 v[144:147], a[224:227], a[0:3], v[144:147]\n"
"ds_read_b128 a[248:251], v1 offset:14336\n"
"v_mfma_f32_16x16x32_bf16 v[144:147], a[228:231], a[16:19], v[144:147]\n"
"v_mfma_f32_16x16x32_bf16 v[148:151], a[224:227], a[4:7], v[148:151]\n"
"v_mfma_f32_16x16x32_bf16 v[148:151], a[228:231], a[20:23], v[148:151]\n"
"ds_read_b128 a[252:255], v1 offset:15360\n"
"v_mfma_f32_16x16x32_bf16 v[152:155], a[224:227], a[8:11], v[152:155]\n"
"v_mfma_f32_16x16x32_bf16 v[152:155], a[228:231], a[24:27], v[152:155]\n"
"v_mfma_f32_16x16x32_bf16 v[156:159], a[224:227], a[12:15], v[156:159]\n"
"s_waitcnt lgkmcnt(4)\n"
"v_mfma_f32_16x16x32_bf16 v[156:159], a[228:231], a[28:31], v[156:159]\n"
"v_mfma_f32_16x16x32_bf16 v[144:147], a[232:235], a[32:35], v[144:147]\n"
"ds_read_b128 a[224:227], v1 offset:16384\n"
"v_mfma_f32_16x16x32_bf16 v[144:147], a[236:239], a[48:51], v[144:147]\n"
"v_mfma_f32_16x16x32_bf16 v[148:151], a[232:235], a[36:39], v[148:151]\n"
"v_mfma_f32_16x16x32_bf16 v[148:151], a[236:239], a[52:55], v[148:151]\n"
"ds_read_b128 a[228:231], v1 offset:17408\n"
"v_mfma_f32_16x16x32_bf16 v[152:155], a[232:235], a[40:43], v[152:155]\n"
"v_mfma_f32_16x16x32_bf16 v[152:155], a[236:239], a[56:59], v[152:155]\n"
"v_mfma_f32_16x16x32_bf16 v[156:159], a[232:235], a[44:47], v[156:159]\n"
"s_waitcnt lgkmcnt(4)\n"
"v_mfma_f32_16x16x32_bf16 v[156:159], a[236:239], a[60:63], v[156:159]\n"
"v_mfma_f32_16x16x32_bf16 v[144:147], a[240:243], a[64:67], v[144:147]\n"
"ds_read_b128 a[232:235], v1 offset:18432\n"
"v_mfma_f32_16x16x32_bf16 v[144:147], a[244:247], a[80:83], v[144:147]\n"
"v_mfma_f32_16x16x32_bf16 v[148:151], a[240:243], a[68:71], v[148:151]\n"
"v_mfma_f32_16x16x32_bf16 v[148:151], a[244:247], a[84:87], v[148:151]\n"
"ds_read_b128 a[236:239], v1 offset:19456\n"
"v_mfma_f32_16x16x32_bf16 v[152:155], a[240:243], a[72:75], v[152:155]\n"
"v_mfma_f32_16x16x32_bf16 v[152:155], a[244:247], a[88:91], v[152:155]\n"
"v_mfma_f32_16x16x32_bf16 v[156:159], a[240:243], a[76:79], v[156:159]\n"
"s_waitcnt lgkmcnt(4)\n"
"v_mfma_f32_16x16x32_bf16 v[156:159], a[244:247], a[92:95], v[156:159]\n"
"v_mfma_f32_16x16x32_bf16 v[144:147], a[248:251], a[96:99], v[144:147]\n"
"ds_read_b128 a[240:243], v1 offset:20480\n"
"v_mfma_f32_16x16x32_bf16 v[144:147], a[252:255], a[112:115], v[144:147]\n"
"v_mfma_f32_16x16x32_bf16 v[148:151], a[248:251], a[100:103], v[148:151]\n"
"v_mfma_f32_16x16x32_bf16 v[148:151], a[252:255], a[116:119], v[148:151]\n"
"s_lshl_b32 s80, s101, 1\n"
"s_add_u32 s80, s80, 6\n"
"ds_read_b128 a[244:247], v1 offset:21504\n"
"v_mfma_f32_16x16x32_bf16 v[152:155], a[248:251], a[104:107], v[152:155]\n"
"s_and_b32 s80, s80, 7\n"
"s_lshl_b32 s80, s80, 14\n"
"s_add_u32 s85, s96, s80\n"
"v_mfma_f32_16x16x32_bf16 v[152:155], a[252:255], a[120:123], v[152:155]\n"
"v_mfma_f32_16x16x32_bf16 v[156:159], a[248:251], a[108:111], v[156:159]\n"
"s_waitcnt lgkmcnt(4)\n"
"v_mfma_f32_16x16x32_bf16 v[156:159], a[252:255], a[124:127], v[156:159]\n"
"v_mfma_f32_16x16x32_bf16 v[160:163], a[224:227], v[112:115], v[160:163]\n"
"ds_read_b128 a[248:251], v1 offset:22528\n"
"v_mfma_f32_16x16x32_bf16 v[160:163], a[228:231], v[128:131], v[160:163]\n"
"v_mfma_f32_16x16x32_bf16 v[164:167], a[224:227], v[116:119], v[164:167]\n"
"v_mfma_f32_16x16x32_bf16 v[164:167], a[228:231], v[132:135], v[164:167]\n"
"ds_read_b128 a[252:255], v1 offset:23552\n"
"v_cvt_pk_bf16_f32 v144, v144, v145\n"
"v_mfma_f32_16x16x32_bf16 v[168:171], a[224:227], v[120:123], v[168:171]\n"
"v_cvt_pk_bf16_f32 v145, v146, v147\n"
"v_cvt_pk_bf16_f32 v146, v148, v149\n"
"v_mfma_f32_16x16x32_bf16 v[168:171], a[228:231], v[136:139], v[168:171]\n"
"v_cvt_pk_bf16_f32 v147, v150, v151\n"
"v_cvt_pk_bf16_f32 v148, v152, v153\n"
"v_mfma_f32_16x16x32_bf16 v[172:175], a[224:227], v[124:127], v[172:175]\n"
"s_waitcnt lgkmcnt(4)\n"
"v_cvt_pk_bf16_f32 v149, v154, v155\n"
"v_cvt_pk_bf16_f32 v150, v156, v157\n"
"v_mfma_f32_16x16x32_bf16 v[172:175], a[228:231], v[140:143], v[172:175]\n"
"v_cvt_pk_bf16_f32 v151, v158, v159\n"
"v_mfma_f32_16x16x32_bf16 v[160:163], a[232:235], a[128:131], v[160:163]\n"
"ds_read_b128 a[224:227], v1 offset:24576\n"
"v_mfma_f32_16x16x32_bf16 v[160:163], a[236:239], a[144:147], v[160:163]\n"
"s_lshl_b32 s78, s73, 2\n"
"buffer_store_dwordx4 v[144:147], v1, s[40:43], s85 offen sc1\n"
"v_mfma_f32_16x16x32_bf16 v[164:167], a[232:235], a[132:135], v[164:167]\n"
"s_add_u32 s78, s78, s71\n"
"v_mfma_f32_16x16x32_bf16 v[164:167], a[236:239], a[148:151], v[164:167]\n"
"s_lshl_b32 s78, s78, 3\n"
"ds_read_b128 a[228:231], v1 offset:25600\n"
"v_mfma_f32_16x16x32_bf16 v[168:171], a[232:235], a[136:139], v[168:171]\n"
"s_add_u32 s78, s78, s100\n"
"buffer_store_dwordx4 v[148:151], v1, s[40:43], s85 offen offset:1024 sc1\n"
"v_mfma_f32_16x16x32_bf16 v[168:171], a[236:239], a[152:155], v[168:171]\n"
"s_lshl_b32 s78, s78, 6\n"
"s_add_u32 s78, s78, 79298560\n"
"v_mov_b32 v8, s92\n"
"v_mfma_f32_16x16x32_bf16 v[172:175], a[232:235], a[140:143], v[172:175]\n"
"s_waitcnt lgkmcnt(4)\n"
"v_mov_b32 v9, s93\n"
"v_mfma_f32_16x16x32_bf16 v[172:175], a[236:239], a[156:159], v[172:175]\n"
"s_waitcnt vmcnt(2)\n"
"buffer_store_dwordx2 v[8:9], v14, s[40:43], s78 offen sc1\n"
"v_mfma_f32_16x16x32_bf16 v[160:163], a[240:243], a[160:163], v[160:163]\n"
"ds_read_b128 a[232:235], v1 offset:26624\n"
"v_mfma_f32_16x16x32_bf16 v[160:163], a[244:247], a[176:179], v[160:163]\n"
"v_mfma_f32_16x16x32_bf16 v[164:167], a[240:243], a[164:167], v[164:167]\n"
"v_mfma_f32_16x16x32_bf16 v[164:167], a[244:247], a[180:183], v[164:167]\n"
"ds_read_b128 a[236:239], v1 offset:27648\n"
"v_mfma_f32_16x16x32_bf16 v[168:171], a[240:243], a[168:171], v[168:171]\n"
"v_mfma_f32_16x16x32_bf16 v[168:171], a[244:247], a[184:187], v[168:171]\n"
"v_mfma_f32_16x16x32_bf16 v[172:175], a[240:243], a[172:175], v[172:175]\n"
"s_waitcnt lgkmcnt(4)\n"
"v_mfma_f32_16x16x32_bf16 v[172:175], a[244:247], a[188:191], v[172:175]\n"
"v_mfma_f32_16x16x32_bf16 v[160:163], a[248:251], a[192:195], v[160:163]\n"
"ds_read_b128 a[240:243], v1 offset:28672\n"
"v_mfma_f32_16x16x32_bf16 v[160:163], a[252:255], a[208:211], v[160:163]\n"
"v_mfma_f32_16x16x32_bf16 v[164:167], a[248:251], a[196:199], v[164:167]\n"
"v_mfma_f32_16x16x32_bf16 v[164:167], a[252:255], a[212:215], v[164:167]\n"
"ds_read_b128 a[244:247], v1 offset:29696\n"
"v_mfma_f32_16x16x32_bf16 v[168:171], a[248:251], a[200:203], v[168:171]\n"
"v_mfma_f32_16x16x32_bf16 v[168:171], a[252:255], a[216:219], v[168:171]\n"
"v_mfma_f32_16x16x32_bf16 v[172:175], a[248:251], a[204:207], v[172:175]\n"
"s_waitcnt lgkmcnt(4)\n"
"v_mfma_f32_16x16x32_bf16 v[172:175], a[252:255], a[220:223], v[172:175]\n"
"v_mfma_f32_16x16x32_bf16 v[160:163], a[224:227], a[0:3], v[160:163]\n"
"ds_read_b128 a[248:251], v1 offset:30720\n"
"v_mfma_f32_16x16x32_bf16 v[160:163], a[228:231], a[16:19], v[160:163]\n"
"v_mfma_f32_16x16x32_bf16 v[164:167], a[224:227], a[4:7], v[164:167]\n"
"v_mfma_f32_16x16x32_bf16 v[164:167], a[228:231], a[20:23], v[164:167]\n"
"ds_read_b128 a[252:255], v1 offset:31744\n"
"v_mfma_f32_16x16x32_bf16 v[168:171], a[224:227], a[8:11], v[168:171]\n"
"v_mfma_f32_16x16x32_bf16 v[168:171], a[228:231], a[24:27], v[168:171]\n"
"v_mfma_f32_16x16x32_bf16 v[172:175], a[224:227], a[12:15], v[172:175]\n"
"s_waitcnt lgkmcnt(4)\n"
"v_mfma_f32_16x16x32_bf16 v[172:175], a[228:231], a[28:31], v[172:175]\n"
"s_barrier\n"
"v_mfma_f32_16x16x32_bf16 v[160:163], a[232:235], a[32:35], v[160:163]\n"
"ds_read_b128 a[224:227], v1 offset:32768\n"
"v_mfma_f32_16x16x32_bf16 v[160:163], a[236:239], a[48:51], v[160:163]\n"
"v_mfma_f32_16x16x32_bf16 v[164:167], a[232:235], a[36:39], v[164:167]\n"
"v_mfma_f32_16x16x32_bf16 v[164:167], a[236:239], a[52:55], v[164:167]\n"
"ds_read_b128 a[228:231], v1 offset:33792\n"
"v_mfma_f32_16x16x32_bf16 v[168:171], a[232:235], a[40:43], v[168:171]\n"
"v_mfma_f32_16x16x32_bf16 v[168:171], a[236:239], a[56:59], v[168:171]\n"
"v_mfma_f32_16x16x32_bf16 v[172:175], a[232:235], a[44:47], v[172:175]\n"
"s_waitcnt lgkmcnt(4)\n"
"v_mfma_f32_16x16x32_bf16 v[172:175], a[236:239], a[60:63], v[172:175]\n"
"v_mfma_f32_16x16x32_bf16 v[160:163], a[240:243], a[64:67], v[160:163]\n"
"ds_read_b128 a[232:235], v1 offset:34816\n"
"v_mfma_f32_16x16x32_bf16 v[160:163], a[244:247], a[80:83], v[160:163]\n"
"v_mfma_f32_16x16x32_bf16 v[164:167], a[240:243], a[68:71], v[164:167]\n"
"v_mfma_f32_16x16x32_bf16 v[164:167], a[244:247], a[84:87], v[164:167]\n"
"ds_read_b128 a[236:239], v1 offset:35840\n"
"v_mfma_f32_16x16x32_bf16 v[168:171], a[240:243], a[72:75], v[168:171]\n"
"v_mfma_f32_16x16x32_bf16 v[168:171], a[244:247], a[88:91], v[168:171]\n"
"v_mfma_f32_16x16x32_bf16 v[172:175], a[240:243], a[76:79], v[172:175]\n"
"s_waitcnt lgkmcnt(4)\n"
"v_mfma_f32_16x16x32_bf16 v[172:175], a[244:247], a[92:95], v[172:175]\n"
"v_mfma_f32_16x16x32_bf16 v[160:163], a[248:251], a[96:99], v[160:163]\n"
"ds_read_b128 a[240:243], v1 offset:36864\n"
"v_mfma_f32_16x16x32_bf16 v[160:163], a[252:255], a[112:115], v[160:163]\n"
"v_mfma_f32_16x16x32_bf16 v[164:167], a[248:251], a[100:103], v[164:167]\n"
"v_mfma_f32_16x16x32_bf16 v[164:167], a[252:255], a[116:119], v[164:167]\n"
"s_lshl_b32 s80, s101, 1\n"
"s_add_u32 s80, s80, 7\n"
"ds_read_b128 a[244:247], v1 offset:37888\n"
"v_mfma_f32_16x16x32_bf16 v[168:171], a[248:251], a[104:107], v[168:171]\n"
"s_and_b32 s80, s80, 7\n"
"s_lshl_b32 s80, s80, 14\n"
"s_add_u32 s85, s96, s80\n"
"v_mfma_f32_16x16x32_bf16 v[168:171], a[252:255], a[120:123], v[168:171]\n"
"v_mfma_f32_16x16x32_bf16 v[172:175], a[248:251], a[108:111], v[172:175]\n"
"s_waitcnt lgkmcnt(4)\n"
"v_mfma_f32_16x16x32_bf16 v[172:175], a[252:255], a[124:127], v[172:175]\n"
"v_mfma_f32_16x16x32_bf16 v[176:179], a[224:227], v[112:115], v[176:179]\n"
"ds_read_b128 a[248:251], v1 offset:38912\n"
"v_mfma_f32_16x16x32_bf16 v[176:179], a[228:231], v[128:131], v[176:179]\n"
"v_mfma_f32_16x16x32_bf16 v[180:183], a[224:227], v[116:119], v[180:183]\n"
"v_mfma_f32_16x16x32_bf16 v[180:183], a[228:231], v[132:135], v[180:183]\n"
"ds_read_b128 a[252:255], v1 offset:39936\n"
"v_cvt_pk_bf16_f32 v160, v160, v161\n"
"v_mfma_f32_16x16x32_bf16 v[184:187], a[224:227], v[120:123], v[184:187]\n"
"v_cvt_pk_bf16_f32 v161, v162, v163\n"
"v_cvt_pk_bf16_f32 v162, v164, v165\n"
"v_mfma_f32_16x16x32_bf16 v[184:187], a[228:231], v[136:139], v[184:187]\n"
"v_cvt_pk_bf16_f32 v163, v166, v167\n"
"v_cvt_pk_bf16_f32 v164, v168, v169\n"
"v_mfma_f32_16x16x32_bf16 v[188:191], a[224:227], v[124:127], v[188:191]\n"
"s_waitcnt lgkmcnt(4)\n"
"v_cvt_pk_bf16_f32 v165, v170, v171\n"
"v_cvt_pk_bf16_f32 v166, v172, v173\n"
"v_mfma_f32_16x16x32_bf16 v[188:191], a[228:231], v[140:143], v[188:191]\n"
"v_cvt_pk_bf16_f32 v167, v174, v175\n"
"v_mfma_f32_16x16x32_bf16 v[176:179], a[232:235], a[128:131], v[176:179]\n"
"ds_read_b128 a[224:227], v1 offset:40960\n"
"v_mfma_f32_16x16x32_bf16 v[176:179], a[236:239], a[144:147], v[176:179]\n"
"buffer_store_dwordx4 v[160:163], v1, s[40:43], s85 offen sc1\n"
"v_mfma_f32_16x16x32_bf16 v[180:183], a[232:235], a[132:135], v[180:183]\n"
"v_mfma_f32_16x16x32_bf16 v[180:183], a[236:239], a[148:151], v[180:183]\n"
"ds_read_b128 a[228:231], v1 offset:41984\n"
"v_mfma_f32_16x16x32_bf16 v[184:187], a[232:235], a[136:139], v[184:187]\n"
"buffer_store_dwordx4 v[164:167], v1, s[40:43], s85 offen offset:1024 sc1\n"
"v_mfma_f32_16x16x32_bf16 v[184:187], a[236:239], a[152:155], v[184:187]\n"
"v_mfma_f32_16x16x32_bf16 v[188:191], a[232:235], a[140:143], v[188:191]\n"
"s_waitcnt lgkmcnt(4)\n"
"v_mfma_f32_16x16x32_bf16 v[188:191], a[236:239], a[156:159], v[188:191]\n"
"v_mfma_f32_16x16x32_bf16 v[176:179], a[240:243], a[160:163], v[176:179]\n"
"ds_read_b128 a[232:235], v1 offset:43008\n"
"v_mfma_f32_16x16x32_bf16 v[176:179], a[244:247], a[176:179], v[176:179]\n"
"v_mfma_f32_16x16x32_bf16 v[180:183], a[240:243], a[164:167], v[180:183]\n"
"v_mfma_f32_16x16x32_bf16 v[180:183], a[244:247], a[180:183], v[180:183]\n"
"ds_read_b128 a[236:239], v1 offset:44032\n"
"v_mfma_f32_16x16x32_bf16 v[184:187], a[240:243], a[168:171], v[184:187]\n"
"v_mfma_f32_16x16x32_bf16 v[184:187], a[244:247], a[184:187], v[184:187]\n"
"v_mfma_f32_16x16x32_bf16 v[188:191], a[240:243], a[172:175], v[188:191]\n"
"s_waitcnt lgkmcnt(4)\n"
"v_mfma_f32_16x16x32_bf16 v[188:191], a[244:247], a[188:191], v[188:191]\n"
"v_mfma_f32_16x16x32_bf16 v[176:179], a[248:251], a[192:195], v[176:179]\n"
"ds_read_b128 a[240:243], v1 offset:45056\n"
"v_mfma_f32_16x16x32_bf16 v[176:179], a[252:255], a[208:211], v[176:179]\n"
"v_mfma_f32_16x16x32_bf16 v[180:183], a[248:251], a[196:199], v[180:183]\n"
"v_mfma_f32_16x16x32_bf16 v[180:183], a[252:255], a[212:215], v[180:183]\n"
"ds_read_b128 a[244:247], v1 offset:46080\n"
"v_mfma_f32_16x16x32_bf16 v[184:187], a[248:251], a[200:203], v[184:187]\n"
"v_mfma_f32_16x16x32_bf16 v[184:187], a[252:255], a[216:219], v[184:187]\n"
"v_mfma_f32_16x16x32_bf16 v[188:191], a[248:251], a[204:207], v[188:191]\n"
"s_waitcnt lgkmcnt(4)\n"
"v_mfma_f32_16x16x32_bf16 v[188:191], a[252:255], a[220:223], v[188:191]\n"
"v_mfma_f32_16x16x32_bf16 v[176:179], a[224:227], a[0:3], v[176:179]\n"
"ds_read_b128 a[248:251], v1 offset:47104\n"
"v_mfma_f32_16x16x32_bf16 v[176:179], a[228:231], a[16:19], v[176:179]\n"
"v_mfma_f32_16x16x32_bf16 v[180:183], a[224:227], a[4:7], v[180:183]\n"
"v_mfma_f32_16x16x32_bf16 v[180:183], a[228:231], a[20:23], v[180:183]\n"
"ds_read_b128 a[252:255], v1 offset:48128\n"
"v_mfma_f32_16x16x32_bf16 v[184:187], a[224:227], a[8:11], v[184:187]\n"
"v_mfma_f32_16x16x32_bf16 v[184:187], a[228:231], a[24:27], v[184:187]\n"
"v_mfma_f32_16x16x32_bf16 v[188:191], a[224:227], a[12:15], v[188:191]\n"
"s_waitcnt lgkmcnt(4)\n"
"v_mfma_f32_16x16x32_bf16 v[188:191], a[228:231], a[28:31], v[188:191]\n"
"v_mfma_f32_16x16x32_bf16 v[176:179], a[232:235], a[32:35], v[176:179]\n"
"ds_read_b128 a[224:227], v1 offset:49152\n"
"v_mfma_f32_16x16x32_bf16 v[176:179], a[236:239], a[48:51], v[176:179]\n"
"v_mfma_f32_16x16x32_bf16 v[180:183], a[232:235], a[36:39], v[180:183]\n"
"v_mfma_f32_16x16x32_bf16 v[180:183], a[236:239], a[52:55], v[180:183]\n"
"ds_read_b128 a[228:231], v1 offset:50176\n"
"v_mfma_f32_16x16x32_bf16 v[184:187], a[232:235], a[40:43], v[184:187]\n"
"v_mfma_f32_16x16x32_bf16 v[184:187], a[236:239], a[56:59], v[184:187]\n"
"v_mfma_f32_16x16x32_bf16 v[188:191], a[232:235], a[44:47], v[188:191]\n"
"s_waitcnt lgkmcnt(4)\n"
"v_mfma_f32_16x16x32_bf16 v[188:191], a[236:239], a[60:63], v[188:191]\n"
"v_mfma_f32_16x16x32_bf16 v[176:179], a[240:243], a[64:67], v[176:179]\n"
"ds_read_b128 a[232:235], v1 offset:51200\n"
"v_mfma_f32_16x16x32_bf16 v[176:179], a[244:247], a[80:83], v[176:179]\n"
"v_mfma_f32_16x16x32_bf16 v[180:183], a[240:243], a[68:71], v[180:183]\n"
"v_mfma_f32_16x16x32_bf16 v[180:183], a[244:247], a[84:87], v[180:183]\n"
"ds_read_b128 a[236:239], v1 offset:52224\n"
"v_mfma_f32_16x16x32_bf16 v[184:187], a[240:243], a[72:75], v[184:187]\n"
"v_mfma_f32_16x16x32_bf16 v[184:187], a[244:247], a[88:91], v[184:187]\n"
"v_mfma_f32_16x16x32_bf16 v[188:191], a[240:243], a[76:79], v[188:191]\n"
"s_waitcnt lgkmcnt(4)\n"
"v_mfma_f32_16x16x32_bf16 v[188:191], a[244:247], a[92:95], v[188:191]\n"
"v_mfma_f32_16x16x32_bf16 v[176:179], a[248:251], a[96:99], v[176:179]\n"
"ds_read_b128 a[240:243], v1 offset:53248\n"
"v_mfma_f32_16x16x32_bf16 v[176:179], a[252:255], a[112:115], v[176:179]\n"
"v_mfma_f32_16x16x32_bf16 v[180:183], a[248:251], a[100:103], v[180:183]\n"
"s_lshl_b32 s78, s73, 2\n"
"v_mfma_f32_16x16x32_bf16 v[180:183], a[252:255], a[116:119], v[180:183]\n"
"s_lshl_b32 s80, s101, 1\n"
"s_add_u32 s80, s80, 8\n"
"ds_read_b128 a[244:247], v1 offset:54272\n"
"v_mfma_f32_16x16x32_bf16 v[184:187], a[248:251], a[104:107], v[184:187]\n"
"s_and_b32 s80, s80, 7\n"
"s_lshl_b32 s80, s80, 14\n"
"s_add_u32 s85, s96, s80\n"
"v_mfma_f32_16x16x32_bf16 v[184:187], a[252:255], a[120:123], v[184:187]\n"
"s_add_u32 s78, s78, s71\n"
"v_mfma_f32_16x16x32_bf16 v[188:191], a[248:251], a[108:111], v[188:191]\n"
"s_waitcnt lgkmcnt(4)\n"
"s_lshl_b32 s78, s78, 3\n"
"s_add_u32 s78, s78, s100\n"
"v_mfma_f32_16x16x32_bf16 v[188:191], a[252:255], a[124:127], v[188:191]\n"
"s_lshl_b32 s78, s78, 6\n"
"s_add_u32 s78, s78, 79364096\n"
"v_mov_b32 v8, s92\n"
"v_mfma_f32_16x16x32_bf16 v[192:195], a[224:227], v[112:115], v[192:195]\n"
"ds_read_b128 a[248:251], v1 offset:55296\n"
"v_mov_b32 v9, s93\n"
"v_mfma_f32_16x16x32_bf16 v[192:195], a[228:231], v[128:131], v[192:195]\n"
"s_waitcnt vmcnt(0)\n"
"buffer_store_dwordx2 v[8:9], v14, s[40:43], s78 offen sc1\n"
"v_mfma_f32_16x16x32_bf16 v[196:199], a[224:227], v[116:119], v[196:199]\n"
"v_mfma_f32_16x16x32_bf16 v[196:199], a[228:231], v[132:135], v[196:199]\n"
"ds_read_b128 a[252:255], v1 offset:56320\n"
"v_cvt_pk_bf16_f32 v176, v176, v177\n"
"v_mfma_f32_16x16x32_bf16 v[200:203], a[224:227], v[120:123], v[200:203]\n"
"v_cvt_pk_bf16_f32 v177, v178, v179\n"
"v_cvt_pk_bf16_f32 v178, v180, v181\n"
"v_mfma_f32_16x16x32_bf16 v[200:203], a[228:231], v[136:139], v[200:203]\n"
"v_cvt_pk_bf16_f32 v179, v182, v183\n"
"v_cvt_pk_bf16_f32 v180, v184, v185\n"
"v_mfma_f32_16x16x32_bf16 v[204:207], a[224:227], v[124:127], v[204:207]\n"
"s_waitcnt lgkmcnt(4)\n"
"v_cvt_pk_bf16_f32 v181, v186, v187\n"
"v_cvt_pk_bf16_f32 v182, v188, v189\n"
"v_mfma_f32_16x16x32_bf16 v[204:207], a[228:231], v[140:143], v[204:207]\n"
"v_cvt_pk_bf16_f32 v183, v190, v191\n"
"v_mfma_f32_16x16x32_bf16 v[192:195], a[232:235], a[128:131], v[192:195]\n"
"ds_read_b128 a[224:227], v1 offset:57344\n"
"v_mfma_f32_16x16x32_bf16 v[192:195], a[236:239], a[144:147], v[192:195]\n"
"buffer_store_dwordx4 v[176:179], v1, s[40:43], s85 offen sc1\n"
"v_mfma_f32_16x16x32_bf16 v[196:199], a[232:235], a[132:135], v[196:199]\n"
"v_mfma_f32_16x16x32_bf16 v[196:199], a[236:239], a[148:151], v[196:199]\n"
"ds_read_b128 a[228:231], v1 offset:58368\n"
"v_mfma_f32_16x16x32_bf16 v[200:203], a[232:235], a[136:139], v[200:203]\n"
"buffer_store_dwordx4 v[180:183], v1, s[40:43], s85 offen offset:1024 sc1\n"
"v_mfma_f32_16x16x32_bf16 v[200:203], a[236:239], a[152:155], v[200:203]\n"
"v_mfma_f32_16x16x32_bf16 v[204:207], a[232:235], a[140:143], v[204:207]\n"
"s_waitcnt lgkmcnt(4)\n"
"v_mfma_f32_16x16x32_bf16 v[204:207], a[236:239], a[156:159], v[204:207]\n"
"v_mfma_f32_16x16x32_bf16 v[192:195], a[240:243], a[160:163], v[192:195]\n"
"ds_read_b128 a[232:235], v1 offset:59392\n"
"v_mfma_f32_16x16x32_bf16 v[192:195], a[244:247], a[176:179], v[192:195]\n"
"v_mfma_f32_16x16x32_bf16 v[196:199], a[240:243], a[164:167], v[196:199]\n"
"v_mfma_f32_16x16x32_bf16 v[196:199], a[244:247], a[180:183], v[196:199]\n"
"ds_read_b128 a[236:239], v1 offset:60416\n"
"v_mfma_f32_16x16x32_bf16 v[200:203], a[240:243], a[168:171], v[200:203]\n"
"v_mfma_f32_16x16x32_bf16 v[200:203], a[244:247], a[184:187], v[200:203]\n"
"v_mfma_f32_16x16x32_bf16 v[204:207], a[240:243], a[172:175], v[204:207]\n"
"s_waitcnt lgkmcnt(4)\n"
"v_mfma_f32_16x16x32_bf16 v[204:207], a[244:247], a[188:191], v[204:207]\n"
"v_mfma_f32_16x16x32_bf16 v[192:195], a[248:251], a[192:195], v[192:195]\n"
"ds_read_b128 a[240:243], v1 offset:61440\n"
"v_mfma_f32_16x16x32_bf16 v[192:195], a[252:255], a[208:211], v[192:195]\n"
"v_mfma_f32_16x16x32_bf16 v[196:199], a[248:251], a[196:199], v[196:199]\n"
"v_mfma_f32_16x16x32_bf16 v[196:199], a[252:255], a[212:215], v[196:199]\n"
"ds_read_b128 a[244:247], v1 offset:62464\n"
"v_mfma_f32_16x16x32_bf16 v[200:203], a[248:251], a[200:203], v[200:203]\n"
"v_mfma_f32_16x16x32_bf16 v[200:203], a[252:255], a[216:219], v[200:203]\n"
"v_mfma_f32_16x16x32_bf16 v[204:207], a[248:251], a[204:207], v[204:207]\n"
"s_waitcnt lgkmcnt(4)\n"
"v_mfma_f32_16x16x32_bf16 v[204:207], a[252:255], a[220:223], v[204:207]\n"
"v_mfma_f32_16x16x32_bf16 v[192:195], a[224:227], a[0:3], v[192:195]\n"
"ds_read_b128 a[248:251], v1 offset:63488\n"
"v_mfma_f32_16x16x32_bf16 v[192:195], a[228:231], a[16:19], v[192:195]\n"
"v_mfma_f32_16x16x32_bf16 v[196:199], a[224:227], a[4:7], v[196:199]\n"
"v_mfma_f32_16x16x32_bf16 v[196:199], a[228:231], a[20:23], v[196:199]\n"
"ds_read_b128 a[252:255], v1 offset:64512\n"
"v_mfma_f32_16x16x32_bf16 v[200:203], a[224:227], a[8:11], v[200:203]\n"
"v_mfma_f32_16x16x32_bf16 v[200:203], a[228:231], a[24:27], v[200:203]\n"
"v_mfma_f32_16x16x32_bf16 v[204:207], a[224:227], a[12:15], v[204:207]\n"
"s_waitcnt lgkmcnt(4)\n"
"v_mfma_f32_16x16x32_bf16 v[204:207], a[228:231], a[28:31], v[204:207]\n"
"s_barrier\n"
"v_mfma_f32_16x16x32_bf16 v[192:195], a[232:235], a[32:35], v[192:195]\n"
"ds_read_b128 a[224:227], v2\n"
"v_mfma_f32_16x16x32_bf16 v[192:195], a[236:239], a[48:51], v[192:195]\n"
"v_mfma_f32_16x16x32_bf16 v[196:199], a[232:235], a[36:39], v[196:199]\n"
"v_mfma_f32_16x16x32_bf16 v[196:199], a[236:239], a[52:55], v[196:199]\n"
"ds_read_b128 a[228:231], v2 offset:1024\n"
"v_mfma_f32_16x16x32_bf16 v[200:203], a[232:235], a[40:43], v[200:203]\n"
"v_mfma_f32_16x16x32_bf16 v[200:203], a[236:239], a[56:59], v[200:203]\n"
"v_mfma_f32_16x16x32_bf16 v[204:207], a[232:235], a[44:47], v[204:207]\n"
"s_waitcnt lgkmcnt(4)\n"
"v_mfma_f32_16x16x32_bf16 v[204:207], a[236:239], a[60:63], v[204:207]\n"
"v_mfma_f32_16x16x32_bf16 v[192:195], a[240:243], a[64:67], v[192:195]\n"
"ds_read_b128 a[232:235], v2 offset:2048\n"
"v_mfma_f32_16x16x32_bf16 v[192:195], a[244:247], a[80:83], v[192:195]\n"
"v_mfma_f32_16x16x32_bf16 v[196:199], a[240:243], a[68:71], v[196:199]\n"
"v_mfma_f32_16x16x32_bf16 v[196:199], a[244:247], a[84:87], v[196:199]\n"
"ds_read_b128 a[236:239], v2 offset:3072\n"
"v_mfma_f32_16x16x32_bf16 v[200:203], a[240:243], a[72:75], v[200:203]\n"
"v_mfma_f32_16x16x32_bf16 v[200:203], a[244:247], a[88:91], v[200:203]\n"
"v_mfma_f32_16x16x32_bf16 v[204:207], a[240:243], a[76:79], v[204:207]\n"
"s_waitcnt lgkmcnt(4)\n"
"v_mfma_f32_16x16x32_bf16 v[204:207], a[244:247], a[92:95], v[204:207]\n"
"v_mfma_f32_16x16x32_bf16 v[192:195], a[248:251], a[96:99], v[192:195]\n"
"ds_read_b128 a[240:243], v2 offset:4096\n"
"v_mfma_f32_16x16x32_bf16 v[192:195], a[252:255], a[112:115], v[192:195]\n"
"v_mfma_f32_16x16x32_bf16 v[196:199], a[248:251], a[100:103], v[196:199]\n"
"v_mfma_f32_16x16x32_bf16 v[196:199], a[252:255], a[116:119], v[196:199]\n"
"s_lshl_b32 s80, s101, 1\n"
"s_add_u32 s80, s80, 9\n"
"ds_read_b128 a[244:247], v2 offset:5120\n"
"v_mfma_f32_16x16x32_bf16 v[200:203], a[248:251], a[104:107], v[200:203]\n"
"s_and_b32 s80, s80, 7\n"
"s_lshl_b32 s80, s80, 14\n"
"s_add_u32 s85, s96, s80\n"
"v_mfma_f32_16x16x32_bf16 v[200:203], a[252:255], a[120:123], v[200:203]\n"
"v_mfma_f32_16x16x32_bf16 v[204:207], a[248:251], a[108:111], v[204:207]\n"
"s_waitcnt lgkmcnt(4)\n"
"v_mfma_f32_16x16x32_bf16 v[204:207], a[252:255], a[124:127], v[204:207]\n"
"v_mfma_f32_16x16x32_bf16 v[208:211], a[224:227], v[112:115], v[208:211]\n"
"ds_read_b128 a[248:251], v2 offset:6144\n"
"v_mfma_f32_16x16x32_bf16 v[208:211], a[228:231], v[128:131], v[208:211]\n"
"v_mfma_f32_16x16x32_bf16 v[212:215], a[224:227], v[116:119], v[212:215]\n"
"v_mfma_f32_16x16x32_bf16 v[212:215], a[228:231], v[132:135], v[212:215]\n"
"ds_read_b128 a[252:255], v2 offset:7168\n"
"v_cvt_pk_bf16_f32 v192, v192, v193\n"
"v_mfma_f32_16x16x32_bf16 v[216:219], a[224:227], v[120:123], v[216:219]\n"
"v_cvt_pk_bf16_f32 v193, v194, v195\n"
"v_cvt_pk_bf16_f32 v194, v196, v197\n"
"v_mfma_f32_16x16x32_bf16 v[216:219], a[228:231], v[136:139], v[216:219]\n"
"v_cvt_pk_bf16_f32 v195, v198, v199\n"
"v_cvt_pk_bf16_f32 v196, v200, v201\n"
"v_mfma_f32_16x16x32_bf16 v[220:223], a[224:227], v[124:127], v[220:223]\n"
"s_waitcnt lgkmcnt(4)\n"
"v_cvt_pk_bf16_f32 v197, v202, v203\n"
"v_cvt_pk_bf16_f32 v198, v204, v205\n"
"v_mfma_f32_16x16x32_bf16 v[220:223], a[228:231], v[140:143], v[220:223]\n"
"v_cvt_pk_bf16_f32 v199, v206, v207\n"
"v_mfma_f32_16x16x32_bf16 v[208:211], a[232:235], a[128:131], v[208:211]\n"
"ds_read_b128 a[224:227], v2 offset:8192\n"
"v_mfma_f32_16x16x32_bf16 v[208:211], a[236:239], a[144:147], v[208:211]\n"
"buffer_store_dwordx4 v[192:195], v1, s[40:43], s85 offen sc1\n"
"v_mfma_f32_16x16x32_bf16 v[212:215], a[232:235], a[132:135], v[212:215]\n"
"v_mfma_f32_16x16x32_bf16 v[212:215], a[236:239], a[148:151], v[212:215]\n"
"ds_read_b128 a[228:231], v2 offset:9216\n"
"v_mfma_f32_16x16x32_bf16 v[216:219], a[232:235], a[136:139], v[216:219]\n"
"buffer_store_dwordx4 v[196:199], v1, s[40:43], s85 offen offset:1024 sc1\n"
"v_mfma_f32_16x16x32_bf16 v[216:219], a[236:239], a[152:155], v[216:219]\n"
"v_mfma_f32_16x16x32_bf16 v[220:223], a[232:235], a[140:143], v[220:223]\n"
"s_waitcnt lgkmcnt(4)\n"
"v_mfma_f32_16x16x32_bf16 v[220:223], a[236:239], a[156:159], v[220:223]\n"
"v_mfma_f32_16x16x32_bf16 v[208:211], a[240:243], a[160:163], v[208:211]\n"
"ds_read_b128 a[232:235], v2 offset:10240\n"
"v_mfma_f32_16x16x32_bf16 v[208:211], a[244:247], a[176:179], v[208:211]\n"
"v_mfma_f32_16x16x32_bf16 v[212:215], a[240:243], a[164:167], v[212:215]\n"
"v_mfma_f32_16x16x32_bf16 v[212:215], a[244:247], a[180:183], v[212:215]\n"
"ds_read_b128 a[236:239], v2 offset:11264\n"
"v_mfma_f32_16x16x32_bf16 v[216:219], a[240:243], a[168:171], v[216:219]\n"
"v_mfma_f32_16x16x32_bf16 v[216:219], a[244:247], a[184:187], v[216:219]\n"
"v_mfma_f32_16x16x32_bf16 v[220:223], a[240:243], a[172:175], v[220:223]\n"
"s_waitcnt lgkmcnt(4)\n"
"v_mfma_f32_16x16x32_bf16 v[220:223], a[244:247], a[188:191], v[220:223]\n"
"v_mfma_f32_16x16x32_bf16 v[208:211], a[248:251], a[192:195], v[208:211]\n"
"ds_read_b128 a[240:243], v2 offset:12288\n"
"v_mfma_f32_16x16x32_bf16 v[208:211], a[252:255], a[208:211], v[208:211]\n"
"v_mfma_f32_16x16x32_bf16 v[212:215], a[248:251], a[196:199], v[212:215]\n"
"v_mfma_f32_16x16x32_bf16 v[212:215], a[252:255], a[212:215], v[212:215]\n"
"ds_read_b128 a[244:247], v2 offset:13312\n"
"v_mfma_f32_16x16x32_bf16 v[216:219], a[248:251], a[200:203], v[216:219]\n"
"v_mfma_f32_16x16x32_bf16 v[216:219], a[252:255], a[216:219], v[216:219]\n"
"v_mfma_f32_16x16x32_bf16 v[220:223], a[248:251], a[204:207], v[220:223]\n"
"s_waitcnt lgkmcnt(4)\n"
"v_mfma_f32_16x16x32_bf16 v[220:223], a[252:255], a[220:223], v[220:223]\n"
"v_mfma_f32_16x16x32_bf16 v[208:211], a[224:227], a[0:3], v[208:211]\n"
"ds_read_b128 a[248:251], v2 offset:14336\n"
"v_mfma_f32_16x16x32_bf16 v[208:211], a[228:231], a[16:19], v[208:211]\n"
"v_mfma_f32_16x16x32_bf16 v[212:215], a[224:227], a[4:7], v[212:215]\n"
"v_mfma_f32_16x16x32_bf16 v[212:215], a[228:231], a[20:23], v[212:215]\n"
"ds_read_b128 a[252:255], v2 offset:15360\n"
"v_mfma_f32_16x16x32_bf16 v[216:219], a[224:227], a[8:11], v[216:219]\n"
"v_mfma_f32_16x16x32_bf16 v[216:219], a[228:231], a[24:27], v[216:219]\n"
"v_mfma_f32_16x16x32_bf16 v[220:223], a[224:227], a[12:15], v[220:223]\n"
"s_waitcnt lgkmcnt(4)\n"
"v_mfma_f32_16x16x32_bf16 v[220:223], a[228:231], a[28:31], v[220:223]\n"
"v_mfma_f32_16x16x32_bf16 v[208:211], a[232:235], a[32:35], v[208:211]\n"
"v_mfma_f32_16x16x32_bf16 v[208:211], a[236:239], a[48:51], v[208:211]\n"
"v_mfma_f32_16x16x32_bf16 v[212:215], a[232:235], a[36:39], v[212:215]\n"
"v_mfma_f32_16x16x32_bf16 v[212:215], a[236:239], a[52:55], v[212:215]\n"
"v_mfma_f32_16x16x32_bf16 v[216:219], a[232:235], a[40:43], v[216:219]\n"
"v_mfma_f32_16x16x32_bf16 v[216:219], a[236:239], a[56:59], v[216:219]\n"
"v_mfma_f32_16x16x32_bf16 v[220:223], a[232:235], a[44:47], v[220:223]\n"
"s_waitcnt lgkmcnt(2)\n"
"v_mfma_f32_16x16x32_bf16 v[220:223], a[236:239], a[60:63], v[220:223]\n"
"v_mfma_f32_16x16x32_bf16 v[208:211], a[240:243], a[64:67], v[208:211]\n"
"v_mfma_f32_16x16x32_bf16 v[208:211], a[244:247], a[80:83], v[208:211]\n"
"v_mfma_f32_16x16x32_bf16 v[212:215], a[240:243], a[68:71], v[212:215]\n"
"v_mfma_f32_16x16x32_bf16 v[212:215], a[244:247], a[84:87], v[212:215]\n"
"v_mfma_f32_16x16x32_bf16 v[216:219], a[240:243], a[72:75], v[216:219]\n"
"v_mfma_f32_16x16x32_bf16 v[216:219], a[244:247], a[88:91], v[216:219]\n"
"v_mfma_f32_16x16x32_bf16 v[220:223], a[240:243], a[76:79], v[220:223]\n"
"s_waitcnt lgkmcnt(0)\n"
"v_mfma_f32_16x16x32_bf16 v[220:223], a[244:247], a[92:95], v[220:223]\n"
"v_mfma_f32_16x16x32_bf16 v[208:211], a[248:251], a[96:99], v[208:211]\n"
"v_mfma_f32_16x16x32_bf16 v[208:211], a[252:255], a[112:115], v[208:211]\n"
"v_mfma_f32_16x16x32_bf16 v[212:215], a[248:251], a[100:103], v[212:215]\n"
"s_lshl_b32 s78, s73, 2\n"
"v_mfma_f32_16x16x32_bf16 v[212:215], a[252:255], a[116:119], v[212:215]\n"
"s_add_u32 s78, s78, s71\n"
"v_mfma_f32_16x16x32_bf16 v[216:219], a[248:251], a[104:107], v[216:219]\n"
"s_lshl_b32 s78, s78, 3\n"
"v_mfma_f32_16x16x32_bf16 v[216:219], a[252:255], a[120:123], v[216:219]\n"
"s_add_u32 s78, s78, s100\n"
"v_mfma_f32_16x16x32_bf16 v[220:223], a[248:251], a[108:111], v[220:223]\n"
"s_lshl_b32 s78, s78, 6\n"
"s_add_u32 s78, s78, 79429632\n"
"v_mov_b32 v8, s92\n"
"v_mfma_f32_16x16x32_bf16 v[220:223], a[252:255], a[124:127], v[220:223]\n"
"v_mov_b32 v9, s93\n"
"s_waitcnt vmcnt(0)\n"
"buffer_store_dwordx2 v[8:9], v14, s[40:43], s78 offen sc1\n"
"s_add_u32 s85, s96, 131072\n"
"v_cvt_pk_bf16_f32 v208, v208, v209\n"
"v_cvt_pk_bf16_f32 v209, v210, v211\n"
"v_cvt_pk_bf16_f32 v210, v212, v213\n"
"v_cvt_pk_bf16_f32 v211, v214, v215\n"
"v_cvt_pk_bf16_f32 v212, v216, v217\n"
"v_cvt_pk_bf16_f32 v213, v218, v219\n"
"v_cvt_pk_bf16_f32 v214, v220, v221\n"
"v_cvt_pk_bf16_f32 v215, v222, v223\n"
"buffer_store_dwordx4 v[208:211], v1, s[40:43], s85 offen sc1\n"
"buffer_store_dwordx4 v[212:215], v1, s[40:43], s85 offen offset:1024 sc1\n"
"s_lshl_b32 s78, s73, 2\n"
"s_add_u32 s78, s78, s71\n"
"s_lshl_b32 s78, s78, 3\n"
"s_add_u32 s78, s78, s100\n"
"s_lshl_b32 s78, s78, 6\n"
"s_add_u32 s78, s78, 79495168\n"
"v_mov_b32 v8, s92\n"
"v_mov_b32 v9, s93\n"
"s_waitcnt vmcnt(0)\n"
"buffer_store_dwordx2 v[8:9], v14, s[40:43], s78 offen sc1\n"
"s_lshl_b32 s23, s73, 2\n"
"s_add_u32 s23, s23, s71\n"
"s_lshl_b32 s23, s23, 3\n"
"s_add_u32 s23, s23, s100\n"
"s_lshl_b32 s23, s23, 6\n"
"s_and_b32 s23, s23, 0xfffffe00\n"
"s_add_u32 s23, s23, 79495168\n"
"v_and_b32 v15, 7, v0\n"
"v_lshlrev_b32 v15, 6, v15\n"
"buffer_load_dwordx2 v[10:11], v15, s[40:43], s23 offen sc1\n"
"s_waitcnt vmcnt(0)\n"
"v_cmp_ne_u64 vcc, s[92:93], v[10:11]\n"
"s_nop 0\n"
"s_cbranch_vccnz L_slow_47_%=\n"
"L_back_48_%=:\n"
"s_add_u32 s78, s52, 0\n"
"buffer_load_dwordx2 v[16:17], v1, s[40:43], s78 offen sc1\n"
"s_add_u32 s78, s52, 2048\n"
"buffer_load_dwordx2 v[18:19], v1, s[40:43], s78 offen sc1\n"
"s_add_u32 s78, s52, 4096\n"
"buffer_load_dwordx2 v[20:21], v1, s[40:43], s78 offen sc1\n"
"s_add_u32 s78, s52, 6144\n"
"buffer_load_dwordx2 v[22:23], v1, s[40:43], s78 offen sc1\n"
"s_add_u32 s78, s52, 8192\n"
"buffer_load_dwordx2 v[24:25], v1, s[40:43], s78 offen sc1\n"
"s_add_u32 s78, s52, 10240\n"
"buffer_load_dwordx2 v[26:27], v1, s[40:43], s78 offen sc1\n"
"s_add_u32 s78, s52, 12288\n"
"buffer_load_dwordx2 v[28:29], v1, s[40:43], s78 offen sc1\n"
"s_add_u32 s78, s52, 14336\n"
"buffer_load_dwordx2 v[30:31], v1, s[40:43], s78 offen sc1\n"
"s_waitcnt vmcnt(7)\n"
"v_lshlrev_b32 v32, 16, v16\n"
"v_and_b32 v33, 0xffff0000, v16\n"
"v_lshlrev_b32 v34, 16, v17\n"
"v_and_b32 v35, 0xffff0000, v17\n"
"s_waitcnt vmcnt(3)\n"
"v_lshlrev_b32 v36, 16, v24\n"
"v_and_b32 v37, 0xffff0000, v24\n"
"v_lshlrev_b32 v38, 16, v25\n"
"v_and_b32 v39, 0xffff0000, v25\n"
"v_dot2c_f32_bf16 v32, s88, v18\n"
"v_dot2c_f32_bf16 v33, s65, v18\n"
"v_dot2c_f32_bf16 v34, s88, v19\n"
"v_dot2c_f32_bf16 v35, s65, v19\n"
"s_waitcnt vmcnt(2)\n"
"v_dot2c_f32_bf16 v36, s88, v26\n"
"v_dot2c_f32_bf16 v37, s65, v26\n"
"v_dot2c_f32_bf16 v38, s88, v27\n"
"v_dot2c_f32_bf16 v39, s65, v27\n"
"v_dot2c_f32_bf16 v32, s88, v20\n"
"v_dot2c_f32_bf16 v33, s65, v20\n"
"v_dot2c_f32_bf16 v34, s88, v21\n"
"v_dot2c_f32_bf16 v35, s65, v21\n"
"s_waitcnt vmcnt(1)\n"
"v_dot2c_f32_bf16 v36, s88, v28\n"
"v_dot2c_f32_bf16 v37, s65, v28\n"
"v_dot2c_f32_bf16 v38, s88, v29\n"
"v_dot2c_f32_bf16 v39, s65, v29\n"
"v_dot2c_f32_bf16 v32, s88, v22\n"
"v_dot2c_f32_bf16 v33, s65, v22\n"
"v_dot2c_f32_bf16 v34, s88, v23\n"
"v_dot2c_f32_bf16 v35, s65, v23\n"
"s_waitcnt vmcnt(0)\n"
"v_dot2c_f32_bf16 v36, s88, v30\n"
"v_dot2c_f32_bf16 v37, s65, v30\n"
"v_dot2c_f32_bf16 v38, s88, v31\n"
"v_dot2c_f32_bf16 v39, s65, v31\n"
"s_nop 2\n"
"v_pk_mul_f32 v[40:41], s[86:87], v[32:33] op_sel_hi:[0,1]\n"
"v_pk_mul_f32 v[42:43], s[86:87], v[34:35] op_sel_hi:[0,1]\n"
"v_exp_f32 v40, v40\n"
"v_exp_f32 v41, v41\n"
"v_exp_f32 v42, v42\n"
"v_exp_f32 v43, v43\n"
"v_pk_mul_f32 v[36:37], v[36:37], v[32:33]\n"
"v_pk_mul_f32 v[38:39], v[38:39], v[34:35]\n"
"v_lshlrev_b32 v32, 16, v224\n"
"v_and_b32 v33, 0xffff0000, v224\n"
"v_lshlrev_b32 v34, 16, v225\n"
"v_and_b32 v35, 0xffff0000, v225\n"
"v_pk_add_f32 v[40:41], 1.0, v[40:41] op_sel_hi:[0,1]\n"
"v_pk_add_f32 v[42:43], 1.0, v[42:43] op_sel_hi:[0,1]\n"
"v_rcp_f32 v40, v40\n"
"v_rcp_f32 v41, v41\n"
"v_rcp_f32 v42, v42\n"
"v_rcp_f32 v43, v43\n"
"v_pk_fma_f32 v[36:37], v[36:37], v[40:41], v[32:33]\n"
"v_pk_fma_f32 v[38:39], v[38:39], v[42:43], v[34:35]\n"
"v_cvt_pk_bf16_f32 v36, v36, v37\n"
"v_cvt_pk_bf16_f32 v37, v38, v39\n"
"buffer_store_dwordx2 v[36:37], v5, s[48:51], s53 offen sc1\n"
"s_endpgm\n"
".p2align 6\n"
"L_att1_5_%=:\n"
"v_mfma_f32_16x16x32_bf16 v[32:35], a[128:131], v[152:155], v[116:119]\n"
"v_exp_f32 v16, v16\n"
"v_mfma_f32_16x16x32_bf16 v[32:35], a[132:135], v[156:159], v[32:35]\n"
"v_cvt_pk_bf16_f32 v95, v46, v47\n"
"v_readlane_b32 s52, v232, s55\n"
"v_mfma_f32_16x16x32_bf16 v[32:35], a[136:139], v[56:59], v[32:35]\n"
"s_waitcnt vmcnt(20) lgkmcnt(0)\n"
"v_exp_f32 v17, v17\n"
"v_mfma_f32_16x16x32_bf16 v[32:35], a[140:143], v[60:63], v[32:35]\n"
"v_mfma_f32_16x16x32_bf16 v[36:39], a[144:147], v[152:155], v[116:119]\n"
"v_exp_f32 v18, v18\n"
"v_mfma_f32_16x16x32_bf16 v[36:39], a[148:151], v[156:159], v[36:39]\n"
"s_barrier\n"
"v_cvt_pk_bf16_f32 v80, v16, v17\n"
"v_mfma_f32_16x16x32_bf16 v[36:39], a[152:155], v[56:59], v[36:39]\n"
"v_exp_f32 v19, v19\n"
"v_mfma_f32_16x16x32_bf16 v[36:39], a[156:159], v[60:63], v[36:39]\n"
"ds_read_b64_tr_b16 v[240:241], v235\n"
"ds_read_b64_tr_b16 v[242:243], v235 offset:2048\n"
"v_mfma_f32_16x16x32_bf16 v[40:43], a[160:163], v[152:155], v[116:119]\n"
"v_exp_f32 v20, v20\n"
"v_mfma_f32_16x16x32_bf16 v[40:43], a[164:167], v[156:159], v[40:43]\n"
"s_cmp_eq_u32 s55, 21\n"
"s_cselect_b32 s59, s57, 0x7fff0000\n"
"v_cvt_pk_bf16_f32 v81, v18, v19\n"
"v_mfma_f32_16x16x32_bf16 v[40:43], a[168:171], v[56:59], v[40:43]\n"
"v_exp_f32 v21, v21\n"
"v_mfma_f32_16x16x32_bf16 v[40:43], a[172:175], v[60:63], v[40:43]\n"
"buffer_store_dwordx2 v[236:237], v14, s[40:43], s59 offen sc1\n"
"v_mfma_f32_16x16x32_bf16 v[44:47], a[176:179], v[152:155], v[116:119]\n"
"s_and_b32 s78, s56, 7168\n"
"v_exp_f32 v22, v22\n"
"v_mfma_f32_16x16x32_bf16 v[44:47], a[180:183], v[156:159], v[44:47]\n"
"s_waitcnt lgkmcnt(0)\n"
"s_add_u32 m0, s54, s78\n"
"v_cvt_pk_bf16_f32 v82, v20, v21\n"
"v_mfma_f32_16x16x32_bf16 v[44:47], a[184:187], v[56:59], v[44:47]\n"
"v_exp_f32 v23, v23\n"
"v_mfma_f32_16x16x32_bf16 v[44:47], a[188:191], v[60:63], v[44:47]\n"
"buffer_load_dwordx4 v234, s[28:31], s52 offen nt lds\n"
"v_mfma_f32_16x16x32_bf16 a[12:15], a[192:195], v[88:91], a[12:15]\n"
"v_exp_f32 v24, v24\n"
"v_mfma_f32_16x16x32_bf16 a[12:15], a[196:199], v[92:95], a[12:15]\n"
"ds_read_b64_tr_b16 a[192:193], v254 offset:16384\n"
"v_cvt_pk_bf16_f32 v83, v22, v23\n"
"v_mfma_f32_16x16x32_bf16 a[28:31], a[200:203], v[88:91], a[28:31]\n"
"v_exp_f32 v25, v25\n"
"v_mfma_f32_16x16x32_bf16 a[28:31], a[204:207], v[92:95], a[28:31]\n"
"ds_read_b64_tr_b16 a[194:195], v254 offset:20480\n"
"ds_read_b64_tr_b16 a[196:197], v254 offset:24576\n"
"v_mfma_f32_16x16x32_bf16 a[44:47], a[208:211], v[88:91], a[44:47]\n"
"v_exp_f32 v26, v26\n"
"v_mfma_f32_16x16x32_bf16 a[44:47], a[212:215], v[92:95], a[44:47]\n"
"ds_read_b64_tr_b16 a[198:199], v254 offset:28672\n"
"v_cvt_pk_bf16_f32 v84, v24, v25\n"
"v_mfma_f32_16x16x32_bf16 a[60:63], a[216:219], v[88:91], a[60:63]\n"
"s_add_u32 s80, s55, 4\n"
"v_exp_f32 v27, v27\n"
"v_mfma_f32_16x16x32_bf16 a[60:63], a[220:223], v[92:95], a[60:63]\n"
"s_and_b32 s79, s80, 1\n"
"ds_read_b64_tr_b16 a[200:201], v255 offset:16384\n"
"ds_read_b64_tr_b16 a[202:203], v255 offset:20480\n"
"v_mfma_f32_16x16x32_bf16 a[76:79], a[224:227], v[88:91], a[76:79]\n"
"s_lshl_b32 s76, s79, 5\n"
"v_exp_f32 v28, v28\n"
"v_mfma_f32_16x16x32_bf16 a[76:79], a[228:231], v[92:95], a[76:79]\n"
"s_and_b32 s79, s80, 2\n"
"ds_read_b64_tr_b16 a[204:205], v255 offset:24576\n"
"v_cvt_pk_bf16_f32 v85, v26, v27\n"
"v_smfmac_f32_16x16x64_bf16 v[140:143], v[228:231], v[88:95], v239\n"
"ds_read_b64_tr_b16 a[206:207], v255 offset:28672\n"
"v_mfma_f32_16x16x32_bf16 a[92:95], a[232:235], v[88:91], a[92:95]\n"
"s_lshl_b32 s79, s79, 8\n"
"v_exp_f32 v29, v29\n"
"v_mfma_f32_16x16x32_bf16 a[92:95], a[236:239], v[92:95], a[92:95]\n"
"s_or_b32 s76, s76, s79\n"
"ds_read_b64_tr_b16 a[208:209], v254 offset:16896\n"
"ds_read_b64_tr_b16 a[210:211], v254 offset:20992\n"
"v_mfma_f32_16x16x32_bf16 a[108:111], a[240:243], v[88:91], a[108:111]\n"
"s_and_b32 s79, s80, 4\n"
"v_exp_f32 v30, v30\n"
"v_mfma_f32_16x16x32_bf16 a[108:111], a[244:247], v[92:95], a[108:111]\n"
"s_lshl_b32 s79, s79, 10\n"
"ds_read_b64_tr_b16 a[212:213], v254 offset:25088\n"
"v_cvt_pk_bf16_f32 v86, v28, v29\n"
"v_mfma_f32_16x16x32_bf16 a[124:127], a[248:251], v[88:91], a[124:127]\n"
"v_exp_f32 v31, v31\n"
"v_mfma_f32_16x16x32_bf16 a[124:127], a[252:255], v[92:95], a[124:127]\n"
"s_or_b32 s76, s76, s79\n"
"ds_read_b64_tr_b16 a[214:215], v254 offset:29184\n"
"ds_read_b64_tr_b16 a[216:217], v255 offset:16896\n"
"v_mfma_f32_16x16x32_bf16 v[16:19], a[128:131], v[160:163], v[120:123]\n"
"v_exp_f32 v32, v32\n"
"v_mfma_f32_16x16x32_bf16 v[16:19], a[132:135], v[164:167], v[16:19]\n"
"ds_read_b64_tr_b16 a[218:219], v255 offset:20992\n"
"v_cvt_pk_bf16_f32 v87, v30, v31\n"
"v_mfma_f32_16x16x32_bf16 v[16:19], a[136:139], v[64:67], v[16:19]\n"
"v_exp_f32 v33, v33\n"
"v_mfma_f32_16x16x32_bf16 v[16:19], a[140:143], v[68:71], v[16:19]\n"
"ds_read_b64_tr_b16 a[220:221], v255 offset:25088\n"
"ds_read_b64_tr_b16 a[222:223], v255 offset:29184\n"
"v_mfma_f32_16x16x32_bf16 v[20:23], a[144:147], v[160:163], v[120:123]\n"
"v_exp_f32 v34, v34\n"
"v_mfma_f32_16x16x32_bf16 v[20:23], a[148:151], v[164:167], v[20:23]\n"
"ds_read_b64_tr_b16 a[224:225], v254 offset:17408\n"
"v_cvt_pk_bf16_f32 v88, v32, v33\n"
"v_mfma_f32_16x16x32_bf16 v[20:23], a[152:155], v[64:67], v[20:23]\n"
"v_exp_f32 v35, v35\n"
"v_mfma_f32_16x16x32_bf16 v[20:23], a[156:159], v[68:71], v[20:23]\n"
"ds_read_b64_tr_b16 a[226:227], v254 offset:21504\n"
"ds_read_b64_tr_b16 a[228:229], v254 offset:25600\n"
"v_mfma_f32_16x16x32_bf16 v[24:27], a[160:163], v[160:163], v[120:123]\n"
"v_exp_f32 v36, v36\n"
"v_mfma_f32_16x16x32_bf16 v[24:27], a[164:167], v[164:167], v[24:27]\n"
"ds_read_b64_tr_b16 a[230:231], v254 offset:29696\n"
"v_cvt_pk_bf16_f32 v89, v34, v35\n"
"v_mfma_f32_16x16x32_bf16 v[24:27], a[168:171], v[64:67], v[24:27]\n"
"v_exp_f32 v37, v37\n"
"v_mfma_f32_16x16x32_bf16 v[24:27], a[172:175], v[68:71], v[24:27]\n"
"ds_read_b64_tr_b16 a[232:233], v255 offset:17408\n"
"ds_read_b64_tr_b16 a[234:235], v255 offset:21504\n"
"v_smfmac_f32_16x16x64_bf16 v[128:131], v[228:231], v[80:87], v239\n"
"ds_read_b64_tr_b16 a[236:237], v255 offset:25600\n"
"v_mfma_f32_16x16x32_bf16 v[28:31], a[176:179], v[160:163], v[120:123]\n"
"v_exp_f32 v38, v38\n"
"v_mfma_f32_16x16x32_bf16 v[28:31], a[180:183], v[164:167], v[28:31]\n"
"ds_read_b64_tr_b16 a[238:239], v255 offset:29696\n"
"v_cvt_pk_bf16_f32 v90, v36, v37\n"
"v_mfma_f32_16x16x32_bf16 v[28:31], a[184:187], v[64:67], v[28:31]\n"
"s_waitcnt lgkmcnt(15)\n"
"v_exp_f32 v39, v39\n"
"v_mfma_f32_16x16x32_bf16 v[28:31], a[188:191], v[68:71], v[28:31]\n"
"ds_read_b64_tr_b16 a[240:241], v254 offset:17920\n"
"ds_read_b64_tr_b16 a[242:243], v254 offset:22016\n"
"v_mfma_f32_16x16x32_bf16 a[0:3], a[192:195], v[80:83], a[0:3]\n"
"v_exp_f32 v40, v40\n"
"v_mfma_f32_16x16x32_bf16 a[0:3], a[196:199], v[84:87], a[0:3]\n"
"ds_read_b64_tr_b16 a[244:245], v254 offset:26112\n"
"v_cvt_pk_bf16_f32 v91, v38, v39\n"
"v_mfma_f32_16x16x32_bf16 a[16:19], a[200:203], v[80:83], a[16:19]\n"
"s_waitcnt lgkmcnt(11)\n"
"v_exp_f32 v41, v41\n"
"v_mfma_f32_16x16x32_bf16 a[16:19], a[204:207], v[84:87], a[16:19]\n"
"ds_read_b64_tr_b16 a[246:247], v254 offset:30208\n"
"ds_read_b64_tr_b16 a[248:249], v255 offset:17920\n"
"v_mfma_f32_16x16x32_bf16 a[32:35], a[208:211], v[80:83], a[32:35]\n"
"v_exp_f32 v42, v42\n"
"v_mfma_f32_16x16x32_bf16 a[32:35], a[212:215], v[84:87], a[32:35]\n"
"ds_read_b64_tr_b16 a[250:251], v255 offset:22016\n"
"v_cvt_pk_bf16_f32 v92, v40, v41\n"
"v_mfma_f32_16x16x32_bf16 a[48:51], a[216:219], v[80:83], a[48:51]\n"
"s_waitcnt lgkmcnt(6)\n"
"v_exp_f32 v43, v43\n"
"v_mfma_f32_16x16x32_bf16 a[48:51], a[220:223], v[84:87], a[48:51]\n"
"ds_read_b64_tr_b16 a[252:253], v255 offset:26112\n"
"ds_read_b64_tr_b16 a[254:255], v255 offset:30208\n"
"v_mfma_f32_16x16x32_bf16 a[64:67], a[224:227], v[80:83], a[64:67]\n"
"v_exp_f32 v44, v44\n"
"v_mfma_f32_16x16x32_bf16 a[64:67], a[228:231], v[84:87], a[64:67]\n"
"s_add_u32 m0, s90, 32768\n"
"v_cvt_pk_bf16_f32 v93, v42, v43\n"
"v_mfma_f32_16x16x32_bf16 a[80:83], a[232:235], v[80:83], a[80:83]\n"
"s_waitcnt lgkmcnt(4)\n"
"v_exp_f32 v45, v45\n"
"v_mfma_f32_16x16x32_bf16 a[80:83], a[236:239], v[84:87], a[80:83]\n"
"buffer_load_dwordx4 v244, s[32:35], s89 offen nt lds\n"
"v_mfma_f32_16x16x32_bf16 a[96:99], a[240:243], v[80:83], a[96:99]\n"
"s_waitcnt lgkmcnt(0)\n"
"v_exp_f32 v46, v46\n"
"v_mfma_f32_16x16x32_bf16 a[96:99], a[244:247], v[84:87], a[96:99]\n"
"v_cvt_pk_bf16_f32 v94, v44, v45\n"
"v_xor_b32 v235, s76, v4\n"
"v_mfma_f32_16x16x32_bf16 a[112:115], a[248:251], v[80:83], a[112:115]\n"
"v_exp_f32 v47, v47\n"
"v_mfma_f32_16x16x32_bf16 a[112:115], a[252:255], v[84:87], a[112:115]\n"
"buffer_load_dwordx4 v245, s[32:35], s89 offen offset:1024 nt lds\n"
"v_mfma_f32_16x16x32_bf16 v[32:35], a[128:131], v[168:171], v[124:127]\n"
"v_exp_f32 v16, v16\n"
"v_mfma_f32_16x16x32_bf16 v[32:35], a[132:135], v[172:175], v[32:35]\n"
"ds_read_b128 a[128:131], v250\n"
"v_cvt_pk_bf16_f32 v95, v46, v47\n"
"v_mfma_f32_16x16x32_bf16 v[32:35], a[136:139], v[72:75], v[32:35]\n"
"v_exp_f32 v17, v17\n"
"v_mfma_f32_16x16x32_bf16 v[32:35], a[140:143], v[76:79], v[32:35]\n"
"ds_read_b128 a[132:135], v251\n"
"ds_read_b128 a[136:139], v252\n"
"v_mfma_f32_16x16x32_bf16 v[36:39], a[144:147], v[168:171], v[124:127]\n"
"v_exp_f32 v18, v18\n"
"v_mfma_f32_16x16x32_bf16 v[36:39], a[148:151], v[172:175], v[36:39]\n"
"ds_read_b128 a[140:143], v253\n"
"v_cvt_pk_bf16_f32 v80, v16, v17\n"
"v_mfma_f32_16x16x32_bf16 v[36:39], a[152:155], v[72:75], v[36:39]\n"
"v_exp_f32 v19, v19\n"
"v_mfma_f32_16x16x32_bf16 v[36:39], a[156:159], v[76:79], v[36:39]\n"
"ds_read_b128 a[144:147], v250 offset:4096\n"
"ds_read_b128 a[148:151], v251 offset:4096\n"
"v_mfma_f32_16x16x32_bf16 v[40:43], a[160:163], v[168:171], v[124:127]\n"
"v_exp_f32 v20, v20\n"
"v_mfma_f32_16x16x32_bf16 v[40:43], a[164:167], v[172:175], v[40:43]\n"
"ds_read_b128 a[152:155], v252 offset:4096\n"
"v_cvt_pk_bf16_f32 v81, v18, v19\n"
"v_mfma_f32_16x16x32_bf16 v[40:43], a[168:171], v[72:75], v[40:43]\n"
"v_exp_f32 v21, v21\n"
"v_mfma_f32_16x16x32_bf16 v[40:43], a[172:175], v[76:79], v[40:43]\n"
"ds_read_b128 a[156:159], v253 offset:4096\n"
"ds_read_b128 a[160:163], v250 offset:8192\n"
"v_mfma_f32_16x16x32_bf16 v[44:47], a[176:179], v[168:171], v[124:127]\n"
"v_exp_f32 v22, v22\n"
"v_mfma_f32_16x16x32_bf16 v[44:47], a[180:183], v[172:175], v[44:47]\n"
"ds_read_b128 a[164:167], v251 offset:8192\n"
"v_cvt_pk_bf16_f32 v82, v20, v21\n"
"v_mfma_f32_16x16x32_bf16 v[44:47], a[184:187], v[72:75], v[44:47]\n"
"v_exp_f32 v23, v23\n"
"v_mfma_f32_16x16x32_bf16 v[44:47], a[188:191], v[76:79], v[44:47]\n"
"ds_read_b128 a[168:171], v252 offset:8192\n"
"ds_read_b128 a[172:175], v253 offset:8192\n"
"v_mfma_f32_16x16x32_bf16 a[4:7], a[192:195], v[88:91], a[4:7]\n"
"v_exp_f32 v24, v24\n"
"v_mfma_f32_16x16x32_bf16 a[4:7], a[196:199], v[92:95], a[4:7]\n"
"ds_read_b128 a[176:179], v250 offset:12288\n"
"v_cvt_pk_bf16_f32 v83, v22, v23\n"
"v_mfma_f32_16x16x32_bf16 a[20:23], a[200:203], v[88:91], a[20:23]\n"
"v_exp_f32 v25, v25\n"
"v_mfma_f32_16x16x32_bf16 a[20:23], a[204:207], v[92:95], a[20:23]\n"
"ds_read_b128 a[180:183], v251 offset:12288\n"
"ds_read_b128 a[184:187], v252 offset:12288\n"
"v_mfma_f32_16x16x32_bf16 a[36:39], a[208:211], v[88:91], a[36:39]\n"
"v_exp_f32 v26, v26\n"
"v_mfma_f32_16x16x32_bf16 a[36:39], a[212:215], v[92:95], a[36:39]\n"
"ds_read_b128 a[188:191], v253 offset:12288\n"
"v_cvt_pk_bf16_f32 v84, v24, v25\n"
"v_mfma_f32_16x16x32_bf16 a[52:55], a[216:219], v[88:91], a[52:55]\n"
"v_exp_f32 v27, v27\n"
"v_mfma_f32_16x16x32_bf16 a[52:55], a[220:223], v[92:95], a[52:55]\n"
"buffer_load_dwordx4 v246, s[32:35], s89 offen offset:2048 nt lds\n"
"v_mfma_f32_16x16x32_bf16 a[68:71], a[224:227], v[88:91], a[68:71]\n"
"v_exp_f32 v28, v28\n"
"v_mfma_f32_16x16x32_bf16 a[68:71], a[228:231], v[92:95], a[68:71]\n"
"v_cvt_pk_bf16_f32 v85, v26, v27\n"
"v_mfma_f32_16x16x32_bf16 a[84:87], a[232:235], v[88:91], a[84:87]\n"
"v_exp_f32 v29, v29\n"
"v_mfma_f32_16x16x32_bf16 a[84:87], a[236:239], v[92:95], a[84:87]\n"
"buffer_load_dwordx4 v247, s[32:35], s89 offen offset:3072 nt lds\n"
"v_smfmac_f32_16x16x64_bf16 v[132:135], v[228:231], v[88:95], v239\n"
"v_mfma_f32_16x16x32_bf16 a[100:103], a[240:243], v[88:91], a[100:103]\n"
"s_sub_u32 s82, s89, 16384\n"
"v_exp_f32 v30, v30\n"
"v_mfma_f32_16x16x32_bf16 a[100:103], a[244:247], v[92:95], a[100:103]\n"
"s_add_u32 m0, s90, 49152\n"
"v_cvt_pk_bf16_f32 v86, v28, v29\n"
"v_mfma_f32_16x16x32_bf16 a[116:119], a[248:251], v[88:91], a[116:119]\n"
"s_waitcnt lgkmcnt(0)\n"
"v_exp_f32 v31, v31\n"
"v_mfma_f32_16x16x32_bf16 a[116:119], a[252:255], v[92:95], a[116:119]\n"
"buffer_load_dwordx4 v248, s[36:39], s82 offen nt lds\n"
"v_mfma_f32_16x16x32_bf16 v[16:19], a[128:131], v[144:147], v[112:115]\n"
"v_exp_f32 v32, v32\n"
"v_mfma_f32_16x16x32_bf16 v[16:19], a[132:135], v[148:151], v[16:19]\n"
"v_cvt_pk_bf16_f32 v87, v30, v31\n"
"v_mfma_f32_16x16x32_bf16 v[16:19], a[136:139], v[48:51], v[16:19]\n"
"v_exp_f32 v33, v33\n"
"v_mfma_f32_16x16x32_bf16 v[16:19], a[140:143], v[52:55], v[16:19]\n"
"buffer_load_dwordx4 v249, s[36:39], s82 offen offset:1024 nt lds\n"
"v_mfma_f32_16x16x32_bf16 v[20:23], a[144:147], v[144:147], v[112:115]\n"
"v_exp_f32 v34, v34\n"
"v_mfma_f32_16x16x32_bf16 v[20:23], a[148:151], v[148:151], v[20:23]\n"
"v_cvt_pk_bf16_f32 v88, v32, v33\n"
"v_mfma_f32_16x16x32_bf16 v[20:23], a[152:155], v[48:51], v[20:23]\n"
"v_exp_f32 v35, v35\n"
"v_mfma_f32_16x16x32_bf16 v[20:23], a[156:159], v[52:55], v[20:23]\n"
"buffer_load_dwordx4 v248, s[36:39], s82 offen offset:2048 nt lds\n"
"v_mfma_f32_16x16x32_bf16 v[24:27], a[160:163], v[144:147], v[112:115]\n"
"v_exp_f32 v36, v36\n"
"v_mfma_f32_16x16x32_bf16 v[24:27], a[164:167], v[148:151], v[24:27]\n"
"v_cvt_pk_bf16_f32 v89, v34, v35\n"
"v_mfma_f32_16x16x32_bf16 v[24:27], a[168:171], v[48:51], v[24:27]\n"
"v_exp_f32 v37, v37\n"
"v_mfma_f32_16x16x32_bf16 v[24:27], a[172:175], v[52:55], v[24:27]\n"
"buffer_load_dwordx4 v249, s[36:39], s82 offen offset:3072 nt lds\n"
"v_mfma_f32_16x16x32_bf16 v[28:31], a[176:179], v[144:147], v[112:115]\n"
"v_exp_f32 v38, v38\n"
"v_mfma_f32_16x16x32_bf16 v[28:31], a[180:183], v[148:151], v[28:31]\n"
"v_cvt_pk_bf16_f32 v90, v36, v37\n"
"v_readlane_b32 s53, v233, s55\n"
"v_mfma_f32_16x16x32_bf16 v[28:31], a[184:187], v[48:51], v[28:31]\n"
"v_exp_f32 v39, v39\n"
"v_mfma_f32_16x16x32_bf16 v[28:31], a[188:191], v[52:55], v[28:31]\n"
"v_mfma_f32_16x16x32_bf16 a[8:11], a[192:195], v[80:83], a[8:11]\n"
"v_exp_f32 v40, v40\n"
"v_mfma_f32_16x16x32_bf16 a[8:11], a[196:199], v[84:87], a[8:11]\n"
"v_cvt_pk_bf16_f32 v91, v38, v39\n"
"v_mfma_f32_16x16x32_bf16 a[24:27], a[200:203], v[80:83], a[24:27]\n"
"v_exp_f32 v41, v41\n"
"v_mfma_f32_16x16x32_bf16 a[24:27], a[204:207], v[84:87], a[24:27]\n"
"buffer_store_dwordx4 v[240:243], v1, s[40:43], s53 offen sc1\n"
"v_mfma_f32_16x16x32_bf16 a[40:43], a[208:211], v[80:83], a[40:43]\n"
"v_exp_f32 v42, v42\n"
"v_mfma_f32_16x16x32_bf16 a[40:43], a[212:215], v[84:87], a[40:43]\n"
"s_add_u32 s89, s89, 16384\n"
"v_cvt_pk_bf16_f32 v92, v40, v41\n"
"v_mfma_f32_16x16x32_bf16 a[56:59], a[216:219], v[80:83], a[56:59]\n"
"s_add_u32 s55, s55, 1\n"
"v_exp_f32 v43, v43\n"
"v_mfma_f32_16x16x32_bf16 a[56:59], a[220:223], v[84:87], a[56:59]\n"
"v_mfma_f32_16x16x32_bf16 a[72:75], a[224:227], v[80:83], a[72:75]\n"
"s_add_u32 s56, s56, 1024\n"
"v_exp_f32 v44, v44\n"
"v_mfma_f32_16x16x32_bf16 a[72:75], a[228:231], v[84:87], a[72:75]\n"
"v_cvt_pk_bf16_f32 v93, v42, v43\n"
"v_mfma_f32_16x16x32_bf16 a[88:91], a[232:235], v[80:83], a[88:91]\n"
"v_exp_f32 v45, v45\n"
"v_mfma_f32_16x16x32_bf16 a[88:91], a[236:239], v[84:87], a[88:91]\n"
"v_smfmac_f32_16x16x64_bf16 v[136:139], v[228:231], v[80:87], v239\n"
"v_mfma_f32_16x16x32_bf16 a[104:107], a[240:243], v[80:83], a[104:107]\n"
"v_exp_f32 v46, v46\n"
"v_mfma_f32_16x16x32_bf16 a[104:107], a[244:247], v[84:87], a[104:107]\n"
"v_cvt_pk_bf16_f32 v94, v44, v45\n"
"v_mfma_f32_16x16x32_bf16 a[120:123], a[248:251], v[80:83], a[120:123]\n"
"v_exp_f32 v47, v47\n"
"v_mfma_f32_16x16x32_bf16 a[120:123], a[252:255], v[84:87], a[120:123]\n"
"v_mfma_f32_16x16x32_bf16 v[32:35], a[128:131], v[152:155], v[116:119]\n"
"v_exp_f32 v16, v16\n"
"v_mfma_f32_16x16x32_bf16 v[32:35], a[132:135], v[156:159], v[32:35]\n"
"v_cvt_pk_bf16_f32 v95, v46, v47\n"
"v_readlane_b32 s52, v232, s55\n"
"v_mfma_f32_16x16x32_bf16 v[32:35], a[136:139], v[56:59], v[32:35]\n"
"s_waitcnt vmcnt(20) lgkmcnt(0)\n"
"v_exp_f32 v17, v17\n"
"v_mfma_f32_16x16x32_bf16 v[32:35], a[140:143], v[60:63], v[32:35]\n"
"v_mfma_f32_16x16x32_bf16 v[36:39], a[144:147], v[152:155], v[116:119]\n"
"v_exp_f32 v18, v18\n"
"v_mfma_f32_16x16x32_bf16 v[36:39], a[148:151], v[156:159], v[36:39]\n"
"s_barrier\n"
"v_cvt_pk_bf16_f32 v80, v16, v17\n"
"v_mfma_f32_16x16x32_bf16 v[36:39], a[152:155], v[56:59], v[36:39]\n"
"v_exp_f32 v19, v19\n"
"v_mfma_f32_16x16x32_bf16 v[36:39], a[156:159], v[60:63], v[36:39]\n"
"ds_read_b64_tr_b16 v[240:241], v235\n"
"ds_read_b64_tr_b16 v[242:243], v235 offset:2048\n"
"v_mfma_f32_16x16x32_bf16 v[40:43], a[160:163], v[152:155], v[116:119]\n"
"v_exp_f32 v20, v20\n"
"v_mfma_f32_16x16x32_bf16 v[40:43], a[164:167], v[156:159], v[40:43]\n"
"s_cmp_eq_u32 s55, 12\n"
"s_cbranch_scc1 L_retile1x_6_%=\n"
"L_retile1xb_7_%=:\n"
"s_cmp_eq_u32 s55, 24\n"
"s_cselect_b32 s59, s96, 0x7fff0000\n"
"v_cvt_pk_bf16_f32 v81, v18, v19\n"
"v_mfma_f32_16x16x32_bf16 v[40:43], a[168:171], v[56:59], v[40:43]\n"
"v_exp_f32 v21, v21\n"
"v_mfma_f32_16x16x32_bf16 v[40:43], a[172:175], v[60:63], v[40:43]\n"
"buffer_load_dword v227, v1, s[16:19], s59 offen\n"
"v_mfma_f32_16x16x32_bf16 v[44:47], a[176:179], v[152:155], v[116:119]\n"
"s_and_b32 s78, s56, 7168\n"
"v_exp_f32 v22, v22\n"
"v_mfma_f32_16x16x32_bf16 v[44:47], a[180:183], v[156:159], v[44:47]\n"
"s_waitcnt lgkmcnt(0)\n"
"s_add_u32 m0, s54, s78\n"
"v_cvt_pk_bf16_f32 v82, v20, v21\n"
"v_mfma_f32_16x16x32_bf16 v[44:47], a[184:187], v[56:59], v[44:47]\n"
"v_exp_f32 v23, v23\n"
"v_mfma_f32_16x16x32_bf16 v[44:47], a[188:191], v[60:63], v[44:47]\n"
"buffer_load_dwordx4 v234, s[28:31], s52 offen nt lds\n"
"v_mfma_f32_16x16x32_bf16 a[12:15], a[192:195], v[88:91], a[12:15]\n"
"v_exp_f32 v24, v24\n"
"v_mfma_f32_16x16x32_bf16 a[12:15], a[196:199], v[92:95], a[12:15]\n"
"ds_read_b64_tr_b16 a[192:193], v254 offset:49152\n"
"v_cvt_pk_bf16_f32 v83, v22, v23\n"
"v_mfma_f32_16x16x32_bf16 a[28:31], a[200:203], v[88:91], a[28:31]\n"
"v_exp_f32 v25, v25\n"
"v_mfma_f32_16x16x32_bf16 a[28:31], a[204:207], v[92:95], a[28:31]\n"
"ds_read_b64_tr_b16 a[194:195], v254 offset:53248\n"
"ds_read_b64_tr_b16 a[196:197], v254 offset:57344\n"
"v_mfma_f32_16x16x32_bf16 a[44:47], a[208:211], v[88:91], a[44:47]\n"
"v_exp_f32 v26, v26\n"
"v_mfma_f32_16x16x32_bf16 a[44:47], a[212:215], v[92:95], a[44:47]\n"
"ds_read_b64_tr_b16 a[198:199], v254 offset:61440\n"
"v_cvt_pk_bf16_f32 v84, v24, v25\n"
"v_mfma_f32_16x16x32_bf16 a[60:63], a[216:219], v[88:91], a[60:63]\n"
"s_add_u32 s80, s55, 4\n"
"v_exp_f32 v27, v27\n"
"v_mfma_f32_16x16x32_bf16 a[60:63], a[220:223], v[92:95], a[60:63]\n"
"s_and_b32 s79, s80, 1\n"
"ds_read_b64_tr_b16 a[200:201], v255 offset:49152\n"
"ds_read_b64_tr_b16 a[202:203], v255 offset:53248\n"
"v_mfma_f32_16x16x32_bf16 a[76:79], a[224:227], v[88:91], a[76:79]\n"
"s_lshl_b32 s76, s79, 5\n"
"v_exp_f32 v28, v28\n"
"v_mfma_f32_16x16x32_bf16 a[76:79], a[228:231], v[92:95], a[76:79]\n"
"s_and_b32 s79, s80, 2\n"
"ds_read_b64_tr_b16 a[204:205], v255 offset:57344\n"
"v_cvt_pk_bf16_f32 v85, v26, v27\n"
"v_smfmac_f32_16x16x64_bf16 v[140:143], v[228:231], v[88:95], v239\n"
"ds_read_b64_tr_b16 a[206:207], v255 offset:61440\n"
"v_mfma_f32_16x16x32_bf16 a[92:95], a[232:235], v[88:91], a[92:95]\n"
"s_lshl_b32 s79, s79, 8\n"
"v_exp_f32 v29, v29\n"
"v_mfma_f32_16x16x32_bf16 a[92:95], a[236:239], v[92:95], a[92:95]\n"
"s_or_b32 s76, s76, s79\n"
"ds_read_b64_tr_b16 a[208:209], v254 offset:49664\n"
"ds_read_b64_tr_b16 a[210:211], v254 offset:53760\n"
"v_mfma_f32_16x16x32_bf16 a[108:111], a[240:243], v[88:91], a[108:111]\n"
"s_and_b32 s79, s80, 4\n"
"v_exp_f32 v30, v30\n"
"v_mfma_f32_16x16x32_bf16 a[108:111], a[244:247], v[92:95], a[108:111]\n"
"s_lshl_b32 s79, s79, 10\n"
"ds_read_b64_tr_b16 a[212:213], v254 offset:57856\n"
"v_cvt_pk_bf16_f32 v86, v28, v29\n"
"v_mfma_f32_16x16x32_bf16 a[124:127], a[248:251], v[88:91], a[124:127]\n"
"v_exp_f32 v31, v31\n"
"v_mfma_f32_16x16x32_bf16 a[124:127], a[252:255], v[92:95], a[124:127]\n"
"s_or_b32 s76, s76, s79\n"
"ds_read_b64_tr_b16 a[214:215], v254 offset:61952\n"
"ds_read_b64_tr_b16 a[216:217], v255 offset:49664\n"
"v_mfma_f32_16x16x32_bf16 v[16:19], a[128:131], v[160:163], v[120:123]\n"
"v_exp_f32 v32, v32\n"
"v_mfma_f32_16x16x32_bf16 v[16:19], a[132:135], v[164:167], v[16:19]\n"
"ds_read_b64_tr_b16 a[218:219], v255 offset:53760\n"
"v_cvt_pk_bf16_f32 v87, v30, v31\n"
"v_mfma_f32_16x16x32_bf16 v[16:19], a[136:139], v[64:67], v[16:19]\n"
"v_exp_f32 v33, v33\n"
"v_mfma_f32_16x16x32_bf16 v[16:19], a[140:143], v[68:71], v[16:19]\n"
"ds_read_b64_tr_b16 a[220:221], v255 offset:57856\n"
"ds_read_b64_tr_b16 a[222:223], v255 offset:61952\n"
"v_mfma_f32_16x16x32_bf16 v[20:23], a[144:147], v[160:163], v[120:123]\n"
"v_exp_f32 v34, v34\n"
"v_mfma_f32_16x16x32_bf16 v[20:23], a[148:151], v[164:167], v[20:23]\n"
"ds_read_b64_tr_b16 a[224:225], v254 offset:50176\n"
"v_cvt_pk_bf16_f32 v88, v32, v33\n"
"v_mfma_f32_16x16x32_bf16 v[20:23], a[152:155], v[64:67], v[20:23]\n"
"v_exp_f32 v35, v35\n"
"v_mfma_f32_16x16x32_bf16 v[20:23], a[156:159], v[68:71], v[20:23]\n"
"ds_read_b64_tr_b16 a[226:227], v254 offset:54272\n"
"ds_read_b64_tr_b16 a[228:229], v254 offset:58368\n"
"v_mfma_f32_16x16x32_bf16 v[24:27], a[160:163], v[160:163], v[120:123]\n"
"v_exp_f32 v36, v36\n"
"v_mfma_f32_16x16x32_bf16 v[24:27], a[164:167], v[164:167], v[24:27]\n"
"ds_read_b64_tr_b16 a[230:231], v254 offset:62464\n"
"v_cvt_pk_bf16_f32 v89, v34, v35\n"
"v_mfma_f32_16x16x32_bf16 v[24:27], a[168:171], v[64:67], v[24:27]\n"
"v_exp_f32 v37, v37\n"
"v_mfma_f32_16x16x32_bf16 v[24:27], a[172:175], v[68:71], v[24:27]\n"
"ds_read_b64_tr_b16 a[232:233], v255 offset:50176\n"
"ds_read_b64_tr_b16 a[234:235], v255 offset:54272\n"
"v_smfmac_f32_16x16x64_bf16 v[128:131], v[228:231], v[80:87], v239\n"
"ds_read_b64_tr_b16 a[236:237], v255 offset:58368\n"
"v_mfma_f32_16x16x32_bf16 v[28:31], a[176:179], v[160:163], v[120:123]\n"
"v_exp_f32 v38, v38\n"
"v_mfma_f32_16x16x32_bf16 v[28:31], a[180:183], v[164:167], v[28:31]\n"
"ds_read_b64_tr_b16 a[238:239], v255 offset:62464\n"
"v_cvt_pk_bf16_f32 v90, v36, v37\n"
"v_mfma_f32_16x16x32_bf16 v[28:31], a[184:187], v[64:67], v[28:31]\n"
"s_waitcnt lgkmcnt(15)\n"
"v_exp_f32 v39, v39\n"
"v_mfma_f32_16x16x32_bf16 v[28:31], a[188:191], v[68:71], v[28:31]\n"
"ds_read_b64_tr_b16 a[240:241], v254 offset:50688\n"
"ds_read_b64_tr_b16 a[242:243], v254 offset:54784\n"
"v_mfma_f32_16x16x32_bf16 a[0:3], a[192:195], v[80:83], a[0:3]\n"
"v_exp_f32 v40, v40\n"
"v_mfma_f32_16x16x32_bf16 a[0:3], a[196:199], v[84:87], a[0:3]\n"
"ds_read_b64_tr_b16 a[244:245], v254 offset:58880\n"
"v_cvt_pk_bf16_f32 v91, v38, v39\n"
"v_mfma_f32_16x16x32_bf16 a[16:19], a[200:203], v[80:83], a[16:19]\n"
"s_waitcnt lgkmcnt(11)\n"
"v_exp_f32 v41, v41\n"
"v_mfma_f32_16x16x32_bf16 a[16:19], a[204:207], v[84:87], a[16:19]\n"
"ds_read_b64_tr_b16 a[246:247], v254 offset:62976\n"
"ds_read_b64_tr_b16 a[248:249], v255 offset:50688\n"
"v_mfma_f32_16x16x32_bf16 a[32:35], a[208:211], v[80:83], a[32:35]\n"
"v_exp_f32 v42, v42\n"
"v_mfma_f32_16x16x32_bf16 a[32:35], a[212:215], v[84:87], a[32:35]\n"
"ds_read_b64_tr_b16 a[250:251], v255 offset:54784\n"
"v_cvt_pk_bf16_f32 v92, v40, v41\n"
"v_mfma_f32_16x16x32_bf16 a[48:51], a[216:219], v[80:83], a[48:51]\n"
"s_waitcnt lgkmcnt(6)\n"
"v_exp_f32 v43, v43\n"
"v_mfma_f32_16x16x32_bf16 a[48:51], a[220:223], v[84:87], a[48:51]\n"
"ds_read_b64_tr_b16 a[252:253], v255 offset:58880\n"
"ds_read_b64_tr_b16 a[254:255], v255 offset:62976\n"
"v_mfma_f32_16x16x32_bf16 a[64:67], a[224:227], v[80:83], a[64:67]\n"
"s_xor_b32 s90, s90, 0x10000\n"
"v_exp_f32 v44, v44\n"
"v_mfma_f32_16x16x32_bf16 a[64:67], a[228:231], v[84:87], a[64:67]\n"
"s_add_u32 m0, s90, 0\n"
"v_cvt_pk_bf16_f32 v93, v42, v43\n"
"v_mfma_f32_16x16x32_bf16 a[80:83], a[232:235], v[80:83], a[80:83]\n"
"s_waitcnt lgkmcnt(4)\n"
"v_exp_f32 v45, v45\n"
"v_mfma_f32_16x16x32_bf16 a[80:83], a[236:239], v[84:87], a[80:83]\n"
"buffer_load_dwordx4 v244, s[32:35], s89 offen nt lds\n"
"v_mfma_f32_16x16x32_bf16 a[96:99], a[240:243], v[80:83], a[96:99]\n"
"s_waitcnt lgkmcnt(0)\n"
"v_exp_f32 v46, v46\n"
"v_mfma_f32_16x16x32_bf16 a[96:99], a[244:247], v[84:87], a[96:99]\n"
"v_cvt_pk_bf16_f32 v94, v44, v45\n"
"v_xor_b32 v235, s76, v4\n"
"v_mfma_f32_16x16x32_bf16 a[112:115], a[248:251], v[80:83], a[112:115]\n"
"v_exp_f32 v47, v47\n"
"v_mfma_f32_16x16x32_bf16 a[112:115], a[252:255], v[84:87], a[112:115]\n"
"buffer_load_dwordx4 v245, s[32:35], s89 offen offset:1024 nt lds\n"
"v_mfma_f32_16x16x32_bf16 v[32:35], a[128:131], v[168:171], v[124:127]\n"
"v_exp_f32 v16, v16\n"
"v_mfma_f32_16x16x32_bf16 v[32:35], a[132:135], v[172:175], v[32:35]\n"
"ds_read_b128 a[128:131], v250 offset:32768\n"
"v_cvt_pk_bf16_f32 v95, v46, v47\n"
"v_mfma_f32_16x16x32_bf16 v[32:35], a[136:139], v[72:75], v[32:35]\n"
"v_exp_f32 v17, v17\n"
"v_mfma_f32_16x16x32_bf16 v[32:35], a[140:143], v[76:79], v[32:35]\n"
"ds_read_b128 a[132:135], v251 offset:32768\n"
"ds_read_b128 a[136:139], v252 offset:32768\n"
"v_mfma_f32_16x16x32_bf16 v[36:39], a[144:147], v[168:171], v[124:127]\n"
"v_exp_f32 v18, v18\n"
"v_mfma_f32_16x16x32_bf16 v[36:39], a[148:151], v[172:175], v[36:39]\n"
"ds_read_b128 a[140:143], v253 offset:32768\n"
"v_cvt_pk_bf16_f32 v80, v16, v17\n"
"v_mfma_f32_16x16x32_bf16 v[36:39], a[152:155], v[72:75], v[36:39]\n"
"v_exp_f32 v19, v19\n"
"v_mfma_f32_16x16x32_bf16 v[36:39], a[156:159], v[76:79], v[36:39]\n"
"ds_read_b128 a[144:147], v250 offset:36864\n"
"ds_read_b128 a[148:151], v251 offset:36864\n"
"v_mfma_f32_16x16x32_bf16 v[40:43], a[160:163], v[168:171], v[124:127]\n"
"v_exp_f32 v20, v20\n"
"v_mfma_f32_16x16x32_bf16 v[40:43], a[164:167], v[172:175], v[40:43]\n"
"ds_read_b128 a[152:155], v252 offset:36864\n"
"v_cvt_pk_bf16_f32 v81, v18, v19\n"
"v_mfma_f32_16x16x32_bf16 v[40:43], a[168:171], v[72:75], v[40:43]\n"
"v_exp_f32 v21, v21\n"
"v_mfma_f32_16x16x32_bf16 v[40:43], a[172:175], v[76:79], v[40:43]\n"
"ds_read_b128 a[156:159], v253 offset:36864\n"
"ds_read_b128 a[160:163], v250 offset:40960\n"
"v_mfma_f32_16x16x32_bf16 v[44:47], a[176:179], v[168:171], v[124:127]\n"
"v_exp_f32 v22, v22\n"
"v_mfma_f32_16x16x32_bf16 v[44:47], a[180:183], v[172:175], v[44:47]\n"
"ds_read_b128 a[164:167], v251 offset:40960\n"
"v_cvt_pk_bf16_f32 v82, v20, v21\n"
"v_mfma_f32_16x16x32_bf16 v[44:47], a[184:187], v[72:75], v[44:47]\n"
"v_exp_f32 v23, v23\n"
"v_mfma_f32_16x16x32_bf16 v[44:47], a[188:191], v[76:79], v[44:47]\n"
"ds_read_b128 a[168:171], v252 offset:40960\n"
"ds_read_b128 a[172:175], v253 offset:40960\n"
"v_mfma_f32_16x16x32_bf16 a[4:7], a[192:195], v[88:91], a[4:7]\n"
"v_exp_f32 v24, v24\n"
"v_mfma_f32_16x16x32_bf16 a[4:7], a[196:199], v[92:95], a[4:7]\n"
"ds_read_b128 a[176:179], v250 offset:45056\n"
"v_cvt_pk_bf16_f32 v83, v22, v23\n"
"v_mfma_f32_16x16x32_bf16 a[20:23], a[200:203], v[88:91], a[20:23]\n"
"v_exp_f32 v25, v25\n"
"v_mfma_f32_16x16x32_bf16 a[20:23], a[204:207], v[92:95], a[20:23]\n"
"ds_read_b128 a[180:183], v251 offset:45056\n"
"ds_read_b128 a[184:187], v252 offset:45056\n"
"v_mfma_f32_16x16x32_bf16 a[36:39], a[208:211], v[88:91], a[36:39]\n"
"v_exp_f32 v26, v26\n"
"v_mfma_f32_16x16x32_bf16 a[36:39], a[212:215], v[92:95], a[36:39]\n"
"ds_read_b128 a[188:191], v253 offset:45056\n"
"v_cvt_pk_bf16_f32 v84, v24, v25\n"
"v_mfma_f32_16x16x32_bf16 a[52:55], a[216:219], v[88:91], a[52:55]\n"
"v_exp_f32 v27, v27\n"
"v_mfma_f32_16x16x32_bf16 a[52:55], a[220:223], v[92:95], a[52:55]\n"
"buffer_load_dwordx4 v246, s[32:35], s89 offen offset:2048 nt lds\n"
"v_mfma_f32_16x16x32_bf16 a[68:71], a[224:227], v[88:91], a[68:71]\n"
"v_exp_f32 v28, v28\n"
"v_mfma_f32_16x16x32_bf16 a[68:71], a[228:231], v[92:95], a[68:71]\n"
"v_cvt_pk_bf16_f32 v85, v26, v27\n"
"v_mfma_f32_16x16x32_bf16 a[84:87], a[232:235], v[88:91], a[84:87]\n"
"v_exp_f32 v29, v29\n"
"v_mfma_f32_16x16x32_bf16 a[84:87], a[236:239], v[92:95], a[84:87]\n"
"buffer_load_dwordx4 v247, s[32:35], s89 offen offset:3072 nt lds\n"
"v_smfmac_f32_16x16x64_bf16 v[132:135], v[228:231], v[88:95], v239\n"
"v_mfma_f32_16x16x32_bf16 a[100:103], a[240:243], v[88:91], a[100:103]\n"
"s_sub_u32 s82, s89, 16384\n"
"v_exp_f32 v30, v30\n"
"v_mfma_f32_16x16x32_bf16 a[100:103], a[244:247], v[92:95], a[100:103]\n"
"s_add_u32 m0, s90, 16384\n"
"v_cvt_pk_bf16_f32 v86, v28, v29\n"
"v_mfma_f32_16x16x32_bf16 a[116:119], a[248:251], v[88:91], a[116:119]\n"
"s_waitcnt lgkmcnt(0)\n"
"v_exp_f32 v31, v31\n"
"v_mfma_f32_16x16x32_bf16 a[116:119], a[252:255], v[92:95], a[116:119]\n"
"buffer_load_dwordx4 v248, s[36:39], s82 offen nt lds\n"
"v_mfma_f32_16x16x32_bf16 v[16:19], a[128:131], v[144:147], v[112:115]\n"
"v_exp_f32 v32, v32\n"
"v_mfma_f32_16x16x32_bf16 v[16:19], a[132:135], v[148:151], v[16:19]\n"
"v_cvt_pk_bf16_f32 v87, v30, v31\n"
"v_xor_b32 v254, 0x10000, v254\n"
"v_mfma_f32_16x16x32_bf16 v[16:19], a[136:139], v[48:51], v[16:19]\n"
"v_exp_f32 v33, v33\n"
"v_mfma_f32_16x16x32_bf16 v[16:19], a[140:143], v[52:55], v[16:19]\n"
"buffer_load_dwordx4 v249, s[36:39], s82 offen offset:1024 nt lds\n"
"v_mfma_f32_16x16x32_bf16 v[20:23], a[144:147], v[144:147], v[112:115]\n"
"v_exp_f32 v34, v34\n"
"v_mfma_f32_16x16x32_bf16 v[20:23], a[148:151], v[148:151], v[20:23]\n"
"v_cvt_pk_bf16_f32 v88, v32, v33\n"
"v_xor_b32 v255, 0x10000, v255\n"
"v_mfma_f32_16x16x32_bf16 v[20:23], a[152:155], v[48:51], v[20:23]\n"
"v_exp_f32 v35, v35\n"
"v_mfma_f32_16x16x32_bf16 v[20:23], a[156:159], v[52:55], v[20:23]\n"
"buffer_load_dwordx4 v248, s[36:39], s82 offen offset:2048 nt lds\n"
"v_mfma_f32_16x16x32_bf16 v[24:27], a[160:163], v[144:147], v[112:115]\n"
"v_exp_f32 v36, v36\n"
"v_mfma_f32_16x16x32_bf16 v[24:27], a[164:167], v[148:151], v[24:27]\n"
"v_cvt_pk_bf16_f32 v89, v34, v35\n"
"v_xor_b32 v250, 0x10000, v250\n"
"v_mfma_f32_16x16x32_bf16 v[24:27], a[168:171], v[48:51], v[24:27]\n"
"v_exp_f32 v37, v37\n"
"v_mfma_f32_16x16x32_bf16 v[24:27], a[172:175], v[52:55], v[24:27]\n"
"buffer_load_dwordx4 v249, s[36:39], s82 offen offset:3072 nt lds\n"
"v_mfma_f32_16x16x32_bf16 v[28:31], a[176:179], v[144:147], v[112:115]\n"
"v_exp_f32 v38, v38\n"
"v_mfma_f32_16x16x32_bf16 v[28:31], a[180:183], v[148:151], v[28:31]\n"
"v_cvt_pk_bf16_f32 v90, v36, v37\n"
"v_readlane_b32 s53, v233, s55\n"
"v_mfma_f32_16x16x32_bf16 v[28:31], a[184:187], v[48:51], v[28:31]\n"
"v_exp_f32 v39, v39\n"
"v_mfma_f32_16x16x32_bf16 v[28:31], a[188:191], v[52:55], v[28:31]\n"
"v_mfma_f32_16x16x32_bf16 a[8:11], a[192:195], v[80:83], a[8:11]\n"
"v_exp_f32 v40, v40\n"
"v_mfma_f32_16x16x32_bf16 a[8:11], a[196:199], v[84:87], a[8:11]\n"
"v_cvt_pk_bf16_f32 v91, v38, v39\n"
"v_xor_b32 v251, 0x10000, v251\n"
"v_mfma_f32_16x16x32_bf16 a[24:27], a[200:203], v[80:83], a[24:27]\n"
"v_exp_f32 v41, v41\n"
"v_mfma_f32_16x16x32_bf16 a[24:27], a[204:207], v[84:87], a[24:27]\n"
"buffer_store_dwordx4 v[240:243], v1, s[40:43], s53 offen sc1\n"
"v_mfma_f32_16x16x32_bf16 a[40:43], a[208:211], v[80:83], a[40:43]\n"
"v_exp_f32 v42, v42\n"
"v_mfma_f32_16x16x32_bf16 a[40:43], a[212:215], v[84:87], a[40:43]\n"
"s_add_u32 s89, s89, 16384\n"
"v_cvt_pk_bf16_f32 v92, v40, v41\n"
"v_xor_b32 v252, 0x10000, v252\n"
"v_mfma_f32_16x16x32_bf16 a[56:59], a[216:219], v[80:83], a[56:59]\n"
"s_add_u32 s55, s55, 1\n"
"v_exp_f32 v43, v43\n"
"v_mfma_f32_16x16x32_bf16 a[56:59], a[220:223], v[84:87], a[56:59]\n"
"v_mfma_f32_16x16x32_bf16 a[72:75], a[224:227], v[80:83], a[72:75]\n"
"s_add_u32 s56, s56, 1024\n"
"v_exp_f32 v44, v44\n"
"v_mfma_f32_16x16x32_bf16 a[72:75], a[228:231], v[84:87], a[72:75]\n"
"v_cvt_pk_bf16_f32 v93, v42, v43\n"
"v_xor_b32 v253, 0x10000, v253\n"
"v_mfma_f32_16x16x32_bf16 a[88:91], a[232:235], v[80:83], a[88:91]\n"
"v_exp_f32 v45, v45\n"
"v_mfma_f32_16x16x32_bf16 a[88:91], a[236:239], v[84:87], a[88:91]\n"
"v_smfmac_f32_16x16x64_bf16 v[136:139], v[228:231], v[80:87], v239\n"
"v_mfma_f32_16x16x32_bf16 a[104:107], a[240:243], v[80:83], a[104:107]\n"
"v_exp_f32 v46, v46\n"
"v_mfma_f32_16x16x32_bf16 a[104:107], a[244:247], v[84:87], a[104:107]\n"
"v_cvt_pk_bf16_f32 v94, v44, v45\n"
"v_mfma_f32_16x16x32_bf16 a[120:123], a[248:251], v[80:83], a[120:123]\n"
"v_exp_f32 v47, v47\n"
"v_mfma_f32_16x16x32_bf16 a[120:123], a[252:255], v[84:87], a[120:123]\n"
"s_cmp_lg_u32 s55, 29\n"
"s_cbranch_scc1 L_att1_5_%=\n"
"s_branch L_attdone_50_%=\n"
"L_retile1x_6_%=:\n"
"s_mov_b64 s[28:29], s[60:61]\n"
"s_mov_b64 s[30:31], s[62:63]\n"
"v_mov_b32 v234, v238\n"
"s_branch L_retile1xb_7_%=\n"
"L_slow_1_%=:\n"
"s_mov_b32 s81, 400000\n"
"L_spin_3_%=:\n"
"s_sleep 1\n"
"buffer_load_dwordx2 v[10:11], v15, s[40:43], s23 offen sc1\n"
"s_waitcnt vmcnt(0)\n"
"v_cmp_ne_u64 vcc, s[92:93], v[10:11]\n"
"s_nop 1\n"
"s_cbranch_vccz L_back_2_%=\n"
"s_sub_u32 s81, s81, 1\n"
"s_cbranch_scc0 L_spin_3_%=\n"
"s_branch L_back_2_%=\n"
"L_retile2_6_%=:\n"
"s_mov_b64 s[28:29], s[60:61]\n"
"s_mov_b64 s[30:31], s[62:63]\n"
"v_mov_b32 v234, v238\n"
"s_branch L_retile2b_7_%=\n"
"L_slow_8_%=:\n"
"s_mov_b32 s81, 400000\n"
"L_spin_10_%=:\n"
"s_sleep 1\n"
"buffer_load_dwordx2 v[10:11], v15, s[40:43], s23 offen sc1\n"
"s_waitcnt vmcnt(0)\n"
"v_cmp_ne_u64 vcc, s[94:95], v[10:11]\n"
"s_nop 1\n"
"s_cbranch_vccz L_back_9_%=\n"
"s_sub_u32 s81, s81, 1\n"
"s_cbranch_scc0 L_spin_10_%=\n"
"s_branch L_back_9_%=\n"
"L_fallback_11_%=:\n"
"s_waitcnt vmcnt(0) lgkmcnt(0)\n"
"s_mov_b32 s22, 1\n"
"s_mov_b32 s89, s66\n"
"s_mov_b32 s88, 32\n"
"v_mov_b32 v112, 0xff800000\n"
"v_mov_b32 v116, 0xff800000\n"
"v_mov_b32 v120, 0xff800000\n"
"v_mov_b32 v124, 0xff800000\n"
"L_mloop_12_%=:\n"
"s_barrier\n"
"s_add_u32 m0, s66, 65536\n"
"s_nop 0\n"
"buffer_load_dwordx4 v244, s[32:35], s89 offen lds\n"
"buffer_load_dwordx4 v245, s[32:35], s89 offen offset:1024 lds\n"
"buffer_load_dwordx4 v246, s[32:35], s89 offen offset:2048 lds\n"
"buffer_load_dwordx4 v247, s[32:35], s89 offen offset:3072 lds\n"
"s_waitcnt vmcnt(0)\n"
"s_barrier\n"
"ds_read_b128 a[128:131], v250\n"
"ds_read_b128 a[132:135], v251\n"
"ds_read_b128 a[136:139], v252\n"
"ds_read_b128 a[140:143], v253\n"
"ds_read_b128 a[144:147], v250 offset:4096\n"
"ds_read_b128 a[148:151], v251 offset:4096\n"
"ds_read_b128 a[152:155], v252 offset:4096\n"
"ds_read_b128 a[156:159], v253 offset:4096\n"
"ds_read_b128 a[160:163], v250 offset:8192\n"
"ds_read_b128 a[164:167], v251 offset:8192\n"
"ds_read_b128 a[168:171], v252 offset:8192\n"
"ds_read_b128 a[172:175], v253 offset:8192\n"
"ds_read_b128 a[176:179], v250 offset:12288\n"
"ds_read_b128 a[180:183], v251 offset:12288\n"
"ds_read_b128 a[184:187], v252 offset:12288\n"
"ds_read_b128 a[188:191], v253 offset:12288\n"
"s_waitcnt lgkmcnt(0)\n"
"v_mfma_f32_16x16x32_bf16 v[16:19], a[128:131], v[144:147], 0\n"
"v_mfma_f32_16x16x32_bf16 v[16:19], a[132:135], v[148:151], v[16:19]\n"
"v_mfma_f32_16x16x32_bf16 v[16:19], a[136:139], v[48:51], v[16:19]\n"
"v_mfma_f32_16x16x32_bf16 v[16:19], a[140:143], v[52:55], v[16:19]\n"
"v_mfma_f32_16x16x32_bf16 v[20:23], a[144:147], v[144:147], 0\n"
"v_mfma_f32_16x16x32_bf16 v[20:23], a[148:151], v[148:151], v[20:23]\n"
"v_mfma_f32_16x16x32_bf16 v[20:23], a[152:155], v[48:51], v[20:23]\n"
"v_mfma_f32_16x16x32_bf16 v[20:23], a[156:159], v[52:55], v[20:23]\n"
"v_mfma_f32_16x16x32_bf16 v[24:27], a[160:163], v[144:147], 0\n"
"v_mfma_f32_16x16x32_bf16 v[24:27], a[164:167], v[148:151], v[24:27]\n"
"v_mfma_f32_16x16x32_bf16 v[24:27], a[168:171], v[48:51], v[24:27]\n"
"v_mfma_f32_16x16x32_bf16 v[24:27], a[172:175], v[52:55], v[24:27]\n"
"v_mfma_f32_16x16x32_bf16 v[28:31], a[176:179], v[144:147], 0\n"
"v_mfma_f32_16x16x32_bf16 v[28:31], a[180:183], v[148:151], v[28:31]\n"
"v_mfma_f32_16x16x32_bf16 v[28:31], a[184:187], v[48:51], v[28:31]\n"
"v_mfma_f32_16x16x32_bf16 v[28:31], a[188:191], v[52:55], v[28:31]\n"
"s_nop 7\n"
"v_max3_f32 v112, v112, v16, v17\n"
"v_max3_f32 v112, v112, v18, v19\n"
"v_max3_f32 v112, v112, v20, v21\n"
"v_max3_f32 v112, v112, v22, v23\n"
"v_max3_f32 v112, v112, v24, v25\n"
"v_max3_f32 v112, v112, v26, v27\n"
"v_max3_f32 v112, v112, v28, v29\n"
"v_max3_f32 v112, v112, v30, v31\n"
"s_nop 1\n"
"v_mfma_f32_16x16x32_bf16 v[32:35], a[128:131], v[152:155], 0\n"
"v_mfma_f32_16x16x32_bf16 v[32:35], a[132:135], v[156:159], v[32:35]\n"
"v_mfma_f32_16x16x32_bf16 v[32:35], a[136:139], v[56:59], v[32:35]\n"
"v_mfma_f32_16x16x32_bf16 v[32:35], a[140:143], v[60:63], v[32:35]\n"
"v_mfma_f32_16x16x32_bf16 v[36:39], a[144:147], v[152:155], 0\n"
"v_mfma_f32_16x16x32_bf16 v[36:39], a[148:151], v[156:159], v[36:39]\n"
"v_mfma_f32_16x16x32_bf16 v[36:39], a[152:155], v[56:59], v[36:39]\n"
"v_mfma_f32_16x16x32_bf16 v[36:39], a[156:159], v[60:63], v[36:39]\n"
"v_mfma_f32_16x16x32_bf16 v[40:43], a[160:163], v[152:155], 0\n"
"v_mfma_f32_16x16x32_bf16 v[40:43], a[164:167], v[156:159], v[40:43]\n"
"v_mfma_f32_16x16x32_bf16 v[40:43], a[168:171], v[56:59], v[40:43]\n"
"v_mfma_f32_16x16x32_bf16 v[40:43], a[172:175], v[60:63], v[40:43]\n"
"v_mfma_f32_16x16x32_bf16 v[44:47], a[176:179], v[152:155], 0\n"
"v_mfma_f32_16x16x32_bf16 v[44:47], a[180:183], v[156:159], v[44:47]\n"
"v_mfma_f32_16x16x32_bf16 v[44:47], a[184:187], v[56:59], v[44:47]\n"
"v_mfma_f32_16x16x32_bf16 v[44:47], a[188:191], v[60:63], v[44:47]\n"
"s_nop 7\n"
"v_max3_f32 v116, v116, v32, v33\n"
"v_max3_f32 v116, v116, v34, v35\n"
"v_max3_f32 v116, v116, v36, v37\n"
"v_max3_f32 v116, v116, v38, v39\n"
"v_max3_f32 v116, v116, v40, v41\n"
"v_max3_f32 v116, v116, v42, v43\n"
"v_max3_f32 v116, v116, v44, v45\n"
"v_max3_f32 v116, v116, v46, v47\n"
"s_nop 1\n"
"v_mfma_f32_16x16x32_bf16 v[16:19], a[128:131], v[160:163], 0\n"
"v_mfma_f32_16x16x32_bf16 v[16:19], a[132:135], v[164:167], v[16:19]\n"
"v_mfma_f32_16x16x32_bf16 v[16:19], a[136:139], v[64:67], v[16:19]\n"
"v_mfma_f32_16x16x32_bf16 v[16:19], a[140:143], v[68:71], v[16:19]\n"
"v_mfma_f32_16x16x32_bf16 v[20:23], a[144:147], v[160:163], 0\n"
"v_mfma_f32_16x16x32_bf16 v[20:23], a[148:151], v[164:167], v[20:23]\n"
"v_mfma_f32_16x16x32_bf16 v[20:23], a[152:155], v[64:67], v[20:23]\n"
"v_mfma_f32_16x16x32_bf16 v[20:23], a[156:159], v[68:71], v[20:23]\n"
"v_mfma_f32_16x16x32_bf16 v[24:27], a[160:163], v[160:163], 0\n"
"v_mfma_f32_16x16x32_bf16 v[24:27], a[164:167], v[164:167], v[24:27]\n"
"v_mfma_f32_16x16x32_bf16 v[24:27], a[168:171], v[64:67], v[24:27]\n"
"v_mfma_f32_16x16x32_bf16 v[24:27], a[172:175], v[68:71], v[24:27]\n"
"v_mfma_f32_16x16x32_bf16 v[28:31], a[176:179], v[160:163], 0\n"
"v_mfma_f32_16x16x32_bf16 v[28:31], a[180:183], v[164:167], v[28:31]\n"
"v_mfma_f32_16x16x32_bf16 v[28:31], a[184:187], v[64:67], v[28:31]\n"
"v_mfma_f32_16x16x32_bf16 v[28:31], a[188:191], v[68:71], v[28:31]\n"
"s_nop 7\n"
"v_max3_f32 v120, v120, v16, v17\n"
"v_max3_f32 v120, v120, v18, v19\n"
"v_max3_f32 v120, v120, v20, v21\n"
"v_max3_f32 v120, v120, v22, v23\n"
"v_max3_f32 v120, v120, v24, v25\n"
"v_max3_f32 v120, v120, v26, v27\n"
"v_max3_f32 v120, v120, v28, v29\n"
"v_max3_f32 v120, v120, v30, v31\n"
"s_nop 1\n"
"v_mfma_f32_16x16x32_bf16 v[32:35], a[128:131], v[168:171], 0\n"
"v_mfma_f32_16x16x32_bf16 v[32:35], a[132:135], v[172:175], v[32:35]\n"
"v_mfma_f32_16x16x32_bf16 v[32:35], a[136:139], v[72:75], v[32:35]\n"
"v_mfma_f32_16x16x32_bf16 v[32:35], a[140:143], v[76:79], v[32:35]\n"
"v_mfma_f32_16x16x32_bf16 v[36:39], a[144:147], v[168:171], 0\n"
"v_mfma_f32_16x16x32_bf16 v[36:39], a[148:151], v[172:175], v[36:39]\n"
"v_mfma_f32_16x16x32_bf16 v[36:39], a[152:155], v[72:75], v[36:39]\n"
"v_mfma_f32_16x16x32_bf16 v[36:39], a[156:159], v[76:79], v[36:39]\n"
"v_mfma_f32_16x16x32_bf16 v[40:43], a[160:163], v[168:171], 0\n"
"v_mfma_f32_16x16x32_bf16 v[40:43], a[164:167], v[172:175], v[40:43]\n"
"v_mfma_f32_16x16x32_bf16 v[40:43], a[168:171], v[72:75], v[40:43]\n"
"v_mfma_f32_16x16x32_bf16 v[40:43], a[172:175], v[76:79], v[40:43]\n"
"v_mfma_f32_16x16x32_bf16 v[44:47], a[176:179], v[168:171], 0\n"
"v_mfma_f32_16x16x32_bf16 v[44:47], a[180:183], v[172:175], v[44:47]\n"
"v_mfma_f32_16x16x32_bf16 v[44:47], a[184:187], v[72:75], v[44:47]\n"
"v_mfma_f32_16x16x32_bf16 v[44:47], a[188:191], v[76:79], v[44:47]\n"
"s_nop 7\n"
"v_max3_f32 v124, v124, v32, v33\n"
"v_max3_f32 v124, v124, v34, v35\n"
"v_max3_f32 v124, v124, v36, v37\n"
"v_max3_f32 v124, v124, v38, v39\n"
"v_max3_f32 v124, v124, v40, v41\n"
"v_max3_f32 v124, v124, v42, v43\n"
"v_max3_f32 v124, v124, v44, v45\n"
"v_max3_f32 v124, v124, v46, v47\n"
"s_nop 1\n"
"s_add_u32 s89, s89, 16384\n"
"s_sub_u32 s88, s88, 1\n"
"s_cmp_lg_u32 s88, 0\n"
"s_cbranch_scc1 L_mloop_12_%=\n"
"v_mov_b32 v113, v112\n"
"s_nop 1\n"
"v_permlane16_swap_b32 v112, v113\n"
"v_max_f32 v112, v112, v113\n"
"v_mov_b32 v113, v112\n"
"s_nop 1\n"
"v_permlane32_swap_b32 v112, v113\n"
"v_max_f32 v112, v112, v113\n"
"v_sub_f32 v113, 0, v112\n"
"v_sub_f32 v114, 0, v112\n"
"v_sub_f32 v115, 0, v112\n"
"v_sub_f32 v112, 0, v112\n"
"v_mov_b32 v117, v116\n"
"s_nop 1\n"
"v_permlane16_swap_b32 v116, v117\n"
"v_max_f32 v116, v116, v117\n"
"v_mov_b32 v117, v116\n"
"s_nop 1\n"
"v_permlane32_swap_b32 v116, v117\n"
"v_max_f32 v116, v116, v117\n"
"v_sub_f32 v117, 0, v116\n"
"v_sub_f32 v118, 0, v116\n"
"v_sub_f32 v119, 0, v116\n"
"v_sub_f32 v116, 0, v116\n"
"v_mov_b32 v121, v120\n"
"s_nop 1\n"
"v_permlane16_swap_b32 v120, v121\n"
"v_max_f32 v120, v120, v121\n"
"v_mov_b32 v121, v120\n"
"s_nop 1\n"
"v_permlane32_swap_b32 v120, v121\n"
"v_max_f32 v120, v120, v121\n"
"v_sub_f32 v121, 0, v120\n"
"v_sub_f32 v122, 0, v120\n"
"v_sub_f32 v123, 0, v120\n"
"v_sub_f32 v120, 0, v120\n"
"v_mov_b32 v125, v124\n"
"s_nop 1\n"
"v_permlane16_swap_b32 v124, v125\n"
"v_max_f32 v124, v124, v125\n"
"v_mov_b32 v125, v124\n"
"s_nop 1\n"
"v_permlane32_swap_b32 v124, v125\n"
"v_max_f32 v124, v124, v125\n"
"v_sub_f32 v125, 0, v124\n"
"v_sub_f32 v126, 0, v124\n"
"v_sub_f32 v127, 0, v124\n"
"v_sub_f32 v124, 0, v124\n"
"v_and_b32 v8, 3, v0\n"
"v_cmp_gt_u32 vcc, 2, v8\n"
"v_mov_b32 v9, 0x3f803f80\n"
"s_nop 0\n"
"v_cndmask_b32 v228, 0, v9, vcc\n"
"v_mov_b32 v229, v228\n"
"v_mov_b32 v230, v228\n"
"v_mov_b32 v231, v228\n"
"v_cmp_eq_u32 vcc, 0, v8\n"
"v_mov_b32 v9, 0x44444444\n"
"v_mov_b32 v10, 0xeeeeeeee\n"
"v_cndmask_b32 v239, v10, v9, vcc\n"
"s_barrier\n"
"s_add_u32 s82, s66, 0\n"
"s_add_u32 m0, s66, 65536\n"
"s_nop 0\n"
"buffer_load_dwordx4 v244, s[32:35], s82 offen lds\n"
"buffer_load_dwordx4 v245, s[32:35], s82 offen offset:1024 lds\n"
"buffer_load_dwordx4 v246, s[32:35], s82 offen offset:2048 lds\n"
"buffer_load_dwordx4 v247, s[32:35], s82 offen offset:3072 lds\n"
"s_add_u32 s82, s66, 16384\n"
"s_add_u32 m0, s66, 98304\n"
"s_nop 0\n"
"buffer_load_dwordx4 v244, s[32:35], s82 offen lds\n"
"buffer_load_dwordx4 v245, s[32:35], s82 offen offset:1024 lds\n"
"buffer_load_dwordx4 v246, s[32:35], s82 offen offset:2048 lds\n"
"buffer_load_dwordx4 v247, s[32:35], s82 offen offset:3072 lds\n"
"s_add_u32 s82, s66, 0\n"
"s_add_u32 m0, s66, 114688\n"
"s_nop 0\n"
"buffer_load_dwordx4 v248, s[36:39], s82 offen lds\n"
"buffer_load_dwordx4 v249, s[36:39], s82 offen offset:1024 lds\n"
"buffer_load_dwordx4 v248, s[36:39], s82 offen offset:2048 lds\n"
"buffer_load_dwordx4 v249, s[36:39], s82 offen offset:3072 lds\n"
"s_add_u32 s82, s66, 32768\n"
"s_add_u32 m0, s66, 0\n"
"s_nop 0\n"
"buffer_load_dwordx4 v244, s[32:35], s82 offen lds\n"
"buffer_load_dwordx4 v245, s[32:35], s82 offen offset:1024 lds\n"
"buffer_load_dwordx4 v246, s[32:35], s82 offen offset:2048 lds\n"
"buffer_load_dwordx4 v247, s[32:35], s82 offen offset:3072 lds\n"
"s_add_u32 s82, s66, 16384\n"
"s_add_u32 m0, s66, 16384\n"
"s_nop 0\n"
"buffer_load_dwordx4 v248, s[36:39], s82 offen lds\n"
"buffer_load_dwordx4 v249, s[36:39], s82 offen offset:1024 lds\n"
"buffer_load_dwordx4 v248, s[36:39], s82 offen offset:2048 lds\n"
"buffer_load_dwordx4 v249, s[36:39], s82 offen offset:3072 lds\n"
"s_add_u32 s82, s66, 49152\n"
"s_add_u32 m0, s66, 32768\n"
"s_nop 0\n"
"buffer_load_dwordx4 v244, s[32:35], s82 offen lds\n"
"buffer_load_dwordx4 v245, s[32:35], s82 offen offset:1024 lds\n"
"buffer_load_dwordx4 v246, s[32:35], s82 offen offset:2048 lds\n"
"buffer_load_dwordx4 v247, s[32:35], s82 offen offset:3072 lds\n"
"s_add_u32 s82, s66, 32768\n"
"s_add_u32 m0, s66, 49152\n"
"s_nop 0\n"
"buffer_load_dwordx4 v248, s[36:39], s82 offen lds\n"
"buffer_load_dwordx4 v249, s[36:39], s82 offen offset:1024 lds\n"
"buffer_load_dwordx4 v248, s[36:39], s82 offen offset:2048 lds\n"
"buffer_load_dwordx4 v249, s[36:39], s82 offen offset:3072 lds\n"
"s_waitcnt vmcnt(0)\n"
"s_barrier\n"
"ds_read_b128 a[128:131], v250\n"
"ds_read_b128 a[132:135], v251\n"
"ds_read_b128 a[136:139], v252\n"
"ds_read_b128 a[140:143], v253\n"
"ds_read_b128 a[144:147], v250 offset:4096\n"
"ds_read_b128 a[148:151], v251 offset:4096\n"
"ds_read_b128 a[152:155], v252 offset:4096\n"
"ds_read_b128 a[156:159], v253 offset:4096\n"
"ds_read_b128 a[160:163], v250 offset:8192\n"
"ds_read_b128 a[164:167], v251 offset:8192\n"
"ds_read_b128 a[168:171], v252 offset:8192\n"
"ds_read_b128 a[172:175], v253 offset:8192\n"
"ds_read_b128 a[176:179], v250 offset:12288\n"
"ds_read_b128 a[180:183], v251 offset:12288\n"
"ds_read_b128 a[184:187], v252 offset:12288\n"
"ds_read_b128 a[188:191], v253 offset:12288\n"
"s_waitcnt vmcnt(0) lgkmcnt(0)\n"
"s_nop 1\n"
"s_branch L_attn_4_%=\n"
"L_slow_13_%=:\n"
"s_mov_b32 s81, 400000\n"
"L_spin_15_%=:\n"
"s_sleep 1\n"
"buffer_load_dwordx2 v[10:11], v15, s[40:43], s23 offen sc1\n"
"s_waitcnt vmcnt(0)\n"
"v_cmp_ne_u64 vcc, s[94:95], v[10:11]\n"
"s_nop 1\n"
"s_cbranch_vccz L_back_14_%=\n"
"s_sub_u32 s81, s81, 1\n"
"s_cbranch_scc0 L_spin_15_%=\n"
"s_branch L_back_14_%=\n"
"L_slow_16_%=:\n"
"s_mov_b32 s81, 400000\n"
"L_spin_18_%=:\n"
"s_sleep 1\n"
"buffer_load_dwordx2 v[10:11], v15, s[40:43], s23 offen sc1\n"
"s_waitcnt vmcnt(0)\n"
"v_cmp_ne_u64 vcc, s[92:93], v[10:11]\n"
"s_nop 1\n"
"s_cbranch_vccz L_back_17_%=\n"
"s_sub_u32 s81, s81, 1\n"
"s_cbranch_scc0 L_spin_18_%=\n"
"s_branch L_back_17_%=\n"
"L_slow_20_%=:\n"
"s_mov_b32 s81, 400000\n"
"L_spin_22_%=:\n"
"s_sleep 1\n"
"buffer_load_dwordx2 v[10:11], v15, s[40:43], s23 offen sc1\n"
"s_waitcnt vmcnt(0)\n"
"v_cmp_ne_u64 vcc, s[92:93], v[10:11]\n"
"s_nop 1\n"
"s_cbranch_vccz L_back_21_%=\n"
"s_sub_u32 s81, s81, 1\n"
"s_cbranch_scc0 L_spin_22_%=\n"
"s_branch L_back_21_%=\n"
"L_slow_23_%=:\n"
"s_mov_b32 s81, 400000\n"
"L_spin_25_%=:\n"
"s_sleep 1\n"
"buffer_load_dwordx2 v[10:11], v15, s[40:43], s23 offen sc1\n"
"s_waitcnt vmcnt(0)\n"
"v_cmp_ne_u64 vcc, s[92:93], v[10:11]\n"
"s_nop 1\n"
"s_cbranch_vccz L_back_24_%=\n"
"s_sub_u32 s81, s81, 1\n"
"s_cbranch_scc0 L_spin_25_%=\n"
"s_branch L_back_24_%=\n"
"L_slow_26_%=:\n"
"s_mov_b32 s81, 400000\n"
"L_spin_28_%=:\n"
"s_sleep 1\n"
"buffer_load_dwordx2 v[10:11], v15, s[40:43], s23 offen sc1\n"
"s_waitcnt vmcnt(0)\n"
"v_cmp_ne_u64 vcc, s[92:93], v[10:11]\n"
"s_nop 1\n"
"s_cbranch_vccz L_back_27_%=\n"
"s_sub_u32 s81, s81, 1\n"
"s_cbranch_scc0 L_spin_28_%=\n"
"s_branch L_back_27_%=\n"
"L_slow_47_%=:\n"
"s_mov_b32 s81, 400000\n"
"L_spin_49_%=:\n"
"s_sleep 4\n"
"buffer_load_dwordx2 v[10:11], v15, s[40:43], s23 offen sc1\n"
"s_waitcnt vmcnt(0)\n"
"v_cmp_ne_u64 vcc, s[92:93], v[10:11]\n"
"s_nop 1\n"
"s_cbranch_vccz L_back_48_%=\n"
"s_sub_u32 s81, s81, 1\n"
"s_cbranch_scc0 L_spin_49_%=\n"
"s_branch L_back_48_%=\n"
"L_late_29_%=:\n"
"s_sleep 1\n"
"buffer_load_dwordx2 v[10:11], v15, s[40:43], s23 offen sc1\n"
"s_waitcnt vmcnt(0)\n"
"v_cmp_ne_u64 vcc, s[92:93], v[10:11]\n"
"s_nop 0\n"
"s_mov_b32 s56, vcc_lo\n"
"s_sub_u32 s81, s81, 1\n"
"s_cselect_b32 s56, 0, s56\n"
"s_andn2_b32 s80, s54, s56\n"
"s_cbranch_scc0 L_late_29_%=\n"
"L_late2_46_%=:\n"
"s_bitcmp1_b32 s80, 0\n"
"s_cbranch_scc0 L_skip_39_%=\n"
"s_add_u32 s79, s101, 3\n"
"s_and_b32 s79, s79, 3\n"
"s_lshl_b32 s79, s79, 11\n"
"s_add_u32 s79, s79, s52\n"
"s_add_u32 s79, s79, 8192\n"
"buffer_load_dwordx4 v[16:19], v1, s[40:43], s79 offen sc1\n"
"buffer_load_dwordx4 v[20:23], v1, s[40:43], s79 offen offset:1024 sc1\n"
"s_add_u32 s79, s79, 16384\n"
"buffer_load_dwordx4 v[24:27], v1, s[40:43], s79 offen sc1\n"
"buffer_load_dwordx4 v[28:31], v1, s[40:43], s79 offen offset:1024 sc1\n"
"L_skip_39_%=:\n"
"s_bitcmp1_b32 s80, 1\n"
"s_cbranch_scc0 L_skip_40_%=\n"
"s_add_u32 s79, s101, 2\n"
"s_and_b32 s79, s79, 3\n"
"s_lshl_b32 s79, s79, 11\n"
"s_add_u32 s79, s79, s52\n"
"s_add_u32 s79, s79, 8192\n"
"buffer_load_dwordx4 v[32:35], v1, s[40:43], s79 offen sc1\n"
"buffer_load_dwordx4 v[36:39], v1, s[40:43], s79 offen offset:1024 sc1\n"
"s_add_u32 s79, s79, 16384\n"
"buffer_load_dwordx4 v[40:43], v1, s[40:43], s79 offen sc1\n"
"buffer_load_dwordx4 v[44:47], v1, s[40:43], s79 offen offset:1024 sc1\n"
"L_skip_40_%=:\n"
"s_bitcmp1_b32 s80, 2\n"
"s_cbranch_scc0 L_skip_41_%=\n"
"s_add_u32 s79, s101, 1\n"
"s_and_b32 s79, s79, 3\n"
"s_lshl_b32 s79, s79, 11\n"
"s_add_u32 s79, s79, s52\n"
"s_add_u32 s79, s79, 8192\n"
"buffer_load_dwordx4 v[48:51], v1, s[40:43], s79 offen sc1\n"
"buffer_load_dwordx4 v[52:55], v1, s[40:43], s79 offen offset:1024 sc1\n"
"s_add_u32 s79, s79, 16384\n"
"buffer_load_dwordx4 v[56:59], v1, s[40:43], s79 offen sc1\n"
"buffer_load_dwordx4 v[60:63], v1, s[40:43], s79 offen offset:1024 sc1\n"
"L_skip_41_%=:\n"
"s_bitcmp1_b32 s80, 3\n"
"s_cbranch_scc0 L_skip_42_%=\n"
"s_add_u32 s79, s101, 3\n"
"s_and_b32 s79, s79, 3\n"
"s_lshl_b32 s79, s79, 11\n"
"s_add_u32 s79, s79, s52\n"
"s_add_u32 s79, s79, 0\n"
"buffer_load_dwordx4 v[64:67], v1, s[40:43], s79 offen sc1\n"
"buffer_load_dwordx4 v[68:71], v1, s[40:43], s79 offen offset:1024 sc1\n"
"s_add_u32 s79, s79, 16384\n"
"buffer_load_dwordx4 v[72:75], v1, s[40:43], s79 offen sc1\n"
"buffer_load_dwordx4 v[76:79], v1, s[40:43], s79 offen offset:1024 sc1\n"
"L_skip_42_%=:\n"
"s_bitcmp1_b32 s80, 4\n"
"s_cbranch_scc0 L_skip_43_%=\n"
"s_add_u32 s79, s101, 2\n"
"s_and_b32 s79, s79, 3\n"
"s_lshl_b32 s79, s79, 11\n"
"s_add_u32 s79, s79, s52\n"
"s_add_u32 s79, s79, 0\n"
"buffer_load_dwordx4 v[144:147], v1, s[40:43], s79 offen sc1\n"
"buffer_load_dwordx4 v[148:151], v1, s[40:43], s79 offen offset:1024 sc1\n"
"s_add_u32 s79, s79, 16384\n"
"buffer_load_dwordx4 v[152:155], v1, s[40:43], s79 offen sc1\n"
"buffer_load_dwordx4 v[156:159], v1, s[40:43], s79 offen offset:1024 sc1\n"
"L_skip_43_%=:\n"
"s_bitcmp1_b32 s80, 5\n"
"s_cbranch_scc0 L_skip_44_%=\n"
"s_add_u32 s79, s101, 0\n"
"s_and_b32 s79, s79, 3\n"
"s_lshl_b32 s79, s79, 11\n"
"s_add_u32 s79, s79, s52\n"
"s_add_u32 s79, s79, 8192\n"
"buffer_load_dwordx4 v[160:163], v1, s[40:43], s79 offen sc1\n"
"buffer_load_dwordx4 v[164:167], v1, s[40:43], s79 offen offset:1024 sc1\n"
"s_add_u32 s79, s79, 16384\n"
"buffer_load_dwordx4 v[168:171], v1, s[40:43], s79 offen sc1\n"
"buffer_load_dwordx4 v[172:175], v1, s[40:43], s79 offen offset:1024 sc1\n"
"L_skip_44_%=:\n"
"s_bitcmp1_b32 s80, 6\n"
"s_cbranch_scc0 L_skip_45_%=\n"
"s_add_u32 s79, s101, 1\n"
"s_and_b32 s79, s79, 3\n"
"s_lshl_b32 s79, s79, 11\n"
"s_add_u32 s79, s79, s52\n"
"s_add_u32 s79, s79, 0\n"
"buffer_load_dwordx4 v[176:179], v1, s[40:43], s79 offen sc1\n"
"buffer_load_dwordx4 v[180:183], v1, s[40:43], s79 offen offset:1024 sc1\n"
"s_add_u32 s79, s79, 16384\n"
"buffer_load_dwordx4 v[184:187], v1, s[40:43], s79 offen sc1\n"
"buffer_load_dwordx4 v[188:191], v1, s[40:43], s79 offen offset:1024 sc1\n"
"L_skip_45_%=:\n"
"s_andn2_b32 s54, s54, s80\n"
"s_waitcnt vmcnt(0)\n"
"s_bitcmp1_b32 s80, 0\n"
"s_cbranch_scc0 L_skip_32_%=\n"
"v_lshlrev_b32 v12, 16, v16\n"
"v_and_b32 v13, 0xffff0000, v16\n"
"v_add_f32 v224, v224, v12\n"
"v_add_f32 v225, v225, v13\n"
"v_lshlrev_b32 v12, 16, v17\n"
"v_and_b32 v13, 0xffff0000, v17\n"
"v_add_f32 v226, v226, v12\n"
"v_add_f32 v227, v227, v13\n"
"v_lshlrev_b32 v12, 16, v18\n"
"v_and_b32 v13, 0xffff0000, v18\n"
"v_add_f32 v228, v228, v12\n"
"v_add_f32 v229, v229, v13\n"
"v_lshlrev_b32 v12, 16, v19\n"
"v_and_b32 v13, 0xffff0000, v19\n"
"v_add_f32 v230, v230, v12\n"
"v_add_f32 v231, v231, v13\n"
"v_lshlrev_b32 v12, 16, v20\n"
"v_and_b32 v13, 0xffff0000, v20\n"
"v_add_f32 v232, v232, v12\n"
"v_add_f32 v233, v233, v13\n"
"v_lshlrev_b32 v12, 16, v21\n"
"v_and_b32 v13, 0xffff0000, v21\n"
"v_add_f32 v234, v234, v12\n"
"v_add_f32 v235, v235, v13\n"
"v_lshlrev_b32 v12, 16, v22\n"
"v_and_b32 v13, 0xffff0000, v22\n"
"v_add_f32 v236, v236, v12\n"
"v_add_f32 v237, v237, v13\n"
"v_lshlrev_b32 v12, 16, v23\n"
"v_and_b32 v13, 0xffff0000, v23\n"
"v_add_f32 v238, v238, v12\n"
"v_add_f32 v239, v239, v13\n"
"v_lshlrev_b32 v12, 16, v24\n"
"v_and_b32 v13, 0xffff0000, v24\n"
"v_add_f32 v240, v240, v12\n"
"v_add_f32 v241, v241, v13\n"
"v_lshlrev_b32 v12, 16, v25\n"
"v_and_b32 v13, 0xffff0000, v25\n"
"v_add_f32 v242, v242, v12\n"
"v_add_f32 v243, v243, v13\n"
"v_lshlrev_b32 v12, 16, v26\n"
"v_and_b32 v13, 0xffff0000, v26\n"
"v_add_f32 v244, v244, v12\n"
"v_add_f32 v245, v245, v13\n"
"v_lshlrev_b32 v12, 16, v27\n"
"v_and_b32 v13, 0xffff0000, v27\n"
"v_add_f32 v246, v246, v12\n"
"v_add_f32 v247, v247, v13\n"
"v_lshlrev_b32 v12, 16, v28\n"
"v_and_b32 v13, 0xffff0000, v28\n"
"v_add_f32 v248, v248, v12\n"
"v_add_f32 v249, v249, v13\n"
"v_lshlrev_b32 v12, 16, v29\n"
"v_and_b32 v13, 0xffff0000, v29\n"
"v_add_f32 v250, v250, v12\n"
"v_add_f32 v251, v251, v13\n"
"v_lshlrev_b32 v12, 16, v30\n"
"v_and_b32 v13, 0xffff0000, v30\n"
"v_add_f32 v252, v252, v12\n"
"v_add_f32 v253, v253, v13\n"
"v_lshlrev_b32 v12, 16, v31\n"
"v_and_b32 v13, 0xffff0000, v31\n"
"v_add_f32 v254, v254, v12\n"
"v_add_f32 v255, v255, v13\n"
"s_xor_b32 s59, s59, 1\n"
"L_skip_32_%=:\n"
"s_bitcmp1_b32 s80, 1\n"
"s_cbranch_scc0 L_skip_33_%=\n"
"v_lshlrev_b32 v12, 16, v32\n"
"v_and_b32 v13, 0xffff0000, v32\n"
"v_add_f32 v224, v224, v12\n"
"v_add_f32 v225, v225, v13\n"
"v_lshlrev_b32 v12, 16, v33\n"
"v_and_b32 v13, 0xffff0000, v33\n"
"v_add_f32 v226, v226, v12\n"
"v_add_f32 v227, v227, v13\n"
"v_lshlrev_b32 v12, 16, v34\n"
"v_and_b32 v13, 0xffff0000, v34\n"
"v_add_f32 v228, v228, v12\n"
"v_add_f32 v229, v229, v13\n"
"v_lshlrev_b32 v12, 16, v35\n"
"v_and_b32 v13, 0xffff0000, v35\n"
"v_add_f32 v230, v230, v12\n"
"v_add_f32 v231, v231, v13\n"
"v_lshlrev_b32 v12, 16, v36\n"
"v_and_b32 v13, 0xffff0000, v36\n"
"v_add_f32 v232, v232, v12\n"
"v_add_f32 v233, v233, v13\n"
"v_lshlrev_b32 v12, 16, v37\n"
"v_and_b32 v13, 0xffff0000, v37\n"
"v_add_f32 v234, v234, v12\n"
"v_add_f32 v235, v235, v13\n"
"v_lshlrev_b32 v12, 16, v38\n"
"v_and_b32 v13, 0xffff0000, v38\n"
"v_add_f32 v236, v236, v12\n"
"v_add_f32 v237, v237, v13\n"
"v_lshlrev_b32 v12, 16, v39\n"
"v_and_b32 v13, 0xffff0000, v39\n"
"v_add_f32 v238, v238, v12\n"
"v_add_f32 v239, v239, v13\n"
"v_lshlrev_b32 v12, 16, v40\n"
"v_and_b32 v13, 0xffff0000, v40\n"
"v_add_f32 v240, v240, v12\n"
"v_add_f32 v241, v241, v13\n"
"v_lshlrev_b32 v12, 16, v41\n"
"v_and_b32 v13, 0xffff0000, v41\n"
"v_add_f32 v242, v242, v12\n"
"v_add_f32 v243, v243, v13\n"
"v_lshlrev_b32 v12, 16, v42\n"
"v_and_b32 v13, 0xffff0000, v42\n"
"v_add_f32 v244, v244, v12\n"
"v_add_f32 v245, v245, v13\n"
"v_lshlrev_b32 v12, 16, v43\n"
"v_and_b32 v13, 0xffff0000, v43\n"
"v_add_f32 v246, v246, v12\n"
"v_add_f32 v247, v247, v13\n"
"v_lshlrev_b32 v12, 16, v44\n"
"v_and_b32 v13, 0xffff0000, v44\n"
"v_add_f32 v248, v248, v12\n"
"v_add_f32 v249, v249, v13\n"
"v_lshlrev_b32 v12, 16, v45\n"
"v_and_b32 v13, 0xffff0000, v45\n"
"v_add_f32 v250, v250, v12\n"
"v_add_f32 v251, v251, v13\n"
"v_lshlrev_b32 v12, 16, v46\n"
"v_and_b32 v13, 0xffff0000, v46\n"
"v_add_f32 v252, v252, v12\n"
"v_add_f32 v253, v253, v13\n"
"v_lshlrev_b32 v12, 16, v47\n"
"v_and_b32 v13, 0xffff0000, v47\n"
"v_add_f32 v254, v254, v12\n"
"v_add_f32 v255, v255, v13\n"
"s_xor_b32 s59, s59, 2\n"
"L_skip_33_%=:\n"
"s_branch L_accum_30_%=\n"
"L_end_%=:\n"
    :
    : [ka] "s"(__builtin_amdgcn_kernarg_segment_ptr()), [blk] "s"(blockIdx.x), [wid] "s"(wid), [lds] "s"(ldsb)
    :
      "v0","v1","v2","v3","v4","v5","v6","v7","v8","v9","v10","v11","v12","v13","v14","v15","v16","v17","v18","v19","v20","v21","v22","v23","v24","v25","v26","v27","v28","v29","v30","v31",
      "v32","v33","v34","v35","v36","v37","v38","v39","v40","v41","v42","v43","v44","v45","v46","v47","v48","v49","v50","v51","v52","v53","v54","v55","v56","v57","v58","v59","v60","v61","v62","v63",
      "v64","v65","v66","v67","v68","v69","v70","v71","v72","v73","v74","v75","v76","v77","v78","v79","v80","v81","v82","v83","v84","v85","v86","v87","v88","v89","v90","v91","v92","v93","v94","v95",
      "v96","v97","v98","v99","v100","v101","v102","v103","v104","v105","v106","v107","v108","v109","v110","v111","v112","v113","v114","v115","v116","v117","v118","v119","v120","v121","v122","v123","v124","v125","v126","v127",
      "v128","v129","v130","v131","v132","v133","v134","v135","v136","v137","v138","v139","v140","v141","v142","v143","v144","v145","v146","v147","v148","v149","v150","v151","v152","v153","v154","v155","v156","v157","v158","v159",
      "v160","v161","v162","v163","v164","v165","v166","v167","v168","v169","v170","v171","v172","v173","v174","v175","v176","v177","v178","v179","v180","v181","v182","v183","v184","v185","v186","v187","v188","v189","v190","v191",
      "v192","v193","v194","v195","v196","v197","v198","v199","v200","v201","v202","v203","v204","v205","v206","v207","v208","v209","v210","v211","v212","v213","v214","v215","v216","v217","v218","v219","v220","v221","v222","v223",
      "v224","v225","v226","v227","v228","v229","v230","v231","v232","v233","v234","v235","v236","v237","v238","v239","v240","v241","v242","v243","v244","v245","v246","v247","v248","v249","v250","v251","v252","v253","v254","v255",
      "a0","a1","a2","a3","a4","a5","a6","a7","a8","a9","a10","a11","a12","a13","a14","a15","a16","a17","a18","a19","a20","a21","a22","a23","a24","a25","a26","a27","a28","a29","a30","a31",
      "a32","a33","a34","a35","a36","a37","a38","a39","a40","a41","a42","a43","a44","a45","a46","a47","a48","a49","a50","a51","a52","a53","a54","a55","a56","a57","a58","a59","a60","a61","a62","a63",
      "a64","a65","a66","a67","a68","a69","a70","a71","a72","a73","a74","a75","a76","a77","a78","a79","a80","a81","a82","a83","a84","a85","a86","a87","a88","a89","a90","a91","a92","a93","a94","a95",
      "a96","a97","a98","a99","a100","a101","a102","a103","a104","a105","a106","a107","a108","a109","a110","a111","a112","a113","a114","a115","a116","a117","a118","a119","a120","a121","a122","a123","a124","a125","a126","a127",
      "a128","a129","a130","a131","a132","a133","a134","a135","a136","a137","a138","a139","a140","a141","a142","a143","a144","a145","a146","a147","a148","a149","a150","a151","a152","a153","a154","a155","a156","a157","a158","a159",
      "a160","a161","a162","a163","a164","a165","a166","a167","a168","a169","a170","a171","a172","a173","a174","a175","a176","a177","a178","a179","a180","a181","a182","a183","a184","a185","a186","a187","a188","a189","a190","a191",
      "a192","a193","a194","a195","a196","a197","a198","a199","a200","a201","a202","a203","a204","a205","a206","a207","a208","a209","a210","a211","a212","a213","a214","a215","a216","a217","a218","a219","a220","a221","a222","a223",
      "a224","a225","a226","a227","a228","a229","a230","a231","a232","a233","a234","a235","a236","a237","a238","a239","a240","a241","a242","a243","a244","a245","a246","a247","a248","a249","a250","a251","a252","a253","a254","a255",
      "s16","s17","s18","s19","s20","s21","s22","s23","s24","s25","s26","s27","s28","s29","s30","s31","s32","s33","s34","s35","s36","s37","s38","s39","s40","s41","s42","s43","s44","s45","s46","s47",
      "s48","s49","s50","s51","s52","s53","s54","s55","s56","s57","s58","s59","s60","s61","s62","s63","s64","s65","s66","s67","s68","s69","s70","s71","s72","s73","s74","s75","s76","s77","s78","s79",
      "s80","s81","s82","s83","s84","s85","s86","s87","s88","s89","s90","s91","s92","s93","s94","s95","s96","s97","s98","s99","s100","s101","vcc","scc","memory"

  );
}

extern "C" void kernel_launch(void* const* d_in, const int* in_sizes, int n_in, void* d_out, int out_size, void* d_ws, size_t ws_size, hipStream_t stream) {
  fused_block_kernel<<<256, 256, 0, stream>>>(d_in[0], d_ws, d_in[6], d_in[7], d_in[3], d_in[1], d_in[2], d_out, d_in[4], d_in[8]);
}
```
